```python
import torch

def _softmax(x, axis=-1):
    return torch.softmax(x, dim=axis)
def _gelu(x):
    return torch.nn.functional.gelu(x, approximate="tanh")
def _silu(x):
    return torch.nn.functional.silu(x)
def _sigmoid(x):
    return torch.sigmoid(x)
def _layernorm(x, axis=-1, eps=1e-5):
    m = x.mean(dim=axis, keepdim=True)
    v = x.var(dim=axis, keepdim=True, unbiased=False)
    return (x - m) / torch.sqrt(v + eps)
def _scatter_add(tgt, idx, src, axis=0):
    return tgt.clone().index_add_(axis, idx, src)
def _segment_sum(src, seg, n):
    out = torch.zeros((n,) + src.shape[1:], dtype=src.dtype)
    return out.index_add_(0, seg, src)
def _pad(x, pw):
    flat = tuple(v for pair in reversed(pw) for v in pair)
    return torch.nn.functional.pad(x, flat)

kernel_name = 'sequential_preLN__compressed_kv_cla__mixer_token_mlp__prompt'

def setup_inputs():

    _w = (lambda a: (lambda t: t.to(torch.bfloat16) if t.is_floating_point() else t)(torch.as_tensor(a)))
    def _A_setup():
        B, H, Sq, Sk, D, M = 1, 32, 2048, 2048, 128, 512
        Dc = 4 * D
        return {
            "x": _w((lambda *s: torch.randn(*s, dtype=torch.bfloat16) * 0.05)(B, Sq, M)),
            "c_kv": _w((lambda *s: torch.randn(*s, dtype=torch.bfloat16) * 0.2)(B, Sk, Dc)),
            "w_q": _w((lambda *s: torch.randn(*s, dtype=torch.bfloat16) * 3.2)(M, H, D) * (M ** -0.5)),
            "w_uk": _w((lambda *s: torch.randn(*s, dtype=torch.bfloat16) * 6.4)(Dc, H, D) * (Dc ** -0.5)),
            "w_uv": _w((lambda *s: torch.randn(*s, dtype=torch.bfloat16) * 0.2)(Dc, H, D) * (Dc ** -0.5)),
            "w_o": _w((lambda *s: torch.randn(*s, dtype=torch.bfloat16) * 0.2)(H, D, M) * ((H * D) ** -0.5)),
        }
    _A_in = _A_setup()

    def _B_setup():
        B, S, D, F = 1, 2048, 512, 1792
        return {
            "x": _w((lambda *s: torch.randn(*s, dtype=torch.bfloat16) * 0.05)(B, S, D)),
            "w1": _w((lambda *s: torch.randn(*s, dtype=torch.bfloat16) * 0.4)(S, F) * (S ** -0.5)),
            "b1": _w((lambda *s: torch.randn(*s, dtype=torch.bfloat16) * 3.2)(F) * 0.02),
            "w2": _w((lambda *s: torch.randn(*s, dtype=torch.bfloat16) * 0.2)(F, S) * (F ** -0.5)),
            "b2": _w((lambda *s: torch.randn(*s, dtype=torch.bfloat16) * 0.4)(S) * 0.02),
        }
    _B_in = _B_setup()

    return {
        "A_x": _A_in["x"],
        "A_c_kv": _A_in["c_kv"],
        "A_w_q": _A_in["w_q"],
        "A_w_uk": _A_in["w_uk"],
        "A_w_uv": _A_in["w_uv"],
        "A_w_o": _A_in["w_o"],
        "B_x": _B_in["x"],
        "B_w1": _B_in["w1"],
        "B_b1": _B_in["b1"],
        "B_w2": _B_in["w2"],
        "B_b2": _B_in["b2"],
    }

def reference(A_x, A_c_kv, A_w_q, A_w_uk, A_w_uv, A_w_o, B_x, B_w1, B_b1, B_w2, B_b2):
    def _A_ref(x, c_kv, w_q, w_uk, w_uv, w_o):
        B, Sq, M = x.shape[0], x.shape[1], x.shape[2]
        Sk = c_kv.shape[1]
        H, D = w_q.shape[1], w_q.shape[2]
        q = torch.einsum("bqm,mhd->bhqd", x, w_q)
        k = torch.einsum("bkc,chd->bhkd", c_kv, w_uk)
        v = torch.einsum("bkc,chd->bhkd", c_kv, w_uv)
        scores = torch.einsum("bhqd,bhkd->bhqk", q, k) * (D ** -0.5)
        off = Sk - Sq
        qi = torch.arange(Sq)[:, None]
        kj = torch.arange(Sk)[None, :]
        cmask = kj <= (qi + off)
        scores = torch.where(cmask[None, None], scores, -1e9)
        p = _softmax(scores, axis=-1)
        ctx = torch.einsum("bhqk,bhkd->bhqd", p, v)
        return torch.einsum("bhqd,hdm->bqm", ctx, w_o)

    def _B_ref(x, w1, b1, w2, b2):
        B, S, D = x.shape
        h = _layernorm(x, axis=-1)
        u = torch.einsum("bsd,sf->bdf", h, w1) + b1
        u = _gelu(u)
        y = torch.einsum("bdf,fs->bds", u, w2) + b2
        return (lambda x, *d: x.permute(*d))(y, 0, 2, 1)

    x = A_x
    h = x + _A_ref(_layernorm(x, axis=-1), A_c_kv, A_w_q, A_w_uk, A_w_uv, A_w_o)
    h2 = h + _B_ref(_layernorm(h, axis=-1), B_w1, B_b1, B_w2, B_b2)
    return h2
```

```cpp
#include <hip/hip_runtime.h>
#include <hip/hip_bf16.h>
typedef unsigned short u16;
__device__ __forceinline__ float bf2f(u16 v){ return __uint_as_float(((unsigned)v)<<16); }
__device__ __forceinline__ u16 f2bf(float f){ __hip_bfloat16 b = __float2bfloat16(f); return *(u16*)&b; }

__global__ void ln_kernel(const u16* x, u16* y, int twice){
  int row = blockIdx.x; int t = threadIdx.x;
  float v[8]; float s=0;
  for(int i=0;i<8;i++){ v[i]=bf2f(x[row*512+t*8+i]); }
  for(int r=0;r<=twice;r++){
    s=0; for(int i=0;i<8;i++) s+=v[i];
    for(int o=32;o;o>>=1) s+=__shfl_xor(s,o);
    float m=s/512.f; float q=0;
    for(int i=0;i<8;i++){ float d=v[i]-m; q+=d*d; }
    for(int o=32;o;o>>=1) q+=__shfl_xor(q,o);
    float rs=1.0f/sqrtf(q/512.f+1e-5f);
    for(int i=0;i<8;i++) v[i]=(v[i]-m)*rs;
  }
  for(int i=0;i<8;i++) y[row*512+t*8+i]=f2bf(v[i]);
}
__global__ void gemm_kernel(const u16* A, int sam, int sak, const u16* B, int N, int K, u16* C, int mode, const u16* aux, const u16* aux2){
  __shared__ float As[16][65], Bs[16][65];
  int tx=threadIdx.x&15, ty=threadIdx.x>>4;
  int m0=blockIdx.y*64, n0=blockIdx.x*64;
  float acc[4][4]={};
  for(int k0=0;k0<K;k0+=16){
    for(int i=threadIdx.x;i<1024;i+=256){
      int kk, mm;
      if(sak==1){ kk=i&15; mm=i>>4; } else { mm=i&63; kk=i>>6; }
      As[kk][mm]=bf2f(A[(long)(m0+mm)*sam+(long)(k0+kk)*sak]);
      int nn=i&63, k2=i>>6;
      Bs[k2][nn]=bf2f(B[(long)(k0+k2)*N+n0+nn]);
    }
    __syncthreads();
    for(int kk=0;kk<16;kk++){
      float a[4],b[4];
      for(int i=0;i<4;i++){a[i]=As[kk][ty*4+i]; b[i]=Bs[kk][tx*4+i];}
      for(int i=0;i<4;i++)for(int j=0;j<4;j++)acc[i][j]+=a[i]*b[j];
    }
    __syncthreads();
  }
  for(int i=0;i<4;i++)for(int j=0;j<4;j++){
    int m=m0+ty*4+i, n=n0+tx*4+j; float v=acc[i][j];
    if(mode==0) C[(long)m*N+n]=f2bf(v);
    else if(mode==1) C[(long)m*N+n]=f2bf(v+bf2f(aux[(long)m*N+n]));
    else if(mode==2){ v+=bf2f(aux[n]); float t=tanhf(0.7978845608f*(v+0.044715f*v*v*v)); C[(long)m*N+n]=f2bf(0.5f*v*(1.f+t)); }
    else { v+=bf2f(aux[n]); C[(long)n*512+m]=f2bf(v+bf2f(aux2[(long)n*512+m])); }
  }
}
__global__ void attn_kernel(const u16* q, const u16* k, const u16* v, u16* ctx){
  __shared__ float p[2048]; __shared__ float qs[128];
  int row=blockIdx.x, h=blockIdx.y, t=threadIdx.x;
  qs[t]=bf2f(q[row*4096+h*128+t]); qs[t+64]=bf2f(q[row*4096+h*128+t+64]);
  __syncthreads();
  float mx=-1e30f;
  for(int j=t;j<=row;j+=64){ float s=0; const u16* kp=k+j*4096+h*128; for(int d=0;d<128;d++) s+=qs[d]*bf2f(kp[d]); s*=0.08838834764f; p[j]=s; mx=fmaxf(mx,s);}
  for(int o=32;o;o>>=1) mx=fmaxf(mx,__shfl_xor(mx,o));
  float l=0; for(int j=t;j<=row;j+=64){ float e=__expf(p[j]-mx); p[j]=e; l+=e; }
  for(int o=32;o;o>>=1) l+=__shfl_xor(l,o);
  __syncthreads();
  float a0=0,a1=0; for(int j=0;j<=row;j++){ float e=p[j]; a0+=e*bf2f(v[j*4096+h*128+t]); a1+=e*bf2f(v[j*4096+h*128+64+t]); }
  ctx[row*4096+h*128+t]=f2bf(a0/l); ctx[row*4096+h*128+64+t]=f2bf(a1/l);
}
extern "C" void kernel_launch(void* const* d_in, const int* in_sizes, int n_in, void* d_out, int out_size, void* d_ws, size_t ws_size, hipStream_t stream){
  const u16 *x=(const u16*)d_in[0], *ckv=(const u16*)d_in[1], *wq=(const u16*)d_in[2], *wuk=(const u16*)d_in[3], *wuv=(const u16*)d_in[4], *wo=(const u16*)d_in[5];
  const u16 *w1=(const u16*)d_in[7], *b1=(const u16*)d_in[8], *w2=(const u16*)d_in[9], *b2=(const u16*)d_in[10];
  u16* ws=(u16*)d_ws;
  u16 *xn=ws, *q=xn+2048*512, *k=q+2048*4096, *v=k+2048*4096, *ctx=v+2048*4096, *h=ctx+2048*4096, *hnn=h+2048*512, *u=hnn+2048*512;
  ln_kernel<<<2048,64,0,stream>>>(x,xn,0);
  gemm_kernel<<<dim3(64,32),256,0,stream>>>(xn,512,1,wq,4096,512,q,0,nullptr,nullptr);
  gemm_kernel<<<dim3(64,32),256,0,stream>>>(ckv,512,1,wuk,4096,512,k,0,nullptr,nullptr);
  gemm_kernel<<<dim3(64,32),256,0,stream>>>(ckv,512,1,wuv,4096,512,v,0,nullptr,nullptr);
  attn_kernel<<<dim3(2048,32),64,0,stream>>>(q,k,v,ctx);
  gemm_kernel<<<dim3(8,32),256,0,stream>>>(ctx,4096,1,wo,512,4096,h,1,x,nullptr);
  ln_kernel<<<2048,64,0,stream>>>(h,hnn,1);
  gemm_kernel<<<dim3(28,8),256,0,stream>>>(hnn,1,512,w1,1792,2048,u,2,b1,nullptr);
  gemm_kernel<<<dim3(32,8),256,0,stream>>>(u,1792,1,w2,2048,1792,(u16*)d_out,3,b2,h);
}
```

```cpp
#include <hip/hip_runtime.h>
#include <hip/hip_bf16.h>
typedef unsigned short u16;
__global__ __launch_bounds__(256, 1) __attribute__((aligned(4096))) void fused_block(const void* x, const void* ckv, const void* wq, const void* wuk, const void* wuv, const void* wo,
                                                      const void* w1, const void* b1, const void* w2, const void* b2, void* out, void* ws) {
  __shared__ __attribute__((aligned(1024))) char lds[163840];
  const unsigned wid = __builtin_amdgcn_readfirstlane(threadIdx.x >> 6);
  asm volatile(
  ".p2align 5\n"
  "s_load_dwordx8 s[8:15], %0, 0x0\n"
  "s_load_dwordx8 s[16:23], %0, 0x20\n"
  "s_load_dwordx8 s[24:31], %0, 0x40\n"
  "v_mbcnt_lo_u32_b32 v160, -1, 0\n"
  "v_mbcnt_hi_u32_b32 v160, -1, v160\n"
  "v_lshlrev_b32 v244, 4, v160\n"
  "s_getpc_b64 s[72:73]\n"
  "mk_pc:\n"
  "s_mov_b32 s74, mk_end-mk_pc\n"
  "s_mov_b32 s75, 0x20000\n"
  "s_lshr_b32 s59, %2, 3\n"
  "s_xor_b32 s59, s59, 28\n"
  "s_lshl_b32 s59, s59, 2\n"
  "s_add_u32 s59, s59, %1\n"
  "s_lshl_b32 s59, s59, 13\n"
  "v_lshlrev_b32 v164, 7, v160\n"
  "buffer_load_dword v252, v164, s[72:75], s59 offen\n"
  "s_and_b32 s61, %2, 7\n"
  "s_lshl_b32 s53, s61, 2\n"
  "s_bfe_u32 s59, %2, 0x20003\n"
  "s_add_u32 s53, s53, s59\n"
  "s_lshr_b32 s52, %2, 5\n"
  "s_add_u32 s54, s52, s61\n"
  "s_and_b32 s54, s54, 7\n"
  "s_lshl_b32 s58, %1, 13\n"
  "s_lshl_b32 s57, %1, 12\n"
  "s_add_u32 s57, s57, 0x18000\n"
  "v_lshrrev_b32 v161, 4, v160\n"
  "v_and_b32 v162, 15, v160\n"
  "v_lshlrev_b32 v163, 1, v161\n"
  "v_xor_b32 v162, v162, v163\n"
  "v_lshlrev_b32 v162, 4, v162\n"
  "s_lshl_b32 s59, %1, 4\n"
  "v_add_u32 v163, s59, v161\n"
  "v_lshl_add_u32 v232, v163, 13, v162\n"
  "v_add_u32 v233, 0x7c00, v232\n"
  "v_xor_b32 v234, 0x80, v232\n"
  "v_add_u32 v234, 0xf800, v234\n"
  "v_add_u32 v235, 0x7c00, v234\n"
  "v_and_b32 v162, 7, v160\n"
  "v_xor_b32 v162, v162, v161\n"
  "v_lshlrev_b32 v162, 4, v162\n"
  "v_lshrrev_b32 v163, 3, v160\n"
  "v_lshl_add_u32 v236, v163, 10, v162\n"
  "v_xor_b32 v237, 64, v236\n"
  "v_add_u32 v237, 7168, v237\n"
  "v_add_u32 v238, 14336, v236\n"
  "v_add_u32 v239, 14336, v237\n"
  "s_lshl_b32 s61, s53, 8\n"
  "s_sub_u32 s38, 0x400000, s61\n"
  "s_mov_b32 s39, 0x20000\n"
  "s_lshl_b32 s59, s52, 2\n"
  "s_add_u32 s59, s59, %1\n"
  "s_lshl_b32 s59, s59, 16\n"
  "s_mov_b32 s46, 0x10000\n"
  "s_mov_b32 s47, 0x20000\n"
  "s_mov_b32 s50, 0x8000000\n"
  "s_mov_b32 s51, 0x20000\n"
  "s_lshl_b32 s62, s52, 4\n"
  "s_add_u32 s62, s62, s53\n"
  "s_and_b32 s62, s62, 31\n"
  "s_lshl_b32 s96, s62, 21\n"
  "s_mul_i32 s63, s52, 0x44500\n"
  "s_add_u32 s96, s96, s63\n"
  "s_add_u32 s96, s96, 0x4000000\n"
  "s_andn2_b32 s96, s96, 0x2000\n"
  "s_cmp_eq_u32 %1, 0\n"
  "s_cselect_b32 s96, s96, 0x7fff0000\n"
  "s_add_u32 s97, s96, 0x2000\n"
  "v_cmp_gt_u32 vcc, 8, v160\n"
  "v_mov_b32 v165, 0x7fff0000\n"
  "s_waitcnt lgkmcnt(0)\n"
  "v_cndmask_b32 v164, v165, v244, vcc\n"
  "s_add_u32 s48, s30, 0x1fffff\n"
  "s_addc_u32 s49, s31, 0\n"
  "s_and_b32 s48, s48, 0xffe00000\n"
  "buffer_store_dwordx4 v[232:235], v164, s[48:51], s96 offen sc1\n"
  "buffer_store_dwordx4 v[232:235], v164, s[48:51], s97 offen sc1\n"
  "s_barrier\n"
  "s_add_u32 s36, s14, s61\n"
  "s_addc_u32 s37, s15, 0\n"
  "s_add_u32 s44, s10, s59\n"
  "s_addc_u32 s45, s11, 0\n"
  "s_lshl_b32 s55, s54, 19\n"
  "s_add_u32 m0, s57, 0x0\n"
  "s_nop 0\n"
  "buffer_load_dwordx4 v232, s[36:39], s55 offen offset:0 lds\n"
  "buffer_load_dwordx4 v233, s[36:39], s55 offen offset:1024 lds\n"
  "buffer_load_dwordx4 v234, s[36:39], s55 offen offset:2048 lds\n"
  "buffer_load_dwordx4 v235, s[36:39], s55 offen offset:3072 lds\n"
  "s_lshl_b32 s56, s54, 7\n"
  "s_add_u32 m0, s58, 0x0\n"
  "s_nop 0\n"
  "buffer_load_dwordx4 v236, s[44:47], s56 offen offset:0 lds\n"
  "buffer_load_dwordx4 v237, s[44:47], s56 offen offset:1024 lds\n"
  "buffer_load_dwordx4 v238, s[44:47], s56 offen offset:2048 lds\n"
  "buffer_load_dwordx4 v239, s[44:47], s56 offen offset:3072 lds\n"
  "s_cmp_lg_u32 %1, 0\n"
  "s_cbranch_scc1 mk_noprobe\n"
  "s_waitcnt vmcnt(9)\n"
  "s_memtime s[64:65]\n"
  "s_waitcnt vmcnt(8)\n"
  "s_memtime s[66:67]\n"
  "s_waitcnt lgkmcnt(0)\n"
  "s_sub_u32 s35, s66, s64\n"
  "mk_noprobe:\n"
  "s_add_u32 s56, s56, 0x8000\n"
  "s_add_u32 m0, s58, 0x1000\n"
  "s_nop 0\n"
  "buffer_load_dwordx4 v236, s[44:47], s56 offen offset:0 lds\n"
  "buffer_load_dwordx4 v237, s[44:47], s56 offen offset:1024 lds\n"
  "buffer_load_dwordx4 v238, s[44:47], s56 offen offset:2048 lds\n"
  "buffer_load_dwordx4 v239, s[44:47], s56 offen offset:3072 lds\n"
  "s_nop 0\n"
  "s_cmp_lt_u32 s35, 40\n"
  "s_cselect_b32 s35, 16, 0\n"
  "s_xor_b32 s35, s35, s53\n"
  "s_and_b32 s35, s35, 16\n"
  "s_lshl_b32 s35, s35, 4\n"
  "s_add_u32 s35, s35, 1\n"
  "s_lshl_b32 s33, s62, 7\n"
  "s_add_u32 s33, s33, 0x6638000\n"
  "s_cmp_eq_u32 %1, 0\n"
  "s_cselect_b32 s33, s33, 0x7fff0000\n"
  "s_lshl_b32 s60, s53, 20\n"
  "s_lshr_b32 s59, s59, 1\n"
  "s_add_u32 s60, s60, s59\n"
  "s_lshl_b32 s59, s52, 3\n"
  "s_add_u32 s40, s59, %1\n"
  "s_add_u32 s41, s40, 4\n"
  "s_sub_u32 s42, %1, s59\n"
  "s_add_u32 s42, s42, 120\n"
  "s_add_u32 s43, s42, 4\n"
  "s_lshl_b32 s59, s52, 5\n"
  "s_add_u32 s61, s59, s53\n"
  "s_lshl_b32 s61, s61, 8\n"
  "s_lshl_b32 s62, %1, 3\n"
  "v_add_u32 v162, s62, v160\n"
  "s_lshl_b32 s62, %1, 1\n"
  "s_add_u32 s62, s62, 24\n"
  "v_add_u32 v163, s62, v160\n"
  "v_cmp_gt_u32 vcc, 8, v160\n"
  "v_cmp_gt_u32_e64 s[64:65], 10, v160\n"
  "v_mov_b32 v165, 0x7fff0000\n"
  "v_cndmask_b32 v164, v163, v162, vcc\n"
  "v_lshlrev_b32 v164, 2, v164\n"
  "v_add_u32 v164, s61, v164\n"
  "v_cndmask_b32_e64 v249, v165, v164, s[64:65]\n"
  "v_add_u32 v162, s59, v162\n"
  "v_lshlrev_b32 v162, 8, v162\n"
  "s_lshl_b32 s61, s53, 2\n"
  "v_add_u32 v162, s61, v162\n"
  "v_subrev_u32 v163, 32, v163\n"
  "v_lshlrev_b32 v163, 13, v163\n"
  "s_lshl_b32 s61, s53, 8\n"
  "s_lshl_b32 s59, s52, 2\n"
  "s_add_u32 s61, s61, s59\n"
  "s_add_u32 s61, s61, 128\n"
  "v_add_u32 v163, s61, v163\n"
  "v_cndmask_b32_e64 v163, v165, v163, s[64:65]\n"
  "v_cndmask_b32 v250, v165, v162, vcc\n"
  "v_cndmask_b32 v251, v163, v165, vcc\n"
  "s_mov_b32 s96, 0x3200000\n"
  "s_mov_b32 s97, 0x3210000\n"
  "buffer_load_dword v248, v249, s[48:51], s96 offen sc1\n"
  "s_lshr_b32 s59, s53, 1\n"
  "s_lshl_b32 s61, s52, 3\n"
  "s_add_u32 s62, s61, s59\n"
  "s_sub_u32 s63, s59, s61\n"
  "s_add_u32 s63, s63, 112\n"
  "s_cmp_lt_u32 s59, 8\n"
  "s_cselect_b32 s62, s62, s63\n"
  "s_and_b32 s59, s53, 1\n"
  "s_lshl_b32 s59, s59, 2\n"
  "s_add_u32 s59, s59, %1\n"
  "s_lshl_b32 s62, s62, 14\n"
  "s_lshl_b32 s61, s59, 11\n"
  "s_add_u32 s61, s61, s62\n"
  "s_mov_b32 s68, s8\n"
  "s_mov_b32 s69, s9\n"
  "s_mov_b32 s70, 0x200000\n"
  "s_mov_b32 s71, 0x20000\n"
  "v_and_b32 v162, 1, v160\n"
  "v_lshrrev_b32 v163, 1, v160\n"
  "v_lshlrev_b32 v164, 4, v163\n"
  "v_lshl_add_u32 v164, v162, 10, v164\n"
  "buffer_load_dwordx4 v[128:131], v164, s[68:71], s61 offen offset:0\n"
  "buffer_load_dwordx4 v[132:135], v164, s[68:71], s61 offen offset:512\n"
  "v_and_b32 v164, 3, v163\n"
  "v_xor_b32 v164, %1, v164\n"
  "v_lshlrev_b32 v164, 5, v164\n"
  "v_lshl_add_u32 v164, v163, 7, v164\n"
  "v_lshl_add_u32 v164, v162, 4, v164\n"
  "v_add_u32 v154, 0x10000, v164\n"
  "s_lshl_b32 s61, %1, 11\n"
  "s_add_u32 s61, s61, 0x10000\n"
  "v_bfe_u32 v164, v160, 3, 2\n"
  "v_lshlrev_b32 v164, 5, v164\n"
  "v_xor_b32 v164, v164, v244\n"
  "v_add_u32 v155, s61, v164\n"
  "s_lshl_b32 s62, s62, 0\n"
  "s_and_b32 s59, s53, 1\n"
  "s_lshl_b32 s59, s59, 7\n"
  "s_add_u32 s62, s62, s59\n"
  "s_lshl_b32 s59, %1, 12\n"
  "s_add_u32 s62, s62, s59\n"
  "s_and_b32 s59, s59, 0\n"
  "s_add_u32 s62, s62, s59\n"
  "s_add_u32 s62, s62, 0x3000000\n"
  "v_and_b32 v164, 0x380, v244\n"
  "v_add_u32 v164, v164, v244\n"
  "v_add_u32 v156, s62, v164\n"
  "v_add_u32 v158, 0x2000, v156\n"
  "s_lshl_b32 s59, %2, 2\n"
  "s_add_u32 s59, s59, %1\n"
  "s_lshl_b32 s59, s59, 8\n"
  "v_cmp_gt_u32 vcc, 16, v160\n"
  "s_add_u32 s59, s59, 0x6604000\n"
  "v_add_u32 v164, s59, v244\n"
  "v_cndmask_b32 v157, v165, v164, vcc\n"
  "v_mov_b32 v168, 0\n"
  "v_mov_b32 v169, 0\n"
  "v_mov_b32 v170, 0\n"
  "v_mov_b32 v171, 0\n"
  "s_lshl_b32 s98, s53, 2\n"
  "s_add_u32 s98, s98, %1\n"
  "s_lshl_b32 s98, s98, 7\n"
  "s_add_u32 s98, s98, 0x6634000\n"
  "v_bfe_u32 v162, v160, 2, 2\n"
  "v_and_b32 v163, 3, v160\n"
  "v_lshl_add_u32 v164, v161, 3, v162\n"
  "v_lshlrev_b32 v164, 8, v164\n"
  "v_lshl_add_u32 v164, v163, 3, v164\n"
  "v_add_u32 v164, 0x18000, v164\n"
  "v_and_b32 v165, 1, v161\n"
  "v_lshl_add_u32 v165, v165, 2, v162\n"
  "v_xor_b32 v163, 0, v165\n"
  "v_lshl_add_u32 v224, v163, 5, v164\n"
  "v_xor_b32 v163, 1, v165\n"
  "v_lshl_add_u32 v225, v163, 5, v164\n"
  "v_xor_b32 v163, 2, v165\n"
  "v_lshl_add_u32 v226, v163, 5, v164\n"
  "v_xor_b32 v163, 3, v165\n"
  "v_lshl_add_u32 v227, v163, 5, v164\n"
  "v_xor_b32 v163, 4, v165\n"
  "v_lshl_add_u32 v228, v163, 5, v164\n"
  "v_xor_b32 v163, 5, v165\n"
  "v_lshl_add_u32 v229, v163, 5, v164\n"
  "v_xor_b32 v163, 6, v165\n"
  "v_lshl_add_u32 v230, v163, 5, v164\n"
  "v_xor_b32 v163, 7, v165\n"
  "v_lshl_add_u32 v231, v163, 5, v164\n"
  "v_and_b32 v162, 15, v160\n"
  "v_lshrrev_b32 v163, 1, v162\n"
  "v_xor_b32 v163, v163, v161\n"
  "v_lshlrev_b32 v163, 4, v163\n"
  "v_lshl_add_u32 v163, v162, 7, v163\n"
  "v_add_u32 v240, s58, v163\n"
  "v_xor_b32 v241, 64, v240\n"
  "v_lshlrev_b32 v163, 2, v161\n"
  "v_sub_u32 v162, v162, v163\n"
  "s_lshl_b32 s59, %1, 4\n"
  "v_add_u32 v162, s59, v162\n"
  "v_cmp_gt_i32_e64 s[64:65], 0, v162\n"
  "v_cmp_gt_i32_e64 s[66:67], 1, v162\n"
  "v_cmp_gt_i32_e64 s[68:69], 2, v162\n"
  "v_cmp_gt_i32_e64 s[70:71], 3, v162\n"
  "v_cmp_gt_i32_e64 s[72:73], 16, v162\n"
  "v_cmp_gt_i32_e64 s[74:75], 17, v162\n"
  "v_cmp_gt_i32_e64 s[76:77], 18, v162\n"
  "v_cmp_gt_i32_e64 s[78:79], 19, v162\n"
  "v_cmp_gt_i32_e64 s[80:81], 32, v162\n"
  "v_cmp_gt_i32_e64 s[82:83], 33, v162\n"
  "v_cmp_gt_i32_e64 s[84:85], 34, v162\n"
  "v_cmp_gt_i32_e64 s[86:87], 35, v162\n"
  "v_cmp_gt_i32_e64 s[88:89], 48, v162\n"
  "v_cmp_gt_i32_e64 s[90:91], 49, v162\n"
  "v_cmp_gt_i32_e64 s[92:93], 50, v162\n"
  "v_cmp_gt_i32_e64 s[94:95], 51, v162\n"
  "v_mov_b32 v245, v244\n"
  "v_mov_b32 v246, 0xff000000\n"
  "s_mov_b64 exec, 0\n"
  "s_cmp_eq_u32 %1, 0\n"
  "s_cbranch_scc1 mk_tp0\n"
  "s_cmp_eq_u32 %1, 1\n"
  "s_cbranch_scc1 mk_tp1\n"
  "s_cmp_eq_u32 %1, 2\n"
  "s_cbranch_scc1 mk_tp2\n"
  "s_branch mk_tp3\n"
  "mk_tret:\n"
  "s_mov_b64 exec, -1\n"
  "s_add_u32 s54, s54, 1\n"
  "s_and_b32 s54, s54, 7\n"
  "s_lshl_b32 s55, s54, 19\n"
  "s_add_u32 m0, s57, 0x4000\n"
  "s_nop 0\n"
  "buffer_load_dwordx4 v232, s[36:39], s55 offen offset:0 lds\n"
  "buffer_load_dwordx4 v233, s[36:39], s55 offen offset:1024 lds\n"
  "buffer_load_dwordx4 v234, s[36:39], s55 offen offset:2048 lds\n"
  "buffer_load_dwordx4 v235, s[36:39], s55 offen offset:3072 lds\n"
  "s_lshl_b32 s56, s54, 7\n"
  "s_add_u32 m0, s58, 0x8000\n"
  "s_nop 0\n"
  "buffer_load_dwordx4 v236, s[44:47], s56 offen offset:0 lds\n"
  "buffer_load_dwordx4 v237, s[44:47], s56 offen offset:1024 lds\n"
  "buffer_load_dwordx4 v238, s[44:47], s56 offen offset:2048 lds\n"
  "buffer_load_dwordx4 v239, s[44:47], s56 offen offset:3072 lds\n"
  "s_add_u32 s56, s56, 0x8000\n"
  "s_add_u32 m0, s58, 0x9000\n"
  "s_nop 0\n"
  "buffer_load_dwordx4 v236, s[44:47], s56 offen offset:0 lds\n"
  "buffer_load_dwordx4 v237, s[44:47], s56 offen offset:1024 lds\n"
  "buffer_load_dwordx4 v238, s[44:47], s56 offen offset:2048 lds\n"
  "buffer_load_dwordx4 v239, s[44:47], s56 offen offset:3072 lds\n"
  "s_waitcnt vmcnt(15)\n"
  "s_barrier\n"
  "ds_read_b64_tr_b16 v[192:193], v224 offset:0\n"
  "ds_read_b64_tr_b16 v[194:195], v224 offset:1024\n"
  "ds_read_b64_tr_b16 v[196:197], v224 offset:8192\n"
  "ds_read_b64_tr_b16 v[198:199], v224 offset:9216\n"
  "ds_read_b128 a[0:3], v240 offset:0\n"
  "ds_read_b128 a[4:7], v241 offset:0\n"
  "ds_read_b128 a[8:11], v240 offset:2048\n"
  "ds_read_b128 a[12:15], v241 offset:2048\n"
  "ds_read_b128 a[16:19], v240 offset:4096\n"
  "ds_read_b128 a[20:23], v241 offset:4096\n"
  "ds_read_b128 a[24:27], v240 offset:6144\n"
  "ds_read_b128 a[28:31], v241 offset:6144\n"
  "ds_read_b64_tr_b16 v[200:201], v225 offset:0\n"
  "ds_read_b64_tr_b16 v[202:203], v225 offset:1024\n"
  "ds_read_b64_tr_b16 v[204:205], v225 offset:8192\n"
  "ds_read_b64_tr_b16 v[206:207], v225 offset:9216\n"
  "s_waitcnt lgkmcnt(11)\n"
  "v_mfma_f32_16x16x32_bf16 v[0:3], v[192:195], a[0:3], 0\n"
  "s_waitcnt lgkmcnt(10)\n"
  "s_add_u32 s54, s54, 1\n"
  "ds_read_b64_tr_b16 v[208:209], v226 offset:0\n"
  "v_mfma_f32_16x16x32_bf16 v[0:3], v[196:199], a[4:7], v[0:3]\n"
  "s_waitcnt lgkmcnt(10)\n"
  "s_and_b32 s54, s54, 7\n"
  "ds_read_b64_tr_b16 v[210:211], v226 offset:1024\n"
  "v_mfma_f32_16x16x32_bf16 v[4:7], v[192:195], a[8:11], 0\n"
  "s_waitcnt lgkmcnt(10)\n"
  "s_lshl_b32 s55, s54, 19\n"
  "v_mfma_f32_16x16x32_bf16 v[4:7], v[196:199], a[12:15], v[4:7]\n"
  "s_waitcnt lgkmcnt(9)\n"
  "s_add_u32 m0, s57, 0x8000\n"
  "ds_read_b64_tr_b16 v[212:213], v226 offset:8192\n"
  "v_mfma_f32_16x16x32_bf16 v[8:11], v[192:195], a[16:19], 0\n"
  "s_waitcnt lgkmcnt(9)\n"
  "v_mfma_f32_16x16x32_bf16 v[8:11], v[196:199], a[20:23], v[8:11]\n"
  "s_waitcnt lgkmcnt(8)\n"
  "buffer_load_dwordx4 v232, s[36:39], s55 offen offset:0 lds\n"
  "v_mfma_f32_16x16x32_bf16 v[12:15], v[192:195], a[24:27], 0\n"
  "s_waitcnt lgkmcnt(7)\n"
  "ds_read_b64_tr_b16 v[214:215], v226 offset:9216\n"
  "v_mfma_f32_16x16x32_bf16 v[12:15], v[196:199], a[28:31], v[12:15]\n"
  "s_waitcnt lgkmcnt(4)\n"
  "ds_read_b64_tr_b16 v[216:217], v227 offset:0\n"
  "v_mfma_f32_16x16x32_bf16 v[16:19], v[200:203], a[0:3], 0\n"
  "buffer_load_dwordx4 v233, s[36:39], s55 offen offset:1024 lds\n"
  "v_mfma_f32_16x16x32_bf16 v[16:19], v[204:207], a[4:7], v[16:19]\n"
  "ds_read_b64_tr_b16 v[218:219], v227 offset:1024\n"
  "v_mfma_f32_16x16x32_bf16 v[20:23], v[200:203], a[8:11], 0\n"
  "v_mfma_f32_16x16x32_bf16 v[20:23], v[204:207], a[12:15], v[20:23]\n"
  "buffer_load_dwordx4 v234, s[36:39], s55 offen offset:2048 lds\n"
  "v_mfma_f32_16x16x32_bf16 v[24:27], v[200:203], a[16:19], 0\n"
  "ds_read_b64_tr_b16 v[220:221], v227 offset:8192\n"
  "v_mfma_f32_16x16x32_bf16 v[24:27], v[204:207], a[20:23], v[24:27]\n"
  "ds_read_b64_tr_b16 v[222:223], v227 offset:9216\n"
  "v_mfma_f32_16x16x32_bf16 v[28:31], v[200:203], a[24:27], 0\n"
  "buffer_load_dwordx4 v235, s[36:39], s55 offen offset:3072 lds\n"
  "v_mfma_f32_16x16x32_bf16 v[28:31], v[204:207], a[28:31], v[28:31]\n"
  "s_waitcnt lgkmcnt(3)\n"
  "s_lshl_b32 s56, s54, 7\n"
  "ds_read_b64_tr_b16 v[192:193], v228 offset:0\n"
  "v_mfma_f32_16x16x32_bf16 v[32:35], v[208:211], a[0:3], 0\n"
  "s_add_u32 m0, s58, 0x0\n"
  "v_mfma_f32_16x16x32_bf16 v[32:35], v[212:215], a[4:7], v[32:35]\n"
  "ds_read_b64_tr_b16 v[194:195], v228 offset:1024\n"
  "v_mfma_f32_16x16x32_bf16 v[36:39], v[208:211], a[8:11], 0\n"
  "v_mfma_f32_16x16x32_bf16 v[36:39], v[212:215], a[12:15], v[36:39]\n"
  "buffer_load_dwordx4 v236, s[44:47], s56 offen offset:0 lds\n"
  "v_mfma_f32_16x16x32_bf16 v[40:43], v[208:211], a[16:19], 0\n"
  "ds_read_b64_tr_b16 v[196:197], v228 offset:8192\n"
  "v_mfma_f32_16x16x32_bf16 v[40:43], v[212:215], a[20:23], v[40:43]\n"
  "ds_read_b64_tr_b16 v[198:199], v228 offset:9216\n"
  "v_mfma_f32_16x16x32_bf16 v[44:47], v[208:211], a[24:27], 0\n"
  "buffer_load_dwordx4 v237, s[44:47], s56 offen offset:1024 lds\n"
  "v_mfma_f32_16x16x32_bf16 v[44:47], v[212:215], a[28:31], v[44:47]\n"
  "s_waitcnt lgkmcnt(3)\n"
  "ds_read_b64_tr_b16 v[200:201], v229 offset:0\n"
  "v_mfma_f32_16x16x32_bf16 v[48:51], v[216:219], a[0:3], 0\n"
  "v_mfma_f32_16x16x32_bf16 v[48:51], v[220:223], a[4:7], v[48:51]\n"
  "buffer_load_dwordx4 v238, s[44:47], s56 offen offset:2048 lds\n"
  "v_mfma_f32_16x16x32_bf16 v[52:55], v[216:219], a[8:11], 0\n"
  "ds_read_b64_tr_b16 v[202:203], v229 offset:1024\n"
  "v_mfma_f32_16x16x32_bf16 v[52:55], v[220:223], a[12:15], v[52:55]\n"
  "ds_read_b64_tr_b16 v[204:205], v229 offset:8192\n"
  "v_mfma_f32_16x16x32_bf16 v[56:59], v[216:219], a[16:19], 0\n"
  "buffer_load_dwordx4 v239, s[44:47], s56 offen offset:3072 lds\n"
  "v_mfma_f32_16x16x32_bf16 v[56:59], v[220:223], a[20:23], v[56:59]\n"
  "s_add_u32 s56, s56, 0x8000\n"
  "ds_read_b64_tr_b16 v[206:207], v229 offset:9216\n"
  "v_mfma_f32_16x16x32_bf16 v[60:63], v[216:219], a[24:27], 0\n"
  "s_add_u32 m0, s58, 0x1000\n"
  "v_mfma_f32_16x16x32_bf16 v[60:63], v[220:223], a[28:31], v[60:63]\n"
  "s_waitcnt lgkmcnt(3)\n"
  "buffer_load_dwordx4 v236, s[44:47], s56 offen offset:0 lds\n"
  "v_mfma_f32_16x16x32_bf16 v[64:67], v[192:195], a[0:3], 0\n"
  "ds_read_b64_tr_b16 v[208:209], v230 offset:0\n"
  "v_mfma_f32_16x16x32_bf16 v[64:67], v[196:199], a[4:7], v[64:67]\n"
  "ds_read_b64_tr_b16 v[210:211], v230 offset:1024\n"
  "v_mfma_f32_16x16x32_bf16 v[68:71], v[192:195], a[8:11], 0\n"
  "buffer_load_dwordx4 v237, s[44:47], s56 offen offset:1024 lds\n"
  "v_mfma_f32_16x16x32_bf16 v[68:71], v[196:199], a[12:15], v[68:71]\n"
  "ds_read_b64_tr_b16 v[212:213], v230 offset:8192\n"
  "v_mfma_f32_16x16x32_bf16 v[72:75], v[192:195], a[16:19], 0\n"
  "v_mfma_f32_16x16x32_bf16 v[72:75], v[196:199], a[20:23], v[72:75]\n"
  "buffer_load_dwordx4 v238, s[44:47], s56 offen offset:2048 lds\n"
  "v_mfma_f32_16x16x32_bf16 v[76:79], v[192:195], a[24:27], 0\n"
  "ds_read_b64_tr_b16 v[214:215], v230 offset:9216\n"
  "v_mfma_f32_16x16x32_bf16 v[76:79], v[196:199], a[28:31], v[76:79]\n"
  "s_waitcnt lgkmcnt(4)\n"
  "ds_read_b64_tr_b16 v[216:217], v231 offset:0\n"
  "v_mfma_f32_16x16x32_bf16 v[80:83], v[200:203], a[0:3], 0\n"
  "buffer_load_dwordx4 v239, s[44:47], s56 offen offset:3072 lds\n"
  "v_mfma_f32_16x16x32_bf16 v[80:83], v[204:207], a[4:7], v[80:83]\n"
  "ds_read_b64_tr_b16 v[218:219], v231 offset:1024\n"
  "v_mfma_f32_16x16x32_bf16 v[84:87], v[200:203], a[8:11], 0\n"
  "v_mfma_f32_16x16x32_bf16 v[84:87], v[204:207], a[12:15], v[84:87]\n"
  "ds_read_b64_tr_b16 v[220:221], v231 offset:8192\n"
  "v_mfma_f32_16x16x32_bf16 v[88:91], v[200:203], a[16:19], 0\n"
  "v_mfma_f32_16x16x32_bf16 v[88:91], v[204:207], a[20:23], v[88:91]\n"
  "ds_read_b64_tr_b16 v[222:223], v231 offset:9216\n"
  "v_mfma_f32_16x16x32_bf16 v[92:95], v[200:203], a[24:27], 0\n"
  "s_waitcnt vmcnt(12)\n"
  "v_mfma_f32_16x16x32_bf16 v[92:95], v[204:207], a[28:31], v[92:95]\n"
  "s_barrier\n"
  "ds_read_b64_tr_b16 v[192:193], v224 offset:16384\n"
  "ds_read_b128 a[32:35], v240 offset:32768\n"
  "s_waitcnt lgkmcnt(5)\n"
  "v_mfma_f32_16x16x32_bf16 v[96:99], v[208:211], a[0:3], 0\n"
  "s_add_u32 s54, s54, 1\n"
  "ds_read_b128 a[36:39], v241 offset:32768\n"
  "v_mfma_f32_16x16x32_bf16 v[96:99], v[212:215], a[4:7], v[96:99]\n"
  "s_and_b32 s54, s54, 7\n"
  "ds_read_b64_tr_b16 v[194:195], v224 offset:17408\n"
  "ds_read_b128 a[40:43], v240 offset:34816\n"
  "v_mfma_f32_16x16x32_bf16 v[100:103], v[208:211], a[8:11], 0\n"
  "s_lshl_b32 s55, s54, 19\n"
  "ds_read_b128 a[44:47], v241 offset:34816\n"
  "v_mfma_f32_16x16x32_bf16 v[100:103], v[212:215], a[12:15], v[100:103]\n"
  "s_add_u32 m0, s57, 0xc000\n"
  "ds_read_b64_tr_b16 v[196:197], v224 offset:24576\n"
  "ds_read_b128 a[48:51], v240 offset:36864\n"
  "v_mfma_f32_16x16x32_bf16 v[104:107], v[208:211], a[16:19], 0\n"
  "ds_read_b128 a[52:55], v241 offset:36864\n"
  "v_mfma_f32_16x16x32_bf16 v[104:107], v[212:215], a[20:23], v[104:107]\n"
  "buffer_load_dwordx4 v232, s[36:39], s55 offen offset:0 lds\n"
  "v_mfma_f32_16x16x32_bf16 v[108:111], v[208:211], a[24:27], 0\n"
  "ds_read_b64_tr_b16 v[198:199], v224 offset:25600\n"
  "ds_read_b128 a[56:59], v240 offset:38912\n"
  "v_mfma_f32_16x16x32_bf16 v[108:111], v[212:215], a[28:31], v[108:111]\n"
  "s_waitcnt lgkmcnt(9)\n"
  "ds_read_b128 a[60:63], v241 offset:38912\n"
  "ds_read_b64_tr_b16 v[200:201], v225 offset:16384\n"
  "v_mfma_f32_16x16x32_bf16 v[112:115], v[216:219], a[0:3], 0\n"
  "v_mfma_f32_16x16x32_bf16 v[112:115], v[220:223], a[4:7], v[112:115]\n"
  "buffer_load_dwordx4 v233, s[36:39], s55 offen offset:1024 lds\n"
  "v_mfma_f32_16x16x32_bf16 v[116:119], v[216:219], a[8:11], 0\n"
  "ds_read_b64_tr_b16 v[202:203], v225 offset:17408\n"
  "v_mfma_f32_16x16x32_bf16 v[116:119], v[220:223], a[12:15], v[116:119]\n"
  "ds_read_b64_tr_b16 v[204:205], v225 offset:24576\n"
  "v_mfma_f32_16x16x32_bf16 v[120:123], v[216:219], a[16:19], 0\n"
  "v_mfma_f32_16x16x32_bf16 v[120:123], v[220:223], a[20:23], v[120:123]\n"
  "buffer_load_dwordx4 v234, s[36:39], s55 offen offset:2048 lds\n"
  "v_mfma_f32_16x16x32_bf16 v[124:127], v[216:219], a[24:27], 0\n"
  "ds_read_b64_tr_b16 v[206:207], v225 offset:25600\n"
  "v_mfma_f32_16x16x32_bf16 v[124:127], v[220:223], a[28:31], v[124:127]\n"
  "s_waitcnt lgkmcnt(3)\n"
  "ds_read_b64_tr_b16 v[208:209], v226 offset:16384\n"
  "v_mfma_f32_16x16x32_bf16 v[0:3], v[192:195], a[32:35], v[0:3]\n"
  "v_mfma_f32_16x16x32_bf16 v[0:3], v[196:199], a[36:39], v[0:3]\n"
  "buffer_load_dwordx4 v235, s[36:39], s55 offen offset:3072 lds\n"
  "v_mfma_f32_16x16x32_bf16 v[4:7], v[192:195], a[40:43], v[4:7]\n"
  "s_lshl_b32 s56, s54, 7\n"
  "ds_read_b64_tr_b16 v[210:211], v226 offset:17408\n"
  "v_mfma_f32_16x16x32_bf16 v[4:7], v[196:199], a[44:47], v[4:7]\n"
  "s_add_u32 m0, s58, 0x8000\n"
  "ds_read_b64_tr_b16 v[212:213], v226 offset:24576\n"
  "v_mfma_f32_16x16x32_bf16 v[8:11], v[192:195], a[48:51], v[8:11]\n"
  "v_mfma_f32_16x16x32_bf16 v[8:11], v[196:199], a[52:55], v[8:11]\n"
  "ds_read_b64_tr_b16 v[214:215], v226 offset:25600\n"
  "v_mfma_f32_16x16x32_bf16 v[12:15], v[192:195], a[56:59], v[12:15]\n"
  "v_mfma_f32_16x16x32_bf16 v[12:15], v[196:199], a[60:63], v[12:15]\n"
  "s_waitcnt lgkmcnt(3)\n"
  "buffer_load_dwordx4 v236, s[44:47], s56 offen offset:0 lds\n"
  "v_mfma_f32_16x16x32_bf16 v[16:19], v[200:203], a[32:35], v[16:19]\n"
  "ds_read_b64_tr_b16 v[216:217], v227 offset:16384\n"
  "v_mfma_f32_16x16x32_bf16 v[16:19], v[204:207], a[36:39], v[16:19]\n"
  "ds_read_b64_tr_b16 v[218:219], v227 offset:17408\n"
  "v_mfma_f32_16x16x32_bf16 v[20:23], v[200:203], a[40:43], v[20:23]\n"
  "v_mfma_f32_16x16x32_bf16 v[20:23], v[204:207], a[44:47], v[20:23]\n"
  "buffer_load_dwordx4 v237, s[44:47], s56 offen offset:1024 lds\n"
  "v_mfma_f32_16x16x32_bf16 v[24:27], v[200:203], a[48:51], v[24:27]\n"
  "ds_read_b64_tr_b16 v[220:221], v227 offset:24576\n"
  "v_mfma_f32_16x16x32_bf16 v[24:27], v[204:207], a[52:55], v[24:27]\n"
  "ds_read_b64_tr_b16 v[222:223], v227 offset:25600\n"
  "v_mfma_f32_16x16x32_bf16 v[28:31], v[200:203], a[56:59], v[28:31]\n"
  "v_mfma_f32_16x16x32_bf16 v[28:31], v[204:207], a[60:63], v[28:31]\n"
  "s_waitcnt lgkmcnt(4)\n"
  "buffer_load_dwordx4 v238, s[44:47], s56 offen offset:2048 lds\n"
  "v_mfma_f32_16x16x32_bf16 v[32:35], v[208:211], a[32:35], v[32:35]\n"
  "ds_read_b64_tr_b16 v[192:193], v228 offset:16384\n"
  "v_mfma_f32_16x16x32_bf16 v[32:35], v[212:215], a[36:39], v[32:35]\n"
  "ds_read_b64_tr_b16 v[194:195], v228 offset:17408\n"
  "v_mfma_f32_16x16x32_bf16 v[36:39], v[208:211], a[40:43], v[36:39]\n"
  "v_mfma_f32_16x16x32_bf16 v[36:39], v[212:215], a[44:47], v[36:39]\n"
  "buffer_load_dwordx4 v239, s[44:47], s56 offen offset:3072 lds\n"
  "v_mfma_f32_16x16x32_bf16 v[40:43], v[208:211], a[48:51], v[40:43]\n"
  "s_add_u32 s56, s56, 0x8000\n"
  "ds_read_b64_tr_b16 v[196:197], v228 offset:24576\n"
  "v_mfma_f32_16x16x32_bf16 v[40:43], v[212:215], a[52:55], v[40:43]\n"
  "s_add_u32 m0, s58, 0x9000\n"
  "ds_read_b64_tr_b16 v[198:199], v228 offset:25600\n"
  "v_mfma_f32_16x16x32_bf16 v[44:47], v[208:211], a[56:59], v[44:47]\n"
  "v_mfma_f32_16x16x32_bf16 v[44:47], v[212:215], a[60:63], v[44:47]\n"
  "s_waitcnt lgkmcnt(4)\n"
  "buffer_load_dwordx4 v236, s[44:47], s56 offen offset:0 lds\n"
  "v_mfma_f32_16x16x32_bf16 v[48:51], v[216:219], a[32:35], v[48:51]\n"
  "ds_read_b64_tr_b16 v[200:201], v229 offset:16384\n"
  "v_mfma_f32_16x16x32_bf16 v[48:51], v[220:223], a[36:39], v[48:51]\n"
  "ds_read_b64_tr_b16 v[202:203], v229 offset:17408\n"
  "v_mfma_f32_16x16x32_bf16 v[52:55], v[216:219], a[40:43], v[52:55]\n"
  "v_mfma_f32_16x16x32_bf16 v[52:55], v[220:223], a[44:47], v[52:55]\n"
  "buffer_load_dwordx4 v237, s[44:47], s56 offen offset:1024 lds\n"
  "v_mfma_f32_16x16x32_bf16 v[56:59], v[216:219], a[48:51], v[56:59]\n"
  "ds_read_b64_tr_b16 v[204:205], v229 offset:24576\n"
  "v_mfma_f32_16x16x32_bf16 v[56:59], v[220:223], a[52:55], v[56:59]\n"
  "ds_read_b64_tr_b16 v[206:207], v229 offset:25600\n"
  "v_mfma_f32_16x16x32_bf16 v[60:63], v[216:219], a[56:59], v[60:63]\n"
  "v_mfma_f32_16x16x32_bf16 v[60:63], v[220:223], a[60:63], v[60:63]\n"
  "s_waitcnt lgkmcnt(4)\n"
  "buffer_load_dwordx4 v238, s[44:47], s56 offen offset:2048 lds\n"
  "v_mfma_f32_16x16x32_bf16 v[64:67], v[192:195], a[32:35], v[64:67]\n"
  "ds_read_b64_tr_b16 v[208:209], v230 offset:16384\n"
  "v_mfma_f32_16x16x32_bf16 v[64:67], v[196:199], a[36:39], v[64:67]\n"
  "ds_read_b64_tr_b16 v[210:211], v230 offset:17408\n"
  "v_mfma_f32_16x16x32_bf16 v[68:71], v[192:195], a[40:43], v[68:71]\n"
  "v_mfma_f32_16x16x32_bf16 v[68:71], v[196:199], a[44:47], v[68:71]\n"
  "buffer_load_dwordx4 v239, s[44:47], s56 offen offset:3072 lds\n"
  "v_mfma_f32_16x16x32_bf16 v[72:75], v[192:195], a[48:51], v[72:75]\n"
  "ds_read_b64_tr_b16 v[212:213], v230 offset:24576\n"
  "v_mfma_f32_16x16x32_bf16 v[72:75], v[196:199], a[52:55], v[72:75]\n"
  "ds_read_b64_tr_b16 v[214:215], v230 offset:25600\n"
  "v_mfma_f32_16x16x32_bf16 v[76:79], v[192:195], a[56:59], v[76:79]\n"
  "v_mfma_f32_16x16x32_bf16 v[76:79], v[196:199], a[60:63], v[76:79]\n"
  "s_waitcnt lgkmcnt(4)\n"
  "ds_read_b64_tr_b16 v[216:217], v231 offset:16384\n"
  "v_mfma_f32_16x16x32_bf16 v[80:83], v[200:203], a[32:35], v[80:83]\n"
  "v_mfma_f32_16x16x32_bf16 v[80:83], v[204:207], a[36:39], v[80:83]\n"
  "ds_read_b64_tr_b16 v[218:219], v231 offset:17408\n"
  "v_mfma_f32_16x16x32_bf16 v[84:87], v[200:203], a[40:43], v[84:87]\n"
  "v_mfma_f32_16x16x32_bf16 v[84:87], v[204:207], a[44:47], v[84:87]\n"
  "ds_read_b64_tr_b16 v[220:221], v231 offset:24576\n"
  "v_mfma_f32_16x16x32_bf16 v[88:91], v[200:203], a[48:51], v[88:91]\n"
  "v_mfma_f32_16x16x32_bf16 v[88:91], v[204:207], a[52:55], v[88:91]\n"
  "ds_read_b64_tr_b16 v[222:223], v231 offset:25600\n"
  "v_mfma_f32_16x16x32_bf16 v[92:95], v[200:203], a[56:59], v[92:95]\n"
  "s_waitcnt vmcnt(12)\n"
  "v_mfma_f32_16x16x32_bf16 v[92:95], v[204:207], a[60:63], v[92:95]\n"
  "s_barrier\n"
  "ds_read_b64_tr_b16 v[192:193], v224 offset:32768\n"
  "ds_read_b128 a[64:67], v240 offset:0\n"
  "s_waitcnt lgkmcnt(5)\n"
  "v_mfma_f32_16x16x32_bf16 v[96:99], v[208:211], a[32:35], v[96:99]\n"
  "s_add_u32 s54, s54, 1\n"
  "ds_read_b128 a[68:71], v241 offset:0\n"
  "v_lshlrev_b32 v136, 16, v128\n"
  "v_mfma_f32_16x16x32_bf16 v[96:99], v[212:215], a[36:39], v[96:99]\n"
  "s_and_b32 s54, s54, 7\n"
  "ds_read_b64_tr_b16 v[194:195], v224 offset:33792\n"
  "ds_read_b128 a[72:75], v240 offset:2048\n"
  "v_mfma_f32_16x16x32_bf16 v[100:103], v[208:211], a[40:43], v[100:103]\n"
  "s_lshl_b32 s55, s54, 19\n"
  "ds_read_b128 a[76:79], v241 offset:2048\n"
  "v_and_b32 v137, 0xffff0000, v128\n"
  "v_mfma_f32_16x16x32_bf16 v[100:103], v[212:215], a[44:47], v[100:103]\n"
  "s_add_u32 m0, s57, 0x0\n"
  "ds_read_b64_tr_b16 v[196:197], v224 offset:40960\n"
  "ds_read_b128 a[80:83], v240 offset:4096\n"
  "v_mfma_f32_16x16x32_bf16 v[104:107], v[208:211], a[48:51], v[104:107]\n"
  "ds_read_b128 a[84:87], v241 offset:4096\n"
  "v_lshlrev_b32 v138, 16, v129\n"
  "v_mfma_f32_16x16x32_bf16 v[104:107], v[212:215], a[52:55], v[104:107]\n"
  "buffer_load_dwordx4 v232, s[36:39], s55 offen offset:0 lds\n"
  "v_mfma_f32_16x16x32_bf16 v[108:111], v[208:211], a[56:59], v[108:111]\n"
  "ds_read_b64_tr_b16 v[198:199], v224 offset:41984\n"
  "ds_read_b128 a[88:91], v240 offset:6144\n"
  "v_mfma_f32_16x16x32_bf16 v[108:111], v[212:215], a[60:63], v[108:111]\n"
  "s_waitcnt lgkmcnt(9)\n"
  "ds_read_b128 a[92:95], v241 offset:6144\n"
  "ds_read_b64_tr_b16 v[200:201], v225 offset:32768\n"
  "v_mfma_f32_16x16x32_bf16 v[112:115], v[216:219], a[32:35], v[112:115]\n"
  "v_and_b32 v139, 0xffff0000, v129\n"
  "v_lshlrev_b32 v140, 16, v130\n"
  "v_mfma_f32_16x16x32_bf16 v[112:115], v[220:223], a[36:39], v[112:115]\n"
  "buffer_load_dwordx4 v233, s[36:39], s55 offen offset:1024 lds\n"
  "v_mfma_f32_16x16x32_bf16 v[116:119], v[216:219], a[40:43], v[116:119]\n"
  "ds_read_b64_tr_b16 v[202:203], v225 offset:33792\n"
  "v_and_b32 v141, 0xffff0000, v130\n"
  "v_mfma_f32_16x16x32_bf16 v[116:119], v[220:223], a[44:47], v[116:119]\n"
  "ds_read_b64_tr_b16 v[204:205], v225 offset:40960\n"
  "v_lshlrev_b32 v142, 16, v131\n"
  "v_mfma_f32_16x16x32_bf16 v[120:123], v[216:219], a[48:51], v[120:123]\n"
  "v_and_b32 v143, 0xffff0000, v131\n"
  "v_lshlrev_b32 v144, 16, v132\n"
  "v_mfma_f32_16x16x32_bf16 v[120:123], v[220:223], a[52:55], v[120:123]\n"
  "buffer_load_dwordx4 v234, s[36:39], s55 offen offset:2048 lds\n"
  "v_mfma_f32_16x16x32_bf16 v[124:127], v[216:219], a[56:59], v[124:127]\n"
  "ds_read_b64_tr_b16 v[206:207], v225 offset:41984\n"
  "v_and_b32 v145, 0xffff0000, v132\n"
  "v_mfma_f32_16x16x32_bf16 v[124:127], v[220:223], a[60:63], v[124:127]\n"
  "s_waitcnt lgkmcnt(3)\n"
  "ds_read_b64_tr_b16 v[208:209], v226 offset:32768\n"
  "v_lshlrev_b32 v146, 16, v133\n"
  "v_mfma_f32_16x16x32_bf16 v[0:3], v[192:195], a[64:67], v[0:3]\n"
  "v_and_b32 v147, 0xffff0000, v133\n"
  "v_lshlrev_b32 v148, 16, v134\n"
  "v_mfma_f32_16x16x32_bf16 v[0:3], v[196:199], a[68:71], v[0:3]\n"
  "mk_tp0:\n"
  "s_cbranch_execz mk_tp4\n"
  "buffer_load_dwordx4 v235, s[36:39], s55 offen offset:3072 lds\n"
  "v_mfma_f32_16x16x32_bf16 v[4:7], v[192:195], a[72:75], v[4:7]\n"
  "s_lshl_b32 s56, s54, 7\n"
  "ds_read_b64_tr_b16 v[210:211], v226 offset:33792\n"
  "v_and_b32 v149, 0xffff0000, v134\n"
  "v_mfma_f32_16x16x32_bf16 v[4:7], v[196:199], a[76:79], v[4:7]\n"
  "s_add_u32 m0, s58, 0x0\n"
  "ds_read_b64_tr_b16 v[212:213], v226 offset:40960\n"
  "v_lshlrev_b32 v150, 16, v135\n"
  "v_mfma_f32_16x16x32_bf16 v[8:11], v[192:195], a[80:83], v[8:11]\n"
  "v_and_b32 v151, 0xffff0000, v135\n"
  "v_add_f32 v152, v136, v137\n"
  "v_mfma_f32_16x16x32_bf16 v[8:11], v[196:199], a[84:87], v[8:11]\n"
  "ds_read_b64_tr_b16 v[214:215], v226 offset:41984\n"
  "v_add_f32 v153, v138, v139\n"
  "v_mfma_f32_16x16x32_bf16 v[12:15], v[192:195], a[88:91], v[12:15]\n"
  "v_add_f32 v152, v152, v140\n"
  "v_add_f32 v153, v153, v141\n"
  "v_mfma_f32_16x16x32_bf16 v[12:15], v[196:199], a[92:95], v[12:15]\n"
  "s_waitcnt lgkmcnt(3)\n"
  "buffer_load_dwordx4 v236, s[44:47], s56 offen offset:0 lds\n"
  "v_mfma_f32_16x16x32_bf16 v[16:19], v[200:203], a[64:67], v[16:19]\n"
  "ds_read_b64_tr_b16 v[216:217], v227 offset:32768\n"
  "v_add_f32 v152, v152, v142\n"
  "v_mfma_f32_16x16x32_bf16 v[16:19], v[204:207], a[68:71], v[16:19]\n"
  "ds_read_b64_tr_b16 v[218:219], v227 offset:33792\n"
  "v_add_f32 v153, v153, v143\n"
  "v_mfma_f32_16x16x32_bf16 v[20:23], v[200:203], a[72:75], v[20:23]\n"
  "v_add_f32 v152, v152, v144\n"
  "v_add_f32 v153, v153, v145\n"
  "v_mfma_f32_16x16x32_bf16 v[20:23], v[204:207], a[76:79], v[20:23]\n"
  "buffer_load_dwordx4 v237, s[44:47], s56 offen offset:1024 lds\n"
  "v_mfma_f32_16x16x32_bf16 v[24:27], v[200:203], a[80:83], v[24:27]\n"
  "ds_read_b64_tr_b16 v[220:221], v227 offset:40960\n"
  "v_add_f32 v152, v152, v146\n"
  "v_mfma_f32_16x16x32_bf16 v[24:27], v[204:207], a[84:87], v[24:27]\n"
  "ds_read_b64_tr_b16 v[222:223], v227 offset:41984\n"
  "v_add_f32 v153, v153, v147\n"
  "v_mfma_f32_16x16x32_bf16 v[28:31], v[200:203], a[88:91], v[28:31]\n"
  "v_add_f32 v152, v152, v148\n"
  "v_add_f32 v153, v153, v149\n"
  "v_mfma_f32_16x16x32_bf16 v[28:31], v[204:207], a[92:95], v[28:31]\n"
  "s_waitcnt lgkmcnt(4)\n"
  "buffer_load_dwordx4 v238, s[44:47], s56 offen offset:2048 lds\n"
  "v_mfma_f32_16x16x32_bf16 v[32:35], v[208:211], a[64:67], v[32:35]\n"
  "ds_read_b64_tr_b16 v[192:193], v228 offset:32768\n"
  "v_add_f32 v152, v152, v150\n"
  "v_mfma_f32_16x16x32_bf16 v[32:35], v[212:215], a[68:71], v[32:35]\n"
  "ds_read_b64_tr_b16 v[194:195], v228 offset:33792\n"
  "v_add_f32 v153, v153, v151\n"
  "v_mfma_f32_16x16x32_bf16 v[36:39], v[208:211], a[72:75], v[36:39]\n"
  "v_add_f32 v152, v152, v153\n"
  "v_mfma_f32_16x16x32_bf16 v[36:39], v[212:215], a[76:79], v[36:39]\n"
  "buffer_load_dwordx4 v239, s[44:47], s56 offen offset:3072 lds\n"
  "v_mfma_f32_16x16x32_bf16 v[40:43], v[208:211], a[80:83], v[40:43]\n"
  "s_add_u32 s56, s56, 0x8000\n"
  "ds_read_b64_tr_b16 v[196:197], v228 offset:40960\n"
  "v_add_f32_dpp v152, v152, v152 row_ror:8 row_mask:0xf bank_mask:0xf\n"
  "v_mfma_f32_16x16x32_bf16 v[40:43], v[212:215], a[84:87], v[40:43]\n"
  "s_add_u32 m0, s58, 0x1000\n"
  "ds_read_b64_tr_b16 v[198:199], v228 offset:41984\n"
  "v_mfma_f32_16x16x32_bf16 v[44:47], v[208:211], a[88:91], v[44:47]\n"
  "v_add_f32_dpp v152, v152, v152 row_ror:4 row_mask:0xf bank_mask:0xf\n"
  "v_mfma_f32_16x16x32_bf16 v[44:47], v[212:215], a[92:95], v[44:47]\n"
  "s_waitcnt lgkmcnt(4)\n"
  "buffer_load_dwordx4 v236, s[44:47], s56 offen offset:0 lds\n"
  "v_mfma_f32_16x16x32_bf16 v[48:51], v[216:219], a[64:67], v[48:51]\n"
  "ds_read_b64_tr_b16 v[200:201], v229 offset:32768\n"
  "v_add_f32_dpp v152, v152, v152 row_ror:2 row_mask:0xf bank_mask:0xf\n"
  "v_mfma_f32_16x16x32_bf16 v[48:51], v[220:223], a[68:71], v[48:51]\n"
  "ds_read_b64_tr_b16 v[202:203], v229 offset:33792\n"
  "v_mov_b32 v153, v152\n"
  "v_mfma_f32_16x16x32_bf16 v[52:55], v[216:219], a[72:75], v[52:55]\n"
  "v_mfma_f32_16x16x32_bf16 v[52:55], v[220:223], a[76:79], v[52:55]\n"
  "buffer_load_dwordx4 v237, s[44:47], s56 offen offset:1024 lds\n"
  "v_mfma_f32_16x16x32_bf16 v[56:59], v[216:219], a[80:83], v[56:59]\n"
  "ds_read_b64_tr_b16 v[204:205], v229 offset:40960\n"
  "v_permlane16_swap_b32 v152, v153\n"
  "v_mfma_f32_16x16x32_bf16 v[56:59], v[220:223], a[84:87], v[56:59]\n"
  "ds_read_b64_tr_b16 v[206:207], v229 offset:41984\n"
  "v_add_f32 v152, v152, v153\n"
  "v_mfma_f32_16x16x32_bf16 v[60:63], v[216:219], a[88:91], v[60:63]\n"
  "v_mov_b32 v153, v152\n"
  "v_mfma_f32_16x16x32_bf16 v[60:63], v[220:223], a[92:95], v[60:63]\n"
  "s_waitcnt lgkmcnt(4)\n"
  "buffer_load_dwordx4 v238, s[44:47], s56 offen offset:2048 lds\n"
  "v_mfma_f32_16x16x32_bf16 v[64:67], v[192:195], a[64:67], v[64:67]\n"
  "ds_read_b64_tr_b16 v[208:209], v230 offset:32768\n"
  "v_permlane32_swap_b32 v152, v153\n"
  "v_mfma_f32_16x16x32_bf16 v[64:67], v[196:199], a[68:71], v[64:67]\n"
  "ds_read_b64_tr_b16 v[210:211], v230 offset:33792\n"
  "v_add_f32 v152, v152, v153\n"
  "v_mfma_f32_16x16x32_bf16 v[68:71], v[192:195], a[72:75], v[68:71]\n"
  "v_fmac_f32 v136, 0xbb000000, v152\n"
  "v_fmac_f32 v137, 0xbb000000, v152\n"
  "v_mfma_f32_16x16x32_bf16 v[68:71], v[196:199], a[76:79], v[68:71]\n"
  "buffer_load_dwordx4 v239, s[44:47], s56 offen offset:3072 lds\n"
  "v_mfma_f32_16x16x32_bf16 v[72:75], v[192:195], a[80:83], v[72:75]\n"
  "ds_read_b64_tr_b16 v[212:213], v230 offset:40960\n"
  "v_add_u32 v253, 1, v248\n"
  "v_mfma_f32_16x16x32_bf16 v[72:75], v[196:199], a[84:87], v[72:75]\n"
  "ds_read_b64_tr_b16 v[214:215], v230 offset:41984\n"
  "v_min_u32 v252, v250, v251\n"
  "v_mfma_f32_16x16x32_bf16 v[76:79], v[192:195], a[88:91], v[76:79]\n"
  "v_fmac_f32 v138, 0xbb000000, v152\n"
  "v_fmac_f32 v139, 0xbb000000, v152\n"
  "v_mfma_f32_16x16x32_bf16 v[76:79], v[196:199], a[92:95], v[76:79]\n"
  "s_waitcnt lgkmcnt(4)\n"
  "ds_read_b64_tr_b16 v[216:217], v231 offset:32768\n"
  "v_fmac_f32 v140, 0xbb000000, v152\n"
  "v_mfma_f32_16x16x32_bf16 v[80:83], v[200:203], a[64:67], v[80:83]\n"
  "buffer_store_dword v253, v252, s[48:51], s97 offen sc1\n"
  "v_mfma_f32_16x16x32_bf16 v[80:83], v[204:207], a[68:71], v[80:83]\n"
  "ds_read_b64_tr_b16 v[218:219], v231 offset:33792\n"
  "v_fmac_f32 v141, 0xbb000000, v152\n"
  "v_mfma_f32_16x16x32_bf16 v[84:87], v[200:203], a[72:75], v[84:87]\n"
  "v_fmac_f32 v142, 0xbb000000, v152\n"
  "v_fmac_f32 v143, 0xbb000000, v152\n"
  "v_mfma_f32_16x16x32_bf16 v[84:87], v[204:207], a[76:79], v[84:87]\n"
  "ds_read_b64_tr_b16 v[220:221], v231 offset:40960\n"
  "v_fmac_f32 v144, 0xbb000000, v152\n"
  "v_mfma_f32_16x16x32_bf16 v[88:91], v[200:203], a[80:83], v[88:91]\n"
  "v_fmac_f32 v145, 0xbb000000, v152\n"
  "v_fmac_f32 v146, 0xbb000000, v152\n"
  "v_mfma_f32_16x16x32_bf16 v[88:91], v[204:207], a[84:87], v[88:91]\n"
  "ds_read_b64_tr_b16 v[222:223], v231 offset:41984\n"
  "v_fmac_f32 v147, 0xbb000000, v152\n"
  "v_mfma_f32_16x16x32_bf16 v[92:95], v[200:203], a[88:91], v[92:95]\n"
  "s_waitcnt vmcnt(13)\n"
  "v_fmac_f32 v148, 0xbb000000, v152\n"
  "v_fmac_f32 v149, 0xbb000000, v152\n"
  "v_mfma_f32_16x16x32_bf16 v[92:95], v[204:207], a[92:95], v[92:95]\n"
  "s_barrier\n"
  "ds_read_b64_tr_b16 v[192:193], v224 offset:49152\n"
  "ds_read_b128 a[96:99], v240 offset:32768\n"
  "s_waitcnt lgkmcnt(5)\n"
  "v_mfma_f32_16x16x32_bf16 v[96:99], v[208:211], a[64:67], v[96:99]\n"
  "s_add_u32 s54, s54, 1\n"
  "ds_read_b128 a[100:103], v241 offset:32768\n"
  "v_fmac_f32 v150, 0xbb000000, v152\n"
  "v_mfma_f32_16x16x32_bf16 v[96:99], v[212:215], a[68:71], v[96:99]\n"
  "s_and_b32 s54, s54, 7\n"
  "ds_read_b64_tr_b16 v[194:195], v224 offset:50176\n"
  "ds_read_b128 a[104:107], v240 offset:34816\n"
  "v_mfma_f32_16x16x32_bf16 v[100:103], v[208:211], a[72:75], v[100:103]\n"
  "s_lshl_b32 s55, s54, 19\n"
  "ds_read_b128 a[108:111], v241 offset:34816\n"
  "v_fmac_f32 v151, 0xbb000000, v152\n"
  "v_mfma_f32_16x16x32_bf16 v[100:103], v[212:215], a[76:79], v[100:103]\n"
  "s_add_u32 m0, s57, 0x4000\n"
  "ds_read_b64_tr_b16 v[196:197], v224 offset:57344\n"
  "ds_read_b128 a[112:115], v240 offset:36864\n"
  "v_mfma_f32_16x16x32_bf16 v[104:107], v[208:211], a[80:83], v[104:107]\n"
  "ds_read_b128 a[116:119], v241 offset:36864\n"
  "v_mul_f32 v152, v136, v136\n"
  "v_mfma_f32_16x16x32_bf16 v[104:107], v[212:215], a[84:87], v[104:107]\n"
  "buffer_load_dwordx4 v232, s[36:39], s55 offen offset:0 lds\n"
  "v_mfma_f32_16x16x32_bf16 v[108:111], v[208:211], a[88:91], v[108:111]\n"
  "ds_read_b64_tr_b16 v[198:199], v224 offset:58368\n"
  "ds_read_b128 a[120:123], v240 offset:38912\n"
  "v_mfma_f32_16x16x32_bf16 v[108:111], v[212:215], a[92:95], v[108:111]\n"
  "s_waitcnt lgkmcnt(9)\n"
  "ds_read_b128 a[124:127], v241 offset:38912\n"
  "ds_read_b64_tr_b16 v[200:201], v225 offset:49152\n"
  "v_mfma_f32_16x16x32_bf16 v[112:115], v[216:219], a[64:67], v[112:115]\n"
  "v_mul_f32 v153, v137, v137\n"
  "v_fmac_f32 v152, v138, v138\n"
  "v_mfma_f32_16x16x32_bf16 v[112:115], v[220:223], a[68:71], v[112:115]\n"
  "buffer_load_dwordx4 v233, s[36:39], s55 offen offset:1024 lds\n"
  "v_mfma_f32_16x16x32_bf16 v[116:119], v[216:219], a[72:75], v[116:119]\n"
  "ds_read_b64_tr_b16 v[202:203], v225 offset:50176\n"
  "v_fmac_f32 v153, v139, v139\n"
  "v_mfma_f32_16x16x32_bf16 v[116:119], v[220:223], a[76:79], v[116:119]\n"
  "ds_read_b64_tr_b16 v[204:205], v225 offset:57344\n"
  "v_fmac_f32 v152, v140, v140\n"
  "v_mfma_f32_16x16x32_bf16 v[120:123], v[216:219], a[80:83], v[120:123]\n"
  "v_fmac_f32 v153, v141, v141\n"
  "v_fmac_f32 v152, v142, v142\n"
  "v_mfma_f32_16x16x32_bf16 v[120:123], v[220:223], a[84:87], v[120:123]\n"
  "buffer_load_dwordx4 v234, s[36:39], s55 offen offset:2048 lds\n"
  "v_mfma_f32_16x16x32_bf16 v[124:127], v[216:219], a[88:91], v[124:127]\n"
  "ds_read_b64_tr_b16 v[206:207], v225 offset:58368\n"
  "v_fmac_f32 v153, v143, v143\n"
  "v_mfma_f32_16x16x32_bf16 v[124:127], v[220:223], a[92:95], v[124:127]\n"
  "s_waitcnt lgkmcnt(3)\n"
  "ds_read_b64_tr_b16 v[208:209], v226 offset:49152\n"
  "v_fmac_f32 v152, v144, v144\n"
  "v_mfma_f32_16x16x32_bf16 v[0:3], v[192:195], a[96:99], v[0:3]\n"
  "v_fmac_f32 v153, v145, v145\n"
  "v_fmac_f32 v152, v146, v146\n"
  "v_mfma_f32_16x16x32_bf16 v[0:3], v[196:199], a[100:103], v[0:3]\n"
  "buffer_load_dwordx4 v235, s[36:39], s55 offen offset:3072 lds\n"
  "v_mfma_f32_16x16x32_bf16 v[4:7], v[192:195], a[104:107], v[4:7]\n"
  "s_lshl_b32 s56, s54, 7\n"
  "ds_read_b64_tr_b16 v[210:211], v226 offset:50176\n"
  "v_fmac_f32 v153, v147, v147\n"
  "v_mfma_f32_16x16x32_bf16 v[4:7], v[196:199], a[108:111], v[4:7]\n"
  "s_add_u32 m0, s58, 0x8000\n"
  "ds_read_b64_tr_b16 v[212:213], v226 offset:57344\n"
  "v_fmac_f32 v152, v148, v148\n"
  "v_mfma_f32_16x16x32_bf16 v[8:11], v[192:195], a[112:115], v[8:11]\n"
  "v_fmac_f32 v153, v149, v149\n"
  "v_fmac_f32 v152, v150, v150\n"
  "v_mfma_f32_16x16x32_bf16 v[8:11], v[196:199], a[116:119], v[8:11]\n"
  "ds_read_b64_tr_b16 v[214:215], v226 offset:58368\n"
  "v_fmac_f32 v153, v151, v151\n"
  "v_mfma_f32_16x16x32_bf16 v[12:15], v[192:195], a[120:123], v[12:15]\n"
  "v_add_f32 v152, v152, v153\n"
  "v_mfma_f32_16x16x32_bf16 v[12:15], v[196:199], a[124:127], v[12:15]\n"
  "s_waitcnt lgkmcnt(3)\n"
  "buffer_load_dwordx4 v236, s[44:47], s56 offen offset:0 lds\n"
  "v_mfma_f32_16x16x32_bf16 v[16:19], v[200:203], a[96:99], v[16:19]\n"
  "ds_read_b64_tr_b16 v[216:217], v227 offset:49152\n"
  "v_add_f32_dpp v152, v152, v152 row_ror:8 row_mask:0xf bank_mask:0xf\n"
  "v_mfma_f32_16x16x32_bf16 v[16:19], v[204:207], a[100:103], v[16:19]\n"
  "ds_read_b64_tr_b16 v[218:219], v227 offset:50176\n"
  "v_mfma_f32_16x16x32_bf16 v[20:23], v[200:203], a[104:107], v[20:23]\n"
  "v_add_f32_dpp v152, v152, v152 row_ror:4 row_mask:0xf bank_mask:0xf\n"
  "v_mfma_f32_16x16x32_bf16 v[20:23], v[204:207], a[108:111], v[20:23]\n"
  "buffer_load_dwordx4 v237, s[44:47], s56 offen offset:1024 lds\n"
  "v_mfma_f32_16x16x32_bf16 v[24:27], v[200:203], a[112:115], v[24:27]\n"
  "ds_read_b64_tr_b16 v[220:221], v227 offset:57344\n"
  "v_add_f32_dpp v152, v152, v152 row_ror:2 row_mask:0xf bank_mask:0xf\n"
  "v_mfma_f32_16x16x32_bf16 v[24:27], v[204:207], a[116:119], v[24:27]\n"
  "ds_read_b64_tr_b16 v[222:223], v227 offset:58368\n"
  "v_mov_b32 v153, v152\n"
  "v_mfma_f32_16x16x32_bf16 v[28:31], v[200:203], a[120:123], v[28:31]\n"
  "v_mfma_f32_16x16x32_bf16 v[28:31], v[204:207], a[124:127], v[28:31]\n"
  "s_waitcnt lgkmcnt(4)\n"
  "buffer_load_dwordx4 v238, s[44:47], s56 offen offset:2048 lds\n"
  "v_mfma_f32_16x16x32_bf16 v[32:35], v[208:211], a[96:99], v[32:35]\n"
  "ds_read_b64_tr_b16 v[192:193], v228 offset:49152\n"
  "v_permlane16_swap_b32 v152, v153\n"
  "v_mfma_f32_16x16x32_bf16 v[32:35], v[212:215], a[100:103], v[32:35]\n"
  "ds_read_b64_tr_b16 v[194:195], v228 offset:50176\n"
  "v_add_f32 v152, v152, v153\n"
  "v_mfma_f32_16x16x32_bf16 v[36:39], v[208:211], a[104:107], v[36:39]\n"
  "v_mov_b32 v153, v152\n"
  "v_mfma_f32_16x16x32_bf16 v[36:39], v[212:215], a[108:111], v[36:39]\n"
  "buffer_load_dwordx4 v239, s[44:47], s56 offen offset:3072 lds\n"
  "v_mfma_f32_16x16x32_bf16 v[40:43], v[208:211], a[112:115], v[40:43]\n"
  "s_add_u32 s56, s56, 0x8000\n"
  "ds_read_b64_tr_b16 v[196:197], v228 offset:57344\n"
  "v_permlane32_swap_b32 v152, v153\n"
  "v_mfma_f32_16x16x32_bf16 v[40:43], v[212:215], a[116:119], v[40:43]\n"
  "s_add_u32 m0, s58, 0x9000\n"
  "ds_read_b64_tr_b16 v[198:199], v228 offset:58368\n"
  "v_add_f32 v152, v152, v153\n"
  "v_mfma_f32_16x16x32_bf16 v[44:47], v[208:211], a[120:123], v[44:47]\n"
  "v_mov_b32 v153, 0x3727c5ac\n"
  "v_fmac_f32 v153, 0x3b000000, v152\n"
  "v_mfma_f32_16x16x32_bf16 v[44:47], v[212:215], a[124:127], v[44:47]\n"
  "s_waitcnt lgkmcnt(4)\n"
  "buffer_load_dwordx4 v236, s[44:47], s56 offen offset:0 lds\n"
  "v_mfma_f32_16x16x32_bf16 v[48:51], v[216:219], a[96:99], v[48:51]\n"
  "ds_read_b64_tr_b16 v[200:201], v229 offset:49152\n"
  "v_mfma_f32_16x16x32_bf16 v[48:51], v[220:223], a[100:103], v[48:51]\n"
  "ds_read_b64_tr_b16 v[202:203], v229 offset:50176\n"
  "v_mfma_f32_16x16x32_bf16 v[52:55], v[216:219], a[104:107], v[52:55]\n"
  "v_rsq_f32 v153, v153\n"
  "v_mfma_f32_16x16x32_bf16 v[52:55], v[220:223], a[108:111], v[52:55]\n"
  "buffer_load_dwordx4 v237, s[44:47], s56 offen offset:1024 lds\n"
  "v_mfma_f32_16x16x32_bf16 v[56:59], v[216:219], a[112:115], v[56:59]\n"
  "ds_read_b64_tr_b16 v[204:205], v229 offset:57344\n"
  "v_mul_f32 v153, 0x3e0293ee, v153\n"
  "v_mfma_f32_16x16x32_bf16 v[56:59], v[220:223], a[116:119], v[56:59]\n"
  "ds_read_b64_tr_b16 v[206:207], v229 offset:58368\n"
  "v_mul_f32 v136, v136, v153\n"
  "v_mfma_f32_16x16x32_bf16 v[60:63], v[216:219], a[120:123], v[60:63]\n"
  "v_mul_f32 v137, v137, v153\n"
  "v_mul_f32 v138, v138, v153\n"
  "v_mfma_f32_16x16x32_bf16 v[60:63], v[220:223], a[124:127], v[60:63]\n"
  "s_waitcnt lgkmcnt(4)\n"
  "buffer_load_dwordx4 v238, s[44:47], s56 offen offset:2048 lds\n"
  "v_mfma_f32_16x16x32_bf16 v[64:67], v[192:195], a[96:99], v[64:67]\n"
  "ds_read_b64_tr_b16 v[208:209], v230 offset:49152\n"
  "v_mul_f32 v139, v139, v153\n"
  "v_mfma_f32_16x16x32_bf16 v[64:67], v[196:199], a[100:103], v[64:67]\n"
  "ds_read_b64_tr_b16 v[210:211], v230 offset:50176\n"
  "v_mul_f32 v140, v140, v153\n"
  "v_mfma_f32_16x16x32_bf16 v[68:71], v[192:195], a[104:107], v[68:71]\n"
  "v_mul_f32 v141, v141, v153\n"
  "v_mul_f32 v142, v142, v153\n"
  "v_mfma_f32_16x16x32_bf16 v[68:71], v[196:199], a[108:111], v[68:71]\n"
  "buffer_load_dwordx4 v239, s[44:47], s56 offen offset:3072 lds\n"
  "v_mfma_f32_16x16x32_bf16 v[72:75], v[192:195], a[112:115], v[72:75]\n"
  "ds_read_b64_tr_b16 v[212:213], v230 offset:57344\n"
  "v_mul_f32 v143, v143, v153\n"
  "v_mfma_f32_16x16x32_bf16 v[72:75], v[196:199], a[116:119], v[72:75]\n"
  "ds_read_b64_tr_b16 v[214:215], v230 offset:58368\n"
  "v_mul_f32 v144, v144, v153\n"
  "v_mfma_f32_16x16x32_bf16 v[76:79], v[192:195], a[120:123], v[76:79]\n"
  "v_mul_f32 v145, v145, v153\n"
  "v_mul_f32 v146, v146, v153\n"
  "v_mfma_f32_16x16x32_bf16 v[76:79], v[196:199], a[124:127], v[76:79]\n"
  "s_waitcnt lgkmcnt(4)\n"
  "ds_read_b64_tr_b16 v[216:217], v231 offset:49152\n"
  "v_mul_f32 v147, v147, v153\n"
  "v_mfma_f32_16x16x32_bf16 v[80:83], v[200:203], a[96:99], v[80:83]\n"
  "v_mul_f32 v148, v148, v153\n"
  "v_mul_f32 v149, v149, v153\n"
  "v_mfma_f32_16x16x32_bf16 v[80:83], v[204:207], a[100:103], v[80:83]\n"
  "ds_read_b64_tr_b16 v[218:219], v231 offset:50176\n"
  "v_mul_f32 v150, v150, v153\n"
  "v_mfma_f32_16x16x32_bf16 v[84:87], v[200:203], a[104:107], v[84:87]\n"
  "v_mul_f32 v151, v151, v153\n"
  "v_cvt_pk_bf16_f32 v128, v136, v137\n"
  "v_mfma_f32_16x16x32_bf16 v[84:87], v[204:207], a[108:111], v[84:87]\n"
  "ds_read_b64_tr_b16 v[220:221], v231 offset:57344\n"
  "v_cvt_pk_bf16_f32 v129, v138, v139\n"
  "v_mfma_f32_16x16x32_bf16 v[88:91], v[200:203], a[112:115], v[88:91]\n"
  "v_cvt_pk_bf16_f32 v130, v140, v141\n"
  "v_cvt_pk_bf16_f32 v131, v142, v143\n"
  "v_mfma_f32_16x16x32_bf16 v[88:91], v[204:207], a[116:119], v[88:91]\n"
  "ds_read_b64_tr_b16 v[222:223], v231 offset:58368\n"
  "v_cvt_pk_bf16_f32 v132, v144, v145\n"
  "v_mfma_f32_16x16x32_bf16 v[92:95], v[200:203], a[120:123], v[92:95]\n"
  "s_waitcnt vmcnt(13)\n"
  "v_cvt_pk_bf16_f32 v133, v146, v147\n"
  "v_cvt_pk_bf16_f32 v134, v148, v149\n"
  "v_mfma_f32_16x16x32_bf16 v[92:95], v[204:207], a[124:127], v[92:95]\n"
  "s_barrier\n"
  "ds_read_b64_tr_b16 v[192:193], v224 offset:0\n"
  "ds_read_b128 a[128:131], v240 offset:0\n"
  "s_waitcnt lgkmcnt(5)\n"
  "v_mfma_f32_16x16x32_bf16 v[96:99], v[208:211], a[96:99], v[96:99]\n"
  "s_add_u32 s54, s54, 1\n"
  "ds_read_b128 a[132:135], v241 offset:0\n"
  "v_cvt_pk_bf16_f32 v135, v150, v151\n"
  "v_mfma_f32_16x16x32_bf16 v[96:99], v[212:215], a[100:103], v[96:99]\n"
  "s_and_b32 s54, s54, 7\n"
  "ds_read_b64_tr_b16 v[194:195], v224 offset:1024\n"
  "ds_read_b128 a[136:139], v240 offset:2048\n"
  "v_mfma_f32_16x16x32_bf16 v[100:103], v[208:211], a[104:107], v[100:103]\n"
  "s_lshl_b32 s55, s54, 19\n"
  "ds_read_b128 a[140:143], v241 offset:2048\n"
  "v_mfma_f32_16x16x32_bf16 v[100:103], v[212:215], a[108:111], v[100:103]\n"
  "s_add_u32 m0, s57, 0x8000\n"
  "ds_read_b64_tr_b16 v[196:197], v224 offset:8192\n"
  "ds_read_b128 a[144:147], v240 offset:4096\n"
  "v_mfma_f32_16x16x32_bf16 v[104:107], v[208:211], a[112:115], v[104:107]\n"
  "ds_read_b128 a[148:151], v241 offset:4096\n"
  "v_mfma_f32_16x16x32_bf16 v[104:107], v[212:215], a[116:119], v[104:107]\n"
  "buffer_load_dwordx4 v232, s[36:39], s55 offen offset:0 lds\n"
  "v_mfma_f32_16x16x32_bf16 v[108:111], v[208:211], a[120:123], v[108:111]\n"
  "ds_read_b64_tr_b16 v[198:199], v224 offset:9216\n"
  "ds_read_b128 a[152:155], v240 offset:6144\n"
  "v_mfma_f32_16x16x32_bf16 v[108:111], v[212:215], a[124:127], v[108:111]\n"
  "s_waitcnt lgkmcnt(9)\n"
  "ds_read_b128 a[156:159], v241 offset:6144\n"
  "ds_read_b64_tr_b16 v[200:201], v225 offset:0\n"
  "v_mfma_f32_16x16x32_bf16 v[112:115], v[216:219], a[96:99], v[112:115]\n"
  "ds_write_b128 v154, v[128:131]\n"
  "v_mfma_f32_16x16x32_bf16 v[112:115], v[220:223], a[100:103], v[112:115]\n"
  "buffer_load_dwordx4 v233, s[36:39], s55 offen offset:1024 lds\n"
  "v_mfma_f32_16x16x32_bf16 v[116:119], v[216:219], a[104:107], v[116:119]\n"
  "ds_read_b64_tr_b16 v[202:203], v225 offset:1024\n"
  "v_mfma_f32_16x16x32_bf16 v[116:119], v[220:223], a[108:111], v[116:119]\n"
  "ds_read_b64_tr_b16 v[204:205], v225 offset:8192\n"
  "v_mfma_f32_16x16x32_bf16 v[120:123], v[216:219], a[112:115], v[120:123]\n"
  "ds_write_b128 v154, v[132:135] offset:4096\n"
  "v_mfma_f32_16x16x32_bf16 v[120:123], v[220:223], a[116:119], v[120:123]\n"
  "buffer_load_dwordx4 v234, s[36:39], s55 offen offset:2048 lds\n"
  "v_mfma_f32_16x16x32_bf16 v[124:127], v[216:219], a[120:123], v[124:127]\n"
  "ds_read_b64_tr_b16 v[206:207], v225 offset:9216\n"
  "v_mfma_f32_16x16x32_bf16 v[124:127], v[220:223], a[124:127], v[124:127]\n"
  "s_waitcnt lgkmcnt(5)\n"
  "ds_read_b64_tr_b16 v[208:209], v226 offset:0\n"
  "v_mfma_f32_16x16x32_bf16 v[0:3], v[192:195], a[128:131], v[0:3]\n"
  "v_mfma_f32_16x16x32_bf16 v[0:3], v[196:199], a[132:135], v[0:3]\n"
  "buffer_load_dwordx4 v235, s[36:39], s55 offen offset:3072 lds\n"
  "v_mfma_f32_16x16x32_bf16 v[4:7], v[192:195], a[136:139], v[4:7]\n"
  "s_lshl_b32 s56, s54, 7\n"
  "ds_read_b64_tr_b16 v[210:211], v226 offset:1024\n"
  "v_mfma_f32_16x16x32_bf16 v[4:7], v[196:199], a[140:143], v[4:7]\n"
  "s_add_u32 m0, s58, 0x0\n"
  "ds_read_b64_tr_b16 v[212:213], v226 offset:8192\n"
  "v_mfma_f32_16x16x32_bf16 v[8:11], v[192:195], a[144:147], v[8:11]\n"
  "v_mfma_f32_16x16x32_bf16 v[8:11], v[196:199], a[148:151], v[8:11]\n"
  "ds_read_b64_tr_b16 v[214:215], v226 offset:9216\n"
  "v_mfma_f32_16x16x32_bf16 v[12:15], v[192:195], a[152:155], v[12:15]\n"
  "v_mfma_f32_16x16x32_bf16 v[12:15], v[196:199], a[156:159], v[12:15]\n"
  "s_waitcnt lgkmcnt(3)\n"
  "buffer_load_dwordx4 v236, s[44:47], s56 offen offset:0 lds\n"
  "v_mfma_f32_16x16x32_bf16 v[16:19], v[200:203], a[128:131], v[16:19]\n"
  "ds_read_b64_tr_b16 v[216:217], v227 offset:0\n"
  "v_mfma_f32_16x16x32_bf16 v[16:19], v[204:207], a[132:135], v[16:19]\n"
  "ds_read_b64_tr_b16 v[218:219], v227 offset:1024\n"
  "v_mfma_f32_16x16x32_bf16 v[20:23], v[200:203], a[136:139], v[20:23]\n"
  "v_mfma_f32_16x16x32_bf16 v[20:23], v[204:207], a[140:143], v[20:23]\n"
  "buffer_load_dwordx4 v237, s[44:47], s56 offen offset:1024 lds\n"
  "v_mfma_f32_16x16x32_bf16 v[24:27], v[200:203], a[144:147], v[24:27]\n"
  "ds_read_b64_tr_b16 v[220:221], v227 offset:8192\n"
  "v_mfma_f32_16x16x32_bf16 v[24:27], v[204:207], a[148:151], v[24:27]\n"
  "ds_read_b64_tr_b16 v[222:223], v227 offset:9216\n"
  "v_mfma_f32_16x16x32_bf16 v[28:31], v[200:203], a[152:155], v[28:31]\n"
  "v_mfma_f32_16x16x32_bf16 v[28:31], v[204:207], a[156:159], v[28:31]\n"
  "s_waitcnt lgkmcnt(4)\n"
  "buffer_load_dwordx4 v238, s[44:47], s56 offen offset:2048 lds\n"
  "v_mfma_f32_16x16x32_bf16 v[32:35], v[208:211], a[128:131], v[32:35]\n"
  "ds_read_b64_tr_b16 v[192:193], v228 offset:0\n"
  "v_mfma_f32_16x16x32_bf16 v[32:35], v[212:215], a[132:135], v[32:35]\n"
  "ds_read_b64_tr_b16 v[194:195], v228 offset:1024\n"
  "v_mfma_f32_16x16x32_bf16 v[36:39], v[208:211], a[136:139], v[36:39]\n"
  "v_mfma_f32_16x16x32_bf16 v[36:39], v[212:215], a[140:143], v[36:39]\n"
  "buffer_load_dwordx4 v239, s[44:47], s56 offen offset:3072 lds\n"
  "v_mfma_f32_16x16x32_bf16 v[40:43], v[208:211], a[144:147], v[40:43]\n"
  "s_add_u32 s56, s56, 0x8000\n"
  "ds_read_b64_tr_b16 v[196:197], v228 offset:8192\n"
  "v_mfma_f32_16x16x32_bf16 v[40:43], v[212:215], a[148:151], v[40:43]\n"
  "s_add_u32 m0, s58, 0x1000\n"
  "ds_read_b64_tr_b16 v[198:199], v228 offset:9216\n"
  "v_mfma_f32_16x16x32_bf16 v[44:47], v[208:211], a[152:155], v[44:47]\n"
  "v_mfma_f32_16x16x32_bf16 v[44:47], v[212:215], a[156:159], v[44:47]\n"
  "s_waitcnt lgkmcnt(4)\n"
  "buffer_load_dwordx4 v236, s[44:47], s56 offen offset:0 lds\n"
  "v_mfma_f32_16x16x32_bf16 v[48:51], v[216:219], a[128:131], v[48:51]\n"
  "ds_read_b64_tr_b16 v[200:201], v229 offset:0\n"
  "v_mfma_f32_16x16x32_bf16 v[48:51], v[220:223], a[132:135], v[48:51]\n"
  "ds_read_b64_tr_b16 v[202:203], v229 offset:1024\n"
  "v_mfma_f32_16x16x32_bf16 v[52:55], v[216:219], a[136:139], v[52:55]\n"
  "v_mfma_f32_16x16x32_bf16 v[52:55], v[220:223], a[140:143], v[52:55]\n"
  "buffer_load_dwordx4 v237, s[44:47], s56 offen offset:1024 lds\n"
  "v_mfma_f32_16x16x32_bf16 v[56:59], v[216:219], a[144:147], v[56:59]\n"
  "ds_read_b64_tr_b16 v[204:205], v229 offset:8192\n"
  "v_mfma_f32_16x16x32_bf16 v[56:59], v[220:223], a[148:151], v[56:59]\n"
  "ds_read_b64_tr_b16 v[206:207], v229 offset:9216\n"
  "v_mfma_f32_16x16x32_bf16 v[60:63], v[216:219], a[152:155], v[60:63]\n"
  "v_mfma_f32_16x16x32_bf16 v[60:63], v[220:223], a[156:159], v[60:63]\n"
  "s_waitcnt lgkmcnt(4)\n"
  "buffer_load_dwordx4 v238, s[44:47], s56 offen offset:2048 lds\n"
  "v_mfma_f32_16x16x32_bf16 v[64:67], v[192:195], a[128:131], v[64:67]\n"
  "ds_read_b64_tr_b16 v[208:209], v230 offset:0\n"
  "v_mfma_f32_16x16x32_bf16 v[64:67], v[196:199], a[132:135], v[64:67]\n"
  "ds_read_b64_tr_b16 v[210:211], v230 offset:1024\n"
  "v_mfma_f32_16x16x32_bf16 v[68:71], v[192:195], a[136:139], v[68:71]\n"
  "v_mfma_f32_16x16x32_bf16 v[68:71], v[196:199], a[140:143], v[68:71]\n"
  "buffer_load_dwordx4 v239, s[44:47], s56 offen offset:3072 lds\n"
  "v_mfma_f32_16x16x32_bf16 v[72:75], v[192:195], a[144:147], v[72:75]\n"
  "ds_read_b64_tr_b16 v[212:213], v230 offset:8192\n"
  "v_mfma_f32_16x16x32_bf16 v[72:75], v[196:199], a[148:151], v[72:75]\n"
  "ds_read_b64_tr_b16 v[214:215], v230 offset:9216\n"
  "v_mfma_f32_16x16x32_bf16 v[76:79], v[192:195], a[152:155], v[76:79]\n"
  "v_mfma_f32_16x16x32_bf16 v[76:79], v[196:199], a[156:159], v[76:79]\n"
  "s_waitcnt lgkmcnt(4)\n"
  "ds_read_b64_tr_b16 v[216:217], v231 offset:0\n"
  "v_mfma_f32_16x16x32_bf16 v[80:83], v[200:203], a[128:131], v[80:83]\n"
  "v_mfma_f32_16x16x32_bf16 v[80:83], v[204:207], a[132:135], v[80:83]\n"
  "ds_read_b64_tr_b16 v[218:219], v231 offset:1024\n"
  "v_mfma_f32_16x16x32_bf16 v[84:87], v[200:203], a[136:139], v[84:87]\n"
  "v_mfma_f32_16x16x32_bf16 v[84:87], v[204:207], a[140:143], v[84:87]\n"
  "ds_read_b64_tr_b16 v[220:221], v231 offset:8192\n"
  "v_mfma_f32_16x16x32_bf16 v[88:91], v[200:203], a[144:147], v[88:91]\n"
  "v_mfma_f32_16x16x32_bf16 v[88:91], v[204:207], a[148:151], v[88:91]\n"
  "ds_read_b64_tr_b16 v[222:223], v231 offset:9216\n"
  "v_mfma_f32_16x16x32_bf16 v[92:95], v[200:203], a[152:155], v[92:95]\n"
  "s_waitcnt vmcnt(12)\n"
  "v_mfma_f32_16x16x32_bf16 v[92:95], v[204:207], a[156:159], v[92:95]\n"
  "s_barrier\n"
  "ds_read_b64_tr_b16 v[192:193], v224 offset:16384\n"
  "ds_read_b128 a[160:163], v240 offset:32768\n"
  "s_waitcnt lgkmcnt(5)\n"
  "v_mfma_f32_16x16x32_bf16 v[96:99], v[208:211], a[128:131], v[96:99]\n"
  "s_add_u32 s54, s54, 1\n"
  "ds_read_b128 a[164:167], v241 offset:32768\n"
  "v_mfma_f32_16x16x32_bf16 v[96:99], v[212:215], a[132:135], v[96:99]\n"
  "s_and_b32 s54, s54, 7\n"
  "ds_read_b64_tr_b16 v[194:195], v224 offset:17408\n"
  "ds_read_b128 a[168:171], v240 offset:34816\n"
  "v_mfma_f32_16x16x32_bf16 v[100:103], v[208:211], a[136:139], v[100:103]\n"
  "s_lshl_b32 s55, s54, 19\n"
  "ds_read_b128 a[172:175], v241 offset:34816\n"
  "v_mfma_f32_16x16x32_bf16 v[100:103], v[212:215], a[140:143], v[100:103]\n"
  "s_add_u32 m0, s57, 0xc000\n"
  "ds_read_b64_tr_b16 v[196:197], v224 offset:24576\n"
  "ds_read_b128 a[176:179], v240 offset:36864\n"
  "v_mfma_f32_16x16x32_bf16 v[104:107], v[208:211], a[144:147], v[104:107]\n"
  "ds_read_b128 a[180:183], v241 offset:36864\n"
  "v_mfma_f32_16x16x32_bf16 v[104:107], v[212:215], a[148:151], v[104:107]\n"
  "buffer_load_dwordx4 v232, s[36:39], s55 offen offset:0 lds\n"
  "v_mfma_f32_16x16x32_bf16 v[108:111], v[208:211], a[152:155], v[108:111]\n"
  "ds_read_b64_tr_b16 v[198:199], v224 offset:25600\n"
  "ds_read_b128 a[184:187], v240 offset:38912\n"
  "v_mfma_f32_16x16x32_bf16 v[108:111], v[212:215], a[156:159], v[108:111]\n"
  "s_waitcnt lgkmcnt(9)\n"
  "ds_read_b128 a[188:191], v241 offset:38912\n"
  "ds_read_b64_tr_b16 v[200:201], v225 offset:16384\n"
  "v_mfma_f32_16x16x32_bf16 v[112:115], v[216:219], a[128:131], v[112:115]\n"
  "v_mfma_f32_16x16x32_bf16 v[112:115], v[220:223], a[132:135], v[112:115]\n"
  "buffer_load_dwordx4 v233, s[36:39], s55 offen offset:1024 lds\n"
  "v_mfma_f32_16x16x32_bf16 v[116:119], v[216:219], a[136:139], v[116:119]\n"
  "ds_read_b64_tr_b16 v[202:203], v225 offset:17408\n"
  "v_mfma_f32_16x16x32_bf16 v[116:119], v[220:223], a[140:143], v[116:119]\n"
  "ds_read_b64_tr_b16 v[204:205], v225 offset:24576\n"
  "v_mfma_f32_16x16x32_bf16 v[120:123], v[216:219], a[144:147], v[120:123]\n"
  "v_mfma_f32_16x16x32_bf16 v[120:123], v[220:223], a[148:151], v[120:123]\n"
  "buffer_load_dwordx4 v234, s[36:39], s55 offen offset:2048 lds\n"
  "v_mfma_f32_16x16x32_bf16 v[124:127], v[216:219], a[152:155], v[124:127]\n"
  "ds_read_b64_tr_b16 v[206:207], v225 offset:25600\n"
  "v_mfma_f32_16x16x32_bf16 v[124:127], v[220:223], a[156:159], v[124:127]\n"
  "s_waitcnt lgkmcnt(3)\n"
  "ds_read_b64_tr_b16 v[208:209], v226 offset:16384\n"
  "v_mfma_f32_16x16x32_bf16 v[0:3], v[192:195], a[160:163], v[0:3]\n"
  "v_mfma_f32_16x16x32_bf16 v[0:3], v[196:199], a[164:167], v[0:3]\n"
  "buffer_load_dwordx4 v235, s[36:39], s55 offen offset:3072 lds\n"
  "v_mfma_f32_16x16x32_bf16 v[4:7], v[192:195], a[168:171], v[4:7]\n"
  "s_lshl_b32 s56, s54, 7\n"
  "ds_read_b64_tr_b16 v[210:211], v226 offset:17408\n"
  "v_mfma_f32_16x16x32_bf16 v[4:7], v[196:199], a[172:175], v[4:7]\n"
  "s_add_u32 m0, s58, 0x8000\n"
  "ds_read_b64_tr_b16 v[212:213], v226 offset:24576\n"
  "v_mfma_f32_16x16x32_bf16 v[8:11], v[192:195], a[176:179], v[8:11]\n"
  "v_mfma_f32_16x16x32_bf16 v[8:11], v[196:199], a[180:183], v[8:11]\n"
  "ds_read_b64_tr_b16 v[214:215], v226 offset:25600\n"
  "v_mfma_f32_16x16x32_bf16 v[12:15], v[192:195], a[184:187], v[12:15]\n"
  "v_mfma_f32_16x16x32_bf16 v[12:15], v[196:199], a[188:191], v[12:15]\n"
  "s_waitcnt lgkmcnt(3)\n"
  "buffer_load_dwordx4 v236, s[44:47], s56 offen offset:0 lds\n"
  "v_mfma_f32_16x16x32_bf16 v[16:19], v[200:203], a[160:163], v[16:19]\n"
  "ds_read_b64_tr_b16 v[216:217], v227 offset:16384\n"
  "v_mfma_f32_16x16x32_bf16 v[16:19], v[204:207], a[164:167], v[16:19]\n"
  "ds_read_b64_tr_b16 v[218:219], v227 offset:17408\n"
  "v_mfma_f32_16x16x32_bf16 v[20:23], v[200:203], a[168:171], v[20:23]\n"
  "v_mfma_f32_16x16x32_bf16 v[20:23], v[204:207], a[172:175], v[20:23]\n"
  "buffer_load_dwordx4 v237, s[44:47], s56 offen offset:1024 lds\n"
  "v_mfma_f32_16x16x32_bf16 v[24:27], v[200:203], a[176:179], v[24:27]\n"
  "ds_read_b64_tr_b16 v[220:221], v227 offset:24576\n"
  "v_mfma_f32_16x16x32_bf16 v[24:27], v[204:207], a[180:183], v[24:27]\n"
  "ds_read_b64_tr_b16 v[222:223], v227 offset:25600\n"
  "v_mfma_f32_16x16x32_bf16 v[28:31], v[200:203], a[184:187], v[28:31]\n"
  "v_mfma_f32_16x16x32_bf16 v[28:31], v[204:207], a[188:191], v[28:31]\n"
  "s_waitcnt lgkmcnt(4)\n"
  "buffer_load_dwordx4 v238, s[44:47], s56 offen offset:2048 lds\n"
  "v_mfma_f32_16x16x32_bf16 v[32:35], v[208:211], a[160:163], v[32:35]\n"
  "ds_read_b64_tr_b16 v[192:193], v228 offset:16384\n"
  "v_mfma_f32_16x16x32_bf16 v[32:35], v[212:215], a[164:167], v[32:35]\n"
  "ds_read_b64_tr_b16 v[194:195], v228 offset:17408\n"
  "v_mfma_f32_16x16x32_bf16 v[36:39], v[208:211], a[168:171], v[36:39]\n"
  "v_mfma_f32_16x16x32_bf16 v[36:39], v[212:215], a[172:175], v[36:39]\n"
  "buffer_load_dwordx4 v239, s[44:47], s56 offen offset:3072 lds\n"
  "v_mfma_f32_16x16x32_bf16 v[40:43], v[208:211], a[176:179], v[40:43]\n"
  "s_add_u32 s56, s56, 0x8000\n"
  "ds_read_b64_tr_b16 v[196:197], v228 offset:24576\n"
  "v_mfma_f32_16x16x32_bf16 v[40:43], v[212:215], a[180:183], v[40:43]\n"
  "s_add_u32 m0, s58, 0x9000\n"
  "ds_read_b64_tr_b16 v[198:199], v228 offset:25600\n"
  "v_mfma_f32_16x16x32_bf16 v[44:47], v[208:211], a[184:187], v[44:47]\n"
  "v_mfma_f32_16x16x32_bf16 v[44:47], v[212:215], a[188:191], v[44:47]\n"
  "s_waitcnt lgkmcnt(4)\n"
  "buffer_load_dwordx4 v236, s[44:47], s56 offen offset:0 lds\n"
  "v_mfma_f32_16x16x32_bf16 v[48:51], v[216:219], a[160:163], v[48:51]\n"
  "ds_read_b64_tr_b16 v[200:201], v229 offset:16384\n"
  "ds_read_b128 v[128:131], v155 offset:0\n"
  "v_mfma_f32_16x16x32_bf16 v[48:51], v[220:223], a[164:167], v[48:51]\n"
  "ds_read_b64_tr_b16 v[202:203], v229 offset:17408\n"
  "ds_read_b128 v[132:135], v155 offset:1024\n"
  "v_mfma_f32_16x16x32_bf16 v[52:55], v[216:219], a[168:171], v[52:55]\n"
  "v_mfma_f32_16x16x32_bf16 v[52:55], v[220:223], a[172:175], v[52:55]\n"
  "buffer_load_dwordx4 v237, s[44:47], s56 offen offset:1024 lds\n"
  "v_mfma_f32_16x16x32_bf16 v[56:59], v[216:219], a[176:179], v[56:59]\n"
  "ds_read_b64_tr_b16 v[204:205], v229 offset:24576\n"
  "v_mfma_f32_16x16x32_bf16 v[56:59], v[220:223], a[180:183], v[56:59]\n"
  "ds_read_b64_tr_b16 v[206:207], v229 offset:25600\n"
  "v_mfma_f32_16x16x32_bf16 v[60:63], v[216:219], a[184:187], v[60:63]\n"
  "v_mfma_f32_16x16x32_bf16 v[60:63], v[220:223], a[188:191], v[60:63]\n"
  "s_waitcnt lgkmcnt(6)\n"
  "buffer_load_dwordx4 v238, s[44:47], s56 offen offset:2048 lds\n"
  "v_mfma_f32_16x16x32_bf16 v[64:67], v[192:195], a[160:163], v[64:67]\n"
  "mk_tp1:\n"
  "s_cbranch_execz mk_tp5\n"
  "ds_read_b64_tr_b16 v[208:209], v230 offset:16384\n"
  "v_mfma_f32_16x16x32_bf16 v[64:67], v[196:199], a[164:167], v[64:67]\n"
  "ds_read_b64_tr_b16 v[210:211], v230 offset:17408\n"
  "v_mfma_f32_16x16x32_bf16 v[68:71], v[192:195], a[168:171], v[68:71]\n"
  "v_mfma_f32_16x16x32_bf16 v[68:71], v[196:199], a[172:175], v[68:71]\n"
  "buffer_load_dwordx4 v239, s[44:47], s56 offen offset:3072 lds\n"
  "v_mfma_f32_16x16x32_bf16 v[72:75], v[192:195], a[176:179], v[72:75]\n"
  "ds_read_b64_tr_b16 v[212:213], v230 offset:24576\n"
  "v_mfma_f32_16x16x32_bf16 v[72:75], v[196:199], a[180:183], v[72:75]\n"
  "ds_read_b64_tr_b16 v[214:215], v230 offset:25600\n"
  "v_mfma_f32_16x16x32_bf16 v[76:79], v[192:195], a[184:187], v[76:79]\n"
  "v_mfma_f32_16x16x32_bf16 v[76:79], v[196:199], a[188:191], v[76:79]\n"
  "s_waitcnt lgkmcnt(4)\n"
  "buffer_store_dwordx4 v[128:131], v156, s[48:51], 0 offen offset:0 sc1\n"
  "v_mfma_f32_16x16x32_bf16 v[80:83], v[200:203], a[160:163], v[80:83]\n"
  "ds_read_b64_tr_b16 v[216:217], v231 offset:16384\n"
  "v_mfma_f32_16x16x32_bf16 v[80:83], v[204:207], a[164:167], v[80:83]\n"
  "buffer_store_dwordx4 v[132:135], v156, s[48:51], 0 offen offset:2048 sc1\n"
  "v_mfma_f32_16x16x32_bf16 v[84:87], v[200:203], a[168:171], v[84:87]\n"
  "ds_read_b64_tr_b16 v[218:219], v231 offset:17408\n"
  "v_mfma_f32_16x16x32_bf16 v[84:87], v[204:207], a[172:175], v[84:87]\n"
  "ds_read_b64_tr_b16 v[220:221], v231 offset:24576\n"
  "v_mfma_f32_16x16x32_bf16 v[88:91], v[200:203], a[176:179], v[88:91]\n"
  "v_mfma_f32_16x16x32_bf16 v[88:91], v[204:207], a[180:183], v[88:91]\n"
  "ds_read_b64_tr_b16 v[222:223], v231 offset:25600\n"
  "v_mfma_f32_16x16x32_bf16 v[92:95], v[200:203], a[184:187], v[92:95]\n"
  "s_waitcnt vmcnt(14)\n"
  "v_mfma_f32_16x16x32_bf16 v[92:95], v[204:207], a[188:191], v[92:95]\n"
  "s_barrier\n"
  "buffer_store_dwordx4 v[168:171], v157, s[48:51], 0 offen sc1\n"
  "s_waitcnt lgkmcnt(4)\n"
  "v_mfma_f32_16x16x32_bf16 v[96:99], v[208:211], a[160:163], v[96:99]\n"
  "ds_read_b64_tr_b16 v[192:193], v224 offset:32768\n"
  "ds_read_b128 a[192:195], v240 offset:0\n"
  "v_mfma_f32_16x16x32_bf16 v[96:99], v[212:215], a[164:167], v[96:99]\n"
  "buffer_load_dword v253, v249, s[48:51], s97 offen sc1\n"
  "v_mfma_f32_16x16x32_bf16 v[100:103], v[208:211], a[168:171], v[100:103]\n"
  "s_lshl_b32 s61, s53, 8\n"
  "ds_read_b128 a[196:199], v241 offset:0\n"
  "ds_read_b64_tr_b16 v[194:195], v224 offset:33792\n"
  "v_mfma_f32_16x16x32_bf16 v[100:103], v[212:215], a[172:175], v[100:103]\n"
  "s_add_u32 s36, s16, s61\n"
  "s_addc_u32 s37, s17, 0\n"
  "ds_read_b128 a[200:203], v240 offset:2048\n"
  "ds_read_b128 a[204:207], v241 offset:2048\n"
  "v_mfma_f32_16x16x32_bf16 v[104:107], v[208:211], a[176:179], v[104:107]\n"
  "s_add_u32 s54, s54, 1\n"
  "ds_read_b64_tr_b16 v[196:197], v224 offset:40960\n"
  "ds_read_b128 a[208:211], v240 offset:4096\n"
  "v_mfma_f32_16x16x32_bf16 v[104:107], v[212:215], a[180:183], v[104:107]\n"
  "s_and_b32 s54, s54, 7\n"
  "ds_read_b128 a[212:215], v241 offset:4096\n"
  "ds_read_b64_tr_b16 v[198:199], v224 offset:41984\n"
  "v_mfma_f32_16x16x32_bf16 v[108:111], v[208:211], a[184:187], v[108:111]\n"
  "s_lshl_b32 s55, s54, 19\n"
  "ds_read_b128 a[216:219], v240 offset:6144\n"
  "ds_read_b128 a[220:223], v241 offset:6144\n"
  "v_mfma_f32_16x16x32_bf16 v[108:111], v[212:215], a[188:191], v[108:111]\n"
  "s_waitcnt lgkmcnt(12)\n"
  "s_add_u32 m0, s57, 0x0\n"
  "ds_read_b64_tr_b16 v[200:201], v225 offset:32768\n"
  "v_mfma_f32_16x16x32_bf16 v[112:115], v[216:219], a[160:163], v[112:115]\n"
  "buffer_load_dwordx4 v232, s[36:39], s55 offen offset:0 lds\n"
  "v_mfma_f32_16x16x32_bf16 v[112:115], v[220:223], a[164:167], v[112:115]\n"
  "buffer_load_dwordx4 v233, s[36:39], s55 offen offset:1024 lds\n"
  "v_mfma_f32_16x16x32_bf16 v[116:119], v[216:219], a[168:171], v[116:119]\n"
  "ds_read_b64_tr_b16 v[202:203], v225 offset:33792\n"
  "v_mfma_f32_16x16x32_bf16 v[116:119], v[220:223], a[172:175], v[116:119]\n"
  "ds_read_b64_tr_b16 v[204:205], v225 offset:40960\n"
  "v_mfma_f32_16x16x32_bf16 v[120:123], v[216:219], a[176:179], v[120:123]\n"
  "v_mfma_f32_16x16x32_bf16 v[120:123], v[220:223], a[180:183], v[120:123]\n"
  "buffer_load_dwordx4 v234, s[36:39], s55 offen offset:2048 lds\n"
  "v_mfma_f32_16x16x32_bf16 v[124:127], v[216:219], a[184:187], v[124:127]\n"
  "ds_read_b64_tr_b16 v[206:207], v225 offset:41984\n"
  "v_mfma_f32_16x16x32_bf16 v[124:127], v[220:223], a[188:191], v[124:127]\n"
  "s_waitcnt lgkmcnt(3)\n"
  "ds_read_b64_tr_b16 v[208:209], v226 offset:32768\n"
  "v_mfma_f32_16x16x32_bf16 v[0:3], v[192:195], a[192:195], v[0:3]\n"
  "v_mfma_f32_16x16x32_bf16 v[0:3], v[196:199], a[196:199], v[0:3]\n"
  "buffer_load_dwordx4 v235, s[36:39], s55 offen offset:3072 lds\n"
  "v_mfma_f32_16x16x32_bf16 v[4:7], v[192:195], a[200:203], v[4:7]\n"
  "ds_read_b64_tr_b16 v[210:211], v226 offset:33792\n"
  "v_mfma_f32_16x16x32_bf16 v[4:7], v[196:199], a[204:207], v[4:7]\n"
  "ds_read_b64_tr_b16 v[212:213], v226 offset:40960\n"
  "v_mfma_f32_16x16x32_bf16 v[8:11], v[192:195], a[208:211], v[8:11]\n"
  "v_mfma_f32_16x16x32_bf16 v[8:11], v[196:199], a[212:215], v[8:11]\n"
  "ds_read_b64_tr_b16 v[214:215], v226 offset:41984\n"
  "v_mfma_f32_16x16x32_bf16 v[12:15], v[192:195], a[216:219], v[12:15]\n"
  "v_mfma_f32_16x16x32_bf16 v[12:15], v[196:199], a[220:223], v[12:15]\n"
  "s_waitcnt lgkmcnt(3)\n"
  "ds_read_b64_tr_b16 v[216:217], v227 offset:32768\n"
  "v_mfma_f32_16x16x32_bf16 v[16:19], v[200:203], a[192:195], v[16:19]\n"
  "v_mfma_f32_16x16x32_bf16 v[16:19], v[204:207], a[196:199], v[16:19]\n"
  "ds_read_b64_tr_b16 v[218:219], v227 offset:33792\n"
  "v_mfma_f32_16x16x32_bf16 v[20:23], v[200:203], a[200:203], v[20:23]\n"
  "v_mfma_f32_16x16x32_bf16 v[20:23], v[204:207], a[204:207], v[20:23]\n"
  "ds_read_b64_tr_b16 v[220:221], v227 offset:40960\n"
  "v_mfma_f32_16x16x32_bf16 v[24:27], v[200:203], a[208:211], v[24:27]\n"
  "v_mfma_f32_16x16x32_bf16 v[24:27], v[204:207], a[212:215], v[24:27]\n"
  "ds_read_b64_tr_b16 v[222:223], v227 offset:41984\n"
  "v_mfma_f32_16x16x32_bf16 v[28:31], v[200:203], a[216:219], v[28:31]\n"
  "v_mfma_f32_16x16x32_bf16 v[28:31], v[204:207], a[220:223], v[28:31]\n"
  "s_waitcnt lgkmcnt(3)\n"
  "ds_read_b64_tr_b16 v[192:193], v228 offset:32768\n"
  "v_mfma_f32_16x16x32_bf16 v[32:35], v[208:211], a[192:195], v[32:35]\n"
  "v_mfma_f32_16x16x32_bf16 v[32:35], v[212:215], a[196:199], v[32:35]\n"
  "ds_read_b64_tr_b16 v[194:195], v228 offset:33792\n"
  "v_mfma_f32_16x16x32_bf16 v[36:39], v[208:211], a[200:203], v[36:39]\n"
  "v_mfma_f32_16x16x32_bf16 v[36:39], v[212:215], a[204:207], v[36:39]\n"
  "ds_read_b64_tr_b16 v[196:197], v228 offset:40960\n"
  "v_mfma_f32_16x16x32_bf16 v[40:43], v[208:211], a[208:211], v[40:43]\n"
  "v_mfma_f32_16x16x32_bf16 v[40:43], v[212:215], a[212:215], v[40:43]\n"
  "ds_read_b64_tr_b16 v[198:199], v228 offset:41984\n"
  "v_mfma_f32_16x16x32_bf16 v[44:47], v[208:211], a[216:219], v[44:47]\n"
  "v_mfma_f32_16x16x32_bf16 v[44:47], v[212:215], a[220:223], v[44:47]\n"
  "s_waitcnt lgkmcnt(3)\n"
  "ds_read_b64_tr_b16 v[200:201], v229 offset:32768\n"
  "v_mfma_f32_16x16x32_bf16 v[48:51], v[216:219], a[192:195], v[48:51]\n"
  "v_mfma_f32_16x16x32_bf16 v[48:51], v[220:223], a[196:199], v[48:51]\n"
  "ds_read_b64_tr_b16 v[202:203], v229 offset:33792\n"
  "v_mfma_f32_16x16x32_bf16 v[52:55], v[216:219], a[200:203], v[52:55]\n"
  "v_mfma_f32_16x16x32_bf16 v[52:55], v[220:223], a[204:207], v[52:55]\n"
  "ds_read_b64_tr_b16 v[204:205], v229 offset:40960\n"
  "v_mfma_f32_16x16x32_bf16 v[56:59], v[216:219], a[208:211], v[56:59]\n"
  "v_mfma_f32_16x16x32_bf16 v[56:59], v[220:223], a[212:215], v[56:59]\n"
  "ds_read_b64_tr_b16 v[206:207], v229 offset:41984\n"
  "v_mfma_f32_16x16x32_bf16 v[60:63], v[216:219], a[216:219], v[60:63]\n"
  "v_mfma_f32_16x16x32_bf16 v[60:63], v[220:223], a[220:223], v[60:63]\n"
  "s_waitcnt lgkmcnt(3)\n"
  "ds_read_b64_tr_b16 v[208:209], v230 offset:32768\n"
  "v_mfma_f32_16x16x32_bf16 v[64:67], v[192:195], a[192:195], v[64:67]\n"
  "v_mfma_f32_16x16x32_bf16 v[64:67], v[196:199], a[196:199], v[64:67]\n"
  "ds_read_b64_tr_b16 v[210:211], v230 offset:33792\n"
  "v_mfma_f32_16x16x32_bf16 v[68:71], v[192:195], a[200:203], v[68:71]\n"
  "v_mfma_f32_16x16x32_bf16 v[68:71], v[196:199], a[204:207], v[68:71]\n"
  "ds_read_b64_tr_b16 v[212:213], v230 offset:40960\n"
  "v_mfma_f32_16x16x32_bf16 v[72:75], v[192:195], a[208:211], v[72:75]\n"
  "v_mfma_f32_16x16x32_bf16 v[72:75], v[196:199], a[212:215], v[72:75]\n"
  "ds_read_b64_tr_b16 v[214:215], v230 offset:41984\n"
  "v_mfma_f32_16x16x32_bf16 v[76:79], v[192:195], a[216:219], v[76:79]\n"
  "v_mfma_f32_16x16x32_bf16 v[76:79], v[196:199], a[220:223], v[76:79]\n"
  "s_waitcnt lgkmcnt(3)\n"
  "ds_read_b64_tr_b16 v[216:217], v231 offset:32768\n"
  "v_mfma_f32_16x16x32_bf16 v[80:83], v[200:203], a[192:195], v[80:83]\n"
  "v_mfma_f32_16x16x32_bf16 v[80:83], v[204:207], a[196:199], v[80:83]\n"
  "ds_read_b64_tr_b16 v[218:219], v231 offset:33792\n"
  "v_mfma_f32_16x16x32_bf16 v[84:87], v[200:203], a[200:203], v[84:87]\n"
  "v_mfma_f32_16x16x32_bf16 v[84:87], v[204:207], a[204:207], v[84:87]\n"
  "ds_read_b64_tr_b16 v[220:221], v231 offset:40960\n"
  "v_mfma_f32_16x16x32_bf16 v[88:91], v[200:203], a[208:211], v[88:91]\n"
  "v_mfma_f32_16x16x32_bf16 v[88:91], v[204:207], a[212:215], v[88:91]\n"
  "ds_read_b64_tr_b16 v[222:223], v231 offset:41984\n"
  "v_mfma_f32_16x16x32_bf16 v[92:95], v[200:203], a[216:219], v[92:95]\n"
  "s_waitcnt vmcnt(4)\n"
  "v_mfma_f32_16x16x32_bf16 v[92:95], v[204:207], a[220:223], v[92:95]\n"
  "s_barrier\n"
  "ds_read_b64_tr_b16 v[192:193], v224 offset:49152\n"
  "ds_read_b128 a[224:227], v240 offset:32768\n"
  "s_waitcnt lgkmcnt(5)\n"
  "v_mfma_f32_16x16x32_bf16 v[96:99], v[208:211], a[192:195], v[96:99]\n"
  "buffer_store_dword v253, v250, s[48:51], s96 offen sc1\n"
  "v_mfma_f32_16x16x32_bf16 v[96:99], v[212:215], a[196:199], v[96:99]\n"
  "s_add_u32 s54, s54, 1\n"
  "ds_read_b128 a[228:231], v241 offset:32768\n"
  "ds_read_b64_tr_b16 v[194:195], v224 offset:50176\n"
  "v_mfma_f32_16x16x32_bf16 v[100:103], v[208:211], a[200:203], v[100:103]\n"
  "s_and_b32 s54, s54, 7\n"
  "ds_read_b128 a[232:235], v240 offset:34816\n"
  "ds_read_b128 a[236:239], v241 offset:34816\n"
  "v_mfma_f32_16x16x32_bf16 v[100:103], v[212:215], a[204:207], v[100:103]\n"
  "s_lshl_b32 s55, s54, 19\n"
  "ds_read_b64_tr_b16 v[196:197], v224 offset:57344\n"
  "ds_read_b128 a[240:243], v240 offset:36864\n"
  "v_mfma_f32_16x16x32_bf16 v[104:107], v[208:211], a[208:211], v[104:107]\n"
  "s_add_u32 m0, s57, 0x4000\n"
  "ds_read_b128 a[244:247], v241 offset:36864\n"
  "v_mfma_f32_16x16x32_bf16 v[104:107], v[212:215], a[212:215], v[104:107]\n"
  "buffer_load_dwordx4 v232, s[36:39], s55 offen offset:0 lds\n"
  "v_mfma_f32_16x16x32_bf16 v[108:111], v[208:211], a[216:219], v[108:111]\n"
  "ds_read_b64_tr_b16 v[198:199], v224 offset:58368\n"
  "ds_read_b128 a[248:251], v240 offset:38912\n"
  "v_mfma_f32_16x16x32_bf16 v[108:111], v[212:215], a[220:223], v[108:111]\n"
  "s_waitcnt lgkmcnt(9)\n"
  "ds_read_b128 a[252:255], v241 offset:38912\n"
  "ds_read_b64_tr_b16 v[200:201], v225 offset:49152\n"
  "v_mfma_f32_16x16x32_bf16 v[112:115], v[216:219], a[192:195], v[112:115]\n"
  "v_mfma_f32_16x16x32_bf16 v[112:115], v[220:223], a[196:199], v[112:115]\n"
  "buffer_load_dwordx4 v233, s[36:39], s55 offen offset:1024 lds\n"
  "v_mfma_f32_16x16x32_bf16 v[116:119], v[216:219], a[200:203], v[116:119]\n"
  "ds_read_b64_tr_b16 v[202:203], v225 offset:50176\n"
  "v_mfma_f32_16x16x32_bf16 v[116:119], v[220:223], a[204:207], v[116:119]\n"
  "ds_read_b64_tr_b16 v[204:205], v225 offset:57344\n"
  "v_mfma_f32_16x16x32_bf16 v[120:123], v[216:219], a[208:211], v[120:123]\n"
  "v_mfma_f32_16x16x32_bf16 v[120:123], v[220:223], a[212:215], v[120:123]\n"
  "buffer_load_dwordx4 v234, s[36:39], s55 offen offset:2048 lds\n"
  "v_mfma_f32_16x16x32_bf16 v[124:127], v[216:219], a[216:219], v[124:127]\n"
  "ds_read_b64_tr_b16 v[206:207], v225 offset:58368\n"
  "v_mfma_f32_16x16x32_bf16 v[124:127], v[220:223], a[220:223], v[124:127]\n"
  "s_waitcnt lgkmcnt(3)\n"
  "ds_read_b64_tr_b16 v[208:209], v226 offset:49152\n"
  "v_mfma_f32_16x16x32_bf16 v[0:3], v[192:195], a[224:227], v[0:3]\n"
  "v_mfma_f32_16x16x32_bf16 v[0:3], v[196:199], a[228:231], v[0:3]\n"
  "buffer_load_dwordx4 v235, s[36:39], s55 offen offset:3072 lds\n"
  "v_mfma_f32_16x16x32_bf16 v[4:7], v[192:195], a[232:235], v[4:7]\n"
  "ds_read_b64_tr_b16 v[210:211], v226 offset:50176\n"
  "v_lshlrev_b32 v243, 3, v244\n"
  "v_mfma_f32_16x16x32_bf16 v[4:7], v[196:199], a[236:239], v[4:7]\n"
  "s_mov_b64 s[44:45], s[22:23]\n"
  "ds_read_b64_tr_b16 v[212:213], v226 offset:57344\n"
  "v_mfma_f32_16x16x32_bf16 v[8:11], v[192:195], a[240:243], v[8:11]\n"
  "s_mov_b32 s46, 3584\n"
  "v_mfma_f32_16x16x32_bf16 v[8:11], v[196:199], a[244:247], v[8:11]\n"
  "ds_read_b64_tr_b16 v[214:215], v226 offset:58368\n"
  "v_cvt_pk_bf16_f32 v128, v0, v1\n"
  "v_mfma_f32_16x16x32_bf16 v[12:15], v[192:195], a[248:251], v[12:15]\n"
  "v_cvt_pk_bf16_f32 v129, v2, v3\n"
  "v_mfma_f32_16x16x32_bf16 v[12:15], v[196:199], a[252:255], v[12:15]\n"
  "s_waitcnt lgkmcnt(3)\n"
  "buffer_load_dword v240, v243, s[44:47], 0 offen\n"
  "v_mfma_f32_16x16x32_bf16 v[16:19], v[200:203], a[224:227], v[16:19]\n"
  "s_mov_b64 s[44:45], s[26:27]\n"
  "ds_read_b64_tr_b16 v[216:217], v227 offset:49152\n"
  "v_cvt_pk_bf16_f32 v144, v4, v5\n"
  "v_mfma_f32_16x16x32_bf16 v[16:19], v[204:207], a[228:231], v[16:19]\n"
  "s_mov_b32 s46, 4096\n"
  "ds_read_b64_tr_b16 v[218:219], v227 offset:50176\n"
  "v_cvt_pk_bf16_f32 v145, v6, v7\n"
  "v_mfma_f32_16x16x32_bf16 v[20:23], v[200:203], a[232:235], v[20:23]\n"
  "v_cvt_pk_bf16_f32 v160, v8, v9\n"
  "v_cvt_pk_bf16_f32 v161, v10, v11\n"
  "v_mfma_f32_16x16x32_bf16 v[20:23], v[204:207], a[236:239], v[20:23]\n"
  "ds_read_b64_tr_b16 v[220:221], v227 offset:57344\n"
  "v_cvt_pk_bf16_f32 v176, v12, v13\n"
  "v_mfma_f32_16x16x32_bf16 v[24:27], v[200:203], a[240:243], v[24:27]\n"
  "v_cvt_pk_bf16_f32 v177, v14, v15\n"
  "v_mfma_f32_16x16x32_bf16 v[24:27], v[204:207], a[244:247], v[24:27]\n"
  "buffer_load_dword v242, v243, s[44:47], 0 offen\n"
  "v_mfma_f32_16x16x32_bf16 v[28:31], v[200:203], a[248:251], v[28:31]\n"
  "ds_read_b64_tr_b16 v[222:223], v227 offset:58368\n"
  "v_cvt_pk_bf16_f32 v130, v16, v17\n"
  "v_mfma_f32_16x16x32_bf16 v[28:31], v[204:207], a[252:255], v[28:31]\n"
  "s_waitcnt lgkmcnt(4)\n"
  "ds_read_b64_tr_b16 v[192:193], v228 offset:49152\n"
  "v_cvt_pk_bf16_f32 v131, v18, v19\n"
  "v_mfma_f32_16x16x32_bf16 v[32:35], v[208:211], a[224:227], v[32:35]\n"
  "v_cvt_pk_bf16_f32 v146, v20, v21\n"
  "v_cvt_pk_bf16_f32 v147, v22, v23\n"
  "v_mfma_f32_16x16x32_bf16 v[32:35], v[212:215], a[228:231], v[32:35]\n"
  "ds_read_b64_tr_b16 v[194:195], v228 offset:50176\n"
  "v_cvt_pk_bf16_f32 v162, v24, v25\n"
  "v_mfma_f32_16x16x32_bf16 v[36:39], v[208:211], a[232:235], v[36:39]\n"
  "v_cvt_pk_bf16_f32 v163, v26, v27\n"
  "v_mfma_f32_16x16x32_bf16 v[36:39], v[212:215], a[236:239], v[36:39]\n"
  "ds_read_b64_tr_b16 v[196:197], v228 offset:57344\n"
  "v_cvt_pk_bf16_f32 v178, v28, v29\n"
  "v_mfma_f32_16x16x32_bf16 v[40:43], v[208:211], a[240:243], v[40:43]\n"
  "v_cvt_pk_bf16_f32 v179, v30, v31\n"
  "v_mfma_f32_16x16x32_bf16 v[40:43], v[212:215], a[244:247], v[40:43]\n"
  "ds_read_b64_tr_b16 v[198:199], v228 offset:58368\n"
  "v_cvt_pk_bf16_f32 v132, v32, v33\n"
  "v_mfma_f32_16x16x32_bf16 v[44:47], v[208:211], a[248:251], v[44:47]\n"
  "v_cvt_pk_bf16_f32 v133, v34, v35\n"
  "v_mfma_f32_16x16x32_bf16 v[44:47], v[212:215], a[252:255], v[44:47]\n"
  "s_waitcnt lgkmcnt(3)\n"
  "ds_read_b64_tr_b16 v[200:201], v229 offset:49152\n"
  "v_cvt_pk_bf16_f32 v148, v36, v37\n"
  "v_mfma_f32_16x16x32_bf16 v[48:51], v[216:219], a[224:227], v[48:51]\n"
  "v_cvt_pk_bf16_f32 v149, v38, v39\n"
  "v_mfma_f32_16x16x32_bf16 v[48:51], v[220:223], a[228:231], v[48:51]\n"
  "ds_read_b64_tr_b16 v[202:203], v229 offset:50176\n"
  "v_cvt_pk_bf16_f32 v164, v40, v41\n"
  "v_mfma_f32_16x16x32_bf16 v[52:55], v[216:219], a[232:235], v[52:55]\n"
  "v_cvt_pk_bf16_f32 v165, v42, v43\n"
  "v_mfma_f32_16x16x32_bf16 v[52:55], v[220:223], a[236:239], v[52:55]\n"
  "ds_read_b64_tr_b16 v[204:205], v229 offset:57344\n"
  "v_cvt_pk_bf16_f32 v180, v44, v45\n"
  "v_mfma_f32_16x16x32_bf16 v[56:59], v[216:219], a[240:243], v[56:59]\n"
  "v_cvt_pk_bf16_f32 v181, v46, v47\n"
  "v_mfma_f32_16x16x32_bf16 v[56:59], v[220:223], a[244:247], v[56:59]\n"
  "ds_read_b64_tr_b16 v[206:207], v229 offset:58368\n"
  "v_cvt_pk_bf16_f32 v134, v48, v49\n"
  "v_mfma_f32_16x16x32_bf16 v[60:63], v[216:219], a[248:251], v[60:63]\n"
  "v_cvt_pk_bf16_f32 v135, v50, v51\n"
  "v_mfma_f32_16x16x32_bf16 v[60:63], v[220:223], a[252:255], v[60:63]\n"
  "s_waitcnt lgkmcnt(3)\n"
  "ds_read_b64_tr_b16 v[208:209], v230 offset:49152\n"
  "v_cvt_pk_bf16_f32 v150, v52, v53\n"
  "v_mfma_f32_16x16x32_bf16 v[64:67], v[192:195], a[224:227], v[64:67]\n"
  "v_cvt_pk_bf16_f32 v151, v54, v55\n"
  "v_mfma_f32_16x16x32_bf16 v[64:67], v[196:199], a[228:231], v[64:67]\n"
  "ds_read_b64_tr_b16 v[210:211], v230 offset:50176\n"
  "v_cvt_pk_bf16_f32 v166, v56, v57\n"
  "v_mfma_f32_16x16x32_bf16 v[68:71], v[192:195], a[232:235], v[68:71]\n"
  "v_cvt_pk_bf16_f32 v167, v58, v59\n"
  "v_mfma_f32_16x16x32_bf16 v[68:71], v[196:199], a[236:239], v[68:71]\n"
  "ds_read_b64_tr_b16 v[212:213], v230 offset:57344\n"
  "v_cvt_pk_bf16_f32 v182, v60, v61\n"
  "v_mfma_f32_16x16x32_bf16 v[72:75], v[192:195], a[240:243], v[72:75]\n"
  "v_cvt_pk_bf16_f32 v183, v62, v63\n"
  "v_mfma_f32_16x16x32_bf16 v[72:75], v[196:199], a[244:247], v[72:75]\n"
  "ds_read_b64_tr_b16 v[214:215], v230 offset:58368\n"
  "v_cvt_pk_bf16_f32 v136, v64, v65\n"
  "v_mfma_f32_16x16x32_bf16 v[76:79], v[192:195], a[248:251], v[76:79]\n"
  "v_cvt_pk_bf16_f32 v137, v66, v67\n"
  "v_mfma_f32_16x16x32_bf16 v[76:79], v[196:199], a[252:255], v[76:79]\n"
  "s_waitcnt lgkmcnt(3)\n"
  "ds_read_b64_tr_b16 v[216:217], v231 offset:49152\n"
  "v_cvt_pk_bf16_f32 v152, v68, v69\n"
  "v_mfma_f32_16x16x32_bf16 v[80:83], v[200:203], a[224:227], v[80:83]\n"
  "v_cvt_pk_bf16_f32 v153, v70, v71\n"
  "v_mfma_f32_16x16x32_bf16 v[80:83], v[204:207], a[228:231], v[80:83]\n"
  "ds_read_b64_tr_b16 v[218:219], v231 offset:50176\n"
  "v_cvt_pk_bf16_f32 v168, v72, v73\n"
  "v_mfma_f32_16x16x32_bf16 v[84:87], v[200:203], a[232:235], v[84:87]\n"
  "v_cvt_pk_bf16_f32 v169, v74, v75\n"
  "v_mfma_f32_16x16x32_bf16 v[84:87], v[204:207], a[236:239], v[84:87]\n"
  "ds_read_b64_tr_b16 v[220:221], v231 offset:57344\n"
  "v_cvt_pk_bf16_f32 v184, v76, v77\n"
  "v_mfma_f32_16x16x32_bf16 v[88:91], v[200:203], a[240:243], v[88:91]\n"
  "v_cvt_pk_bf16_f32 v185, v78, v79\n"
  "v_mfma_f32_16x16x32_bf16 v[88:91], v[204:207], a[244:247], v[88:91]\n"
  "ds_read_b64_tr_b16 v[222:223], v231 offset:58368\n"
  "v_cvt_pk_bf16_f32 v138, v80, v81\n"
  "v_mfma_f32_16x16x32_bf16 v[92:95], v[200:203], a[248:251], v[92:95]\n"
  "s_waitcnt vmcnt(7)\n"
  "v_cvt_pk_bf16_f32 v139, v82, v83\n"
  "v_mfma_f32_16x16x32_bf16 v[92:95], v[204:207], a[252:255], v[92:95]\n"
  "s_barrier\n"
  "ds_read_b64_tr_b16 v[192:193], v224 offset:0\n"
  "v_cvt_pk_bf16_f32 v154, v84, v85\n"
  "s_waitcnt lgkmcnt(4)\n"
  "v_mfma_f32_16x16x32_bf16 v[96:99], v[208:211], a[224:227], v[96:99]\n"
  "s_add_u32 s54, s54, 1\n"
  "v_cvt_pk_bf16_f32 v155, v86, v87\n"
  "v_mfma_f32_16x16x32_bf16 v[96:99], v[212:215], a[228:231], v[96:99]\n"
  "s_and_b32 s54, s54, 7\n"
  "ds_read_b64_tr_b16 v[194:195], v224 offset:1024\n"
  "v_cvt_pk_bf16_f32 v170, v88, v89\n"
  "v_mfma_f32_16x16x32_bf16 v[100:103], v[208:211], a[232:235], v[100:103]\n"
  "s_lshl_b32 s55, s54, 19\n"
  "v_cvt_pk_bf16_f32 v171, v90, v91\n"
  "v_mfma_f32_16x16x32_bf16 v[100:103], v[212:215], a[236:239], v[100:103]\n"
  "s_add_u32 m0, s57, 0x8000\n"
  "ds_read_b64_tr_b16 v[196:197], v224 offset:8192\n"
  "v_cvt_pk_bf16_f32 v186, v92, v93\n"
  "v_mfma_f32_16x16x32_bf16 v[104:107], v[208:211], a[240:243], v[104:107]\n"
  "v_cvt_pk_bf16_f32 v187, v94, v95\n"
  "v_mfma_f32_16x16x32_bf16 v[104:107], v[212:215], a[244:247], v[104:107]\n"
  "buffer_load_dwordx4 v232, s[36:39], s55 offen offset:0 lds\n"
  "v_mfma_f32_16x16x32_bf16 v[108:111], v[208:211], a[248:251], v[108:111]\n"
  "ds_read_b64_tr_b16 v[198:199], v224 offset:9216\n"
  "v_cvt_pk_bf16_f32 v140, v96, v97\n"
  "v_mfma_f32_16x16x32_bf16 v[108:111], v[212:215], a[252:255], v[108:111]\n"
  "s_waitcnt lgkmcnt(3)\n"
  "ds_read_b64_tr_b16 v[200:201], v225 offset:0\n"
  "v_cvt_pk_bf16_f32 v141, v98, v99\n"
  "v_mfma_f32_16x16x32_bf16 v[112:115], v[216:219], a[224:227], v[112:115]\n"
  "v_cvt_pk_bf16_f32 v156, v100, v101\n"
  "v_cvt_pk_bf16_f32 v157, v102, v103\n"
  "v_mfma_f32_16x16x32_bf16 v[112:115], v[220:223], a[228:231], v[112:115]\n"
  "buffer_load_dwordx4 v233, s[36:39], s55 offen offset:1024 lds\n"
  "v_mfma_f32_16x16x32_bf16 v[116:119], v[216:219], a[232:235], v[116:119]\n"
  "ds_read_b64_tr_b16 v[202:203], v225 offset:1024\n"
  "v_cvt_pk_bf16_f32 v172, v104, v105\n"
  "v_mfma_f32_16x16x32_bf16 v[116:119], v[220:223], a[236:239], v[116:119]\n"
  "ds_read_b64_tr_b16 v[204:205], v225 offset:8192\n"
  "v_cvt_pk_bf16_f32 v173, v106, v107\n"
  "v_mfma_f32_16x16x32_bf16 v[120:123], v[216:219], a[240:243], v[120:123]\n"
  "v_cvt_pk_bf16_f32 v188, v108, v109\n"
  "v_cvt_pk_bf16_f32 v189, v110, v111\n"
  "v_mfma_f32_16x16x32_bf16 v[120:123], v[220:223], a[244:247], v[120:123]\n"
  "buffer_load_dwordx4 v234, s[36:39], s55 offen offset:2048 lds\n"
  "v_mfma_f32_16x16x32_bf16 v[124:127], v[216:219], a[248:251], v[124:127]\n"
  "ds_read_b64_tr_b16 v[206:207], v225 offset:9216\n"
  "v_cvt_pk_bf16_f32 v142, v112, v113\n"
  "v_mfma_f32_16x16x32_bf16 v[124:127], v[220:223], a[252:255], v[124:127]\n"
  "s_waitcnt lgkmcnt(3)\n"
  "ds_read_b64_tr_b16 v[208:209], v226 offset:0\n"
  "v_cvt_pk_bf16_f32 v143, v114, v115\n"
  "v_mfma_f32_16x16x32_bf16 v[0:3], a[0:3], v[192:195], 0\n"
  "v_cvt_pk_bf16_f32 v158, v116, v117\n"
  "v_cvt_pk_bf16_f32 v159, v118, v119\n"
  "v_mfma_f32_16x16x32_bf16 v[0:3], a[4:7], v[196:199], v[0:3]\n"
  "buffer_load_dwordx4 v235, s[36:39], s55 offen offset:3072 lds\n"
  "v_mfma_f32_16x16x32_bf16 v[4:7], a[8:11], v[192:195], 0\n"
  "s_mov_b32 s58, s98\n"
  "ds_read_b64_tr_b16 v[210:211], v226 offset:1024\n"
  "v_cvt_pk_bf16_f32 v174, v120, v121\n"
  "v_mfma_f32_16x16x32_bf16 v[4:7], a[12:15], v[196:199], v[4:7]\n"
  "s_add_u32 s59, s60, 0x0\n"
  "ds_read_b64_tr_b16 v[212:213], v226 offset:8192\n"
  "v_cvt_pk_bf16_f32 v175, v122, v123\n"
  "v_mfma_f32_16x16x32_bf16 v[8:11], a[16:19], v[192:195], 0\n"
  "v_cvt_pk_bf16_f32 v190, v124, v125\n"
  "v_cvt_pk_bf16_f32 v191, v126, v127\n"
  "v_mfma_f32_16x16x32_bf16 v[8:11], a[20:23], v[196:199], v[8:11]\n"
  "buffer_store_dwordx4 v[128:131], v244, s[48:51], s59 offen offset:0 sc1\n"
  "v_mfma_f32_16x16x32_bf16 v[12:15], a[24:27], v[192:195], 0\n"
  "ds_read_b64_tr_b16 v[214:215], v226 offset:9216\n"
  "v_mfma_f32_16x16x32_bf16 v[12:15], a[28:31], v[196:199], v[12:15]\n"
  "s_waitcnt lgkmcnt(3)\n"
  "ds_read_b64_tr_b16 v[216:217], v227 offset:0\n"
  "v_mfma_f32_16x16x32_bf16 v[16:19], a[0:3], v[200:203], 0\n"
  "v_mfma_f32_16x16x32_bf16 v[16:19], a[4:7], v[204:207], v[16:19]\n"
  "buffer_store_dwordx4 v[132:135], v244, s[48:51], s59 offen offset:1024 sc1\n"
  "v_mfma_f32_16x16x32_bf16 v[20:23], a[8:11], v[200:203], 0\n"
  "ds_read_b64_tr_b16 v[218:219], v227 offset:1024\n"
  "v_mfma_f32_16x16x32_bf16 v[20:23], a[12:15], v[204:207], v[20:23]\n"
  "ds_read_b64_tr_b16 v[220:221], v227 offset:8192\n"
  "v_mfma_f32_16x16x32_bf16 v[24:27], a[16:19], v[200:203], 0\n"
  "v_mfma_f32_16x16x32_bf16 v[24:27], a[20:23], v[204:207], v[24:27]\n"
  "buffer_store_dwordx4 v[136:139], v244, s[48:51], s59 offen offset:2048 sc1\n"
  "v_mfma_f32_16x16x32_bf16 v[28:31], a[24:27], v[200:203], 0\n"
  "ds_read_b64_tr_b16 v[222:223], v227 offset:9216\n"
  "v_mfma_f32_16x16x32_bf16 v[28:31], a[28:31], v[204:207], v[28:31]\n"
  "s_waitcnt lgkmcnt(3)\n"
  "ds_read_b64_tr_b16 v[192:193], v228 offset:0\n"
  "v_mfma_f32_16x16x32_bf16 v[32:35], a[0:3], v[208:211], 0\n"
  "v_mfma_f32_16x16x32_bf16 v[32:35], a[4:7], v[212:215], v[32:35]\n"
  "buffer_store_dwordx4 v[140:143], v244, s[48:51], s59 offen offset:3072 sc1\n"
  "v_mfma_f32_16x16x32_bf16 v[36:39], a[8:11], v[208:211], 0\n"
  "ds_read_b64_tr_b16 v[194:195], v228 offset:1024\n"
  "v_mfma_f32_16x16x32_bf16 v[36:39], a[12:15], v[212:215], v[36:39]\n"
  "ds_read_b64_tr_b16 v[196:197], v228 offset:8192\n"
  "v_mfma_f32_16x16x32_bf16 v[40:43], a[16:19], v[208:211], 0\n"
  "v_mfma_f32_16x16x32_bf16 v[40:43], a[20:23], v[212:215], v[40:43]\n"
  "ds_read_b64_tr_b16 v[198:199], v228 offset:9216\n"
  "v_mfma_f32_16x16x32_bf16 v[44:47], a[24:27], v[208:211], 0\n"
  "v_mfma_f32_16x16x32_bf16 v[44:47], a[28:31], v[212:215], v[44:47]\n"
  "s_waitcnt lgkmcnt(3)\n"
  "ds_read_b64_tr_b16 v[200:201], v229 offset:0\n"
  "v_mfma_f32_16x16x32_bf16 v[48:51], a[0:3], v[216:219], 0\n"
  "v_mfma_f32_16x16x32_bf16 v[48:51], a[4:7], v[220:223], v[48:51]\n"
  "ds_read_b64_tr_b16 v[202:203], v229 offset:1024\n"
  "v_mfma_f32_16x16x32_bf16 v[52:55], a[8:11], v[216:219], 0\n"
  "v_mfma_f32_16x16x32_bf16 v[52:55], a[12:15], v[220:223], v[52:55]\n"
  "ds_read_b64_tr_b16 v[204:205], v229 offset:8192\n"
  "v_mfma_f32_16x16x32_bf16 v[56:59], a[16:19], v[216:219], 0\n"
  "v_mfma_f32_16x16x32_bf16 v[56:59], a[20:23], v[220:223], v[56:59]\n"
  "ds_read_b64_tr_b16 v[206:207], v229 offset:9216\n"
  "v_mfma_f32_16x16x32_bf16 v[60:63], a[24:27], v[216:219], 0\n"
  "v_mfma_f32_16x16x32_bf16 v[60:63], a[28:31], v[220:223], v[60:63]\n"
  "s_waitcnt lgkmcnt(3)\n"
  "ds_read_b64_tr_b16 v[208:209], v230 offset:0\n"
  "v_mfma_f32_16x16x32_bf16 v[64:67], a[0:3], v[192:195], 0\n"
  "v_mfma_f32_16x16x32_bf16 v[64:67], a[4:7], v[196:199], v[64:67]\n"
  "ds_read_b64_tr_b16 v[210:211], v230 offset:1024\n"
  "v_mfma_f32_16x16x32_bf16 v[68:71], a[8:11], v[192:195], 0\n"
  "v_mfma_f32_16x16x32_bf16 v[68:71], a[12:15], v[196:199], v[68:71]\n"
  "ds_read_b64_tr_b16 v[212:213], v230 offset:8192\n"
  "v_mfma_f32_16x16x32_bf16 v[72:75], a[16:19], v[192:195], 0\n"
  "v_mfma_f32_16x16x32_bf16 v[72:75], a[20:23], v[196:199], v[72:75]\n"
  "ds_read_b64_tr_b16 v[214:215], v230 offset:9216\n"
  "v_mfma_f32_16x16x32_bf16 v[76:79], a[24:27], v[192:195], 0\n"
  "v_mfma_f32_16x16x32_bf16 v[76:79], a[28:31], v[196:199], v[76:79]\n"
  "s_waitcnt lgkmcnt(3)\n"
  "ds_read_b64_tr_b16 v[216:217], v231 offset:0\n"
  "v_mfma_f32_16x16x32_bf16 v[80:83], a[0:3], v[200:203], 0\n"
  "v_mfma_f32_16x16x32_bf16 v[80:83], a[4:7], v[204:207], v[80:83]\n"
  "ds_read_b64_tr_b16 v[218:219], v231 offset:1024\n"
  "v_mfma_f32_16x16x32_bf16 v[84:87], a[8:11], v[200:203], 0\n"
  "v_mfma_f32_16x16x32_bf16 v[84:87], a[12:15], v[204:207], v[84:87]\n"
  "ds_read_b64_tr_b16 v[220:221], v231 offset:8192\n"
  "v_mov_b32 v252, 0\n"
  "v_mfma_f32_16x16x32_bf16 v[88:91], a[16:19], v[200:203], 0\n"
  "v_mfma_f32_16x16x32_bf16 v[88:91], a[20:23], v[204:207], v[88:91]\n"
  "ds_read_b64_tr_b16 v[222:223], v231 offset:9216\n"
  "v_mfma_f32_16x16x32_bf16 v[92:95], a[24:27], v[200:203], 0\n"
  "s_waitcnt vmcnt(10)\n"
  "v_mfma_f32_16x16x32_bf16 v[92:95], a[28:31], v[204:207], v[92:95]\n"
  "s_barrier\n"
  "ds_read_b64_tr_b16 v[192:193], v224 offset:16384\n"
  "s_waitcnt lgkmcnt(4)\n"
  "v_mfma_f32_16x16x32_bf16 v[96:99], a[0:3], v[208:211], 0\n"
  "buffer_atomic_add v252, v249, s[48:51], s96 offen sc0 sc1\n"
  "v_mfma_f32_16x16x32_bf16 v[96:99], a[4:7], v[212:215], v[96:99]\n"
  "s_add_u32 s54, s54, 1\n"
  "ds_read_b64_tr_b16 v[194:195], v224 offset:17408\n"
  "v_mfma_f32_16x16x32_bf16 v[100:103], a[8:11], v[208:211], 0\n"
  "s_and_b32 s54, s54, 7\n"
  "v_mfma_f32_16x16x32_bf16 v[100:103], a[12:15], v[212:215], v[100:103]\n"
  "s_lshl_b32 s55, s54, 19\n"
  "ds_read_b64_tr_b16 v[196:197], v224 offset:24576\n"
  "v_mfma_f32_16x16x32_bf16 v[104:107], a[16:19], v[208:211], 0\n"
  "s_add_u32 m0, s57, 0xc000\n"
  "v_mfma_f32_16x16x32_bf16 v[104:107], a[20:23], v[212:215], v[104:107]\n"
  "buffer_load_dwordx4 v232, s[36:39], s55 offen offset:0 lds\n"
  "v_mfma_f32_16x16x32_bf16 v[108:111], a[24:27], v[208:211], 0\n"
  "ds_read_b64_tr_b16 v[198:199], v224 offset:25600\n"
  "v_mfma_f32_16x16x32_bf16 v[108:111], a[28:31], v[212:215], v[108:111]\n"
  "s_waitcnt lgkmcnt(3)\n"
  "ds_read_b64_tr_b16 v[200:201], v225 offset:16384\n"
  "v_mfma_f32_16x16x32_bf16 v[112:115], a[0:3], v[216:219], 0\n"
  "v_mfma_f32_16x16x32_bf16 v[112:115], a[4:7], v[220:223], v[112:115]\n"
  "buffer_load_dwordx4 v233, s[36:39], s55 offen offset:1024 lds\n"
  "v_mfma_f32_16x16x32_bf16 v[116:119], a[8:11], v[216:219], 0\n"
  "ds_read_b64_tr_b16 v[202:203], v225 offset:17408\n"
  "v_mfma_f32_16x16x32_bf16 v[116:119], a[12:15], v[220:223], v[116:119]\n"
  "ds_read_b64_tr_b16 v[204:205], v225 offset:24576\n"
  "v_mfma_f32_16x16x32_bf16 v[120:123], a[16:19], v[216:219], 0\n"
  "v_mfma_f32_16x16x32_bf16 v[120:123], a[20:23], v[220:223], v[120:123]\n"
  "buffer_load_dwordx4 v234, s[36:39], s55 offen offset:2048 lds\n"
  "v_mfma_f32_16x16x32_bf16 v[124:127], a[24:27], v[216:219], 0\n"
  "ds_read_b64_tr_b16 v[206:207], v225 offset:25600\n"
  "v_mfma_f32_16x16x32_bf16 v[124:127], a[28:31], v[220:223], v[124:127]\n"
  "s_waitcnt lgkmcnt(3)\n"
  "ds_read_b64_tr_b16 v[208:209], v226 offset:16384\n"
  "v_mfma_f32_16x16x32_bf16 v[0:3], a[32:35], v[192:195], v[0:3]\n"
  "v_mfma_f32_16x16x32_bf16 v[0:3], a[36:39], v[196:199], v[0:3]\n"
  "buffer_load_dwordx4 v235, s[36:39], s55 offen offset:3072 lds\n"
  "v_mfma_f32_16x16x32_bf16 v[4:7], a[40:43], v[192:195], v[4:7]\n"
  "s_add_u32 s59, s60, 0x1000\n"
  "ds_read_b64_tr_b16 v[210:211], v226 offset:17408\n"
  "v_mfma_f32_16x16x32_bf16 v[4:7], a[44:47], v[196:199], v[4:7]\n"
  "ds_read_b64_tr_b16 v[212:213], v226 offset:24576\n"
  "v_mfma_f32_16x16x32_bf16 v[8:11], a[48:51], v[192:195], v[8:11]\n"
  "v_mfma_f32_16x16x32_bf16 v[8:11], a[52:55], v[196:199], v[8:11]\n"
  "buffer_store_dwordx4 v[144:147], v244, s[48:51], s59 offen offset:0 sc1\n"
  "v_mfma_f32_16x16x32_bf16 v[12:15], a[56:59], v[192:195], v[12:15]\n"
  "ds_read_b64_tr_b16 v[214:215], v226 offset:25600\n"
  "v_mfma_f32_16x16x32_bf16 v[12:15], a[60:63], v[196:199], v[12:15]\n"
  "s_waitcnt lgkmcnt(3)\n"
  "ds_read_b64_tr_b16 v[216:217], v227 offset:16384\n"
  "v_mfma_f32_16x16x32_bf16 v[16:19], a[32:35], v[200:203], v[16:19]\n"
  "v_mfma_f32_16x16x32_bf16 v[16:19], a[36:39], v[204:207], v[16:19]\n"
  "buffer_store_dwordx4 v[148:151], v244, s[48:51], s59 offen offset:1024 sc1\n"
  "v_mfma_f32_16x16x32_bf16 v[20:23], a[40:43], v[200:203], v[20:23]\n"
  "ds_read_b64_tr_b16 v[218:219], v227 offset:17408\n"
  "v_mfma_f32_16x16x32_bf16 v[20:23], a[44:47], v[204:207], v[20:23]\n"
  "ds_read_b64_tr_b16 v[220:221], v227 offset:24576\n"
  "v_mfma_f32_16x16x32_bf16 v[24:27], a[48:51], v[200:203], v[24:27]\n"
  "v_mfma_f32_16x16x32_bf16 v[24:27], a[52:55], v[204:207], v[24:27]\n"
  "buffer_store_dwordx4 v[152:155], v244, s[48:51], s59 offen offset:2048 sc1\n"
  "v_mfma_f32_16x16x32_bf16 v[28:31], a[56:59], v[200:203], v[28:31]\n"
  "ds_read_b64_tr_b16 v[222:223], v227 offset:25600\n"
  "v_mfma_f32_16x16x32_bf16 v[28:31], a[60:63], v[204:207], v[28:31]\n"
  "s_waitcnt lgkmcnt(3)\n"
  "ds_read_b64_tr_b16 v[192:193], v228 offset:16384\n"
  "v_mfma_f32_16x16x32_bf16 v[32:35], a[32:35], v[208:211], v[32:35]\n"
  "v_mfma_f32_16x16x32_bf16 v[32:35], a[36:39], v[212:215], v[32:35]\n"
  "mk_tp2:\n"
  "s_cbranch_execz mk_tp6\n"
  "buffer_store_dwordx4 v[156:159], v244, s[48:51], s59 offen offset:3072 sc1\n"
  "v_mfma_f32_16x16x32_bf16 v[36:39], a[40:43], v[208:211], v[36:39]\n"
  "ds_read_b64_tr_b16 v[194:195], v228 offset:17408\n"
  "v_mfma_f32_16x16x32_bf16 v[36:39], a[44:47], v[212:215], v[36:39]\n"
  "ds_read_b64_tr_b16 v[196:197], v228 offset:24576\n"
  "v_mfma_f32_16x16x32_bf16 v[40:43], a[48:51], v[208:211], v[40:43]\n"
  "v_mfma_f32_16x16x32_bf16 v[40:43], a[52:55], v[212:215], v[40:43]\n"
  "ds_read_b64_tr_b16 v[198:199], v228 offset:25600\n"
  "v_mfma_f32_16x16x32_bf16 v[44:47], a[56:59], v[208:211], v[44:47]\n"
  "v_mfma_f32_16x16x32_bf16 v[44:47], a[60:63], v[212:215], v[44:47]\n"
  "s_waitcnt lgkmcnt(3)\n"
  "ds_read_b64_tr_b16 v[200:201], v229 offset:16384\n"
  "v_mfma_f32_16x16x32_bf16 v[48:51], a[32:35], v[216:219], v[48:51]\n"
  "v_mfma_f32_16x16x32_bf16 v[48:51], a[36:39], v[220:223], v[48:51]\n"
  "ds_read_b64_tr_b16 v[202:203], v229 offset:17408\n"
  "v_mfma_f32_16x16x32_bf16 v[52:55], a[40:43], v[216:219], v[52:55]\n"
  "v_mfma_f32_16x16x32_bf16 v[52:55], a[44:47], v[220:223], v[52:55]\n"
  "ds_read_b64_tr_b16 v[204:205], v229 offset:24576\n"
  "v_mfma_f32_16x16x32_bf16 v[56:59], a[48:51], v[216:219], v[56:59]\n"
  "v_mfma_f32_16x16x32_bf16 v[56:59], a[52:55], v[220:223], v[56:59]\n"
  "ds_read_b64_tr_b16 v[206:207], v229 offset:25600\n"
  "v_mfma_f32_16x16x32_bf16 v[60:63], a[56:59], v[216:219], v[60:63]\n"
  "v_mfma_f32_16x16x32_bf16 v[60:63], a[60:63], v[220:223], v[60:63]\n"
  "s_waitcnt lgkmcnt(3)\n"
  "ds_read_b64_tr_b16 v[208:209], v230 offset:16384\n"
  "v_mfma_f32_16x16x32_bf16 v[64:67], a[32:35], v[192:195], v[64:67]\n"
  "v_mfma_f32_16x16x32_bf16 v[64:67], a[36:39], v[196:199], v[64:67]\n"
  "ds_read_b64_tr_b16 v[210:211], v230 offset:17408\n"
  "v_mfma_f32_16x16x32_bf16 v[68:71], a[40:43], v[192:195], v[68:71]\n"
  "v_mfma_f32_16x16x32_bf16 v[68:71], a[44:47], v[196:199], v[68:71]\n"
  "ds_read_b64_tr_b16 v[212:213], v230 offset:24576\n"
  "v_mfma_f32_16x16x32_bf16 v[72:75], a[48:51], v[192:195], v[72:75]\n"
  "v_mfma_f32_16x16x32_bf16 v[72:75], a[52:55], v[196:199], v[72:75]\n"
  "ds_read_b64_tr_b16 v[214:215], v230 offset:25600\n"
  "v_mfma_f32_16x16x32_bf16 v[76:79], a[56:59], v[192:195], v[76:79]\n"
  "v_mfma_f32_16x16x32_bf16 v[76:79], a[60:63], v[196:199], v[76:79]\n"
  "s_waitcnt lgkmcnt(3)\n"
  "ds_read_b64_tr_b16 v[216:217], v231 offset:16384\n"
  "v_mfma_f32_16x16x32_bf16 v[80:83], a[32:35], v[200:203], v[80:83]\n"
  "v_mfma_f32_16x16x32_bf16 v[80:83], a[36:39], v[204:207], v[80:83]\n"
  "ds_read_b64_tr_b16 v[218:219], v231 offset:17408\n"
  "v_mfma_f32_16x16x32_bf16 v[84:87], a[40:43], v[200:203], v[84:87]\n"
  "v_mfma_f32_16x16x32_bf16 v[84:87], a[44:47], v[204:207], v[84:87]\n"
  "ds_read_b64_tr_b16 v[220:221], v231 offset:24576\n"
  "v_mfma_f32_16x16x32_bf16 v[88:91], a[48:51], v[200:203], v[88:91]\n"
  "s_waitcnt vmcnt(8)\n"
  "v_cmp_eq_u32 vcc, v252, v248\n"
  "v_mfma_f32_16x16x32_bf16 v[88:91], a[52:55], v[204:207], v[88:91]\n"
  "ds_read_b64_tr_b16 v[222:223], v231 offset:25600\n"
  "v_mfma_f32_16x16x32_bf16 v[92:95], a[56:59], v[200:203], v[92:95]\n"
  "s_and_b32 vcc_lo, vcc_lo, 0xff\n"
  "s_cbranch_scc1 mk_spinG\n"
  "mk_spinG_b:\n"
  "v_mfma_f32_16x16x32_bf16 v[92:95], a[60:63], v[204:207], v[92:95]\n"
  "s_barrier\n"
  "ds_read_b64_tr_b16 v[192:193], v224 offset:32768\n"
  "s_waitcnt lgkmcnt(4)\n"
  "v_mfma_f32_16x16x32_bf16 v[96:99], a[32:35], v[208:211], v[96:99]\n"
  "s_add_u32 s54, s54, 1\n"
  "v_mfma_f32_16x16x32_bf16 v[96:99], a[36:39], v[212:215], v[96:99]\n"
  "s_and_b32 s54, s54, 7\n"
  "ds_read_b64_tr_b16 v[194:195], v224 offset:33792\n"
  "v_mfma_f32_16x16x32_bf16 v[100:103], a[40:43], v[208:211], v[100:103]\n"
  "s_lshl_b32 s55, s54, 19\n"
  "v_mfma_f32_16x16x32_bf16 v[100:103], a[44:47], v[212:215], v[100:103]\n"
  "s_add_u32 m0, s57, 0x0\n"
  "ds_read_b64_tr_b16 v[196:197], v224 offset:40960\n"
  "v_mfma_f32_16x16x32_bf16 v[104:107], a[48:51], v[208:211], v[104:107]\n"
  "v_mfma_f32_16x16x32_bf16 v[104:107], a[52:55], v[212:215], v[104:107]\n"
  "buffer_load_dwordx4 v232, s[36:39], s55 offen offset:0 lds\n"
  "v_mfma_f32_16x16x32_bf16 v[108:111], a[56:59], v[208:211], v[108:111]\n"
  "ds_read_b64_tr_b16 v[198:199], v224 offset:41984\n"
  "v_mfma_f32_16x16x32_bf16 v[108:111], a[60:63], v[212:215], v[108:111]\n"
  "s_waitcnt lgkmcnt(3)\n"
  "ds_read_b64_tr_b16 v[200:201], v225 offset:32768\n"
  "v_mfma_f32_16x16x32_bf16 v[112:115], a[32:35], v[216:219], v[112:115]\n"
  "v_mfma_f32_16x16x32_bf16 v[112:115], a[36:39], v[220:223], v[112:115]\n"
  "buffer_load_dwordx4 v233, s[36:39], s55 offen offset:1024 lds\n"
  "v_mfma_f32_16x16x32_bf16 v[116:119], a[40:43], v[216:219], v[116:119]\n"
  "ds_read_b64_tr_b16 v[202:203], v225 offset:33792\n"
  "v_mfma_f32_16x16x32_bf16 v[116:119], a[44:47], v[220:223], v[116:119]\n"
  "ds_read_b64_tr_b16 v[204:205], v225 offset:40960\n"
  "v_mfma_f32_16x16x32_bf16 v[120:123], a[48:51], v[216:219], v[120:123]\n"
  "v_mfma_f32_16x16x32_bf16 v[120:123], a[52:55], v[220:223], v[120:123]\n"
  "buffer_load_dwordx4 v234, s[36:39], s55 offen offset:2048 lds\n"
  "v_mfma_f32_16x16x32_bf16 v[124:127], a[56:59], v[216:219], v[124:127]\n"
  "ds_read_b64_tr_b16 v[206:207], v225 offset:41984\n"
  "v_mfma_f32_16x16x32_bf16 v[124:127], a[60:63], v[220:223], v[124:127]\n"
  "s_waitcnt lgkmcnt(3)\n"
  "ds_read_b64_tr_b16 v[208:209], v226 offset:32768\n"
  "v_mfma_f32_16x16x32_bf16 v[0:3], a[64:67], v[192:195], v[0:3]\n"
  "v_mfma_f32_16x16x32_bf16 v[0:3], a[68:71], v[196:199], v[0:3]\n"
  "buffer_load_dwordx4 v235, s[36:39], s55 offen offset:3072 lds\n"
  "v_mfma_f32_16x16x32_bf16 v[4:7], a[72:75], v[192:195], v[4:7]\n"
  "ds_read_b64_tr_b16 v[210:211], v226 offset:33792\n"
  "v_mfma_f32_16x16x32_bf16 v[4:7], a[76:79], v[196:199], v[4:7]\n"
  "ds_read_b64_tr_b16 v[212:213], v226 offset:40960\n"
  "v_mfma_f32_16x16x32_bf16 v[8:11], a[80:83], v[192:195], v[8:11]\n"
  "v_mfma_f32_16x16x32_bf16 v[8:11], a[84:87], v[196:199], v[8:11]\n"
  "buffer_load_dword v242, v249, s[48:51], s97 offen sc1\n"
  "v_mfma_f32_16x16x32_bf16 v[12:15], a[88:91], v[192:195], v[12:15]\n"
  "s_add_u32 s59, s60, 0x2000\n"
  "ds_read_b64_tr_b16 v[214:215], v226 offset:41984\n"
  "v_mfma_f32_16x16x32_bf16 v[12:15], a[92:95], v[196:199], v[12:15]\n"
  "s_waitcnt lgkmcnt(3)\n"
  "ds_read_b64_tr_b16 v[216:217], v227 offset:32768\n"
  "v_mfma_f32_16x16x32_bf16 v[16:19], a[64:67], v[200:203], v[16:19]\n"
  "v_mfma_f32_16x16x32_bf16 v[16:19], a[68:71], v[204:207], v[16:19]\n"
  "buffer_store_dwordx4 v[160:163], v244, s[48:51], s59 offen offset:0 sc1\n"
  "v_mfma_f32_16x16x32_bf16 v[20:23], a[72:75], v[200:203], v[20:23]\n"
  "ds_read_b64_tr_b16 v[218:219], v227 offset:33792\n"
  "v_mfma_f32_16x16x32_bf16 v[20:23], a[76:79], v[204:207], v[20:23]\n"
  "ds_read_b64_tr_b16 v[220:221], v227 offset:40960\n"
  "v_mfma_f32_16x16x32_bf16 v[24:27], a[80:83], v[200:203], v[24:27]\n"
  "v_mfma_f32_16x16x32_bf16 v[24:27], a[84:87], v[204:207], v[24:27]\n"
  "buffer_store_dwordx4 v[164:167], v244, s[48:51], s59 offen offset:1024 sc1\n"
  "v_mfma_f32_16x16x32_bf16 v[28:31], a[88:91], v[200:203], v[28:31]\n"
  "ds_read_b64_tr_b16 v[222:223], v227 offset:41984\n"
  "v_mfma_f32_16x16x32_bf16 v[28:31], a[92:95], v[204:207], v[28:31]\n"
  "s_waitcnt lgkmcnt(3)\n"
  "ds_read_b64_tr_b16 v[192:193], v228 offset:32768\n"
  "v_mfma_f32_16x16x32_bf16 v[32:35], a[64:67], v[208:211], v[32:35]\n"
  "v_mfma_f32_16x16x32_bf16 v[32:35], a[68:71], v[212:215], v[32:35]\n"
  "buffer_store_dwordx4 v[168:171], v244, s[48:51], s59 offen offset:2048 sc1\n"
  "v_mfma_f32_16x16x32_bf16 v[36:39], a[72:75], v[208:211], v[36:39]\n"
  "ds_read_b64_tr_b16 v[194:195], v228 offset:33792\n"
  "v_mfma_f32_16x16x32_bf16 v[36:39], a[76:79], v[212:215], v[36:39]\n"
  "ds_read_b64_tr_b16 v[196:197], v228 offset:40960\n"
  "v_mfma_f32_16x16x32_bf16 v[40:43], a[80:83], v[208:211], v[40:43]\n"
  "v_mfma_f32_16x16x32_bf16 v[40:43], a[84:87], v[212:215], v[40:43]\n"
  "ds_read_b64_tr_b16 v[198:199], v228 offset:41984\n"
  "v_mfma_f32_16x16x32_bf16 v[44:47], a[88:91], v[208:211], v[44:47]\n"
  "v_mfma_f32_16x16x32_bf16 v[44:47], a[92:95], v[212:215], v[44:47]\n"
  "s_waitcnt lgkmcnt(3)\n"
  "ds_read_b64_tr_b16 v[200:201], v229 offset:32768\n"
  "v_mfma_f32_16x16x32_bf16 v[48:51], a[64:67], v[216:219], v[48:51]\n"
  "v_mfma_f32_16x16x32_bf16 v[48:51], a[68:71], v[220:223], v[48:51]\n"
  "ds_read_b64_tr_b16 v[202:203], v229 offset:33792\n"
  "v_mfma_f32_16x16x32_bf16 v[52:55], a[72:75], v[216:219], v[52:55]\n"
  "v_mfma_f32_16x16x32_bf16 v[52:55], a[76:79], v[220:223], v[52:55]\n"
  "ds_read_b64_tr_b16 v[204:205], v229 offset:40960\n"
  "v_mfma_f32_16x16x32_bf16 v[56:59], a[80:83], v[216:219], v[56:59]\n"
  "v_mfma_f32_16x16x32_bf16 v[56:59], a[84:87], v[220:223], v[56:59]\n"
  "ds_read_b64_tr_b16 v[206:207], v229 offset:41984\n"
  "v_mfma_f32_16x16x32_bf16 v[60:63], a[88:91], v[216:219], v[60:63]\n"
  "v_mfma_f32_16x16x32_bf16 v[60:63], a[92:95], v[220:223], v[60:63]\n"
  "s_waitcnt lgkmcnt(3)\n"
  "ds_read_b64_tr_b16 v[208:209], v230 offset:32768\n"
  "v_mfma_f32_16x16x32_bf16 v[64:67], a[64:67], v[192:195], v[64:67]\n"
  "v_mfma_f32_16x16x32_bf16 v[64:67], a[68:71], v[196:199], v[64:67]\n"
  "ds_read_b64_tr_b16 v[210:211], v230 offset:33792\n"
  "v_mfma_f32_16x16x32_bf16 v[68:71], a[72:75], v[192:195], v[68:71]\n"
  "v_mfma_f32_16x16x32_bf16 v[68:71], a[76:79], v[196:199], v[68:71]\n"
  "ds_read_b64_tr_b16 v[212:213], v230 offset:40960\n"
  "v_mfma_f32_16x16x32_bf16 v[72:75], a[80:83], v[192:195], v[72:75]\n"
  "v_mfma_f32_16x16x32_bf16 v[72:75], a[84:87], v[196:199], v[72:75]\n"
  "ds_read_b64_tr_b16 v[214:215], v230 offset:41984\n"
  "v_mfma_f32_16x16x32_bf16 v[76:79], a[88:91], v[192:195], v[76:79]\n"
  "v_mfma_f32_16x16x32_bf16 v[76:79], a[92:95], v[196:199], v[76:79]\n"
  "s_waitcnt lgkmcnt(3)\n"
  "ds_read_b64_tr_b16 v[216:217], v231 offset:32768\n"
  "v_mfma_f32_16x16x32_bf16 v[80:83], a[64:67], v[200:203], v[80:83]\n"
  "v_mfma_f32_16x16x32_bf16 v[80:83], a[68:71], v[204:207], v[80:83]\n"
  "ds_read_b64_tr_b16 v[218:219], v231 offset:33792\n"
  "v_mfma_f32_16x16x32_bf16 v[84:87], a[72:75], v[200:203], v[84:87]\n"
  "v_mfma_f32_16x16x32_bf16 v[84:87], a[76:79], v[204:207], v[84:87]\n"
  "ds_read_b64_tr_b16 v[220:221], v231 offset:40960\n"
  "v_mfma_f32_16x16x32_bf16 v[88:91], a[80:83], v[200:203], v[88:91]\n"
  "v_mfma_f32_16x16x32_bf16 v[88:91], a[84:87], v[204:207], v[88:91]\n"
  "ds_read_b64_tr_b16 v[222:223], v231 offset:41984\n"
  "v_mfma_f32_16x16x32_bf16 v[92:95], a[88:91], v[200:203], v[92:95]\n"
  "s_waitcnt vmcnt(12)\n"
  "v_mfma_f32_16x16x32_bf16 v[92:95], a[92:95], v[204:207], v[92:95]\n"
  "s_barrier\n"
  "ds_read_b64_tr_b16 v[192:193], v224 offset:49152\n"
  "s_waitcnt lgkmcnt(4)\n"
  "v_mfma_f32_16x16x32_bf16 v[96:99], a[64:67], v[208:211], v[96:99]\n"
  "s_add_u32 s54, s54, 1\n"
  "v_mfma_f32_16x16x32_bf16 v[96:99], a[68:71], v[212:215], v[96:99]\n"
  "s_and_b32 s54, s54, 7\n"
  "ds_read_b64_tr_b16 v[194:195], v224 offset:50176\n"
  "v_mfma_f32_16x16x32_bf16 v[100:103], a[72:75], v[208:211], v[100:103]\n"
  "s_lshl_b32 s55, s54, 19\n"
  "v_mfma_f32_16x16x32_bf16 v[100:103], a[76:79], v[212:215], v[100:103]\n"
  "s_add_u32 m0, s57, 0x4000\n"
  "ds_read_b64_tr_b16 v[196:197], v224 offset:57344\n"
  "v_mfma_f32_16x16x32_bf16 v[104:107], a[80:83], v[208:211], v[104:107]\n"
  "v_mfma_f32_16x16x32_bf16 v[104:107], a[84:87], v[212:215], v[104:107]\n"
  "buffer_load_dwordx4 v232, s[36:39], s55 offen offset:0 lds\n"
  "v_mfma_f32_16x16x32_bf16 v[108:111], a[88:91], v[208:211], v[108:111]\n"
  "ds_read_b64_tr_b16 v[198:199], v224 offset:58368\n"
  "v_mfma_f32_16x16x32_bf16 v[108:111], a[92:95], v[212:215], v[108:111]\n"
  "s_waitcnt lgkmcnt(3)\n"
  "ds_read_b64_tr_b16 v[200:201], v225 offset:49152\n"
  "v_mfma_f32_16x16x32_bf16 v[112:115], a[64:67], v[216:219], v[112:115]\n"
  "v_mfma_f32_16x16x32_bf16 v[112:115], a[68:71], v[220:223], v[112:115]\n"
  "buffer_load_dwordx4 v233, s[36:39], s55 offen offset:1024 lds\n"
  "v_mfma_f32_16x16x32_bf16 v[116:119], a[72:75], v[216:219], v[116:119]\n"
  "ds_read_b64_tr_b16 v[202:203], v225 offset:50176\n"
  "v_mfma_f32_16x16x32_bf16 v[116:119], a[76:79], v[220:223], v[116:119]\n"
  "ds_read_b64_tr_b16 v[204:205], v225 offset:57344\n"
  "v_mfma_f32_16x16x32_bf16 v[120:123], a[80:83], v[216:219], v[120:123]\n"
  "v_mfma_f32_16x16x32_bf16 v[120:123], a[84:87], v[220:223], v[120:123]\n"
  "buffer_load_dwordx4 v234, s[36:39], s55 offen offset:2048 lds\n"
  "v_mfma_f32_16x16x32_bf16 v[124:127], a[88:91], v[216:219], v[124:127]\n"
  "ds_read_b64_tr_b16 v[206:207], v225 offset:58368\n"
  "v_mfma_f32_16x16x32_bf16 v[124:127], a[92:95], v[220:223], v[124:127]\n"
  "s_waitcnt lgkmcnt(3)\n"
  "ds_read_b64_tr_b16 v[208:209], v226 offset:49152\n"
  "v_mfma_f32_16x16x32_bf16 v[0:3], a[96:99], v[192:195], v[0:3]\n"
  "v_mfma_f32_16x16x32_bf16 v[0:3], a[100:103], v[196:199], v[0:3]\n"
  "buffer_load_dwordx4 v235, s[36:39], s55 offen offset:3072 lds\n"
  "v_mfma_f32_16x16x32_bf16 v[4:7], a[104:107], v[192:195], v[4:7]\n"
  "s_add_u32 s59, s60, 0x2000\n"
  "ds_read_b64_tr_b16 v[210:211], v226 offset:50176\n"
  "v_mfma_f32_16x16x32_bf16 v[4:7], a[108:111], v[196:199], v[4:7]\n"
  "ds_read_b64_tr_b16 v[212:213], v226 offset:57344\n"
  "v_mfma_f32_16x16x32_bf16 v[8:11], a[112:115], v[192:195], v[8:11]\n"
  "v_mfma_f32_16x16x32_bf16 v[8:11], a[116:119], v[196:199], v[8:11]\n"
  "buffer_store_dwordx4 v[172:175], v244, s[48:51], s59 offen offset:3072 sc1\n"
  "v_mfma_f32_16x16x32_bf16 v[12:15], a[120:123], v[192:195], v[12:15]\n"
  "s_add_u32 s59, s60, 0x3000\n"
  "ds_read_b64_tr_b16 v[214:215], v226 offset:58368\n"
  "v_mfma_f32_16x16x32_bf16 v[12:15], a[124:127], v[196:199], v[12:15]\n"
  "s_waitcnt lgkmcnt(3)\n"
  "ds_read_b64_tr_b16 v[216:217], v227 offset:49152\n"
  "v_mfma_f32_16x16x32_bf16 v[16:19], a[96:99], v[200:203], v[16:19]\n"
  "v_mfma_f32_16x16x32_bf16 v[16:19], a[100:103], v[204:207], v[16:19]\n"
  "buffer_store_dwordx4 v[176:179], v244, s[48:51], s59 offen offset:0 sc1\n"
  "v_mfma_f32_16x16x32_bf16 v[20:23], a[104:107], v[200:203], v[20:23]\n"
  "s_add_u32 s61, s54, 3\n"
  "ds_read_b64_tr_b16 v[218:219], v227 offset:50176\n"
  "v_mfma_f32_16x16x32_bf16 v[20:23], a[108:111], v[204:207], v[20:23]\n"
  "s_and_b32 s61, s61, 7\n"
  "ds_read_b64_tr_b16 v[220:221], v227 offset:57344\n"
  "v_mfma_f32_16x16x32_bf16 v[24:27], a[112:115], v[200:203], v[24:27]\n"
  "s_lshl_b32 s61, s61, 11\n"
  "v_mfma_f32_16x16x32_bf16 v[24:27], a[116:119], v[204:207], v[24:27]\n"
  "s_add_u32 s61, s61, 0x3000000\n"
  "ds_read_b64_tr_b16 v[222:223], v227 offset:58368\n"
  "v_mfma_f32_16x16x32_bf16 v[28:31], a[120:123], v[200:203], v[28:31]\n"
  "s_lshl_b32 s56, s40, 14\n"
  "v_mfma_f32_16x16x32_bf16 v[28:31], a[124:127], v[204:207], v[28:31]\n"
  "s_waitcnt lgkmcnt(3)\n"
  "s_add_u32 s56, s56, s61\n"
  "buffer_load_dwordx4 a[0:3], v244, s[48:51], s56 offen offset:0 sc1\n"
  "v_mfma_f32_16x16x32_bf16 v[32:35], a[96:99], v[208:211], v[32:35]\n"
  "ds_read_b64_tr_b16 v[192:193], v228 offset:49152\n"
  "v_mfma_f32_16x16x32_bf16 v[32:35], a[100:103], v[212:215], v[32:35]\n"
  "buffer_load_dwordx4 a[4:7], v244, s[48:51], s56 offen offset:1024 sc1\n"
  "v_mfma_f32_16x16x32_bf16 v[36:39], a[104:107], v[208:211], v[36:39]\n"
  "s_lshl_b32 s56, s41, 14\n"
  "ds_read_b64_tr_b16 v[194:195], v228 offset:50176\n"
  "v_mfma_f32_16x16x32_bf16 v[36:39], a[108:111], v[212:215], v[36:39]\n"
  "s_add_u32 s56, s56, s61\n"
  "ds_read_b64_tr_b16 v[196:197], v228 offset:57344\n"
  "v_mfma_f32_16x16x32_bf16 v[40:43], a[112:115], v[208:211], v[40:43]\n"
  "v_mfma_f32_16x16x32_bf16 v[40:43], a[116:119], v[212:215], v[40:43]\n"
  "buffer_load_dwordx4 a[8:11], v244, s[48:51], s56 offen offset:0 sc1\n"
  "v_mfma_f32_16x16x32_bf16 v[44:47], a[120:123], v[208:211], v[44:47]\n"
  "ds_read_b64_tr_b16 v[198:199], v228 offset:58368\n"
  "v_mfma_f32_16x16x32_bf16 v[44:47], a[124:127], v[212:215], v[44:47]\n"
  "s_waitcnt lgkmcnt(4)\n"
  "ds_read_b64_tr_b16 v[200:201], v229 offset:49152\n"
  "v_mfma_f32_16x16x32_bf16 v[48:51], a[96:99], v[216:219], v[48:51]\n"
  "v_mfma_f32_16x16x32_bf16 v[48:51], a[100:103], v[220:223], v[48:51]\n"
  "buffer_load_dwordx4 a[12:15], v244, s[48:51], s56 offen offset:1024 sc1\n"
  "v_mfma_f32_16x16x32_bf16 v[52:55], a[104:107], v[216:219], v[52:55]\n"
  "s_lshl_b32 s56, s42, 14\n"
  "ds_read_b64_tr_b16 v[202:203], v229 offset:50176\n"
  "v_mfma_f32_16x16x32_bf16 v[52:55], a[108:111], v[220:223], v[52:55]\n"
  "s_add_u32 s56, s56, s61\n"
  "ds_read_b64_tr_b16 v[204:205], v229 offset:57344\n"
  "v_mfma_f32_16x16x32_bf16 v[56:59], a[112:115], v[216:219], v[56:59]\n"
  "v_mfma_f32_16x16x32_bf16 v[56:59], a[116:119], v[220:223], v[56:59]\n"
  "buffer_load_dwordx4 a[16:19], v244, s[48:51], s56 offen offset:0 sc1\n"
  "v_mfma_f32_16x16x32_bf16 v[60:63], a[120:123], v[216:219], v[60:63]\n"
  "ds_read_b64_tr_b16 v[206:207], v229 offset:58368\n"
  "v_mfma_f32_16x16x32_bf16 v[60:63], a[124:127], v[220:223], v[60:63]\n"
  "s_waitcnt lgkmcnt(3)\n"
  "ds_read_b64_tr_b16 v[208:209], v230 offset:49152\n"
  "v_mfma_f32_16x16x32_bf16 v[64:67], a[96:99], v[192:195], v[64:67]\n"
  "v_mfma_f32_16x16x32_bf16 v[64:67], a[100:103], v[196:199], v[64:67]\n"
  "buffer_load_dwordx4 a[20:23], v244, s[48:51], s56 offen offset:1024 sc1\n"
  "v_mfma_f32_16x16x32_bf16 v[68:71], a[104:107], v[192:195], v[68:71]\n"
  "ds_read_b64_tr_b16 v[210:211], v230 offset:50176\n"
  "v_mfma_f32_16x16x32_bf16 v[68:71], a[108:111], v[196:199], v[68:71]\n"
  "ds_read_b64_tr_b16 v[212:213], v230 offset:57344\n"
  "v_mfma_f32_16x16x32_bf16 v[72:75], a[112:115], v[192:195], v[72:75]\n"
  "v_mfma_f32_16x16x32_bf16 v[72:75], a[116:119], v[196:199], v[72:75]\n"
  "ds_read_b64_tr_b16 v[214:215], v230 offset:58368\n"
  "v_mfma_f32_16x16x32_bf16 v[76:79], a[120:123], v[192:195], v[76:79]\n"
  "v_mfma_f32_16x16x32_bf16 v[76:79], a[124:127], v[196:199], v[76:79]\n"
  "s_waitcnt lgkmcnt(3)\n"
  "ds_read_b64_tr_b16 v[216:217], v231 offset:49152\n"
  "v_mfma_f32_16x16x32_bf16 v[80:83], a[96:99], v[200:203], v[80:83]\n"
  "v_mfma_f32_16x16x32_bf16 v[80:83], a[100:103], v[204:207], v[80:83]\n"
  "ds_read_b64_tr_b16 v[218:219], v231 offset:50176\n"
  "v_mfma_f32_16x16x32_bf16 v[84:87], a[104:107], v[200:203], v[84:87]\n"
  "v_mfma_f32_16x16x32_bf16 v[84:87], a[108:111], v[204:207], v[84:87]\n"
  "ds_read_b64_tr_b16 v[220:221], v231 offset:57344\n"
  "v_mfma_f32_16x16x32_bf16 v[88:91], a[112:115], v[200:203], v[88:91]\n"
  "v_mfma_f32_16x16x32_bf16 v[88:91], a[116:119], v[204:207], v[88:91]\n"
  "ds_read_b64_tr_b16 v[222:223], v231 offset:58368\n"
  "v_mfma_f32_16x16x32_bf16 v[92:95], a[120:123], v[200:203], v[92:95]\n"
  "s_waitcnt vmcnt(15)\n"
  "v_mfma_f32_16x16x32_bf16 v[92:95], a[124:127], v[204:207], v[92:95]\n"
  "s_barrier\n"
  "ds_read_b64_tr_b16 v[192:193], v224 offset:0\n"
  "s_waitcnt lgkmcnt(4)\n"
  "v_mfma_f32_16x16x32_bf16 v[96:99], a[96:99], v[208:211], v[96:99]\n"
  "s_add_u32 s54, s54, 1\n"
  "v_mfma_f32_16x16x32_bf16 v[96:99], a[100:103], v[212:215], v[96:99]\n"
  "s_and_b32 s54, s54, 7\n"
  "ds_read_b64_tr_b16 v[194:195], v224 offset:1024\n"
  "v_mfma_f32_16x16x32_bf16 v[100:103], a[104:107], v[208:211], v[100:103]\n"
  "s_lshl_b32 s55, s54, 19\n"
  "v_mfma_f32_16x16x32_bf16 v[100:103], a[108:111], v[212:215], v[100:103]\n"
  "s_add_u32 m0, s57, 0x8000\n"
  "ds_read_b64_tr_b16 v[196:197], v224 offset:8192\n"
  "v_mfma_f32_16x16x32_bf16 v[104:107], a[112:115], v[208:211], v[104:107]\n"
  "v_mfma_f32_16x16x32_bf16 v[104:107], a[116:119], v[212:215], v[104:107]\n"
  "buffer_load_dwordx4 v232, s[36:39], s55 offen offset:0 lds\n"
  "v_mfma_f32_16x16x32_bf16 v[108:111], a[120:123], v[208:211], v[108:111]\n"
  "ds_read_b64_tr_b16 v[198:199], v224 offset:9216\n"
  "v_mfma_f32_16x16x32_bf16 v[108:111], a[124:127], v[212:215], v[108:111]\n"
  "s_waitcnt lgkmcnt(3)\n"
  "ds_read_b64_tr_b16 v[200:201], v225 offset:0\n"
  "v_mfma_f32_16x16x32_bf16 v[112:115], a[96:99], v[216:219], v[112:115]\n"
  "v_mfma_f32_16x16x32_bf16 v[112:115], a[100:103], v[220:223], v[112:115]\n"
  "buffer_load_dwordx4 v233, s[36:39], s55 offen offset:1024 lds\n"
  "v_mfma_f32_16x16x32_bf16 v[116:119], a[104:107], v[216:219], v[116:119]\n"
  "ds_read_b64_tr_b16 v[202:203], v225 offset:1024\n"
  "v_mfma_f32_16x16x32_bf16 v[116:119], a[108:111], v[220:223], v[116:119]\n"
  "ds_read_b64_tr_b16 v[204:205], v225 offset:8192\n"
  "v_mfma_f32_16x16x32_bf16 v[120:123], a[112:115], v[216:219], v[120:123]\n"
  "v_mfma_f32_16x16x32_bf16 v[120:123], a[116:119], v[220:223], v[120:123]\n"
  "buffer_load_dwordx4 v234, s[36:39], s55 offen offset:2048 lds\n"
  "v_mfma_f32_16x16x32_bf16 v[124:127], a[120:123], v[216:219], v[124:127]\n"
  "ds_read_b64_tr_b16 v[206:207], v225 offset:9216\n"
  "v_mfma_f32_16x16x32_bf16 v[124:127], a[124:127], v[220:223], v[124:127]\n"
  "s_waitcnt lgkmcnt(3)\n"
  "ds_read_b64_tr_b16 v[208:209], v226 offset:0\n"
  "v_mfma_f32_16x16x32_bf16 v[0:3], a[128:131], v[192:195], v[0:3]\n"
  "v_mfma_f32_16x16x32_bf16 v[0:3], a[132:135], v[196:199], v[0:3]\n"
  "buffer_load_dwordx4 v235, s[36:39], s55 offen offset:3072 lds\n"
  "v_mfma_f32_16x16x32_bf16 v[4:7], a[136:139], v[192:195], v[4:7]\n"
  "ds_read_b64_tr_b16 v[210:211], v226 offset:1024\n"
  "v_cmp_ne_u32 vcc, v242, v253\n"
  "v_mfma_f32_16x16x32_bf16 v[4:7], a[140:143], v[196:199], v[4:7]\n"
  "ds_read_b64_tr_b16 v[212:213], v226 offset:8192\n"
  "v_max_u32 v243, v250, v251\n"
  "v_mfma_f32_16x16x32_bf16 v[8:11], a[144:147], v[192:195], v[8:11]\n"
  "v_mov_b32 v253, v242\n"
  "v_cndmask_b32 v243, v243, v250, vcc\n"
  "v_mfma_f32_16x16x32_bf16 v[8:11], a[148:151], v[196:199], v[8:11]\n"
  "ds_read_b64_tr_b16 v[214:215], v226 offset:9216\n"
  "v_mfma_f32_16x16x32_bf16 v[12:15], a[152:155], v[192:195], v[12:15]\n"
  "v_mfma_f32_16x16x32_bf16 v[12:15], a[156:159], v[196:199], v[12:15]\n"
  "s_waitcnt lgkmcnt(3)\n"
  "buffer_store_dword v253, v243, s[48:51], s96 offen sc1\n"
  "v_mfma_f32_16x16x32_bf16 v[16:19], a[128:131], v[200:203], v[16:19]\n"
  "s_add_u32 s59, s60, 0x3000\n"
  "ds_read_b64_tr_b16 v[216:217], v227 offset:0\n"
  "v_mfma_f32_16x16x32_bf16 v[16:19], a[132:135], v[204:207], v[16:19]\n"
  "ds_read_b64_tr_b16 v[218:219], v227 offset:1024\n"
  "v_mfma_f32_16x16x32_bf16 v[20:23], a[136:139], v[200:203], v[20:23]\n"
  "v_mfma_f32_16x16x32_bf16 v[20:23], a[140:143], v[204:207], v[20:23]\n"
  "buffer_store_dwordx4 v[180:183], v244, s[48:51], s59 offen offset:1024 sc1\n"
  "v_mfma_f32_16x16x32_bf16 v[24:27], a[144:147], v[200:203], v[24:27]\n"
  "ds_read_b64_tr_b16 v[220:221], v227 offset:8192\n"
  "v_mfma_f32_16x16x32_bf16 v[24:27], a[148:151], v[204:207], v[24:27]\n"
  "ds_read_b64_tr_b16 v[222:223], v227 offset:9216\n"
  "v_mfma_f32_16x16x32_bf16 v[28:31], a[152:155], v[200:203], v[28:31]\n"
  "v_mfma_f32_16x16x32_bf16 v[28:31], a[156:159], v[204:207], v[28:31]\n"
  "s_waitcnt lgkmcnt(4)\n"
  "buffer_store_dwordx4 v[184:187], v244, s[48:51], s59 offen offset:2048 sc1\n"
  "v_mfma_f32_16x16x32_bf16 v[32:35], a[128:131], v[208:211], v[32:35]\n"
  "s_add_u32 s61, s54, 2\n"
  "ds_read_b64_tr_b16 v[192:193], v228 offset:0\n"
  "v_mfma_f32_16x16x32_bf16 v[32:35], a[132:135], v[212:215], v[32:35]\n"
  "s_and_b32 s61, s61, 7\n"
  "ds_read_b64_tr_b16 v[194:195], v228 offset:1024\n"
  "v_mfma_f32_16x16x32_bf16 v[36:39], a[136:139], v[208:211], v[36:39]\n"
  "s_lshl_b32 s61, s61, 11\n"
  "v_mfma_f32_16x16x32_bf16 v[36:39], a[140:143], v[212:215], v[36:39]\n"
  "s_add_u32 s61, s61, 0x3000000\n"
  "ds_read_b64_tr_b16 v[196:197], v228 offset:8192\n"
  "v_mfma_f32_16x16x32_bf16 v[40:43], a[144:147], v[208:211], v[40:43]\n"
  "s_lshl_b32 s56, s43, 14\n"
  "v_mfma_f32_16x16x32_bf16 v[40:43], a[148:151], v[212:215], v[40:43]\n"
  "s_add_u32 s56, s56, s61\n"
  "buffer_load_dwordx4 a[24:27], v244, s[48:51], s56 offen offset:0 sc1\n"
  "v_mfma_f32_16x16x32_bf16 v[44:47], a[152:155], v[208:211], v[44:47]\n"
  "ds_read_b64_tr_b16 v[198:199], v228 offset:9216\n"
  "v_mfma_f32_16x16x32_bf16 v[44:47], a[156:159], v[212:215], v[44:47]\n"
  "s_waitcnt lgkmcnt(4)\n"
  "buffer_load_dwordx4 a[28:31], v244, s[48:51], s56 offen offset:1024 sc1\n"
  "v_mfma_f32_16x16x32_bf16 v[48:51], a[128:131], v[216:219], v[48:51]\n"
  "s_add_u32 s61, s54, 3\n"
  "ds_read_b64_tr_b16 v[200:201], v229 offset:0\n"
  "v_mfma_f32_16x16x32_bf16 v[48:51], a[132:135], v[220:223], v[48:51]\n"
  "s_and_b32 s61, s61, 7\n"
  "ds_read_b64_tr_b16 v[202:203], v229 offset:1024\n"
  "v_mfma_f32_16x16x32_bf16 v[52:55], a[136:139], v[216:219], v[52:55]\n"
  "s_lshl_b32 s61, s61, 11\n"
  "v_mfma_f32_16x16x32_bf16 v[52:55], a[140:143], v[220:223], v[52:55]\n"
  "s_add_u32 s61, s61, 0x3000000\n"
  "ds_read_b64_tr_b16 v[204:205], v229 offset:8192\n"
  "v_mfma_f32_16x16x32_bf16 v[56:59], a[144:147], v[216:219], v[56:59]\n"
  "s_lshl_b32 s56, s40, 14\n"
  "v_mfma_f32_16x16x32_bf16 v[56:59], a[148:151], v[220:223], v[56:59]\n"
  "s_add_u32 s56, s56, s61\n"
  "buffer_load_dwordx4 a[32:35], v244, s[48:51], s56 offen offset:0 sc1\n"
  "v_mfma_f32_16x16x32_bf16 v[60:63], a[152:155], v[216:219], v[60:63]\n"
  "ds_read_b64_tr_b16 v[206:207], v229 offset:9216\n"
  "v_mfma_f32_16x16x32_bf16 v[60:63], a[156:159], v[220:223], v[60:63]\n"
  "s_waitcnt lgkmcnt(4)\n"
  "buffer_load_dwordx4 a[36:39], v244, s[48:51], s56 offen offset:1024 sc1\n"
  "v_mfma_f32_16x16x32_bf16 v[64:67], a[128:131], v[192:195], v[64:67]\n"
  "s_lshl_b32 s56, s41, 14\n"
  "ds_read_b64_tr_b16 v[208:209], v230 offset:0\n"
  "v_mfma_f32_16x16x32_bf16 v[64:67], a[132:135], v[196:199], v[64:67]\n"
  "s_add_u32 s56, s56, s61\n"
  "ds_read_b64_tr_b16 v[210:211], v230 offset:1024\n"
  "v_mfma_f32_16x16x32_bf16 v[68:71], a[136:139], v[192:195], v[68:71]\n"
  "v_mfma_f32_16x16x32_bf16 v[68:71], a[140:143], v[196:199], v[68:71]\n"
  "buffer_load_dwordx4 a[40:43], v244, s[48:51], s56 offen offset:0 sc1\n"
  "v_mfma_f32_16x16x32_bf16 v[72:75], a[144:147], v[192:195], v[72:75]\n"
  "ds_read_b64_tr_b16 v[212:213], v230 offset:8192\n"
  "v_mfma_f32_16x16x32_bf16 v[72:75], a[148:151], v[196:199], v[72:75]\n"
  "ds_read_b64_tr_b16 v[214:215], v230 offset:9216\n"
  "v_mfma_f32_16x16x32_bf16 v[76:79], a[152:155], v[192:195], v[76:79]\n"
  "v_mfma_f32_16x16x32_bf16 v[76:79], a[156:159], v[196:199], v[76:79]\n"
  "s_waitcnt lgkmcnt(4)\n"
  "buffer_load_dwordx4 a[44:47], v244, s[48:51], s56 offen offset:1024 sc1\n"
  "v_mfma_f32_16x16x32_bf16 v[80:83], a[128:131], v[200:203], v[80:83]\n"
  "ds_read_b64_tr_b16 v[216:217], v231 offset:0\n"
  "v_mfma_f32_16x16x32_bf16 v[80:83], a[132:135], v[204:207], v[80:83]\n"
  "ds_read_b64_tr_b16 v[218:219], v231 offset:1024\n"
  "v_mfma_f32_16x16x32_bf16 v[84:87], a[136:139], v[200:203], v[84:87]\n"
  "v_mfma_f32_16x16x32_bf16 v[84:87], a[140:143], v[204:207], v[84:87]\n"
  "ds_read_b64_tr_b16 v[220:221], v231 offset:8192\n"
  "v_mfma_f32_16x16x32_bf16 v[88:91], a[144:147], v[200:203], v[88:91]\n"
  "v_mfma_f32_16x16x32_bf16 v[88:91], a[148:151], v[204:207], v[88:91]\n"
  "ds_read_b64_tr_b16 v[222:223], v231 offset:9216\n"
  "v_mfma_f32_16x16x32_bf16 v[92:95], a[152:155], v[200:203], v[92:95]\n"
  "s_waitcnt vmcnt(21)\n"
  "v_mfma_f32_16x16x32_bf16 v[92:95], a[156:159], v[204:207], v[92:95]\n"
  "s_barrier\n"
  "ds_read_b64_tr_b16 v[192:193], v224 offset:16384\n"
  "s_waitcnt lgkmcnt(5)\n"
  "v_mfma_f32_16x16x32_bf16 v[96:99], a[128:131], v[208:211], v[96:99]\n"
  "s_add_u32 s54, s54, 1\n"
  "v_mfma_f32_16x16x32_bf16 v[96:99], a[132:135], v[212:215], v[96:99]\n"
  "s_and_b32 s54, s54, 7\n"
  "ds_read_b64_tr_b16 v[194:195], v224 offset:17408\n"
  "v_mfma_f32_16x16x32_bf16 v[100:103], a[136:139], v[208:211], v[100:103]\n"
  "s_lshl_b32 s55, s54, 19\n"
  "v_mfma_f32_16x16x32_bf16 v[100:103], a[140:143], v[212:215], v[100:103]\n"
  "s_add_u32 m0, s57, 0xc000\n"
  "ds_read_b64_tr_b16 v[196:197], v224 offset:24576\n"
  "v_mfma_f32_16x16x32_bf16 v[104:107], a[144:147], v[208:211], v[104:107]\n"
  "v_mfma_f32_16x16x32_bf16 v[104:107], a[148:151], v[212:215], v[104:107]\n"
  "buffer_load_dwordx4 v232, s[36:39], s55 offen offset:0 lds\n"
  "v_mfma_f32_16x16x32_bf16 v[108:111], a[152:155], v[208:211], v[108:111]\n"
  "ds_read_b64_tr_b16 v[198:199], v224 offset:25600\n"
  "v_mfma_f32_16x16x32_bf16 v[108:111], a[156:159], v[212:215], v[108:111]\n"
  "s_waitcnt lgkmcnt(3)\n"
  "ds_read_b64_tr_b16 v[200:201], v225 offset:16384\n"
  "v_mfma_f32_16x16x32_bf16 v[112:115], a[128:131], v[216:219], v[112:115]\n"
  "v_mfma_f32_16x16x32_bf16 v[112:115], a[132:135], v[220:223], v[112:115]\n"
  "buffer_load_dwordx4 v233, s[36:39], s55 offen offset:1024 lds\n"
  "v_mfma_f32_16x16x32_bf16 v[116:119], a[136:139], v[216:219], v[116:119]\n"
  "ds_read_b64_tr_b16 v[202:203], v225 offset:17408\n"
  "v_mfma_f32_16x16x32_bf16 v[116:119], a[140:143], v[220:223], v[116:119]\n"
  "ds_read_b64_tr_b16 v[204:205], v225 offset:24576\n"
  "v_mfma_f32_16x16x32_bf16 v[120:123], a[144:147], v[216:219], v[120:123]\n"
  "v_mfma_f32_16x16x32_bf16 v[120:123], a[148:151], v[220:223], v[120:123]\n"
  "buffer_load_dwordx4 v234, s[36:39], s55 offen offset:2048 lds\n"
  "v_mfma_f32_16x16x32_bf16 v[124:127], a[152:155], v[216:219], v[124:127]\n"
  "ds_read_b64_tr_b16 v[206:207], v225 offset:25600\n"
  "v_mfma_f32_16x16x32_bf16 v[124:127], a[156:159], v[220:223], v[124:127]\n"
  "s_waitcnt lgkmcnt(3)\n"
  "ds_read_b64_tr_b16 v[208:209], v226 offset:16384\n"
  "v_mfma_f32_16x16x32_bf16 v[0:3], a[160:163], v[192:195], v[0:3]\n"
  "v_mfma_f32_16x16x32_bf16 v[0:3], a[164:167], v[196:199], v[0:3]\n"
  "buffer_load_dwordx4 v235, s[36:39], s55 offen offset:3072 lds\n"
  "v_mfma_f32_16x16x32_bf16 v[4:7], a[168:171], v[192:195], v[4:7]\n"
  "s_add_u32 s59, s60, 0x3000\n"
  "ds_read_b64_tr_b16 v[210:211], v226 offset:17408\n"
  "v_mfma_f32_16x16x32_bf16 v[4:7], a[172:175], v[196:199], v[4:7]\n"
  "ds_read_b64_tr_b16 v[212:213], v226 offset:24576\n"
  "v_mfma_f32_16x16x32_bf16 v[8:11], a[176:179], v[192:195], v[8:11]\n"
  "v_mfma_f32_16x16x32_bf16 v[8:11], a[180:183], v[196:199], v[8:11]\n"
  "buffer_store_dwordx4 v[188:191], v244, s[48:51], s59 offen offset:3072 sc1\n"
  "v_mfma_f32_16x16x32_bf16 v[12:15], a[184:187], v[192:195], v[12:15]\n"
  "s_add_u32 s61, s54, 2\n"
  "ds_read_b64_tr_b16 v[214:215], v226 offset:25600\n"
  "v_mfma_f32_16x16x32_bf16 v[12:15], a[188:191], v[196:199], v[12:15]\n"
  "s_waitcnt lgkmcnt(3)\n"
  "s_and_b32 s61, s61, 7\n"
  "ds_read_b64_tr_b16 v[216:217], v227 offset:16384\n"
  "v_mfma_f32_16x16x32_bf16 v[16:19], a[160:163], v[200:203], v[16:19]\n"
  "s_lshl_b32 s61, s61, 11\n"
  "v_mfma_f32_16x16x32_bf16 v[16:19], a[164:167], v[204:207], v[16:19]\n"
  "s_add_u32 s61, s61, 0x3000000\n"
  "ds_read_b64_tr_b16 v[218:219], v227 offset:17408\n"
  "v_mfma_f32_16x16x32_bf16 v[20:23], a[168:171], v[200:203], v[20:23]\n"
  "s_lshl_b32 s56, s42, 14\n"
  "v_mfma_f32_16x16x32_bf16 v[20:23], a[172:175], v[204:207], v[20:23]\n"
  "s_add_u32 s56, s56, s61\n"
  "buffer_load_dwordx4 a[48:51], v244, s[48:51], s56 offen offset:0 sc1\n"
  "v_mfma_f32_16x16x32_bf16 v[24:27], a[176:179], v[200:203], v[24:27]\n"
  "ds_read_b64_tr_b16 v[220:221], v227 offset:24576\n"
  "v_mfma_f32_16x16x32_bf16 v[24:27], a[180:183], v[204:207], v[24:27]\n"
  "buffer_load_dwordx4 a[52:55], v244, s[48:51], s56 offen offset:1024 sc1\n"
  "v_mfma_f32_16x16x32_bf16 v[28:31], a[184:187], v[200:203], v[28:31]\n"
  "s_lshl_b32 s56, s43, 14\n"
  "ds_read_b64_tr_b16 v[222:223], v227 offset:25600\n"
  "v_mfma_f32_16x16x32_bf16 v[28:31], a[188:191], v[204:207], v[28:31]\n"
  "s_waitcnt lgkmcnt(3)\n"
  "s_add_u32 s56, s56, s61\n"
  "ds_read_b64_tr_b16 v[192:193], v228 offset:16384\n"
  "v_mfma_f32_16x16x32_bf16 v[32:35], a[160:163], v[208:211], v[32:35]\n"
  "v_mfma_f32_16x16x32_bf16 v[32:35], a[164:167], v[212:215], v[32:35]\n"
  "buffer_load_dwordx4 a[56:59], v244, s[48:51], s56 offen offset:0 sc1\n"
  "v_mfma_f32_16x16x32_bf16 v[36:39], a[168:171], v[208:211], v[36:39]\n"
  "ds_read_b64_tr_b16 v[194:195], v228 offset:17408\n"
  "v_mfma_f32_16x16x32_bf16 v[36:39], a[172:175], v[212:215], v[36:39]\n"
  "ds_read_b64_tr_b16 v[196:197], v228 offset:24576\n"
  "v_mfma_f32_16x16x32_bf16 v[40:43], a[176:179], v[208:211], v[40:43]\n"
  "v_mfma_f32_16x16x32_bf16 v[40:43], a[180:183], v[212:215], v[40:43]\n"
  "buffer_load_dwordx4 a[60:63], v244, s[48:51], s56 offen offset:1024 sc1\n"
  "v_mfma_f32_16x16x32_bf16 v[44:47], a[184:187], v[208:211], v[44:47]\n"
  "s_add_u32 s61, s54, 3\n"
  "ds_read_b64_tr_b16 v[198:199], v228 offset:25600\n"
  "v_mfma_f32_16x16x32_bf16 v[44:47], a[188:191], v[212:215], v[44:47]\n"
  "s_waitcnt lgkmcnt(3)\n"
  "s_and_b32 s61, s61, 7\n"
  "ds_read_b64_tr_b16 v[200:201], v229 offset:16384\n"
  "v_mfma_f32_16x16x32_bf16 v[48:51], a[160:163], v[216:219], v[48:51]\n"
  "s_lshl_b32 s61, s61, 11\n"
  "v_mfma_f32_16x16x32_bf16 v[48:51], a[164:167], v[220:223], v[48:51]\n"
  "s_add_u32 s61, s61, 0x3000000\n"
  "ds_read_b64_tr_b16 v[202:203], v229 offset:17408\n"
  "v_mfma_f32_16x16x32_bf16 v[52:55], a[168:171], v[216:219], v[52:55]\n"
  "s_lshl_b32 s56, s40, 14\n"
  "v_mfma_f32_16x16x32_bf16 v[52:55], a[172:175], v[220:223], v[52:55]\n"
  "s_add_u32 s56, s56, s61\n"
  "buffer_load_dwordx4 a[64:67], v244, s[48:51], s56 offen offset:0 sc1\n"
  "v_mfma_f32_16x16x32_bf16 v[56:59], a[176:179], v[216:219], v[56:59]\n"
  "ds_read_b64_tr_b16 v[204:205], v229 offset:24576\n"
  "v_mfma_f32_16x16x32_bf16 v[56:59], a[180:183], v[220:223], v[56:59]\n"
  "buffer_load_dwordx4 a[68:71], v244, s[48:51], s56 offen offset:1024 sc1\n"
  "v_mfma_f32_16x16x32_bf16 v[60:63], a[184:187], v[216:219], v[60:63]\n"
  "ds_read_b64_tr_b16 v[206:207], v229 offset:25600\n"
  "v_mfma_f32_16x16x32_bf16 v[60:63], a[188:191], v[220:223], v[60:63]\n"
  "s_waitcnt lgkmcnt(3)\n"
  "ds_read_b64_tr_b16 v[208:209], v230 offset:16384\n"
  "v_mfma_f32_16x16x32_bf16 v[64:67], a[160:163], v[192:195], v[64:67]\n"
  "mk_tp3:\n"
  "s_cbranch_execz mk_tp7\n"
  "v_mfma_f32_16x16x32_bf16 v[64:67], a[164:167], v[196:199], v[64:67]\n"
  "ds_read_b64_tr_b16 v[210:211], v230 offset:17408\n"
  "v_mfma_f32_16x16x32_bf16 v[68:71], a[168:171], v[192:195], v[68:71]\n"
  "v_mfma_f32_16x16x32_bf16 v[68:71], a[172:175], v[196:199], v[68:71]\n"
  "ds_read_b64_tr_b16 v[212:213], v230 offset:24576\n"
  "v_mfma_f32_16x16x32_bf16 v[72:75], a[176:179], v[192:195], v[72:75]\n"
  "v_mfma_f32_16x16x32_bf16 v[72:75], a[180:183], v[196:199], v[72:75]\n"
  "ds_read_b64_tr_b16 v[214:215], v230 offset:25600\n"
  "v_mfma_f32_16x16x32_bf16 v[76:79], a[184:187], v[192:195], v[76:79]\n"
  "v_mfma_f32_16x16x32_bf16 v[76:79], a[188:191], v[196:199], v[76:79]\n"
  "s_waitcnt lgkmcnt(3)\n"
  "ds_read_b64_tr_b16 v[216:217], v231 offset:16384\n"
  "v_mfma_f32_16x16x32_bf16 v[80:83], a[160:163], v[200:203], v[80:83]\n"
  "v_mfma_f32_16x16x32_bf16 v[80:83], a[164:167], v[204:207], v[80:83]\n"
  "ds_read_b64_tr_b16 v[218:219], v231 offset:17408\n"
  "v_mfma_f32_16x16x32_bf16 v[84:87], a[168:171], v[200:203], v[84:87]\n"
  "v_mfma_f32_16x16x32_bf16 v[84:87], a[172:175], v[204:207], v[84:87]\n"
  "ds_read_b64_tr_b16 v[220:221], v231 offset:24576\n"
  "v_mfma_f32_16x16x32_bf16 v[88:91], a[176:179], v[200:203], v[88:91]\n"
  "v_mfma_f32_16x16x32_bf16 v[88:91], a[180:183], v[204:207], v[88:91]\n"
  "ds_read_b64_tr_b16 v[222:223], v231 offset:25600\n"
  "v_mfma_f32_16x16x32_bf16 v[92:95], a[184:187], v[200:203], v[92:95]\n"
  "s_waitcnt vmcnt(20)\n"
  "v_mfma_f32_16x16x32_bf16 v[92:95], a[188:191], v[204:207], v[92:95]\n"
  "s_barrier\n"
  "ds_read_b64_tr_b16 v[192:193], v224 offset:32768\n"
  "s_waitcnt lgkmcnt(4)\n"
  "v_mfma_f32_16x16x32_bf16 v[96:99], a[160:163], v[208:211], v[96:99]\n"
  "s_lshl_b32 s61, s53, 8\n"
  "v_mfma_f32_16x16x32_bf16 v[96:99], a[164:167], v[212:215], v[96:99]\n"
  "s_add_u32 s36, s12, s61\n"
  "s_addc_u32 s37, s13, 0\n"
  "ds_read_b64_tr_b16 v[194:195], v224 offset:33792\n"
  "v_mfma_f32_16x16x32_bf16 v[100:103], a[168:171], v[208:211], v[100:103]\n"
  "s_add_u32 s54, s54, 1\n"
  "v_mfma_f32_16x16x32_bf16 v[100:103], a[172:175], v[212:215], v[100:103]\n"
  "s_and_b32 s54, s54, 7\n"
  "ds_read_b64_tr_b16 v[196:197], v224 offset:40960\n"
  "v_mfma_f32_16x16x32_bf16 v[104:107], a[176:179], v[208:211], v[104:107]\n"
  "s_lshl_b32 s55, s54, 19\n"
  "v_mfma_f32_16x16x32_bf16 v[104:107], a[180:183], v[212:215], v[104:107]\n"
  "s_add_u32 m0, s57, 0x0\n"
  "ds_read_b64_tr_b16 v[198:199], v224 offset:41984\n"
  "v_mfma_f32_16x16x32_bf16 v[108:111], a[184:187], v[208:211], v[108:111]\n"
  "buffer_load_dwordx4 v232, s[36:39], s55 offen offset:0 lds\n"
  "v_mfma_f32_16x16x32_bf16 v[108:111], a[188:191], v[212:215], v[108:111]\n"
  "s_waitcnt lgkmcnt(3)\n"
  "ds_read_b64_tr_b16 v[200:201], v225 offset:32768\n"
  "v_mfma_f32_16x16x32_bf16 v[112:115], a[160:163], v[216:219], v[112:115]\n"
  "v_mfma_f32_16x16x32_bf16 v[112:115], a[164:167], v[220:223], v[112:115]\n"
  "buffer_load_dwordx4 v233, s[36:39], s55 offen offset:1024 lds\n"
  "v_mfma_f32_16x16x32_bf16 v[116:119], a[168:171], v[216:219], v[116:119]\n"
  "ds_read_b64_tr_b16 v[202:203], v225 offset:33792\n"
  "v_mfma_f32_16x16x32_bf16 v[116:119], a[172:175], v[220:223], v[116:119]\n"
  "ds_read_b64_tr_b16 v[204:205], v225 offset:40960\n"
  "v_mfma_f32_16x16x32_bf16 v[120:123], a[176:179], v[216:219], v[120:123]\n"
  "v_mfma_f32_16x16x32_bf16 v[120:123], a[180:183], v[220:223], v[120:123]\n"
  "buffer_load_dwordx4 v234, s[36:39], s55 offen offset:2048 lds\n"
  "v_mfma_f32_16x16x32_bf16 v[124:127], a[184:187], v[216:219], v[124:127]\n"
  "ds_read_b64_tr_b16 v[206:207], v225 offset:41984\n"
  "v_mfma_f32_16x16x32_bf16 v[124:127], a[188:191], v[220:223], v[124:127]\n"
  "s_waitcnt lgkmcnt(3)\n"
  "ds_read_b64_tr_b16 v[208:209], v226 offset:32768\n"
  "v_mfma_f32_16x16x32_bf16 v[0:3], a[192:195], v[192:195], v[0:3]\n"
  "v_mfma_f32_16x16x32_bf16 v[0:3], a[196:199], v[196:199], v[0:3]\n"
  "buffer_load_dwordx4 v235, s[36:39], s55 offen offset:3072 lds\n"
  "v_mfma_f32_16x16x32_bf16 v[4:7], a[200:203], v[192:195], v[4:7]\n"
  "s_add_u32 s61, s54, 2\n"
  "ds_read_b64_tr_b16 v[210:211], v226 offset:33792\n"
  "v_mfma_f32_16x16x32_bf16 v[4:7], a[204:207], v[196:199], v[4:7]\n"
  "s_and_b32 s61, s61, 7\n"
  "ds_read_b64_tr_b16 v[212:213], v226 offset:40960\n"
  "v_mfma_f32_16x16x32_bf16 v[8:11], a[208:211], v[192:195], v[8:11]\n"
  "s_lshl_b32 s61, s61, 11\n"
  "v_mfma_f32_16x16x32_bf16 v[8:11], a[212:215], v[196:199], v[8:11]\n"
  "s_add_u32 s61, s61, 0x3000000\n"
  "ds_read_b64_tr_b16 v[214:215], v226 offset:41984\n"
  "v_mfma_f32_16x16x32_bf16 v[12:15], a[216:219], v[192:195], v[12:15]\n"
  "s_lshl_b32 s56, s41, 14\n"
  "v_mfma_f32_16x16x32_bf16 v[12:15], a[220:223], v[196:199], v[12:15]\n"
  "s_waitcnt lgkmcnt(3)\n"
  "s_add_u32 s56, s56, s61\n"
  "buffer_load_dwordx4 a[72:75], v244, s[48:51], s56 offen offset:0 sc1\n"
  "v_mfma_f32_16x16x32_bf16 v[16:19], a[192:195], v[200:203], v[16:19]\n"
  "ds_read_b64_tr_b16 v[216:217], v227 offset:32768\n"
  "v_mfma_f32_16x16x32_bf16 v[16:19], a[196:199], v[204:207], v[16:19]\n"
  "buffer_load_dwordx4 a[76:79], v244, s[48:51], s56 offen offset:1024 sc1\n"
  "v_mfma_f32_16x16x32_bf16 v[20:23], a[200:203], v[200:203], v[20:23]\n"
  "s_lshl_b32 s56, s42, 14\n"
  "ds_read_b64_tr_b16 v[218:219], v227 offset:33792\n"
  "v_mfma_f32_16x16x32_bf16 v[20:23], a[204:207], v[204:207], v[20:23]\n"
  "s_add_u32 s56, s56, s61\n"
  "ds_read_b64_tr_b16 v[220:221], v227 offset:40960\n"
  "v_mfma_f32_16x16x32_bf16 v[24:27], a[208:211], v[200:203], v[24:27]\n"
  "v_mfma_f32_16x16x32_bf16 v[24:27], a[212:215], v[204:207], v[24:27]\n"
  "buffer_load_dwordx4 a[80:83], v244, s[48:51], s56 offen offset:0 sc1\n"
  "v_mfma_f32_16x16x32_bf16 v[28:31], a[216:219], v[200:203], v[28:31]\n"
  "ds_read_b64_tr_b16 v[222:223], v227 offset:41984\n"
  "v_mfma_f32_16x16x32_bf16 v[28:31], a[220:223], v[204:207], v[28:31]\n"
  "s_waitcnt lgkmcnt(4)\n"
  "ds_read_b64_tr_b16 v[192:193], v228 offset:32768\n"
  "v_mfma_f32_16x16x32_bf16 v[32:35], a[192:195], v[208:211], v[32:35]\n"
  "v_mfma_f32_16x16x32_bf16 v[32:35], a[196:199], v[212:215], v[32:35]\n"
  "buffer_load_dwordx4 a[84:87], v244, s[48:51], s56 offen offset:1024 sc1\n"
  "v_mfma_f32_16x16x32_bf16 v[36:39], a[200:203], v[208:211], v[36:39]\n"
  "s_lshl_b32 s56, s43, 14\n"
  "ds_read_b64_tr_b16 v[194:195], v228 offset:33792\n"
  "v_mfma_f32_16x16x32_bf16 v[36:39], a[204:207], v[212:215], v[36:39]\n"
  "s_add_u32 s56, s56, s61\n"
  "ds_read_b64_tr_b16 v[196:197], v228 offset:40960\n"
  "v_mfma_f32_16x16x32_bf16 v[40:43], a[208:211], v[208:211], v[40:43]\n"
  "v_mfma_f32_16x16x32_bf16 v[40:43], a[212:215], v[212:215], v[40:43]\n"
  "buffer_load_dwordx4 a[88:91], v244, s[48:51], s56 offen offset:0 sc1\n"
  "v_mfma_f32_16x16x32_bf16 v[44:47], a[216:219], v[208:211], v[44:47]\n"
  "ds_read_b64_tr_b16 v[198:199], v228 offset:41984\n"
  "v_mfma_f32_16x16x32_bf16 v[44:47], a[220:223], v[212:215], v[44:47]\n"
  "s_waitcnt lgkmcnt(3)\n"
  "ds_read_b64_tr_b16 v[200:201], v229 offset:32768\n"
  "v_mfma_f32_16x16x32_bf16 v[48:51], a[192:195], v[216:219], v[48:51]\n"
  "v_mfma_f32_16x16x32_bf16 v[48:51], a[196:199], v[220:223], v[48:51]\n"
  "buffer_load_dwordx4 a[92:95], v244, s[48:51], s56 offen offset:1024 sc1\n"
  "v_mfma_f32_16x16x32_bf16 v[52:55], a[200:203], v[216:219], v[52:55]\n"
  "s_add_u32 s61, s54, 3\n"
  "ds_read_b64_tr_b16 v[202:203], v229 offset:33792\n"
  "v_mfma_f32_16x16x32_bf16 v[52:55], a[204:207], v[220:223], v[52:55]\n"
  "s_and_b32 s61, s61, 7\n"
  "ds_read_b64_tr_b16 v[204:205], v229 offset:40960\n"
  "v_mfma_f32_16x16x32_bf16 v[56:59], a[208:211], v[216:219], v[56:59]\n"
  "s_lshl_b32 s61, s61, 11\n"
  "v_mfma_f32_16x16x32_bf16 v[56:59], a[212:215], v[220:223], v[56:59]\n"
  "s_add_u32 s61, s61, 0x3000000\n"
  "ds_read_b64_tr_b16 v[206:207], v229 offset:41984\n"
  "v_mfma_f32_16x16x32_bf16 v[60:63], a[216:219], v[216:219], v[60:63]\n"
  "s_lshl_b32 s56, s40, 14\n"
  "v_mfma_f32_16x16x32_bf16 v[60:63], a[220:223], v[220:223], v[60:63]\n"
  "s_waitcnt lgkmcnt(3)\n"
  "s_add_u32 s56, s56, s61\n"
  "buffer_load_dwordx4 a[96:99], v244, s[48:51], s56 offen offset:0 sc1\n"
  "v_mfma_f32_16x16x32_bf16 v[64:67], a[192:195], v[192:195], v[64:67]\n"
  "ds_read_b64_tr_b16 v[208:209], v230 offset:32768\n"
  "v_mfma_f32_16x16x32_bf16 v[64:67], a[196:199], v[196:199], v[64:67]\n"
  "buffer_load_dwordx4 a[100:103], v244, s[48:51], s56 offen offset:1024 sc1\n"
  "v_mfma_f32_16x16x32_bf16 v[68:71], a[200:203], v[192:195], v[68:71]\n"
  "ds_read_b64_tr_b16 v[210:211], v230 offset:33792\n"
  "v_mfma_f32_16x16x32_bf16 v[68:71], a[204:207], v[196:199], v[68:71]\n"
  "ds_read_b64_tr_b16 v[212:213], v230 offset:40960\n"
  "v_mfma_f32_16x16x32_bf16 v[72:75], a[208:211], v[192:195], v[72:75]\n"
  "v_mfma_f32_16x16x32_bf16 v[72:75], a[212:215], v[196:199], v[72:75]\n"
  "ds_read_b64_tr_b16 v[214:215], v230 offset:41984\n"
  "v_mfma_f32_16x16x32_bf16 v[76:79], a[216:219], v[192:195], v[76:79]\n"
  "v_mfma_f32_16x16x32_bf16 v[76:79], a[220:223], v[196:199], v[76:79]\n"
  "s_waitcnt lgkmcnt(4)\n"
  "ds_read_b64_tr_b16 v[216:217], v231 offset:32768\n"
  "v_mfma_f32_16x16x32_bf16 v[80:83], a[192:195], v[200:203], v[80:83]\n"
  "v_mfma_f32_16x16x32_bf16 v[80:83], a[196:199], v[204:207], v[80:83]\n"
  "ds_read_b64_tr_b16 v[218:219], v231 offset:33792\n"
  "v_mfma_f32_16x16x32_bf16 v[84:87], a[200:203], v[200:203], v[84:87]\n"
  "v_mfma_f32_16x16x32_bf16 v[84:87], a[204:207], v[204:207], v[84:87]\n"
  "ds_read_b64_tr_b16 v[220:221], v231 offset:40960\n"
  "v_mfma_f32_16x16x32_bf16 v[88:91], a[208:211], v[200:203], v[88:91]\n"
  "v_mfma_f32_16x16x32_bf16 v[88:91], a[212:215], v[204:207], v[88:91]\n"
  "ds_read_b64_tr_b16 v[222:223], v231 offset:41984\n"
  "v_mfma_f32_16x16x32_bf16 v[92:95], a[216:219], v[200:203], v[92:95]\n"
  "s_waitcnt vmcnt(19)\n"
  "v_mfma_f32_16x16x32_bf16 v[92:95], a[220:223], v[204:207], v[92:95]\n"
  "s_barrier\n"
  "ds_read_b64_tr_b16 v[192:193], v224 offset:49152\n"
  "s_waitcnt lgkmcnt(4)\n"
  "v_mfma_f32_16x16x32_bf16 v[96:99], a[192:195], v[208:211], v[96:99]\n"
  "s_add_u32 s54, s54, 1\n"
  "v_mfma_f32_16x16x32_bf16 v[96:99], a[196:199], v[212:215], v[96:99]\n"
  "s_and_b32 s54, s54, 7\n"
  "ds_read_b64_tr_b16 v[194:195], v224 offset:50176\n"
  "v_mfma_f32_16x16x32_bf16 v[100:103], a[200:203], v[208:211], v[100:103]\n"
  "s_lshl_b32 s55, s54, 19\n"
  "v_mfma_f32_16x16x32_bf16 v[100:103], a[204:207], v[212:215], v[100:103]\n"
  "s_add_u32 m0, s57, 0x4000\n"
  "ds_read_b64_tr_b16 v[196:197], v224 offset:57344\n"
  "v_mfma_f32_16x16x32_bf16 v[104:107], a[208:211], v[208:211], v[104:107]\n"
  "v_mfma_f32_16x16x32_bf16 v[104:107], a[212:215], v[212:215], v[104:107]\n"
  "buffer_load_dwordx4 v232, s[36:39], s55 offen offset:0 lds\n"
  "v_mfma_f32_16x16x32_bf16 v[108:111], a[216:219], v[208:211], v[108:111]\n"
  "ds_read_b64_tr_b16 v[198:199], v224 offset:58368\n"
  "v_mfma_f32_16x16x32_bf16 v[108:111], a[220:223], v[212:215], v[108:111]\n"
  "s_waitcnt lgkmcnt(3)\n"
  "ds_read_b64_tr_b16 v[200:201], v225 offset:49152\n"
  "v_mfma_f32_16x16x32_bf16 v[112:115], a[192:195], v[216:219], v[112:115]\n"
  "v_mfma_f32_16x16x32_bf16 v[112:115], a[196:199], v[220:223], v[112:115]\n"
  "buffer_load_dwordx4 v233, s[36:39], s55 offen offset:1024 lds\n"
  "v_mfma_f32_16x16x32_bf16 v[116:119], a[200:203], v[216:219], v[116:119]\n"
  "ds_read_b64_tr_b16 v[202:203], v225 offset:50176\n"
  "v_mfma_f32_16x16x32_bf16 v[116:119], a[204:207], v[220:223], v[116:119]\n"
  "ds_read_b64_tr_b16 v[204:205], v225 offset:57344\n"
  "v_mfma_f32_16x16x32_bf16 v[120:123], a[208:211], v[216:219], v[120:123]\n"
  "v_mfma_f32_16x16x32_bf16 v[120:123], a[212:215], v[220:223], v[120:123]\n"
  "buffer_load_dwordx4 v234, s[36:39], s55 offen offset:2048 lds\n"
  "v_mfma_f32_16x16x32_bf16 v[124:127], a[216:219], v[216:219], v[124:127]\n"
  "ds_read_b64_tr_b16 v[206:207], v225 offset:58368\n"
  "v_mfma_f32_16x16x32_bf16 v[124:127], a[220:223], v[220:223], v[124:127]\n"
  "s_waitcnt lgkmcnt(3)\n"
  "ds_read_b64_tr_b16 v[208:209], v226 offset:49152\n"
  "v_mfma_f32_16x16x32_bf16 v[0:3], a[224:227], v[192:195], v[0:3]\n"
  "v_mfma_f32_16x16x32_bf16 v[0:3], a[228:231], v[196:199], v[0:3]\n"
  "buffer_load_dwordx4 v235, s[36:39], s55 offen offset:3072 lds\n"
  "v_mfma_f32_16x16x32_bf16 v[4:7], a[232:235], v[192:195], v[4:7]\n"
  "s_add_u32 s61, s54, 2\n"
  "ds_read_b64_tr_b16 v[210:211], v226 offset:50176\n"
  "v_mfma_f32_16x16x32_bf16 v[4:7], a[236:239], v[196:199], v[4:7]\n"
  "s_and_b32 s61, s61, 7\n"
  "ds_read_b64_tr_b16 v[212:213], v226 offset:57344\n"
  "v_mfma_f32_16x16x32_bf16 v[8:11], a[240:243], v[192:195], v[8:11]\n"
  "s_lshl_b32 s61, s61, 11\n"
  "v_mfma_f32_16x16x32_bf16 v[8:11], a[244:247], v[196:199], v[8:11]\n"
  "s_add_u32 s61, s61, 0x3000000\n"
  "ds_read_b64_tr_b16 v[214:215], v226 offset:58368\n"
  "v_cvt_pk_bf16_f32 v128, v0, v1\n"
  "v_mfma_f32_16x16x32_bf16 v[12:15], a[248:251], v[192:195], v[12:15]\n"
  "s_lshl_b32 s56, s41, 14\n"
  "v_cvt_pk_bf16_f32 v129, v2, v3\n"
  "v_mfma_f32_16x16x32_bf16 v[12:15], a[252:255], v[196:199], v[12:15]\n"
  "s_waitcnt lgkmcnt(3)\n"
  "s_add_u32 s56, s56, s61\n"
  "buffer_load_dwordx4 a[104:107], v244, s[48:51], s56 offen offset:0 sc1\n"
  "v_mfma_f32_16x16x32_bf16 v[16:19], a[224:227], v[200:203], v[16:19]\n"
  "ds_read_b64_tr_b16 v[216:217], v227 offset:49152\n"
  "v_cvt_pk_bf16_f32 v130, v4, v5\n"
  "v_mfma_f32_16x16x32_bf16 v[16:19], a[228:231], v[204:207], v[16:19]\n"
  "buffer_load_dwordx4 a[108:111], v244, s[48:51], s56 offen offset:1024 sc1\n"
  "v_mfma_f32_16x16x32_bf16 v[20:23], a[232:235], v[200:203], v[20:23]\n"
  "s_lshl_b32 s56, s42, 14\n"
  "ds_read_b64_tr_b16 v[218:219], v227 offset:50176\n"
  "v_cvt_pk_bf16_f32 v131, v6, v7\n"
  "v_mfma_f32_16x16x32_bf16 v[20:23], a[236:239], v[204:207], v[20:23]\n"
  "s_add_u32 s56, s56, s61\n"
  "ds_read_b64_tr_b16 v[220:221], v227 offset:57344\n"
  "v_cvt_pk_bf16_f32 v132, v8, v9\n"
  "v_mfma_f32_16x16x32_bf16 v[24:27], a[240:243], v[200:203], v[24:27]\n"
  "v_cvt_pk_bf16_f32 v133, v10, v11\n"
  "v_cvt_pk_bf16_f32 v134, v12, v13\n"
  "v_mfma_f32_16x16x32_bf16 v[24:27], a[244:247], v[204:207], v[24:27]\n"
  "buffer_load_dwordx4 a[112:115], v244, s[48:51], s56 offen offset:0 sc1\n"
  "v_mfma_f32_16x16x32_bf16 v[28:31], a[248:251], v[200:203], v[28:31]\n"
  "ds_read_b64_tr_b16 v[222:223], v227 offset:58368\n"
  "v_cvt_pk_bf16_f32 v135, v14, v15\n"
  "v_mfma_f32_16x16x32_bf16 v[28:31], a[252:255], v[204:207], v[28:31]\n"
  "s_waitcnt lgkmcnt(4)\n"
  "ds_read_b64_tr_b16 v[192:193], v228 offset:49152\n"
  "v_cvt_pk_bf16_f32 v136, v16, v17\n"
  "v_mfma_f32_16x16x32_bf16 v[32:35], a[224:227], v[208:211], v[32:35]\n"
  "v_cvt_pk_bf16_f32 v137, v18, v19\n"
  "v_cvt_pk_bf16_f32 v138, v20, v21\n"
  "v_mfma_f32_16x16x32_bf16 v[32:35], a[228:231], v[212:215], v[32:35]\n"
  "buffer_load_dwordx4 a[116:119], v244, s[48:51], s56 offen offset:1024 sc1\n"
  "v_mfma_f32_16x16x32_bf16 v[36:39], a[232:235], v[208:211], v[36:39]\n"
  "s_lshl_b32 s56, s43, 14\n"
  "ds_read_b64_tr_b16 v[194:195], v228 offset:50176\n"
  "v_cvt_pk_bf16_f32 v139, v22, v23\n"
  "v_mfma_f32_16x16x32_bf16 v[36:39], a[236:239], v[212:215], v[36:39]\n"
  "s_add_u32 s56, s56, s61\n"
  "ds_read_b64_tr_b16 v[196:197], v228 offset:57344\n"
  "v_cvt_pk_bf16_f32 v140, v24, v25\n"
  "v_mfma_f32_16x16x32_bf16 v[40:43], a[240:243], v[208:211], v[40:43]\n"
  "v_cvt_pk_bf16_f32 v141, v26, v27\n"
  "v_cvt_pk_bf16_f32 v142, v28, v29\n"
  "v_mfma_f32_16x16x32_bf16 v[40:43], a[244:247], v[212:215], v[40:43]\n"
  "buffer_load_dwordx4 a[120:123], v244, s[48:51], s56 offen offset:0 sc1\n"
  "v_mfma_f32_16x16x32_bf16 v[44:47], a[248:251], v[208:211], v[44:47]\n"
  "ds_read_b64_tr_b16 v[198:199], v228 offset:58368\n"
  "v_cvt_pk_bf16_f32 v143, v30, v31\n"
  "v_mfma_f32_16x16x32_bf16 v[44:47], a[252:255], v[212:215], v[44:47]\n"
  "s_waitcnt lgkmcnt(3)\n"
  "ds_read_b64_tr_b16 v[200:201], v229 offset:49152\n"
  "v_cvt_pk_bf16_f32 v144, v32, v33\n"
  "v_mfma_f32_16x16x32_bf16 v[48:51], a[224:227], v[216:219], v[48:51]\n"
  "v_cvt_pk_bf16_f32 v145, v34, v35\n"
  "v_cvt_pk_bf16_f32 v146, v36, v37\n"
  "v_mfma_f32_16x16x32_bf16 v[48:51], a[228:231], v[220:223], v[48:51]\n"
  "buffer_load_dwordx4 a[124:127], v244, s[48:51], s56 offen offset:1024 sc1\n"
  "v_mfma_f32_16x16x32_bf16 v[52:55], a[232:235], v[216:219], v[52:55]\n"
  "s_add_u32 s61, s54, 3\n"
  "ds_read_b64_tr_b16 v[202:203], v229 offset:50176\n"
  "v_cvt_pk_bf16_f32 v147, v38, v39\n"
  "v_mfma_f32_16x16x32_bf16 v[52:55], a[236:239], v[220:223], v[52:55]\n"
  "s_and_b32 s61, s61, 7\n"
  "ds_read_b64_tr_b16 v[204:205], v229 offset:57344\n"
  "v_cvt_pk_bf16_f32 v148, v40, v41\n"
  "v_mfma_f32_16x16x32_bf16 v[56:59], a[240:243], v[216:219], v[56:59]\n"
  "s_lshl_b32 s61, s61, 11\n"
  "v_cvt_pk_bf16_f32 v149, v42, v43\n"
  "v_cvt_pk_bf16_f32 v150, v44, v45\n"
  "v_mfma_f32_16x16x32_bf16 v[56:59], a[244:247], v[220:223], v[56:59]\n"
  "s_add_u32 s61, s61, 0x3000000\n"
  "ds_read_b64_tr_b16 v[206:207], v229 offset:58368\n"
  "v_cvt_pk_bf16_f32 v151, v46, v47\n"
  "v_mfma_f32_16x16x32_bf16 v[60:63], a[248:251], v[216:219], v[60:63]\n"
  "s_lshl_b32 s56, s40, 14\n"
  "v_cvt_pk_bf16_f32 v152, v48, v49\n"
  "v_cvt_pk_bf16_f32 v153, v50, v51\n"
  "v_mfma_f32_16x16x32_bf16 v[60:63], a[252:255], v[220:223], v[60:63]\n"
  "s_waitcnt lgkmcnt(3)\n"
  "s_add_u32 s56, s56, s61\n"
  "buffer_load_dwordx4 a[128:131], v244, s[48:51], s56 offen offset:0 sc1\n"
  "v_mfma_f32_16x16x32_bf16 v[64:67], a[224:227], v[192:195], v[64:67]\n"
  "ds_read_b64_tr_b16 v[208:209], v230 offset:49152\n"
  "v_cvt_pk_bf16_f32 v154, v52, v53\n"
  "v_mfma_f32_16x16x32_bf16 v[64:67], a[228:231], v[196:199], v[64:67]\n"
  "buffer_load_dwordx4 a[132:135], v244, s[48:51], s56 offen offset:1024 sc1\n"
  "v_mfma_f32_16x16x32_bf16 v[68:71], a[232:235], v[192:195], v[68:71]\n"
  "ds_read_b64_tr_b16 v[210:211], v230 offset:50176\n"
  "v_cvt_pk_bf16_f32 v155, v54, v55\n"
  "v_mfma_f32_16x16x32_bf16 v[68:71], a[236:239], v[196:199], v[68:71]\n"
  "ds_read_b64_tr_b16 v[212:213], v230 offset:57344\n"
  "v_cvt_pk_bf16_f32 v156, v56, v57\n"
  "v_mfma_f32_16x16x32_bf16 v[72:75], a[240:243], v[192:195], v[72:75]\n"
  "v_cvt_pk_bf16_f32 v157, v58, v59\n"
  "v_cvt_pk_bf16_f32 v158, v60, v61\n"
  "v_mfma_f32_16x16x32_bf16 v[72:75], a[244:247], v[196:199], v[72:75]\n"
  "ds_read_b64_tr_b16 v[214:215], v230 offset:58368\n"
  "v_cvt_pk_bf16_f32 v159, v62, v63\n"
  "v_mfma_f32_16x16x32_bf16 v[76:79], a[248:251], v[192:195], v[76:79]\n"
  "v_cvt_pk_bf16_f32 v160, v64, v65\n"
  "v_cvt_pk_bf16_f32 v161, v66, v67\n"
  "v_mfma_f32_16x16x32_bf16 v[76:79], a[252:255], v[196:199], v[76:79]\n"
  "s_waitcnt lgkmcnt(4)\n"
  "ds_read_b64_tr_b16 v[216:217], v231 offset:49152\n"
  "v_cvt_pk_bf16_f32 v162, v68, v69\n"
  "v_mfma_f32_16x16x32_bf16 v[80:83], a[224:227], v[200:203], v[80:83]\n"
  "v_cvt_pk_bf16_f32 v163, v70, v71\n"
  "v_mfma_f32_16x16x32_bf16 v[80:83], a[228:231], v[204:207], v[80:83]\n"
  "ds_read_b64_tr_b16 v[218:219], v231 offset:50176\n"
  "v_cvt_pk_bf16_f32 v164, v72, v73\n"
  "v_mfma_f32_16x16x32_bf16 v[84:87], a[232:235], v[200:203], v[84:87]\n"
  "v_cvt_pk_bf16_f32 v165, v74, v75\n"
  "v_mfma_f32_16x16x32_bf16 v[84:87], a[236:239], v[204:207], v[84:87]\n"
  "ds_read_b64_tr_b16 v[220:221], v231 offset:57344\n"
  "v_cvt_pk_bf16_f32 v166, v76, v77\n"
  "v_mfma_f32_16x16x32_bf16 v[88:91], a[240:243], v[200:203], v[88:91]\n"
  "v_cvt_pk_bf16_f32 v167, v78, v79\n"
  "v_mfma_f32_16x16x32_bf16 v[88:91], a[244:247], v[204:207], v[88:91]\n"
  "ds_read_b64_tr_b16 v[222:223], v231 offset:58368\n"
  "v_cvt_pk_bf16_f32 v168, v80, v81\n"
  "v_mfma_f32_16x16x32_bf16 v[92:95], a[248:251], v[200:203], v[92:95]\n"
  "s_waitcnt vmcnt(20)\n"
  "v_cvt_pk_bf16_f32 v169, v82, v83\n"
  "v_mfma_f32_16x16x32_bf16 v[92:95], a[252:255], v[204:207], v[92:95]\n"
  "s_barrier\n"
  "ds_read_b64_tr_b16 v[192:193], v224 offset:0\n"
  "v_cvt_pk_bf16_f32 v170, v84, v85\n"
  "s_waitcnt lgkmcnt(4)\n"
  "v_mfma_f32_16x16x32_bf16 v[96:99], a[224:227], v[208:211], v[96:99]\n"
  "s_add_u32 s54, s54, 1\n"
  "v_cvt_pk_bf16_f32 v171, v86, v87\n"
  "v_mfma_f32_16x16x32_bf16 v[96:99], a[228:231], v[212:215], v[96:99]\n"
  "s_and_b32 s54, s54, 7\n"
  "ds_read_b64_tr_b16 v[194:195], v224 offset:1024\n"
  "v_cvt_pk_bf16_f32 v172, v88, v89\n"
  "v_mfma_f32_16x16x32_bf16 v[100:103], a[232:235], v[208:211], v[100:103]\n"
  "s_lshl_b32 s55, s54, 19\n"
  "v_cvt_pk_bf16_f32 v173, v90, v91\n"
  "v_mfma_f32_16x16x32_bf16 v[100:103], a[236:239], v[212:215], v[100:103]\n"
  "s_add_u32 m0, s57, 0x8000\n"
  "ds_read_b64_tr_b16 v[196:197], v224 offset:8192\n"
  "v_cvt_pk_bf16_f32 v174, v92, v93\n"
  "v_mfma_f32_16x16x32_bf16 v[104:107], a[240:243], v[208:211], v[104:107]\n"
  "v_cvt_pk_bf16_f32 v175, v94, v95\n"
  "v_mfma_f32_16x16x32_bf16 v[104:107], a[244:247], v[212:215], v[104:107]\n"
  "buffer_load_dwordx4 v232, s[36:39], s55 offen offset:0 lds\n"
  "v_mfma_f32_16x16x32_bf16 v[108:111], a[248:251], v[208:211], v[108:111]\n"
  "ds_read_b64_tr_b16 v[198:199], v224 offset:9216\n"
  "v_cvt_pk_bf16_f32 v176, v96, v97\n"
  "v_mfma_f32_16x16x32_bf16 v[108:111], a[252:255], v[212:215], v[108:111]\n"
  "s_waitcnt lgkmcnt(3)\n"
  "ds_read_b64_tr_b16 v[200:201], v225 offset:0\n"
  "v_cvt_pk_bf16_f32 v177, v98, v99\n"
  "v_mfma_f32_16x16x32_bf16 v[112:115], a[224:227], v[216:219], v[112:115]\n"
  "v_cvt_pk_bf16_f32 v178, v100, v101\n"
  "v_cvt_pk_bf16_f32 v179, v102, v103\n"
  "v_mfma_f32_16x16x32_bf16 v[112:115], a[228:231], v[220:223], v[112:115]\n"
  "buffer_load_dwordx4 v233, s[36:39], s55 offen offset:1024 lds\n"
  "v_mfma_f32_16x16x32_bf16 v[116:119], a[232:235], v[216:219], v[116:119]\n"
  "ds_read_b64_tr_b16 v[202:203], v225 offset:1024\n"
  "v_cvt_pk_bf16_f32 v180, v104, v105\n"
  "v_mfma_f32_16x16x32_bf16 v[116:119], a[236:239], v[220:223], v[116:119]\n"
  "ds_read_b64_tr_b16 v[204:205], v225 offset:8192\n"
  "v_cvt_pk_bf16_f32 v181, v106, v107\n"
  "v_mfma_f32_16x16x32_bf16 v[120:123], a[240:243], v[216:219], v[120:123]\n"
  "v_cvt_pk_bf16_f32 v182, v108, v109\n"
  "v_cvt_pk_bf16_f32 v183, v110, v111\n"
  "v_mfma_f32_16x16x32_bf16 v[120:123], a[244:247], v[220:223], v[120:123]\n"
  "buffer_load_dwordx4 v234, s[36:39], s55 offen offset:2048 lds\n"
  "v_mfma_f32_16x16x32_bf16 v[124:127], a[248:251], v[216:219], v[124:127]\n"
  "ds_read_b64_tr_b16 v[206:207], v225 offset:9216\n"
  "v_cvt_pk_bf16_f32 v184, v112, v113\n"
  "v_mfma_f32_16x16x32_bf16 v[124:127], a[252:255], v[220:223], v[124:127]\n"
  "s_waitcnt lgkmcnt(3)\n"
  "ds_read_b64_tr_b16 v[208:209], v226 offset:0\n"
  "v_cvt_pk_bf16_f32 v185, v114, v115\n"
  "v_mfma_f32_16x16x32_bf16 v[0:3], v[192:195], a[0:3], 0\n"
  "v_cvt_pk_bf16_f32 v186, v116, v117\n"
  "v_cvt_pk_bf16_f32 v187, v118, v119\n"
  "v_mfma_f32_16x16x32_bf16 v[0:3], v[196:199], a[4:7], v[0:3]\n"
  "buffer_load_dwordx4 v235, s[36:39], s55 offen offset:3072 lds\n"
  "v_mfma_f32_16x16x32_bf16 v[4:7], v[192:195], a[8:11], 0\n"
  "ds_read_b64_tr_b16 v[210:211], v226 offset:1024\n"
  "v_cvt_pk_bf16_f32 v188, v120, v121\n"
  "v_mfma_f32_16x16x32_bf16 v[4:7], v[196:199], a[12:15], v[4:7]\n"
  "ds_read_b64_tr_b16 v[212:213], v226 offset:8192\n"
  "v_cvt_pk_bf16_f32 v189, v122, v123\n"
  "v_mfma_f32_16x16x32_bf16 v[8:11], v[192:195], a[16:19], 0\n"
  "v_cvt_pk_bf16_f32 v190, v124, v125\n"
  "v_cvt_pk_bf16_f32 v191, v126, v127\n"
  "v_mfma_f32_16x16x32_bf16 v[8:11], v[196:199], a[20:23], v[8:11]\n"
  "buffer_store_dword v253, v251, s[48:51], s96 offen sc1\n"
  "v_mfma_f32_16x16x32_bf16 v[12:15], v[192:195], a[24:27], 0\n"
  "s_add_u32 s59, s60, 0x4000\n"
  "ds_read_b64_tr_b16 v[214:215], v226 offset:9216\n"
  "v_mfma_f32_16x16x32_bf16 v[12:15], v[196:199], a[28:31], v[12:15]\n"
  "s_waitcnt lgkmcnt(3)\n"
  "ds_read_b64_tr_b16 v[216:217], v227 offset:0\n"
  "v_mfma_f32_16x16x32_bf16 v[16:19], v[200:203], a[0:3], 0\n"
  "v_mfma_f32_16x16x32_bf16 v[16:19], v[204:207], a[4:7], v[16:19]\n"
  "buffer_store_dwordx4 v[128:131], v244, s[48:51], s59 offen offset:0 sc1\n"
  "v_mfma_f32_16x16x32_bf16 v[20:23], v[200:203], a[8:11], 0\n"
  "ds_read_b64_tr_b16 v[218:219], v227 offset:1024\n"
  "v_mfma_f32_16x16x32_bf16 v[20:23], v[204:207], a[12:15], v[20:23]\n"
  "ds_read_b64_tr_b16 v[220:221], v227 offset:8192\n"
  "v_mfma_f32_16x16x32_bf16 v[24:27], v[200:203], a[16:19], 0\n"
  "v_mfma_f32_16x16x32_bf16 v[24:27], v[204:207], a[20:23], v[24:27]\n"
  "buffer_store_dwordx4 v[132:135], v244, s[48:51], s59 offen offset:1024 sc1\n"
  "v_mfma_f32_16x16x32_bf16 v[28:31], v[200:203], a[24:27], 0\n"
  "ds_read_b64_tr_b16 v[222:223], v227 offset:9216\n"
  "v_mfma_f32_16x16x32_bf16 v[28:31], v[204:207], a[28:31], v[28:31]\n"
  "s_waitcnt lgkmcnt(3)\n"
  "ds_read_b64_tr_b16 v[192:193], v228 offset:0\n"
  "v_mfma_f32_16x16x32_bf16 v[32:35], v[208:211], a[0:3], 0\n"
  "v_mfma_f32_16x16x32_bf16 v[32:35], v[212:215], a[4:7], v[32:35]\n"
  "buffer_store_dwordx4 v[136:139], v244, s[48:51], s59 offen offset:2048 sc1\n"
  "v_mfma_f32_16x16x32_bf16 v[36:39], v[208:211], a[8:11], 0\n"
  "s_add_u32 s61, s54, 2\n"
  "ds_read_b64_tr_b16 v[194:195], v228 offset:1024\n"
  "v_mfma_f32_16x16x32_bf16 v[36:39], v[212:215], a[12:15], v[36:39]\n"
  "s_and_b32 s61, s61, 7\n"
  "ds_read_b64_tr_b16 v[196:197], v228 offset:8192\n"
  "v_mfma_f32_16x16x32_bf16 v[40:43], v[208:211], a[16:19], 0\n"
  "s_lshl_b32 s61, s61, 11\n"
  "v_mfma_f32_16x16x32_bf16 v[40:43], v[212:215], a[20:23], v[40:43]\n"
  "s_add_u32 s61, s61, 0x3000000\n"
  "ds_read_b64_tr_b16 v[198:199], v228 offset:9216\n"
  "v_mfma_f32_16x16x32_bf16 v[44:47], v[208:211], a[24:27], 0\n"
  "s_lshl_b32 s56, s41, 14\n"
  "v_mfma_f32_16x16x32_bf16 v[44:47], v[212:215], a[28:31], v[44:47]\n"
  "s_waitcnt lgkmcnt(3)\n"
  "s_add_u32 s56, s56, s61\n"
  "buffer_load_dwordx4 a[136:139], v244, s[48:51], s56 offen offset:0 sc1\n"
  "v_mfma_f32_16x16x32_bf16 v[48:51], v[216:219], a[0:3], 0\n"
  "ds_read_b64_tr_b16 v[200:201], v229 offset:0\n"
  "v_mfma_f32_16x16x32_bf16 v[48:51], v[220:223], a[4:7], v[48:51]\n"
  "buffer_load_dwordx4 a[140:143], v244, s[48:51], s56 offen offset:1024 sc1\n"
  "v_mfma_f32_16x16x32_bf16 v[52:55], v[216:219], a[8:11], 0\n"
  "s_lshl_b32 s56, s42, 14\n"
  "ds_read_b64_tr_b16 v[202:203], v229 offset:1024\n"
  "v_mfma_f32_16x16x32_bf16 v[52:55], v[220:223], a[12:15], v[52:55]\n"
  "s_add_u32 s56, s56, s61\n"
  "ds_read_b64_tr_b16 v[204:205], v229 offset:8192\n"
  "v_mfma_f32_16x16x32_bf16 v[56:59], v[216:219], a[16:19], 0\n"
  "v_mfma_f32_16x16x32_bf16 v[56:59], v[220:223], a[20:23], v[56:59]\n"
  "buffer_load_dwordx4 a[144:147], v244, s[48:51], s56 offen offset:0 sc1\n"
  "v_mfma_f32_16x16x32_bf16 v[60:63], v[216:219], a[24:27], 0\n"
  "ds_read_b64_tr_b16 v[206:207], v229 offset:9216\n"
  "v_mfma_f32_16x16x32_bf16 v[60:63], v[220:223], a[28:31], v[60:63]\n"
  "s_waitcnt lgkmcnt(4)\n"
  "ds_read_b64_tr_b16 v[208:209], v230 offset:0\n"
  "v_mfma_f32_16x16x32_bf16 v[64:67], v[192:195], a[0:3], 0\n"
  "v_mfma_f32_16x16x32_bf16 v[64:67], v[196:199], a[4:7], v[64:67]\n"
  "buffer_load_dwordx4 a[148:151], v244, s[48:51], s56 offen offset:1024 sc1\n"
  "v_mfma_f32_16x16x32_bf16 v[68:71], v[192:195], a[8:11], 0\n"
  "s_lshl_b32 s56, s43, 14\n"
  "ds_read_b64_tr_b16 v[210:211], v230 offset:1024\n"
  "v_mfma_f32_16x16x32_bf16 v[68:71], v[196:199], a[12:15], v[68:71]\n"
  "s_add_u32 s56, s56, s61\n"
  "ds_read_b64_tr_b16 v[212:213], v230 offset:8192\n"
  "v_mfma_f32_16x16x32_bf16 v[72:75], v[192:195], a[16:19], 0\n"
  "v_mfma_f32_16x16x32_bf16 v[72:75], v[196:199], a[20:23], v[72:75]\n"
  "buffer_load_dwordx4 a[152:155], v244, s[48:51], s56 offen offset:0 sc1\n"
  "v_mfma_f32_16x16x32_bf16 v[76:79], v[192:195], a[24:27], 0\n"
  "ds_read_b64_tr_b16 v[214:215], v230 offset:9216\n"
  "v_mfma_f32_16x16x32_bf16 v[76:79], v[196:199], a[28:31], v[76:79]\n"
  "s_waitcnt lgkmcnt(3)\n"
  "ds_read_b64_tr_b16 v[216:217], v231 offset:0\n"
  "v_mfma_f32_16x16x32_bf16 v[80:83], v[200:203], a[0:3], 0\n"
  "v_mfma_f32_16x16x32_bf16 v[80:83], v[204:207], a[4:7], v[80:83]\n"
  "buffer_load_dwordx4 a[156:159], v244, s[48:51], s56 offen offset:1024 sc1\n"
  "v_mfma_f32_16x16x32_bf16 v[84:87], v[200:203], a[8:11], 0\n"
  "ds_read_b64_tr_b16 v[218:219], v231 offset:1024\n"
  "v_mfma_f32_16x16x32_bf16 v[84:87], v[204:207], a[12:15], v[84:87]\n"
  "ds_read_b64_tr_b16 v[220:221], v231 offset:8192\n"
  "v_mfma_f32_16x16x32_bf16 v[88:91], v[200:203], a[16:19], 0\n"
  "v_mfma_f32_16x16x32_bf16 v[88:91], v[204:207], a[20:23], v[88:91]\n"
  "ds_read_b64_tr_b16 v[222:223], v231 offset:9216\n"
  "v_mfma_f32_16x16x32_bf16 v[92:95], v[200:203], a[24:27], 0\n"
  "s_waitcnt vmcnt(22)\n"
  "v_mfma_f32_16x16x32_bf16 v[92:95], v[204:207], a[28:31], v[92:95]\n"
  "s_barrier\n"
  "ds_read_b64_tr_b16 v[192:193], v224 offset:16384\n"
  "s_waitcnt lgkmcnt(4)\n"
  "v_mfma_f32_16x16x32_bf16 v[96:99], v[208:211], a[0:3], 0\n"
  "s_add_u32 s54, s54, 1\n"
  "v_mfma_f32_16x16x32_bf16 v[96:99], v[212:215], a[4:7], v[96:99]\n"
  "s_and_b32 s54, s54, 7\n"
  "ds_read_b64_tr_b16 v[194:195], v224 offset:17408\n"
  "v_mfma_f32_16x16x32_bf16 v[100:103], v[208:211], a[8:11], 0\n"
  "s_lshl_b32 s55, s54, 19\n"
  "v_mfma_f32_16x16x32_bf16 v[100:103], v[212:215], a[12:15], v[100:103]\n"
  "s_add_u32 m0, s57, 0xc000\n"
  "ds_read_b64_tr_b16 v[196:197], v224 offset:24576\n"
  "v_mfma_f32_16x16x32_bf16 v[104:107], v[208:211], a[16:19], 0\n"
  "v_mfma_f32_16x16x32_bf16 v[104:107], v[212:215], a[20:23], v[104:107]\n"
  "buffer_load_dwordx4 v232, s[36:39], s55 offen offset:0 lds\n"
  "v_mfma_f32_16x16x32_bf16 v[108:111], v[208:211], a[24:27], 0\n"
  "ds_read_b64_tr_b16 v[198:199], v224 offset:25600\n"
  "v_mfma_f32_16x16x32_bf16 v[108:111], v[212:215], a[28:31], v[108:111]\n"
  "s_waitcnt lgkmcnt(3)\n"
  "ds_read_b64_tr_b16 v[200:201], v225 offset:16384\n"
  "v_mfma_f32_16x16x32_bf16 v[112:115], v[216:219], a[0:3], 0\n"
  "v_mfma_f32_16x16x32_bf16 v[112:115], v[220:223], a[4:7], v[112:115]\n"
  "buffer_load_dwordx4 v233, s[36:39], s55 offen offset:1024 lds\n"
  "v_mfma_f32_16x16x32_bf16 v[116:119], v[216:219], a[8:11], 0\n"
  "ds_read_b64_tr_b16 v[202:203], v225 offset:17408\n"
  "v_mfma_f32_16x16x32_bf16 v[116:119], v[220:223], a[12:15], v[116:119]\n"
  "ds_read_b64_tr_b16 v[204:205], v225 offset:24576\n"
  "v_mfma_f32_16x16x32_bf16 v[120:123], v[216:219], a[16:19], 0\n"
  "v_mfma_f32_16x16x32_bf16 v[120:123], v[220:223], a[20:23], v[120:123]\n"
  "buffer_load_dwordx4 v234, s[36:39], s55 offen offset:2048 lds\n"
  "v_mfma_f32_16x16x32_bf16 v[124:127], v[216:219], a[24:27], 0\n"
  "ds_read_b64_tr_b16 v[206:207], v225 offset:25600\n"
  "v_mfma_f32_16x16x32_bf16 v[124:127], v[220:223], a[28:31], v[124:127]\n"
  "s_waitcnt lgkmcnt(3)\n"
  "ds_read_b64_tr_b16 v[208:209], v226 offset:16384\n"
  "v_mfma_f32_16x16x32_bf16 v[0:3], v[192:195], a[32:35], v[0:3]\n"
  "v_mfma_f32_16x16x32_bf16 v[0:3], v[196:199], a[36:39], v[0:3]\n"
  "buffer_load_dwordx4 v235, s[36:39], s55 offen offset:3072 lds\n"
  "v_mfma_f32_16x16x32_bf16 v[4:7], v[192:195], a[40:43], v[4:7]\n"
  "s_add_u32 s59, s60, 0x4000\n"
  "ds_read_b64_tr_b16 v[210:211], v226 offset:17408\n"
  "v_mfma_f32_16x16x32_bf16 v[4:7], v[196:199], a[44:47], v[4:7]\n"
  "ds_read_b64_tr_b16 v[212:213], v226 offset:24576\n"
  "v_mfma_f32_16x16x32_bf16 v[8:11], v[192:195], a[48:51], v[8:11]\n"
  "v_mfma_f32_16x16x32_bf16 v[8:11], v[196:199], a[52:55], v[8:11]\n"
  "buffer_store_dwordx4 v[140:143], v244, s[48:51], s59 offen offset:3072 sc1\n"
  "v_mfma_f32_16x16x32_bf16 v[12:15], v[192:195], a[56:59], v[12:15]\n"
  "s_add_u32 s59, s60, 0x5000\n"
  "ds_read_b64_tr_b16 v[214:215], v226 offset:25600\n"
  "v_mfma_f32_16x16x32_bf16 v[12:15], v[196:199], a[60:63], v[12:15]\n"
  "s_waitcnt lgkmcnt(3)\n"
  "ds_read_b64_tr_b16 v[216:217], v227 offset:16384\n"
  "v_mfma_f32_16x16x32_bf16 v[16:19], v[200:203], a[32:35], v[16:19]\n"
  "mk_tp4:\n"
  "s_cbranch_execz mk_tp8\n"
  "v_mfma_f32_16x16x32_bf16 v[16:19], v[204:207], a[36:39], v[16:19]\n"
  "buffer_store_dwordx4 v[144:147], v244, s[48:51], s59 offen offset:0 sc1\n"
  "v_mfma_f32_16x16x32_bf16 v[20:23], v[200:203], a[40:43], v[20:23]\n"
  "ds_read_b64_tr_b16 v[218:219], v227 offset:17408\n"
  "v_mfma_f32_16x16x32_bf16 v[20:23], v[204:207], a[44:47], v[20:23]\n"
  "ds_read_b64_tr_b16 v[220:221], v227 offset:24576\n"
  "v_mfma_f32_16x16x32_bf16 v[24:27], v[200:203], a[48:51], v[24:27]\n"
  "v_mfma_f32_16x16x32_bf16 v[24:27], v[204:207], a[52:55], v[24:27]\n"
  "buffer_store_dwordx4 v[148:151], v244, s[48:51], s59 offen offset:1024 sc1\n"
  "v_mfma_f32_16x16x32_bf16 v[28:31], v[200:203], a[56:59], v[28:31]\n"
  "s_add_u32 s61, s54, 2\n"
  "ds_read_b64_tr_b16 v[222:223], v227 offset:25600\n"
  "v_mfma_f32_16x16x32_bf16 v[28:31], v[204:207], a[60:63], v[28:31]\n"
  "s_waitcnt lgkmcnt(3)\n"
  "s_and_b32 s61, s61, 7\n"
  "ds_read_b64_tr_b16 v[192:193], v228 offset:16384\n"
  "v_mfma_f32_16x16x32_bf16 v[32:35], v[208:211], a[32:35], v[32:35]\n"
  "s_lshl_b32 s61, s61, 11\n"
  "v_mfma_f32_16x16x32_bf16 v[32:35], v[212:215], a[36:39], v[32:35]\n"
  "s_add_u32 s61, s61, 0x3000000\n"
  "ds_read_b64_tr_b16 v[194:195], v228 offset:17408\n"
  "v_mfma_f32_16x16x32_bf16 v[36:39], v[208:211], a[40:43], v[36:39]\n"
  "s_lshl_b32 s56, s40, 14\n"
  "v_mfma_f32_16x16x32_bf16 v[36:39], v[212:215], a[44:47], v[36:39]\n"
  "s_add_u32 s56, s56, s61\n"
  "buffer_load_dwordx4 a[160:163], v244, s[48:51], s56 offen offset:0 sc1\n"
  "v_mfma_f32_16x16x32_bf16 v[40:43], v[208:211], a[48:51], v[40:43]\n"
  "ds_read_b64_tr_b16 v[196:197], v228 offset:24576\n"
  "v_mfma_f32_16x16x32_bf16 v[40:43], v[212:215], a[52:55], v[40:43]\n"
  "buffer_load_dwordx4 a[164:167], v244, s[48:51], s56 offen offset:1024 sc1\n"
  "v_mfma_f32_16x16x32_bf16 v[44:47], v[208:211], a[56:59], v[44:47]\n"
  "s_lshl_b32 s56, s41, 14\n"
  "ds_read_b64_tr_b16 v[198:199], v228 offset:25600\n"
  "v_mfma_f32_16x16x32_bf16 v[44:47], v[212:215], a[60:63], v[44:47]\n"
  "s_waitcnt lgkmcnt(3)\n"
  "s_add_u32 s56, s56, s61\n"
  "ds_read_b64_tr_b16 v[200:201], v229 offset:16384\n"
  "v_mfma_f32_16x16x32_bf16 v[48:51], v[216:219], a[32:35], v[48:51]\n"
  "v_mfma_f32_16x16x32_bf16 v[48:51], v[220:223], a[36:39], v[48:51]\n"
  "buffer_load_dwordx4 a[168:171], v244, s[48:51], s56 offen offset:0 sc1\n"
  "v_mfma_f32_16x16x32_bf16 v[52:55], v[216:219], a[40:43], v[52:55]\n"
  "ds_read_b64_tr_b16 v[202:203], v229 offset:17408\n"
  "v_mfma_f32_16x16x32_bf16 v[52:55], v[220:223], a[44:47], v[52:55]\n"
  "ds_read_b64_tr_b16 v[204:205], v229 offset:24576\n"
  "v_mfma_f32_16x16x32_bf16 v[56:59], v[216:219], a[48:51], v[56:59]\n"
  "v_mfma_f32_16x16x32_bf16 v[56:59], v[220:223], a[52:55], v[56:59]\n"
  "buffer_load_dwordx4 a[172:175], v244, s[48:51], s56 offen offset:1024 sc1\n"
  "v_mfma_f32_16x16x32_bf16 v[60:63], v[216:219], a[56:59], v[60:63]\n"
  "s_lshl_b32 s56, s42, 14\n"
  "ds_read_b64_tr_b16 v[206:207], v229 offset:25600\n"
  "v_mfma_f32_16x16x32_bf16 v[60:63], v[220:223], a[60:63], v[60:63]\n"
  "s_waitcnt lgkmcnt(3)\n"
  "s_add_u32 s56, s56, s61\n"
  "ds_read_b64_tr_b16 v[208:209], v230 offset:16384\n"
  "v_mfma_f32_16x16x32_bf16 v[64:67], v[192:195], a[32:35], v[64:67]\n"
  "v_mfma_f32_16x16x32_bf16 v[64:67], v[196:199], a[36:39], v[64:67]\n"
  "buffer_load_dwordx4 a[176:179], v244, s[48:51], s56 offen offset:0 sc1\n"
  "v_mfma_f32_16x16x32_bf16 v[68:71], v[192:195], a[40:43], v[68:71]\n"
  "ds_read_b64_tr_b16 v[210:211], v230 offset:17408\n"
  "v_mfma_f32_16x16x32_bf16 v[68:71], v[196:199], a[44:47], v[68:71]\n"
  "ds_read_b64_tr_b16 v[212:213], v230 offset:24576\n"
  "v_mfma_f32_16x16x32_bf16 v[72:75], v[192:195], a[48:51], v[72:75]\n"
  "v_mfma_f32_16x16x32_bf16 v[72:75], v[196:199], a[52:55], v[72:75]\n"
  "buffer_load_dwordx4 a[180:183], v244, s[48:51], s56 offen offset:1024 sc1\n"
  "v_mfma_f32_16x16x32_bf16 v[76:79], v[192:195], a[56:59], v[76:79]\n"
  "ds_read_b64_tr_b16 v[214:215], v230 offset:25600\n"
  "v_mfma_f32_16x16x32_bf16 v[76:79], v[196:199], a[60:63], v[76:79]\n"
  "s_waitcnt lgkmcnt(3)\n"
  "ds_read_b64_tr_b16 v[216:217], v231 offset:16384\n"
  "v_mfma_f32_16x16x32_bf16 v[80:83], v[200:203], a[32:35], v[80:83]\n"
  "v_mfma_f32_16x16x32_bf16 v[80:83], v[204:207], a[36:39], v[80:83]\n"
  "ds_read_b64_tr_b16 v[218:219], v231 offset:17408\n"
  "v_mfma_f32_16x16x32_bf16 v[84:87], v[200:203], a[40:43], v[84:87]\n"
  "v_mfma_f32_16x16x32_bf16 v[84:87], v[204:207], a[44:47], v[84:87]\n"
  "ds_read_b64_tr_b16 v[220:221], v231 offset:24576\n"
  "v_mov_b32 v252, 0\n"
  "v_mfma_f32_16x16x32_bf16 v[88:91], v[200:203], a[48:51], v[88:91]\n"
  "v_mfma_f32_16x16x32_bf16 v[88:91], v[204:207], a[52:55], v[88:91]\n"
  "ds_read_b64_tr_b16 v[222:223], v231 offset:25600\n"
  "v_mfma_f32_16x16x32_bf16 v[92:95], v[200:203], a[56:59], v[92:95]\n"
  "s_waitcnt vmcnt(23)\n"
  "v_mfma_f32_16x16x32_bf16 v[92:95], v[204:207], a[60:63], v[92:95]\n"
  "s_barrier\n"
  "ds_read_b64_tr_b16 v[192:193], v224 offset:32768\n"
  "s_waitcnt lgkmcnt(4)\n"
  "v_mfma_f32_16x16x32_bf16 v[96:99], v[208:211], a[32:35], v[96:99]\n"
  "buffer_atomic_add v252, v249, s[48:51], s96 offen sc0 sc1\n"
  "v_mfma_f32_16x16x32_bf16 v[96:99], v[212:215], a[36:39], v[96:99]\n"
  "s_add_u32 s54, s54, 1\n"
  "ds_read_b64_tr_b16 v[194:195], v224 offset:33792\n"
  "v_mfma_f32_16x16x32_bf16 v[100:103], v[208:211], a[40:43], v[100:103]\n"
  "s_and_b32 s54, s54, 7\n"
  "v_mfma_f32_16x16x32_bf16 v[100:103], v[212:215], a[44:47], v[100:103]\n"
  "s_lshl_b32 s55, s54, 19\n"
  "ds_read_b64_tr_b16 v[196:197], v224 offset:40960\n"
  "v_mfma_f32_16x16x32_bf16 v[104:107], v[208:211], a[48:51], v[104:107]\n"
  "s_add_u32 m0, s57, 0x0\n"
  "v_mfma_f32_16x16x32_bf16 v[104:107], v[212:215], a[52:55], v[104:107]\n"
  "buffer_load_dwordx4 v232, s[36:39], s55 offen offset:0 lds\n"
  "v_mfma_f32_16x16x32_bf16 v[108:111], v[208:211], a[56:59], v[108:111]\n"
  "ds_read_b64_tr_b16 v[198:199], v224 offset:41984\n"
  "v_mfma_f32_16x16x32_bf16 v[108:111], v[212:215], a[60:63], v[108:111]\n"
  "s_waitcnt lgkmcnt(3)\n"
  "ds_read_b64_tr_b16 v[200:201], v225 offset:32768\n"
  "v_mfma_f32_16x16x32_bf16 v[112:115], v[216:219], a[32:35], v[112:115]\n"
  "v_mfma_f32_16x16x32_bf16 v[112:115], v[220:223], a[36:39], v[112:115]\n"
  "buffer_load_dwordx4 v233, s[36:39], s55 offen offset:1024 lds\n"
  "v_mfma_f32_16x16x32_bf16 v[116:119], v[216:219], a[40:43], v[116:119]\n"
  "ds_read_b64_tr_b16 v[202:203], v225 offset:33792\n"
  "v_mfma_f32_16x16x32_bf16 v[116:119], v[220:223], a[44:47], v[116:119]\n"
  "ds_read_b64_tr_b16 v[204:205], v225 offset:40960\n"
  "v_mfma_f32_16x16x32_bf16 v[120:123], v[216:219], a[48:51], v[120:123]\n"
  "v_mfma_f32_16x16x32_bf16 v[120:123], v[220:223], a[52:55], v[120:123]\n"
  "buffer_load_dwordx4 v234, s[36:39], s55 offen offset:2048 lds\n"
  "v_mfma_f32_16x16x32_bf16 v[124:127], v[216:219], a[56:59], v[124:127]\n"
  "ds_read_b64_tr_b16 v[206:207], v225 offset:41984\n"
  "v_mfma_f32_16x16x32_bf16 v[124:127], v[220:223], a[60:63], v[124:127]\n"
  "s_waitcnt lgkmcnt(3)\n"
  "ds_read_b64_tr_b16 v[208:209], v226 offset:32768\n"
  "v_mfma_f32_16x16x32_bf16 v[0:3], v[192:195], a[64:67], v[0:3]\n"
  "v_mfma_f32_16x16x32_bf16 v[0:3], v[196:199], a[68:71], v[0:3]\n"
  "buffer_load_dwordx4 v235, s[36:39], s55 offen offset:3072 lds\n"
  "v_mfma_f32_16x16x32_bf16 v[4:7], v[192:195], a[72:75], v[4:7]\n"
  "s_add_u32 s59, s60, 0x5000\n"
  "ds_read_b64_tr_b16 v[210:211], v226 offset:33792\n"
  "v_mfma_f32_16x16x32_bf16 v[4:7], v[196:199], a[76:79], v[4:7]\n"
  "ds_read_b64_tr_b16 v[212:213], v226 offset:40960\n"
  "v_mfma_f32_16x16x32_bf16 v[8:11], v[192:195], a[80:83], v[8:11]\n"
  "v_mfma_f32_16x16x32_bf16 v[8:11], v[196:199], a[84:87], v[8:11]\n"
  "buffer_store_dwordx4 v[152:155], v244, s[48:51], s59 offen offset:2048 sc1\n"
  "v_mfma_f32_16x16x32_bf16 v[12:15], v[192:195], a[88:91], v[12:15]\n"
  "ds_read_b64_tr_b16 v[214:215], v226 offset:41984\n"
  "v_mfma_f32_16x16x32_bf16 v[12:15], v[196:199], a[92:95], v[12:15]\n"
  "s_waitcnt lgkmcnt(3)\n"
  "ds_read_b64_tr_b16 v[216:217], v227 offset:32768\n"
  "v_mfma_f32_16x16x32_bf16 v[16:19], v[200:203], a[64:67], v[16:19]\n"
  "v_mfma_f32_16x16x32_bf16 v[16:19], v[204:207], a[68:71], v[16:19]\n"
  "buffer_store_dwordx4 v[156:159], v244, s[48:51], s59 offen offset:3072 sc1\n"
  "v_mfma_f32_16x16x32_bf16 v[20:23], v[200:203], a[72:75], v[20:23]\n"
  "s_add_u32 s59, s60, 0x6000\n"
  "ds_read_b64_tr_b16 v[218:219], v227 offset:33792\n"
  "v_mfma_f32_16x16x32_bf16 v[20:23], v[204:207], a[76:79], v[20:23]\n"
  "ds_read_b64_tr_b16 v[220:221], v227 offset:40960\n"
  "v_mfma_f32_16x16x32_bf16 v[24:27], v[200:203], a[80:83], v[24:27]\n"
  "v_mfma_f32_16x16x32_bf16 v[24:27], v[204:207], a[84:87], v[24:27]\n"
  "buffer_store_dwordx4 v[160:163], v244, s[48:51], s59 offen offset:0 sc1\n"
  "v_mfma_f32_16x16x32_bf16 v[28:31], v[200:203], a[88:91], v[28:31]\n"
  "s_add_u32 s61, s54, 1\n"
  "ds_read_b64_tr_b16 v[222:223], v227 offset:41984\n"
  "v_mfma_f32_16x16x32_bf16 v[28:31], v[204:207], a[92:95], v[28:31]\n"
  "s_waitcnt lgkmcnt(3)\n"
  "s_and_b32 s61, s61, 7\n"
  "ds_read_b64_tr_b16 v[192:193], v228 offset:32768\n"
  "v_mfma_f32_16x16x32_bf16 v[32:35], v[208:211], a[64:67], v[32:35]\n"
  "s_lshl_b32 s61, s61, 11\n"
  "v_mfma_f32_16x16x32_bf16 v[32:35], v[212:215], a[68:71], v[32:35]\n"
  "s_add_u32 s61, s61, 0x3000000\n"
  "ds_read_b64_tr_b16 v[194:195], v228 offset:33792\n"
  "v_mfma_f32_16x16x32_bf16 v[36:39], v[208:211], a[72:75], v[36:39]\n"
  "s_lshl_b32 s56, s43, 14\n"
  "v_mfma_f32_16x16x32_bf16 v[36:39], v[212:215], a[76:79], v[36:39]\n"
  "s_add_u32 s56, s56, s61\n"
  "buffer_load_dwordx4 a[184:187], v244, s[48:51], s56 offen offset:0 sc1\n"
  "v_mfma_f32_16x16x32_bf16 v[40:43], v[208:211], a[80:83], v[40:43]\n"
  "ds_read_b64_tr_b16 v[196:197], v228 offset:40960\n"
  "v_mfma_f32_16x16x32_bf16 v[40:43], v[212:215], a[84:87], v[40:43]\n"
  "buffer_load_dwordx4 a[188:191], v244, s[48:51], s56 offen offset:1024 sc1\n"
  "v_mfma_f32_16x16x32_bf16 v[44:47], v[208:211], a[88:91], v[44:47]\n"
  "s_add_u32 s61, s54, 2\n"
  "ds_read_b64_tr_b16 v[198:199], v228 offset:41984\n"
  "v_mfma_f32_16x16x32_bf16 v[44:47], v[212:215], a[92:95], v[44:47]\n"
  "s_waitcnt lgkmcnt(3)\n"
  "s_and_b32 s61, s61, 7\n"
  "ds_read_b64_tr_b16 v[200:201], v229 offset:32768\n"
  "v_mfma_f32_16x16x32_bf16 v[48:51], v[216:219], a[64:67], v[48:51]\n"
  "s_lshl_b32 s61, s61, 11\n"
  "v_mfma_f32_16x16x32_bf16 v[48:51], v[220:223], a[68:71], v[48:51]\n"
  "s_add_u32 s61, s61, 0x3000000\n"
  "ds_read_b64_tr_b16 v[202:203], v229 offset:33792\n"
  "v_mfma_f32_16x16x32_bf16 v[52:55], v[216:219], a[72:75], v[52:55]\n"
  "s_lshl_b32 s56, s40, 14\n"
  "v_mfma_f32_16x16x32_bf16 v[52:55], v[220:223], a[76:79], v[52:55]\n"
  "s_add_u32 s56, s56, s61\n"
  "buffer_load_dwordx4 a[192:195], v244, s[48:51], s56 offen offset:0 sc1\n"
  "v_mfma_f32_16x16x32_bf16 v[56:59], v[216:219], a[80:83], v[56:59]\n"
  "ds_read_b64_tr_b16 v[204:205], v229 offset:40960\n"
  "v_mfma_f32_16x16x32_bf16 v[56:59], v[220:223], a[84:87], v[56:59]\n"
  "buffer_load_dwordx4 a[196:199], v244, s[48:51], s56 offen offset:1024 sc1\n"
  "v_mfma_f32_16x16x32_bf16 v[60:63], v[216:219], a[88:91], v[60:63]\n"
  "s_lshl_b32 s56, s41, 14\n"
  "ds_read_b64_tr_b16 v[206:207], v229 offset:41984\n"
  "v_mfma_f32_16x16x32_bf16 v[60:63], v[220:223], a[92:95], v[60:63]\n"
  "s_waitcnt lgkmcnt(3)\n"
  "s_add_u32 s56, s56, s61\n"
  "ds_read_b64_tr_b16 v[208:209], v230 offset:32768\n"
  "v_mfma_f32_16x16x32_bf16 v[64:67], v[192:195], a[64:67], v[64:67]\n"
  "v_mfma_f32_16x16x32_bf16 v[64:67], v[196:199], a[68:71], v[64:67]\n"
  "buffer_load_dwordx4 a[200:203], v244, s[48:51], s56 offen offset:0 sc1\n"
  "v_mfma_f32_16x16x32_bf16 v[68:71], v[192:195], a[72:75], v[68:71]\n"
  "ds_read_b64_tr_b16 v[210:211], v230 offset:33792\n"
  "v_mfma_f32_16x16x32_bf16 v[68:71], v[196:199], a[76:79], v[68:71]\n"
  "ds_read_b64_tr_b16 v[212:213], v230 offset:40960\n"
  "v_mfma_f32_16x16x32_bf16 v[72:75], v[192:195], a[80:83], v[72:75]\n"
  "v_mfma_f32_16x16x32_bf16 v[72:75], v[196:199], a[84:87], v[72:75]\n"
  "buffer_load_dwordx4 a[204:207], v244, s[48:51], s56 offen offset:1024 sc1\n"
  "v_mfma_f32_16x16x32_bf16 v[76:79], v[192:195], a[88:91], v[76:79]\n"
  "ds_read_b64_tr_b16 v[214:215], v230 offset:41984\n"
  "v_mfma_f32_16x16x32_bf16 v[76:79], v[196:199], a[92:95], v[76:79]\n"
  "s_waitcnt lgkmcnt(3)\n"
  "ds_read_b64_tr_b16 v[216:217], v231 offset:32768\n"
  "v_mfma_f32_16x16x32_bf16 v[80:83], v[200:203], a[64:67], v[80:83]\n"
  "v_mfma_f32_16x16x32_bf16 v[80:83], v[204:207], a[68:71], v[80:83]\n"
  "ds_read_b64_tr_b16 v[218:219], v231 offset:33792\n"
  "v_mfma_f32_16x16x32_bf16 v[84:87], v[200:203], a[72:75], v[84:87]\n"
  "v_mfma_f32_16x16x32_bf16 v[84:87], v[204:207], a[76:79], v[84:87]\n"
  "ds_read_b64_tr_b16 v[220:221], v231 offset:40960\n"
  "v_mfma_f32_16x16x32_bf16 v[88:91], v[200:203], a[80:83], v[88:91]\n"
  "s_waitcnt vmcnt(13)\n"
  "v_cmp_eq_u32 vcc, v252, v248\n"
  "v_mfma_f32_16x16x32_bf16 v[88:91], v[204:207], a[84:87], v[88:91]\n"
  "ds_read_b64_tr_b16 v[222:223], v231 offset:41984\n"
  "v_mfma_f32_16x16x32_bf16 v[92:95], v[200:203], a[88:91], v[92:95]\n"
  "s_and_b32 vcc_lo, vcc_lo, 0x300\n"
  "s_cbranch_scc1 mk_spinH\n"
  "mk_spinH_b:\n"
  "v_mfma_f32_16x16x32_bf16 v[92:95], v[204:207], a[92:95], v[92:95]\n"
  "s_barrier\n"
  "ds_read_b64_tr_b16 v[192:193], v224 offset:49152\n"
  "s_waitcnt lgkmcnt(4)\n"
  "v_mfma_f32_16x16x32_bf16 v[96:99], v[208:211], a[64:67], v[96:99]\n"
  "s_add_u32 s54, s54, 1\n"
  "v_mfma_f32_16x16x32_bf16 v[96:99], v[212:215], a[68:71], v[96:99]\n"
  "s_and_b32 s54, s54, 7\n"
  "ds_read_b64_tr_b16 v[194:195], v224 offset:50176\n"
  "v_mfma_f32_16x16x32_bf16 v[100:103], v[208:211], a[72:75], v[100:103]\n"
  "s_lshl_b32 s55, s54, 19\n"
  "v_mfma_f32_16x16x32_bf16 v[100:103], v[212:215], a[76:79], v[100:103]\n"
  "s_add_u32 m0, s57, 0x4000\n"
  "ds_read_b64_tr_b16 v[196:197], v224 offset:57344\n"
  "v_mfma_f32_16x16x32_bf16 v[104:107], v[208:211], a[80:83], v[104:107]\n"
  "v_mfma_f32_16x16x32_bf16 v[104:107], v[212:215], a[84:87], v[104:107]\n"
  "buffer_load_dwordx4 v232, s[36:39], s55 offen offset:0 lds\n"
  "v_mfma_f32_16x16x32_bf16 v[108:111], v[208:211], a[88:91], v[108:111]\n"
  "ds_read_b64_tr_b16 v[198:199], v224 offset:58368\n"
  "v_mfma_f32_16x16x32_bf16 v[108:111], v[212:215], a[92:95], v[108:111]\n"
  "s_waitcnt lgkmcnt(3)\n"
  "ds_read_b64_tr_b16 v[200:201], v225 offset:49152\n"
  "v_mfma_f32_16x16x32_bf16 v[112:115], v[216:219], a[64:67], v[112:115]\n"
  "v_mfma_f32_16x16x32_bf16 v[112:115], v[220:223], a[68:71], v[112:115]\n"
  "buffer_load_dwordx4 v233, s[36:39], s55 offen offset:1024 lds\n"
  "v_mfma_f32_16x16x32_bf16 v[116:119], v[216:219], a[72:75], v[116:119]\n"
  "ds_read_b64_tr_b16 v[202:203], v225 offset:50176\n"
  "v_mfma_f32_16x16x32_bf16 v[116:119], v[220:223], a[76:79], v[116:119]\n"
  "ds_read_b64_tr_b16 v[204:205], v225 offset:57344\n"
  "v_mfma_f32_16x16x32_bf16 v[120:123], v[216:219], a[80:83], v[120:123]\n"
  "v_mfma_f32_16x16x32_bf16 v[120:123], v[220:223], a[84:87], v[120:123]\n"
  "buffer_load_dwordx4 v234, s[36:39], s55 offen offset:2048 lds\n"
  "v_mfma_f32_16x16x32_bf16 v[124:127], v[216:219], a[88:91], v[124:127]\n"
  "ds_read_b64_tr_b16 v[206:207], v225 offset:58368\n"
  "v_mfma_f32_16x16x32_bf16 v[124:127], v[220:223], a[92:95], v[124:127]\n"
  "s_waitcnt lgkmcnt(3)\n"
  "ds_read_b64_tr_b16 v[208:209], v226 offset:49152\n"
  "v_mfma_f32_16x16x32_bf16 v[0:3], v[192:195], a[96:99], v[0:3]\n"
  "v_mfma_f32_16x16x32_bf16 v[0:3], v[196:199], a[100:103], v[0:3]\n"
  "buffer_load_dwordx4 v235, s[36:39], s55 offen offset:3072 lds\n"
  "v_mfma_f32_16x16x32_bf16 v[4:7], v[192:195], a[104:107], v[4:7]\n"
  "ds_read_b64_tr_b16 v[210:211], v226 offset:50176\n"
  "v_mfma_f32_16x16x32_bf16 v[4:7], v[196:199], a[108:111], v[4:7]\n"
  "ds_read_b64_tr_b16 v[212:213], v226 offset:57344\n"
  "v_mfma_f32_16x16x32_bf16 v[8:11], v[192:195], a[112:115], v[8:11]\n"
  "v_mfma_f32_16x16x32_bf16 v[8:11], v[196:199], a[116:119], v[8:11]\n"
  "buffer_load_dword v242, v249, s[48:51], s97 offen sc1\n"
  "v_mfma_f32_16x16x32_bf16 v[12:15], v[192:195], a[120:123], v[12:15]\n"
  "s_add_u32 s59, s60, 0x6000\n"
  "ds_read_b64_tr_b16 v[214:215], v226 offset:58368\n"
  "v_mfma_f32_16x16x32_bf16 v[12:15], v[196:199], a[124:127], v[12:15]\n"
  "s_waitcnt lgkmcnt(3)\n"
  "ds_read_b64_tr_b16 v[216:217], v227 offset:49152\n"
  "v_mfma_f32_16x16x32_bf16 v[16:19], v[200:203], a[96:99], v[16:19]\n"
  "v_mfma_f32_16x16x32_bf16 v[16:19], v[204:207], a[100:103], v[16:19]\n"
  "buffer_store_dwordx4 v[164:167], v244, s[48:51], s59 offen offset:1024 sc1\n"
  "v_mfma_f32_16x16x32_bf16 v[20:23], v[200:203], a[104:107], v[20:23]\n"
  "ds_read_b64_tr_b16 v[218:219], v227 offset:50176\n"
  "v_mfma_f32_16x16x32_bf16 v[20:23], v[204:207], a[108:111], v[20:23]\n"
  "ds_read_b64_tr_b16 v[220:221], v227 offset:57344\n"
  "v_mfma_f32_16x16x32_bf16 v[24:27], v[200:203], a[112:115], v[24:27]\n"
  "v_mfma_f32_16x16x32_bf16 v[24:27], v[204:207], a[116:119], v[24:27]\n"
  "buffer_store_dwordx4 v[168:171], v244, s[48:51], s59 offen offset:2048 sc1\n"
  "v_mfma_f32_16x16x32_bf16 v[28:31], v[200:203], a[120:123], v[28:31]\n"
  "ds_read_b64_tr_b16 v[222:223], v227 offset:58368\n"
  "v_mfma_f32_16x16x32_bf16 v[28:31], v[204:207], a[124:127], v[28:31]\n"
  "s_waitcnt lgkmcnt(3)\n"
  "ds_read_b64_tr_b16 v[192:193], v228 offset:49152\n"
  "v_mfma_f32_16x16x32_bf16 v[32:35], v[208:211], a[96:99], v[32:35]\n"
  "v_mfma_f32_16x16x32_bf16 v[32:35], v[212:215], a[100:103], v[32:35]\n"
  "buffer_store_dwordx4 v[172:175], v244, s[48:51], s59 offen offset:3072 sc1\n"
  "v_mfma_f32_16x16x32_bf16 v[36:39], v[208:211], a[104:107], v[36:39]\n"
  "s_add_u32 s61, s54, 1\n"
  "ds_read_b64_tr_b16 v[194:195], v228 offset:50176\n"
  "v_mfma_f32_16x16x32_bf16 v[36:39], v[212:215], a[108:111], v[36:39]\n"
  "s_and_b32 s61, s61, 7\n"
  "ds_read_b64_tr_b16 v[196:197], v228 offset:57344\n"
  "v_mfma_f32_16x16x32_bf16 v[40:43], v[208:211], a[112:115], v[40:43]\n"
  "s_lshl_b32 s61, s61, 11\n"
  "v_mfma_f32_16x16x32_bf16 v[40:43], v[212:215], a[116:119], v[40:43]\n"
  "s_add_u32 s61, s61, 0x3000000\n"
  "ds_read_b64_tr_b16 v[198:199], v228 offset:58368\n"
  "v_mfma_f32_16x16x32_bf16 v[44:47], v[208:211], a[120:123], v[44:47]\n"
  "s_lshl_b32 s56, s42, 14\n"
  "v_mfma_f32_16x16x32_bf16 v[44:47], v[212:215], a[124:127], v[44:47]\n"
  "s_waitcnt lgkmcnt(3)\n"
  "s_add_u32 s56, s56, s61\n"
  "buffer_load_dwordx4 a[208:211], v244, s[48:51], s56 offen offset:0 sc1\n"
  "v_mfma_f32_16x16x32_bf16 v[48:51], v[216:219], a[96:99], v[48:51]\n"
  "ds_read_b64_tr_b16 v[200:201], v229 offset:49152\n"
  "v_mfma_f32_16x16x32_bf16 v[48:51], v[220:223], a[100:103], v[48:51]\n"
  "buffer_load_dwordx4 a[212:215], v244, s[48:51], s56 offen offset:1024 sc1\n"
  "v_mfma_f32_16x16x32_bf16 v[52:55], v[216:219], a[104:107], v[52:55]\n"
  "s_lshl_b32 s56, s43, 14\n"
  "ds_read_b64_tr_b16 v[202:203], v229 offset:50176\n"
  "v_mfma_f32_16x16x32_bf16 v[52:55], v[220:223], a[108:111], v[52:55]\n"
  "s_add_u32 s56, s56, s61\n"
  "ds_read_b64_tr_b16 v[204:205], v229 offset:57344\n"
  "v_mfma_f32_16x16x32_bf16 v[56:59], v[216:219], a[112:115], v[56:59]\n"
  "v_mfma_f32_16x16x32_bf16 v[56:59], v[220:223], a[116:119], v[56:59]\n"
  "buffer_load_dwordx4 a[216:219], v244, s[48:51], s56 offen offset:0 sc1\n"
  "v_mfma_f32_16x16x32_bf16 v[60:63], v[216:219], a[120:123], v[60:63]\n"
  "ds_read_b64_tr_b16 v[206:207], v229 offset:58368\n"
  "v_mfma_f32_16x16x32_bf16 v[60:63], v[220:223], a[124:127], v[60:63]\n"
  "s_waitcnt lgkmcnt(4)\n"
  "ds_read_b64_tr_b16 v[208:209], v230 offset:49152\n"
  "v_mfma_f32_16x16x32_bf16 v[64:67], v[192:195], a[96:99], v[64:67]\n"
  "v_mfma_f32_16x16x32_bf16 v[64:67], v[196:199], a[100:103], v[64:67]\n"
  "buffer_load_dwordx4 a[220:223], v244, s[48:51], s56 offen offset:1024 sc1\n"
  "v_mfma_f32_16x16x32_bf16 v[68:71], v[192:195], a[104:107], v[68:71]\n"
  "s_add_u32 s61, s54, 2\n"
  "ds_read_b64_tr_b16 v[210:211], v230 offset:50176\n"
  "v_mfma_f32_16x16x32_bf16 v[68:71], v[196:199], a[108:111], v[68:71]\n"
  "s_and_b32 s61, s61, 7\n"
  "ds_read_b64_tr_b16 v[212:213], v230 offset:57344\n"
  "v_mfma_f32_16x16x32_bf16 v[72:75], v[192:195], a[112:115], v[72:75]\n"
  "s_lshl_b32 s61, s61, 11\n"
  "v_mfma_f32_16x16x32_bf16 v[72:75], v[196:199], a[116:119], v[72:75]\n"
  "s_add_u32 s61, s61, 0x3000000\n"
  "ds_read_b64_tr_b16 v[214:215], v230 offset:58368\n"
  "v_mfma_f32_16x16x32_bf16 v[76:79], v[192:195], a[120:123], v[76:79]\n"
  "s_lshl_b32 s56, s40, 14\n"
  "v_mfma_f32_16x16x32_bf16 v[76:79], v[196:199], a[124:127], v[76:79]\n"
  "s_waitcnt lgkmcnt(3)\n"
  "s_add_u32 s56, s56, s61\n"
  "buffer_load_dwordx4 a[224:227], v244, s[48:51], s56 offen offset:0 sc1\n"
  "v_mfma_f32_16x16x32_bf16 v[80:83], v[200:203], a[96:99], v[80:83]\n"
  "ds_read_b64_tr_b16 v[216:217], v231 offset:49152\n"
  "v_mfma_f32_16x16x32_bf16 v[80:83], v[204:207], a[100:103], v[80:83]\n"
  "buffer_load_dwordx4 a[228:231], v244, s[48:51], s56 offen offset:1024 sc1\n"
  "v_mfma_f32_16x16x32_bf16 v[84:87], v[200:203], a[104:107], v[84:87]\n"
  "ds_read_b64_tr_b16 v[218:219], v231 offset:50176\n"
  "v_mfma_f32_16x16x32_bf16 v[84:87], v[204:207], a[108:111], v[84:87]\n"
  "ds_read_b64_tr_b16 v[220:221], v231 offset:57344\n"
  "v_mfma_f32_16x16x32_bf16 v[88:91], v[200:203], a[112:115], v[88:91]\n"
  "v_mfma_f32_16x16x32_bf16 v[88:91], v[204:207], a[116:119], v[88:91]\n"
  "ds_read_b64_tr_b16 v[222:223], v231 offset:58368\n"
  "v_mfma_f32_16x16x32_bf16 v[92:95], v[200:203], a[120:123], v[92:95]\n"
  "s_waitcnt vmcnt(23)\n"
  "v_mfma_f32_16x16x32_bf16 v[92:95], v[204:207], a[124:127], v[92:95]\n"
  "s_barrier\n"
  "ds_read_b64_tr_b16 v[192:193], v224 offset:0\n"
  "s_waitcnt lgkmcnt(5)\n"
  "v_mfma_f32_16x16x32_bf16 v[96:99], v[208:211], a[96:99], v[96:99]\n"
  "s_add_u32 s54, s54, 1\n"
  "v_mfma_f32_16x16x32_bf16 v[96:99], v[212:215], a[100:103], v[96:99]\n"
  "s_and_b32 s54, s54, 7\n"
  "ds_read_b64_tr_b16 v[194:195], v224 offset:1024\n"
  "v_mfma_f32_16x16x32_bf16 v[100:103], v[208:211], a[104:107], v[100:103]\n"
  "s_lshl_b32 s55, s54, 19\n"
  "v_mfma_f32_16x16x32_bf16 v[100:103], v[212:215], a[108:111], v[100:103]\n"
  "s_add_u32 m0, s57, 0x8000\n"
  "ds_read_b64_tr_b16 v[196:197], v224 offset:8192\n"
  "v_mfma_f32_16x16x32_bf16 v[104:107], v[208:211], a[112:115], v[104:107]\n"
  "v_mfma_f32_16x16x32_bf16 v[104:107], v[212:215], a[116:119], v[104:107]\n"
  "buffer_load_dwordx4 v232, s[36:39], s55 offen offset:0 lds\n"
  "v_mfma_f32_16x16x32_bf16 v[108:111], v[208:211], a[120:123], v[108:111]\n"
  "ds_read_b64_tr_b16 v[198:199], v224 offset:9216\n"
  "v_mfma_f32_16x16x32_bf16 v[108:111], v[212:215], a[124:127], v[108:111]\n"
  "s_waitcnt lgkmcnt(3)\n"
  "ds_read_b64_tr_b16 v[200:201], v225 offset:0\n"
  "v_mfma_f32_16x16x32_bf16 v[112:115], v[216:219], a[96:99], v[112:115]\n"
  "v_mfma_f32_16x16x32_bf16 v[112:115], v[220:223], a[100:103], v[112:115]\n"
  "buffer_load_dwordx4 v233, s[36:39], s55 offen offset:1024 lds\n"
  "v_mfma_f32_16x16x32_bf16 v[116:119], v[216:219], a[104:107], v[116:119]\n"
  "ds_read_b64_tr_b16 v[202:203], v225 offset:1024\n"
  "v_mfma_f32_16x16x32_bf16 v[116:119], v[220:223], a[108:111], v[116:119]\n"
  "ds_read_b64_tr_b16 v[204:205], v225 offset:8192\n"
  "v_mfma_f32_16x16x32_bf16 v[120:123], v[216:219], a[112:115], v[120:123]\n"
  "v_mfma_f32_16x16x32_bf16 v[120:123], v[220:223], a[116:119], v[120:123]\n"
  "buffer_load_dwordx4 v234, s[36:39], s55 offen offset:2048 lds\n"
  "v_mfma_f32_16x16x32_bf16 v[124:127], v[216:219], a[120:123], v[124:127]\n"
  "ds_read_b64_tr_b16 v[206:207], v225 offset:9216\n"
  "v_mfma_f32_16x16x32_bf16 v[124:127], v[220:223], a[124:127], v[124:127]\n"
  "s_waitcnt lgkmcnt(3)\n"
  "ds_read_b64_tr_b16 v[208:209], v226 offset:0\n"
  "v_mfma_f32_16x16x32_bf16 v[0:3], v[192:195], a[128:131], v[0:3]\n"
  "v_mfma_f32_16x16x32_bf16 v[0:3], v[196:199], a[132:135], v[0:3]\n"
  "buffer_load_dwordx4 v235, s[36:39], s55 offen offset:3072 lds\n"
  "v_mfma_f32_16x16x32_bf16 v[4:7], v[192:195], a[136:139], v[4:7]\n"
  "s_add_u32 s59, s60, 0x7000\n"
  "ds_read_b64_tr_b16 v[210:211], v226 offset:1024\n"
  "v_accvgpr_write_b32 a0, 0\n"
  "v_mfma_f32_16x16x32_bf16 v[4:7], v[196:199], a[140:143], v[4:7]\n"
  "ds_read_b64_tr_b16 v[212:213], v226 offset:8192\n"
  "v_accvgpr_write_b32 a1, 0\n"
  "v_mfma_f32_16x16x32_bf16 v[8:11], v[192:195], a[144:147], v[8:11]\n"
  "v_accvgpr_write_b32 a2, 0\n"
  "v_accvgpr_write_b32 a3, 0\n"
  "v_mfma_f32_16x16x32_bf16 v[8:11], v[196:199], a[148:151], v[8:11]\n"
  "buffer_store_dwordx4 v[176:179], v244, s[48:51], s59 offen offset:0 sc1\n"
  "v_mfma_f32_16x16x32_bf16 v[12:15], v[192:195], a[152:155], v[12:15]\n"
  "ds_read_b64_tr_b16 v[214:215], v226 offset:9216\n"
  "v_accvgpr_write_b32 a4, 0\n"
  "v_mfma_f32_16x16x32_bf16 v[12:15], v[196:199], a[156:159], v[12:15]\n"
  "s_waitcnt lgkmcnt(3)\n"
  "ds_read_b64_tr_b16 v[216:217], v227 offset:0\n"
  "v_accvgpr_write_b32 a5, 0\n"
  "v_mfma_f32_16x16x32_bf16 v[16:19], v[200:203], a[128:131], v[16:19]\n"
  "v_accvgpr_write_b32 a6, 0\n"
  "v_mfma_f32_16x16x32_bf16 v[16:19], v[204:207], a[132:135], v[16:19]\n"
  "buffer_store_dwordx4 v[180:183], v244, s[48:51], s59 offen offset:1024 sc1\n"
  "v_mfma_f32_16x16x32_bf16 v[20:23], v[200:203], a[136:139], v[20:23]\n"
  "s_add_u32 s61, s54, 1\n"
  "ds_read_b64_tr_b16 v[218:219], v227 offset:1024\n"
  "v_accvgpr_write_b32 a7, 0\n"
  "v_mfma_f32_16x16x32_bf16 v[20:23], v[204:207], a[140:143], v[20:23]\n"
  "s_and_b32 s61, s61, 7\n"
  "ds_read_b64_tr_b16 v[220:221], v227 offset:8192\n"
  "v_accvgpr_write_b32 a8, 0\n"
  "v_mfma_f32_16x16x32_bf16 v[24:27], v[200:203], a[144:147], v[24:27]\n"
  "s_lshl_b32 s61, s61, 11\n"
  "v_accvgpr_write_b32 a9, 0\n"
  "v_mfma_f32_16x16x32_bf16 v[24:27], v[204:207], a[148:151], v[24:27]\n"
  "s_add_u32 s61, s61, 0x3000000\n"
  "ds_read_b64_tr_b16 v[222:223], v227 offset:9216\n"
  "v_accvgpr_write_b32 a10, 0\n"
  "v_mfma_f32_16x16x32_bf16 v[28:31], v[200:203], a[152:155], v[28:31]\n"
  "s_lshl_b32 s56, s41, 14\n"
  "v_mfma_f32_16x16x32_bf16 v[28:31], v[204:207], a[156:159], v[28:31]\n"
  "s_waitcnt lgkmcnt(3)\n"
  "s_add_u32 s56, s56, s61\n"
  "buffer_load_dwordx4 a[232:235], v244, s[48:51], s56 offen offset:0 sc1\n"
  "v_mfma_f32_16x16x32_bf16 v[32:35], v[208:211], a[128:131], v[32:35]\n"
  "ds_read_b64_tr_b16 v[192:193], v228 offset:0\n"
  "v_accvgpr_write_b32 a11, 0\n"
  "v_mfma_f32_16x16x32_bf16 v[32:35], v[212:215], a[132:135], v[32:35]\n"
  "buffer_load_dwordx4 a[236:239], v244, s[48:51], s56 offen offset:1024 sc1\n"
  "v_mfma_f32_16x16x32_bf16 v[36:39], v[208:211], a[136:139], v[36:39]\n"
  "s_lshl_b32 s56, s42, 14\n"
  "ds_read_b64_tr_b16 v[194:195], v228 offset:1024\n"
  "v_accvgpr_write_b32 a12, 0\n"
  "v_mfma_f32_16x16x32_bf16 v[36:39], v[212:215], a[140:143], v[36:39]\n"
  "s_add_u32 s56, s56, s61\n"
  "ds_read_b64_tr_b16 v[196:197], v228 offset:8192\n"
  "v_accvgpr_write_b32 a13, 0\n"
  "v_mfma_f32_16x16x32_bf16 v[40:43], v[208:211], a[144:147], v[40:43]\n"
  "v_accvgpr_write_b32 a14, 0\n"
  "v_accvgpr_write_b32 a15, 0\n"
  "v_mfma_f32_16x16x32_bf16 v[40:43], v[212:215], a[148:151], v[40:43]\n"
  "buffer_load_dwordx4 a[240:243], v244, s[48:51], s56 offen offset:0 sc1\n"
  "v_mfma_f32_16x16x32_bf16 v[44:47], v[208:211], a[152:155], v[44:47]\n"
  "ds_read_b64_tr_b16 v[198:199], v228 offset:9216\n"
  "v_accvgpr_write_b32 a16, 0\n"
  "v_mfma_f32_16x16x32_bf16 v[44:47], v[212:215], a[156:159], v[44:47]\n"
  "s_waitcnt lgkmcnt(4)\n"
  "ds_read_b64_tr_b16 v[200:201], v229 offset:0\n"
  "v_accvgpr_write_b32 a17, 0\n"
  "v_mfma_f32_16x16x32_bf16 v[48:51], v[216:219], a[128:131], v[48:51]\n"
  "v_mfma_f32_16x16x32_bf16 v[48:51], v[220:223], a[132:135], v[48:51]\n"
  "buffer_load_dwordx4 a[244:247], v244, s[48:51], s56 offen offset:1024 sc1\n"
  "v_mfma_f32_16x16x32_bf16 v[52:55], v[216:219], a[136:139], v[52:55]\n"
  "s_lshl_b32 s56, s43, 14\n"
  "ds_read_b64_tr_b16 v[202:203], v229 offset:1024\n"
  "v_accvgpr_write_b32 a18, 0\n"
  "v_mfma_f32_16x16x32_bf16 v[52:55], v[220:223], a[140:143], v[52:55]\n"
  "s_add_u32 s56, s56, s61\n"
  "ds_read_b64_tr_b16 v[204:205], v229 offset:8192\n"
  "v_accvgpr_write_b32 a19, 0\n"
  "v_mfma_f32_16x16x32_bf16 v[56:59], v[216:219], a[144:147], v[56:59]\n"
  "v_accvgpr_write_b32 a20, 0\n"
  "v_mfma_f32_16x16x32_bf16 v[56:59], v[220:223], a[148:151], v[56:59]\n"
  "buffer_load_dwordx4 a[248:251], v244, s[48:51], s56 offen offset:0 sc1\n"
  "v_mfma_f32_16x16x32_bf16 v[60:63], v[216:219], a[152:155], v[60:63]\n"
  "ds_read_b64_tr_b16 v[206:207], v229 offset:9216\n"
  "v_accvgpr_write_b32 a21, 0\n"
  "v_mfma_f32_16x16x32_bf16 v[60:63], v[220:223], a[156:159], v[60:63]\n"
  "s_waitcnt lgkmcnt(3)\n"
  "ds_read_b64_tr_b16 v[208:209], v230 offset:0\n"
  "v_accvgpr_write_b32 a22, 0\n"
  "v_mfma_f32_16x16x32_bf16 v[64:67], v[192:195], a[128:131], v[64:67]\n"
  "v_accvgpr_write_b32 a23, 0\n"
  "v_mfma_f32_16x16x32_bf16 v[64:67], v[196:199], a[132:135], v[64:67]\n"
  "buffer_load_dwordx4 a[252:255], v244, s[48:51], s56 offen offset:1024 sc1\n"
  "v_mfma_f32_16x16x32_bf16 v[68:71], v[192:195], a[136:139], v[68:71]\n"
  "ds_read_b64_tr_b16 v[210:211], v230 offset:1024\n"
  "v_accvgpr_write_b32 a24, 0\n"
  "v_mfma_f32_16x16x32_bf16 v[68:71], v[196:199], a[140:143], v[68:71]\n"
  "ds_read_b64_tr_b16 v[212:213], v230 offset:8192\n"
  "v_accvgpr_write_b32 a25, 0\n"
  "v_mfma_f32_16x16x32_bf16 v[72:75], v[192:195], a[144:147], v[72:75]\n"
  "v_accvgpr_write_b32 a26, 0\n"
  "v_mfma_f32_16x16x32_bf16 v[72:75], v[196:199], a[148:151], v[72:75]\n"
  "ds_read_b64_tr_b16 v[214:215], v230 offset:9216\n"
  "v_accvgpr_write_b32 a27, 0\n"
  "v_mfma_f32_16x16x32_bf16 v[76:79], v[192:195], a[152:155], v[76:79]\n"
  "v_mfma_f32_16x16x32_bf16 v[76:79], v[196:199], a[156:159], v[76:79]\n"
  "s_waitcnt lgkmcnt(3)\n"
  "ds_read_b64_tr_b16 v[216:217], v231 offset:0\n"
  "v_accvgpr_write_b32 a28, 0\n"
  "v_mfma_f32_16x16x32_bf16 v[80:83], v[200:203], a[128:131], v[80:83]\n"
  "v_accvgpr_write_b32 a29, 0\n"
  "v_mfma_f32_16x16x32_bf16 v[80:83], v[204:207], a[132:135], v[80:83]\n"
  "ds_read_b64_tr_b16 v[218:219], v231 offset:1024\n"
  "v_accvgpr_write_b32 a30, 0\n"
  "v_mfma_f32_16x16x32_bf16 v[84:87], v[200:203], a[136:139], v[84:87]\n"
  "v_mfma_f32_16x16x32_bf16 v[84:87], v[204:207], a[140:143], v[84:87]\n"
  "ds_read_b64_tr_b16 v[220:221], v231 offset:8192\n"
  "v_accvgpr_write_b32 a31, 0\n"
  "v_mfma_f32_16x16x32_bf16 v[88:91], v[200:203], a[144:147], v[88:91]\n"
  "v_accvgpr_write_b32 a32, 0\n"
  "v_mfma_f32_16x16x32_bf16 v[88:91], v[204:207], a[148:151], v[88:91]\n"
  "ds_read_b64_tr_b16 v[222:223], v231 offset:9216\n"
  "v_mfma_f32_16x16x32_bf16 v[92:95], v[200:203], a[152:155], v[92:95]\n"
  "s_waitcnt vmcnt(21)\n"
  "v_accvgpr_write_b32 a33, 0\n"
  "v_mfma_f32_16x16x32_bf16 v[92:95], v[204:207], a[156:159], v[92:95]\n"
  "s_barrier\n"
  "ds_read_b64_tr_b16 v[192:193], v224 offset:16384\n"
  "v_accvgpr_write_b32 a34, 0\n"
  "s_waitcnt lgkmcnt(4)\n"
  "v_mfma_f32_16x16x32_bf16 v[96:99], v[208:211], a[128:131], v[96:99]\n"
  "s_add_u32 s54, s54, 1\n"
  "v_accvgpr_write_b32 a35, 0\n"
  "v_mfma_f32_16x16x32_bf16 v[96:99], v[212:215], a[132:135], v[96:99]\n"
  "s_and_b32 s54, s54, 7\n"
  "ds_read_b64_tr_b16 v[194:195], v224 offset:17408\n"
  "v_mfma_f32_16x16x32_bf16 v[100:103], v[208:211], a[136:139], v[100:103]\n"
  "s_lshl_b32 s55, s54, 19\n"
  "v_accvgpr_write_b32 a36, 0\n"
  "v_mfma_f32_16x16x32_bf16 v[100:103], v[212:215], a[140:143], v[100:103]\n"
  "s_add_u32 m0, s57, 0xc000\n"
  "ds_read_b64_tr_b16 v[196:197], v224 offset:24576\n"
  "v_accvgpr_write_b32 a37, 0\n"
  "v_mfma_f32_16x16x32_bf16 v[104:107], v[208:211], a[144:147], v[104:107]\n"
  "v_mfma_f32_16x16x32_bf16 v[104:107], v[212:215], a[148:151], v[104:107]\n"
  "buffer_load_dwordx4 v232, s[36:39], s55 offen offset:0 lds\n"
  "v_mfma_f32_16x16x32_bf16 v[108:111], v[208:211], a[152:155], v[108:111]\n"
  "ds_read_b64_tr_b16 v[198:199], v224 offset:25600\n"
  "v_accvgpr_write_b32 a38, 0\n"
  "v_mfma_f32_16x16x32_bf16 v[108:111], v[212:215], a[156:159], v[108:111]\n"
  "s_waitcnt lgkmcnt(3)\n"
  "ds_read_b64_tr_b16 v[200:201], v225 offset:16384\n"
  "v_accvgpr_write_b32 a39, 0\n"
  "v_mfma_f32_16x16x32_bf16 v[112:115], v[216:219], a[128:131], v[112:115]\n"
  "mk_tp5:\n"
  "s_cbranch_execz mk_tp9\n"
  "v_accvgpr_write_b32 a40, 0\n"
  "v_mfma_f32_16x16x32_bf16 v[112:115], v[220:223], a[132:135], v[112:115]\n"
  "buffer_load_dwordx4 v233, s[36:39], s55 offen offset:1024 lds\n"
  "v_mfma_f32_16x16x32_bf16 v[116:119], v[216:219], a[136:139], v[116:119]\n"
  "ds_read_b64_tr_b16 v[202:203], v225 offset:17408\n"
  "v_accvgpr_write_b32 a41, 0\n"
  "v_mfma_f32_16x16x32_bf16 v[116:119], v[220:223], a[140:143], v[116:119]\n"
  "ds_read_b64_tr_b16 v[204:205], v225 offset:24576\n"
  "v_accvgpr_write_b32 a42, 0\n"
  "v_mfma_f32_16x16x32_bf16 v[120:123], v[216:219], a[144:147], v[120:123]\n"
  "v_accvgpr_write_b32 a43, 0\n"
  "v_mfma_f32_16x16x32_bf16 v[120:123], v[220:223], a[148:151], v[120:123]\n"
  "buffer_load_dwordx4 v234, s[36:39], s55 offen offset:2048 lds\n"
  "v_mfma_f32_16x16x32_bf16 v[124:127], v[216:219], a[152:155], v[124:127]\n"
  "ds_read_b64_tr_b16 v[206:207], v225 offset:25600\n"
  "v_accvgpr_write_b32 a44, 0\n"
  "v_mfma_f32_16x16x32_bf16 v[124:127], v[220:223], a[156:159], v[124:127]\n"
  "s_waitcnt lgkmcnt(3)\n"
  "ds_read_b64_tr_b16 v[208:209], v226 offset:16384\n"
  "v_accvgpr_write_b32 a45, 0\n"
  "v_mfma_f32_16x16x32_bf16 v[0:3], v[192:195], a[160:163], v[0:3]\n"
  "v_accvgpr_write_b32 a46, 0\n"
  "v_mfma_f32_16x16x32_bf16 v[0:3], v[196:199], a[164:167], v[0:3]\n"
  "buffer_load_dwordx4 v235, s[36:39], s55 offen offset:3072 lds\n"
  "v_mfma_f32_16x16x32_bf16 v[4:7], v[192:195], a[168:171], v[4:7]\n"
  "ds_read_b64_tr_b16 v[210:211], v226 offset:17408\n"
  "v_cmp_ne_u32 vcc, v242, v253\n"
  "v_mfma_f32_16x16x32_bf16 v[4:7], v[196:199], a[172:175], v[4:7]\n"
  "ds_read_b64_tr_b16 v[212:213], v226 offset:24576\n"
  "v_max_u32 v243, v250, v251\n"
  "v_mfma_f32_16x16x32_bf16 v[8:11], v[192:195], a[176:179], v[8:11]\n"
  "v_mov_b32 v253, v242\n"
  "v_cndmask_b32 v243, v243, v251, vcc\n"
  "v_mfma_f32_16x16x32_bf16 v[8:11], v[196:199], a[180:183], v[8:11]\n"
  "ds_read_b64_tr_b16 v[214:215], v226 offset:25600\n"
  "v_accvgpr_write_b32 a47, 0\n"
  "v_mfma_f32_16x16x32_bf16 v[12:15], v[192:195], a[184:187], v[12:15]\n"
  "v_accvgpr_write_b32 a48, 0\n"
  "v_accvgpr_write_b32 a49, 0\n"
  "v_mfma_f32_16x16x32_bf16 v[12:15], v[196:199], a[188:191], v[12:15]\n"
  "s_waitcnt lgkmcnt(3)\n"
  "buffer_store_dword v253, v243, s[48:51], s96 offen sc1\n"
  "v_mfma_f32_16x16x32_bf16 v[16:19], v[200:203], a[160:163], v[16:19]\n"
  "s_add_u32 s59, s60, 0x7000\n"
  "ds_read_b64_tr_b16 v[216:217], v227 offset:16384\n"
  "v_accvgpr_write_b32 a50, 0\n"
  "v_mfma_f32_16x16x32_bf16 v[16:19], v[204:207], a[164:167], v[16:19]\n"
  "ds_read_b64_tr_b16 v[218:219], v227 offset:17408\n"
  "v_accvgpr_write_b32 a51, 0\n"
  "v_mfma_f32_16x16x32_bf16 v[20:23], v[200:203], a[168:171], v[20:23]\n"
  "v_accvgpr_write_b32 a52, 0\n"
  "v_accvgpr_write_b32 a53, 0\n"
  "v_mfma_f32_16x16x32_bf16 v[20:23], v[204:207], a[172:175], v[20:23]\n"
  "buffer_store_dwordx4 v[184:187], v244, s[48:51], s59 offen offset:2048 sc1\n"
  "v_mfma_f32_16x16x32_bf16 v[24:27], v[200:203], a[176:179], v[24:27]\n"
  "ds_read_b64_tr_b16 v[220:221], v227 offset:24576\n"
  "v_accvgpr_write_b32 a54, 0\n"
  "v_mfma_f32_16x16x32_bf16 v[24:27], v[204:207], a[180:183], v[24:27]\n"
  "ds_read_b64_tr_b16 v[222:223], v227 offset:25600\n"
  "v_accvgpr_write_b32 a55, 0\n"
  "v_mfma_f32_16x16x32_bf16 v[28:31], v[200:203], a[184:187], v[28:31]\n"
  "v_accvgpr_write_b32 a56, 0\n"
  "v_mfma_f32_16x16x32_bf16 v[28:31], v[204:207], a[188:191], v[28:31]\n"
  "s_waitcnt lgkmcnt(4)\n"
  "buffer_store_dwordx4 v[188:191], v244, s[48:51], s59 offen offset:3072 sc1\n"
  "v_mfma_f32_16x16x32_bf16 v[32:35], v[208:211], a[160:163], v[32:35]\n"
  "s_lshl_b32 s61, s53, 20\n"
  "ds_read_b64_tr_b16 v[192:193], v228 offset:16384\n"
  "v_accvgpr_write_b32 a57, 0\n"
  "v_mfma_f32_16x16x32_bf16 v[32:35], v[212:215], a[164:167], v[32:35]\n"
  "s_add_u32 s44, s48, s61\n"
  "s_addc_u32 s45, s49, 0\n"
  "ds_read_b64_tr_b16 v[194:195], v228 offset:17408\n"
  "v_accvgpr_write_b32 a58, 0\n"
  "v_mfma_f32_16x16x32_bf16 v[36:39], v[208:211], a[168:171], v[36:39]\n"
  "s_lshl_b32 s61, s52, 16\n"
  "v_accvgpr_write_b32 a59, 0\n"
  "v_mfma_f32_16x16x32_bf16 v[36:39], v[212:215], a[172:175], v[36:39]\n"
  "s_sub_u32 s46, 0x100000, s61\n"
  "ds_read_b64_tr_b16 v[196:197], v228 offset:24576\n"
  "v_accvgpr_write_b32 a60, 0\n"
  "v_mfma_f32_16x16x32_bf16 v[40:43], v[208:211], a[176:179], v[40:43]\n"
  "s_lshl_b32 s98, %1, 12\n"
  "v_mfma_f32_16x16x32_bf16 v[40:43], v[212:215], a[180:183], v[40:43]\n"
  "s_mov_b32 s96, s98\n"
  "ds_read_b64_tr_b16 v[198:199], v228 offset:25600\n"
  "v_accvgpr_write_b32 a61, 0\n"
  "v_mfma_f32_16x16x32_bf16 v[44:47], v[208:211], a[184:187], v[44:47]\n"
  "s_mov_b32 m0, s98\n"
  "v_accvgpr_write_b32 a62, 0\n"
  "v_mfma_f32_16x16x32_bf16 v[44:47], v[212:215], a[188:191], v[44:47]\n"
  "s_waitcnt lgkmcnt(4)\n"
  "ds_read_b64_tr_b16 v[200:201], v229 offset:16384\n"
  "v_mfma_f32_16x16x32_bf16 v[48:51], v[216:219], a[160:163], v[48:51]\n"
  "v_accvgpr_write_b32 a63, 0\n"
  "v_mfma_f32_16x16x32_bf16 v[48:51], v[220:223], a[164:167], v[48:51]\n"
  "buffer_load_dwordx4 v244, s[44:47], s96 offen offset:0 sc1 lds\n"
  "v_mfma_f32_16x16x32_bf16 v[52:55], v[216:219], a[168:171], v[52:55]\n"
  "ds_read_b64_tr_b16 v[202:203], v229 offset:17408\n"
  "v_accvgpr_write_b32 a64, 0\n"
  "v_mfma_f32_16x16x32_bf16 v[52:55], v[220:223], a[172:175], v[52:55]\n"
  "ds_read_b64_tr_b16 v[204:205], v229 offset:24576\n"
  "v_accvgpr_write_b32 a65, 0\n"
  "v_mfma_f32_16x16x32_bf16 v[56:59], v[216:219], a[176:179], v[56:59]\n"
  "v_accvgpr_write_b32 a66, 0\n"
  "v_mfma_f32_16x16x32_bf16 v[56:59], v[220:223], a[180:183], v[56:59]\n"
  "buffer_load_dwordx4 v244, s[44:47], s96 offen offset:1024 sc1 lds\n"
  "v_mfma_f32_16x16x32_bf16 v[60:63], v[216:219], a[184:187], v[60:63]\n"
  "ds_read_b64_tr_b16 v[206:207], v229 offset:25600\n"
  "v_accvgpr_write_b32 a67, 0\n"
  "v_mfma_f32_16x16x32_bf16 v[60:63], v[220:223], a[188:191], v[60:63]\n"
  "s_waitcnt lgkmcnt(3)\n"
  "ds_read_b64_tr_b16 v[208:209], v230 offset:16384\n"
  "v_accvgpr_write_b32 a68, 0\n"
  "v_mfma_f32_16x16x32_bf16 v[64:67], v[192:195], a[160:163], v[64:67]\n"
  "v_accvgpr_write_b32 a69, 0\n"
  "v_mfma_f32_16x16x32_bf16 v[64:67], v[196:199], a[164:167], v[64:67]\n"
  "buffer_load_dwordx4 v244, s[44:47], s96 offen offset:2048 sc1 lds\n"
  "v_mfma_f32_16x16x32_bf16 v[68:71], v[192:195], a[168:171], v[68:71]\n"
  "ds_read_b64_tr_b16 v[210:211], v230 offset:17408\n"
  "v_accvgpr_write_b32 a70, 0\n"
  "v_mfma_f32_16x16x32_bf16 v[68:71], v[196:199], a[172:175], v[68:71]\n"
  "ds_read_b64_tr_b16 v[212:213], v230 offset:24576\n"
  "v_accvgpr_write_b32 a71, 0\n"
  "v_mfma_f32_16x16x32_bf16 v[72:75], v[192:195], a[176:179], v[72:75]\n"
  "v_accvgpr_write_b32 a72, 0\n"
  "v_mfma_f32_16x16x32_bf16 v[72:75], v[196:199], a[180:183], v[72:75]\n"
  "buffer_load_dwordx4 v244, s[44:47], s96 offen offset:3072 sc1 lds\n"
  "v_mfma_f32_16x16x32_bf16 v[76:79], v[192:195], a[184:187], v[76:79]\n"
  "ds_read_b64_tr_b16 v[214:215], v230 offset:25600\n"
  "v_accvgpr_write_b32 a73, 0\n"
  "v_mfma_f32_16x16x32_bf16 v[76:79], v[196:199], a[188:191], v[76:79]\n"
  "s_waitcnt lgkmcnt(3)\n"
  "ds_read_b64_tr_b16 v[216:217], v231 offset:16384\n"
  "v_accvgpr_write_b32 a74, 0\n"
  "v_mfma_f32_16x16x32_bf16 v[80:83], v[200:203], a[160:163], v[80:83]\n"
  "v_accvgpr_write_b32 a75, 0\n"
  "v_mfma_f32_16x16x32_bf16 v[80:83], v[204:207], a[164:167], v[80:83]\n"
  "ds_read_b64_tr_b16 v[218:219], v231 offset:17408\n"
  "v_mfma_f32_16x16x32_bf16 v[84:87], v[200:203], a[168:171], v[84:87]\n"
  "v_accvgpr_write_b32 a76, 0\n"
  "v_mfma_f32_16x16x32_bf16 v[84:87], v[204:207], a[172:175], v[84:87]\n"
  "ds_read_b64_tr_b16 v[220:221], v231 offset:24576\n"
  "v_accvgpr_write_b32 a77, 0\n"
  "v_mfma_f32_16x16x32_bf16 v[88:91], v[200:203], a[176:179], v[88:91]\n"
  "v_mfma_f32_16x16x32_bf16 v[88:91], v[204:207], a[180:183], v[88:91]\n"
  "ds_read_b64_tr_b16 v[222:223], v231 offset:25600\n"
  "v_accvgpr_write_b32 a78, 0\n"
  "v_mfma_f32_16x16x32_bf16 v[92:95], v[200:203], a[184:187], v[92:95]\n"
  "s_waitcnt vmcnt(19)\n"
  "v_accvgpr_write_b32 a79, 0\n"
  "v_mfma_f32_16x16x32_bf16 v[92:95], v[204:207], a[188:191], v[92:95]\n"
  "s_barrier\n"
  "ds_read_b64_tr_b16 v[192:193], v224 offset:32768\n"
  "v_accvgpr_write_b32 a80, 0\n"
  "s_waitcnt lgkmcnt(4)\n"
  "v_mfma_f32_16x16x32_bf16 v[96:99], v[208:211], a[160:163], v[96:99]\n"
  "s_add_u32 s96, s96, 0x8000\n"
  "v_mfma_f32_16x16x32_bf16 v[96:99], v[212:215], a[164:167], v[96:99]\n"
  "s_add_u32 s98, s98, 0x8000\n"
  "ds_read_b64_tr_b16 v[194:195], v224 offset:33792\n"
  "v_accvgpr_write_b32 a81, 0\n"
  "v_mfma_f32_16x16x32_bf16 v[100:103], v[208:211], a[168:171], v[100:103]\n"
  "s_and_b32 s98, s98, 0x1ffff\n"
  "v_accvgpr_write_b32 a82, 0\n"
  "v_mfma_f32_16x16x32_bf16 v[100:103], v[212:215], a[172:175], v[100:103]\n"
  "s_mov_b32 m0, s98\n"
  "ds_read_b64_tr_b16 v[196:197], v224 offset:40960\n"
  "v_mfma_f32_16x16x32_bf16 v[104:107], v[208:211], a[176:179], v[104:107]\n"
  "v_accvgpr_write_b32 a83, 0\n"
  "v_mfma_f32_16x16x32_bf16 v[104:107], v[212:215], a[180:183], v[104:107]\n"
  "buffer_load_dwordx4 v244, s[44:47], s96 offen offset:0 sc1 lds\n"
  "v_mfma_f32_16x16x32_bf16 v[108:111], v[208:211], a[184:187], v[108:111]\n"
  "ds_read_b64_tr_b16 v[198:199], v224 offset:41984\n"
  "v_accvgpr_write_b32 a84, 0\n"
  "v_mfma_f32_16x16x32_bf16 v[108:111], v[212:215], a[188:191], v[108:111]\n"
  "s_waitcnt lgkmcnt(3)\n"
  "ds_read_b64_tr_b16 v[200:201], v225 offset:32768\n"
  "v_accvgpr_write_b32 a85, 0\n"
  "v_mfma_f32_16x16x32_bf16 v[112:115], v[216:219], a[160:163], v[112:115]\n"
  "v_accvgpr_write_b32 a86, 0\n"
  "v_mfma_f32_16x16x32_bf16 v[112:115], v[220:223], a[164:167], v[112:115]\n"
  "buffer_load_dwordx4 v244, s[44:47], s96 offen offset:1024 sc1 lds\n"
  "v_mfma_f32_16x16x32_bf16 v[116:119], v[216:219], a[168:171], v[116:119]\n"
  "ds_read_b64_tr_b16 v[202:203], v225 offset:33792\n"
  "v_accvgpr_write_b32 a87, 0\n"
  "v_mfma_f32_16x16x32_bf16 v[116:119], v[220:223], a[172:175], v[116:119]\n"
  "ds_read_b64_tr_b16 v[204:205], v225 offset:40960\n"
  "v_accvgpr_write_b32 a88, 0\n"
  "v_mfma_f32_16x16x32_bf16 v[120:123], v[216:219], a[176:179], v[120:123]\n"
  "v_accvgpr_write_b32 a89, 0\n"
  "v_mfma_f32_16x16x32_bf16 v[120:123], v[220:223], a[180:183], v[120:123]\n"
  "buffer_load_dwordx4 v244, s[44:47], s96 offen offset:2048 sc1 lds\n"
  "v_mfma_f32_16x16x32_bf16 v[124:127], v[216:219], a[184:187], v[124:127]\n"
  "ds_read_b64_tr_b16 v[206:207], v225 offset:41984\n"
  "v_accvgpr_write_b32 a90, 0\n"
  "v_mfma_f32_16x16x32_bf16 v[124:127], v[220:223], a[188:191], v[124:127]\n"
  "s_waitcnt lgkmcnt(3)\n"
  "ds_read_b64_tr_b16 v[208:209], v226 offset:32768\n"
  "v_accvgpr_write_b32 a91, 0\n"
  "v_mfma_f32_16x16x32_bf16 v[0:3], v[192:195], a[192:195], v[0:3]\n"
  "s_lshl_b32 s62, s52, 5\n"
  "v_accvgpr_write_b32 a92, 0\n"
  "v_mfma_f32_16x16x32_bf16 v[0:3], v[196:199], a[196:199], v[0:3]\n"
  "s_add_u32 s62, s62, s53\n"
  "buffer_load_dwordx4 v244, s[44:47], s96 offen offset:3072 sc1 lds\n"
  "v_mfma_f32_16x16x32_bf16 v[4:7], v[192:195], a[200:203], v[4:7]\n"
  "s_lshl_b32 s62, s62, 2\n"
  "ds_read_b64_tr_b16 v[210:211], v226 offset:33792\n"
  "v_accvgpr_write_b32 a93, 0\n"
  "v_mfma_f32_16x16x32_bf16 v[4:7], v[196:199], a[204:207], v[4:7]\n"
  "s_add_u32 s62, s62, %1\n"
  "ds_read_b64_tr_b16 v[212:213], v226 offset:40960\n"
  "v_accvgpr_write_b32 a94, 0\n"
  "v_mfma_f32_16x16x32_bf16 v[8:11], v[192:195], a[208:211], v[8:11]\n"
  "s_add_u32 s96, s96, 0x8000\n"
  "v_accvgpr_write_b32 a95, 0\n"
  "v_mfma_f32_16x16x32_bf16 v[8:11], v[196:199], a[212:215], v[8:11]\n"
  "s_add_u32 s98, s98, 0x8000\n"
  "ds_read_b64_tr_b16 v[214:215], v226 offset:41984\n"
  "v_mfma_f32_16x16x32_bf16 v[12:15], v[192:195], a[216:219], v[12:15]\n"
  "s_and_b32 s98, s98, 0x1ffff\n"
  "v_accvgpr_write_b32 a96, 0\n"
  "v_mfma_f32_16x16x32_bf16 v[12:15], v[196:199], a[220:223], v[12:15]\n"
  "s_waitcnt lgkmcnt(3)\n"
  "s_mov_b32 m0, s98\n"
  "ds_read_b64_tr_b16 v[216:217], v227 offset:32768\n"
  "v_accvgpr_write_b32 a97, 0\n"
  "v_mfma_f32_16x16x32_bf16 v[16:19], v[200:203], a[192:195], v[16:19]\n"
  "s_lshl_b32 s62, s62, 13\n"
  "v_mfma_f32_16x16x32_bf16 v[16:19], v[204:207], a[196:199], v[16:19]\n"
  "s_sub_u32 s63, s62, 0x100000\n"
  "ds_read_b64_tr_b16 v[218:219], v227 offset:33792\n"
  "v_accvgpr_write_b32 a98, 0\n"
  "v_mfma_f32_16x16x32_bf16 v[20:23], v[200:203], a[200:203], v[20:23]\n"
  "s_add_u32 s36, s20, s63\n"
  "s_addc_u32 s37, s21, 0\n"
  "v_accvgpr_write_b32 a99, 0\n"
  "v_mfma_f32_16x16x32_bf16 v[20:23], v[204:207], a[204:207], v[20:23]\n"
  "s_sub_u32 s63, s62, 0x200000\n"
  "buffer_load_dwordx4 v244, s[44:47], s96 offen offset:0 sc1 lds\n"
  "v_mfma_f32_16x16x32_bf16 v[24:27], v[200:203], a[208:211], v[24:27]\n"
  "s_add_u32 s10, s24, s63\n"
  "s_addc_u32 s11, s25, 0\n"
  "ds_read_b64_tr_b16 v[220:221], v227 offset:40960\n"
  "v_accvgpr_write_b32 a100, 0\n"
  "v_mfma_f32_16x16x32_bf16 v[24:27], v[204:207], a[212:215], v[24:27]\n"
  "s_add_u32 s63, s62, 0x300000\n"
  "ds_read_b64_tr_b16 v[222:223], v227 offset:41984\n"
  "v_accvgpr_write_b32 a101, 0\n"
  "v_mfma_f32_16x16x32_bf16 v[28:31], v[200:203], a[216:219], v[28:31]\n"
  "s_add_u32 s12, s24, s63\n"
  "s_addc_u32 s13, s25, 0\n"
  "v_accvgpr_write_b32 a102, 0\n"
  "v_mfma_f32_16x16x32_bf16 v[28:31], v[204:207], a[220:223], v[28:31]\n"
  "s_waitcnt lgkmcnt(3)\n"
  "s_sub_u32 s63, s62, 0x400000\n"
  "buffer_load_dwordx4 v244, s[44:47], s96 offen offset:1024 sc1 lds\n"
  "v_mfma_f32_16x16x32_bf16 v[32:35], v[208:211], a[192:195], v[32:35]\n"
  "s_add_u32 s14, s18, s63\n"
  "s_addc_u32 s15, s19, 0\n"
  "ds_read_b64_tr_b16 v[192:193], v228 offset:32768\n"
  "v_accvgpr_write_b32 a103, 0\n"
  "v_mfma_f32_16x16x32_bf16 v[32:35], v[212:215], a[196:199], v[32:35]\n"
  "s_cmp_eq_u32 s52, 3\n"
  "s_cselect_b64 s[12:13], s[12:13], s[14:15]\n"
  "ds_read_b64_tr_b16 v[194:195], v228 offset:33792\n"
  "v_accvgpr_write_b32 a104, 0\n"
  "v_mfma_f32_16x16x32_bf16 v[36:39], v[208:211], a[200:203], v[36:39]\n"
  "s_mov_b32 s38, 0x2000\n"
  "v_accvgpr_write_b32 a105, 0\n"
  "v_mfma_f32_16x16x32_bf16 v[36:39], v[212:215], a[204:207], v[36:39]\n"
  "s_mov_b32 s14, 0xe000\n"
  "buffer_load_dwordx4 v244, s[44:47], s96 offen offset:2048 sc1 lds\n"
  "v_mfma_f32_16x16x32_bf16 v[40:43], v[208:211], a[208:211], v[40:43]\n"
  "s_mov_b32 s62, s52\n"
  "ds_read_b64_tr_b16 v[196:197], v228 offset:40960\n"
  "v_accvgpr_write_b32 a106, 0\n"
  "v_mfma_f32_16x16x32_bf16 v[40:43], v[212:215], a[212:215], v[40:43]\n"
  "s_cmp_eq_u32 s52, 0\n"
  "s_cselect_b32 s63, 1.0, -1.0\n"
  "ds_read_b64_tr_b16 v[198:199], v228 offset:41984\n"
  "v_accvgpr_write_b32 a107, 0\n"
  "v_mfma_f32_16x16x32_bf16 v[44:47], v[208:211], a[216:219], v[44:47]\n"
  "v_mfma_f32_16x16x32_bf16 v[44:47], v[212:215], a[220:223], v[44:47]\n"
  "s_waitcnt lgkmcnt(4)\n"
  "buffer_load_dwordx4 v244, s[44:47], s96 offen offset:3072 sc1 lds\n"
  "v_mfma_f32_16x16x32_bf16 v[48:51], v[216:219], a[192:195], v[48:51]\n"
  "ds_read_b64_tr_b16 v[200:201], v229 offset:32768\n"
  "v_accvgpr_write_b32 a108, 0\n"
  "v_mfma_f32_16x16x32_bf16 v[48:51], v[220:223], a[196:199], v[48:51]\n"
  "ds_read_b64_tr_b16 v[202:203], v229 offset:33792\n"
  "v_accvgpr_write_b32 a109, 0\n"
  "v_mfma_f32_16x16x32_bf16 v[52:55], v[216:219], a[200:203], v[52:55]\n"
  "v_accvgpr_write_b32 a110, 0\n"
  "v_mfma_f32_16x16x32_bf16 v[52:55], v[220:223], a[204:207], v[52:55]\n"
  "ds_read_b64_tr_b16 v[204:205], v229 offset:40960\n"
  "v_accvgpr_write_b32 a111, 0\n"
  "v_mfma_f32_16x16x32_bf16 v[56:59], v[216:219], a[208:211], v[56:59]\n"
  "v_accvgpr_write_b32 a112, 0\n"
  "v_mfma_f32_16x16x32_bf16 v[56:59], v[220:223], a[212:215], v[56:59]\n"
  "ds_read_b64_tr_b16 v[206:207], v229 offset:41984\n"
  "v_mfma_f32_16x16x32_bf16 v[60:63], v[216:219], a[216:219], v[60:63]\n"
  "v_accvgpr_write_b32 a113, 0\n"
  "v_mfma_f32_16x16x32_bf16 v[60:63], v[220:223], a[220:223], v[60:63]\n"
  "s_waitcnt lgkmcnt(4)\n"
  "ds_read_b64_tr_b16 v[208:209], v230 offset:32768\n"
  "v_accvgpr_write_b32 a114, 0\n"
  "v_mfma_f32_16x16x32_bf16 v[64:67], v[192:195], a[192:195], v[64:67]\n"
  "v_accvgpr_write_b32 a115, 0\n"
  "v_mfma_f32_16x16x32_bf16 v[64:67], v[196:199], a[196:199], v[64:67]\n"
  "ds_read_b64_tr_b16 v[210:211], v230 offset:33792\n"
  "v_mfma_f32_16x16x32_bf16 v[68:71], v[192:195], a[200:203], v[68:71]\n"
  "v_accvgpr_write_b32 a116, 0\n"
  "v_mfma_f32_16x16x32_bf16 v[68:71], v[196:199], a[204:207], v[68:71]\n"
  "ds_read_b64_tr_b16 v[212:213], v230 offset:40960\n"
  "v_accvgpr_write_b32 a117, 0\n"
  "v_mfma_f32_16x16x32_bf16 v[72:75], v[192:195], a[208:211], v[72:75]\n"
  "v_mfma_f32_16x16x32_bf16 v[72:75], v[196:199], a[212:215], v[72:75]\n"
  "ds_read_b64_tr_b16 v[214:215], v230 offset:41984\n"
  "v_accvgpr_write_b32 a118, 0\n"
  "v_mfma_f32_16x16x32_bf16 v[76:79], v[192:195], a[216:219], v[76:79]\n"
  "v_accvgpr_write_b32 a119, 0\n"
  "v_mfma_f32_16x16x32_bf16 v[76:79], v[196:199], a[220:223], v[76:79]\n"
  "s_waitcnt lgkmcnt(3)\n"
  "ds_read_b64_tr_b16 v[216:217], v231 offset:32768\n"
  "v_accvgpr_write_b32 a120, 0\n"
  "v_mfma_f32_16x16x32_bf16 v[80:83], v[200:203], a[192:195], v[80:83]\n"
  "v_mfma_f32_16x16x32_bf16 v[80:83], v[204:207], a[196:199], v[80:83]\n"
  "ds_read_b64_tr_b16 v[218:219], v231 offset:33792\n"
  "v_accvgpr_write_b32 a121, 0\n"
  "v_mfma_f32_16x16x32_bf16 v[84:87], v[200:203], a[200:203], v[84:87]\n"
  "v_accvgpr_write_b32 a122, 0\n"
  "v_mfma_f32_16x16x32_bf16 v[84:87], v[204:207], a[204:207], v[84:87]\n"
  "ds_read_b64_tr_b16 v[220:221], v231 offset:40960\n"
  "v_mfma_f32_16x16x32_bf16 v[88:91], v[200:203], a[208:211], v[88:91]\n"
  "v_accvgpr_write_b32 a123, 0\n"
  "v_mfma_f32_16x16x32_bf16 v[88:91], v[204:207], a[212:215], v[88:91]\n"
  "ds_read_b64_tr_b16 v[222:223], v231 offset:41984\n"
  "v_accvgpr_write_b32 a124, 0\n"
  "v_mfma_f32_16x16x32_bf16 v[92:95], v[200:203], a[216:219], v[92:95]\n"
  "s_waitcnt vmcnt(12)\n"
  "v_cmp_eq_u32 vcc, 0, v244\n"
  "v_accvgpr_write_b32 a125, 0\n"
  "v_mfma_f32_16x16x32_bf16 v[92:95], v[204:207], a[220:223], v[92:95]\n"
  "s_barrier\n"
  "ds_read_b64_tr_b16 v[192:193], v224 offset:49152\n"
  "v_mov_b32 v249, 0x7fff0000\n"
  "s_waitcnt lgkmcnt(4)\n"
  "v_mfma_f32_16x16x32_bf16 v[96:99], v[208:211], a[192:195], v[96:99]\n"
  "v_mov_b32 v248, 1\n"
  "v_cndmask_b32 v249, v249, v244, vcc\n"
  "v_mfma_f32_16x16x32_bf16 v[96:99], v[212:215], a[196:199], v[96:99]\n"
  "ds_read_b64_tr_b16 v[194:195], v224 offset:50176\n"
  "v_accvgpr_write_b32 a126, 0\n"
  "v_mfma_f32_16x16x32_bf16 v[100:103], v[208:211], a[200:203], v[100:103]\n"
  "v_accvgpr_write_b32 a127, 0\n"
  "v_mfma_f32_16x16x32_bf16 v[100:103], v[212:215], a[204:207], v[100:103]\n"
  "ds_read_b64_tr_b16 v[196:197], v224 offset:57344\n"
  "v_mfma_f32_16x16x32_bf16 v[104:107], v[208:211], a[208:211], v[104:107]\n"
  "v_mfma_f32_16x16x32_bf16 v[104:107], v[212:215], a[212:215], v[104:107]\n"
  "buffer_atomic_add v248, v249, s[48:51], s58 offen sc1\n"
  "v_mfma_f32_16x16x32_bf16 v[108:111], v[208:211], a[216:219], v[108:111]\n"
  "ds_read_b64_tr_b16 v[198:199], v224 offset:58368\n"
  "v_mov_b32 v253, s35\n"
  "v_mfma_f32_16x16x32_bf16 v[108:111], v[212:215], a[220:223], v[108:111]\n"
  "s_waitcnt lgkmcnt(3)\n"
  "ds_read_b64_tr_b16 v[200:201], v225 offset:49152\n"
  "v_mfma_f32_16x16x32_bf16 v[112:115], v[216:219], a[192:195], v[112:115]\n"
  "v_mfma_f32_16x16x32_bf16 v[112:115], v[220:223], a[196:199], v[112:115]\n"
  "buffer_atomic_add v253, v249, s[48:51], s33 offen sc1\n"
  "v_mfma_f32_16x16x32_bf16 v[116:119], v[216:219], a[200:203], v[116:119]\n"
  "ds_read_b64_tr_b16 v[202:203], v225 offset:50176\n"
  "v_mfma_f32_16x16x32_bf16 v[116:119], v[220:223], a[204:207], v[116:119]\n"
  "ds_read_b64_tr_b16 v[204:205], v225 offset:57344\n"
  "v_mfma_f32_16x16x32_bf16 v[120:123], v[216:219], a[208:211], v[120:123]\n"
  "v_mfma_f32_16x16x32_bf16 v[120:123], v[220:223], a[212:215], v[120:123]\n"
  "ds_read_b64_tr_b16 v[206:207], v225 offset:58368\n"
  "v_mov_b32 v240, 0x3f803f80\n"
  "v_mfma_f32_16x16x32_bf16 v[124:127], v[216:219], a[216:219], v[124:127]\n"
  "v_mov_b32 v241, 0x3f803f80\n"
  "v_mov_b32 v242, 0x3f803f80\n"
  "v_mfma_f32_16x16x32_bf16 v[124:127], v[220:223], a[220:223], v[124:127]\n"
  "s_waitcnt lgkmcnt(3)\n"
  "ds_read_b64_tr_b16 v[208:209], v226 offset:49152\n"
  "v_mov_b32 v243, 0x3f803f80\n"
  "v_mfma_f32_16x16x32_bf16 v[0:3], v[192:195], a[224:227], v[0:3]\n"
  "v_lshlrev_b32 v251, 3, v244\n"
  "v_mul_f32 v250, s63, v246\n"
  "v_mfma_f32_16x16x32_bf16 v[0:3], v[196:199], a[228:231], v[0:3]\n"
  "ds_read_b64_tr_b16 v[210:211], v226 offset:50176\n"
  "v_mfma_f32_16x16x32_bf16 v[4:7], v[192:195], a[232:235], v[4:7]\n"
  "v_mfma_f32_16x16x32_bf16 v[4:7], v[196:199], a[236:239], v[4:7]\n"
  "ds_read_b64_tr_b16 v[212:213], v226 offset:57344\n"
  "v_mfma_f32_16x16x32_bf16 v[8:11], v[192:195], a[240:243], v[8:11]\n"
  "v_mfma_f32_16x16x32_bf16 v[8:11], v[196:199], a[244:247], v[8:11]\n"
  "ds_read_b64_tr_b16 v[214:215], v226 offset:58368\n"
  "v_cvt_pk_bf16_f32 v128, v0, v1\n"
  "v_mfma_f32_16x16x32_bf16 v[12:15], v[192:195], a[248:251], v[12:15]\n"
  "v_cvt_pk_bf16_f32 v129, v2, v3\n"
  "v_mfma_f32_16x16x32_bf16 v[12:15], v[196:199], a[252:255], v[12:15]\n"
  "s_waitcnt lgkmcnt(3)\n"
  "ds_read_b64_tr_b16 v[216:217], v227 offset:49152\n"
  "v_cvt_pk_bf16_f32 v144, v4, v5\n"
  "v_mfma_f32_16x16x32_bf16 v[16:19], v[200:203], a[224:227], v[16:19]\n"
  "v_cvt_pk_bf16_f32 v145, v6, v7\n"
  "v_mfma_f32_16x16x32_bf16 v[16:19], v[204:207], a[228:231], v[16:19]\n"
  "ds_read_b64_tr_b16 v[218:219], v227 offset:50176\n"
  "v_cvt_pk_bf16_f32 v160, v8, v9\n"
  "v_mfma_f32_16x16x32_bf16 v[20:23], v[200:203], a[232:235], v[20:23]\n"
  "v_cvt_pk_bf16_f32 v161, v10, v11\n"
  "v_mfma_f32_16x16x32_bf16 v[20:23], v[204:207], a[236:239], v[20:23]\n"
  "ds_read_b64_tr_b16 v[220:221], v227 offset:57344\n"
  "v_cvt_pk_bf16_f32 v176, v12, v13\n"
  "v_mfma_f32_16x16x32_bf16 v[24:27], v[200:203], a[240:243], v[24:27]\n"
  "v_cvt_pk_bf16_f32 v177, v14, v15\n"
  "v_mfma_f32_16x16x32_bf16 v[24:27], v[204:207], a[244:247], v[24:27]\n"
  "ds_read_b64_tr_b16 v[222:223], v227 offset:58368\n"
  "v_cvt_pk_bf16_f32 v130, v16, v17\n"
  "v_mfma_f32_16x16x32_bf16 v[28:31], v[200:203], a[248:251], v[28:31]\n"
  "v_cvt_pk_bf16_f32 v131, v18, v19\n"
  "v_mfma_f32_16x16x32_bf16 v[28:31], v[204:207], a[252:255], v[28:31]\n"
  "s_waitcnt lgkmcnt(3)\n"
  "ds_read_b64_tr_b16 v[192:193], v228 offset:49152\n"
  "v_cvt_pk_bf16_f32 v146, v20, v21\n"
  "v_mfma_f32_16x16x32_bf16 v[32:35], v[208:211], a[224:227], v[32:35]\n"
  "v_cvt_pk_bf16_f32 v147, v22, v23\n"
  "v_mfma_f32_16x16x32_bf16 v[32:35], v[212:215], a[228:231], v[32:35]\n"
  "ds_read_b64_tr_b16 v[194:195], v228 offset:50176\n"
  "v_cvt_pk_bf16_f32 v162, v24, v25\n"
  "v_mfma_f32_16x16x32_bf16 v[36:39], v[208:211], a[232:235], v[36:39]\n"
  "v_cvt_pk_bf16_f32 v163, v26, v27\n"
  "v_mfma_f32_16x16x32_bf16 v[36:39], v[212:215], a[236:239], v[36:39]\n"
  "ds_read_b64_tr_b16 v[196:197], v228 offset:57344\n"
  "v_cvt_pk_bf16_f32 v178, v28, v29\n"
  "v_mfma_f32_16x16x32_bf16 v[40:43], v[208:211], a[240:243], v[40:43]\n"
  "v_cvt_pk_bf16_f32 v179, v30, v31\n"
  "v_mfma_f32_16x16x32_bf16 v[40:43], v[212:215], a[244:247], v[40:43]\n"
  "ds_read_b64_tr_b16 v[198:199], v228 offset:58368\n"
  "v_cvt_pk_bf16_f32 v132, v32, v33\n"
  "v_mfma_f32_16x16x32_bf16 v[44:47], v[208:211], a[248:251], v[44:47]\n"
  "v_cvt_pk_bf16_f32 v133, v34, v35\n"
  "v_mfma_f32_16x16x32_bf16 v[44:47], v[212:215], a[252:255], v[44:47]\n"
  "s_waitcnt lgkmcnt(3)\n"
  "ds_read_b64_tr_b16 v[200:201], v229 offset:49152\n"
  "v_cvt_pk_bf16_f32 v148, v36, v37\n"
  "v_mfma_f32_16x16x32_bf16 v[48:51], v[216:219], a[224:227], v[48:51]\n"
  "v_cvt_pk_bf16_f32 v149, v38, v39\n"
  "v_mfma_f32_16x16x32_bf16 v[48:51], v[220:223], a[228:231], v[48:51]\n"
  "ds_read_b64_tr_b16 v[202:203], v229 offset:50176\n"
  "v_cvt_pk_bf16_f32 v164, v40, v41\n"
  "v_mfma_f32_16x16x32_bf16 v[52:55], v[216:219], a[232:235], v[52:55]\n"
  "v_cvt_pk_bf16_f32 v165, v42, v43\n"
  "v_mfma_f32_16x16x32_bf16 v[52:55], v[220:223], a[236:239], v[52:55]\n"
  "ds_read_b64_tr_b16 v[204:205], v229 offset:57344\n"
  "v_cvt_pk_bf16_f32 v180, v44, v45\n"
  "v_mfma_f32_16x16x32_bf16 v[56:59], v[216:219], a[240:243], v[56:59]\n"
  "v_cvt_pk_bf16_f32 v181, v46, v47\n"
  "v_mfma_f32_16x16x32_bf16 v[56:59], v[220:223], a[244:247], v[56:59]\n"
  "ds_read_b64_tr_b16 v[206:207], v229 offset:58368\n"
  "v_cvt_pk_bf16_f32 v134, v48, v49\n"
  "v_mfma_f32_16x16x32_bf16 v[60:63], v[216:219], a[248:251], v[60:63]\n"
  "v_cvt_pk_bf16_f32 v135, v50, v51\n"
  "v_mfma_f32_16x16x32_bf16 v[60:63], v[220:223], a[252:255], v[60:63]\n"
  "s_waitcnt lgkmcnt(3)\n"
  "ds_read_b64_tr_b16 v[208:209], v230 offset:49152\n"
  "v_cvt_pk_bf16_f32 v150, v52, v53\n"
  "v_mfma_f32_16x16x32_bf16 v[64:67], v[192:195], a[224:227], v[64:67]\n"
  "v_cvt_pk_bf16_f32 v151, v54, v55\n"
  "v_mfma_f32_16x16x32_bf16 v[64:67], v[196:199], a[228:231], v[64:67]\n"
  "ds_read_b64_tr_b16 v[210:211], v230 offset:50176\n"
  "v_cvt_pk_bf16_f32 v166, v56, v57\n"
  "v_mfma_f32_16x16x32_bf16 v[68:71], v[192:195], a[232:235], v[68:71]\n"
  "v_cvt_pk_bf16_f32 v167, v58, v59\n"
  "v_mfma_f32_16x16x32_bf16 v[68:71], v[196:199], a[236:239], v[68:71]\n"
  "ds_read_b64_tr_b16 v[212:213], v230 offset:57344\n"
  "v_cvt_pk_bf16_f32 v182, v60, v61\n"
  "v_mfma_f32_16x16x32_bf16 v[72:75], v[192:195], a[240:243], v[72:75]\n"
  "v_cvt_pk_bf16_f32 v183, v62, v63\n"
  "v_mfma_f32_16x16x32_bf16 v[72:75], v[196:199], a[244:247], v[72:75]\n"
  "ds_read_b64_tr_b16 v[214:215], v230 offset:58368\n"
  "v_cvt_pk_bf16_f32 v136, v64, v65\n"
  "v_mfma_f32_16x16x32_bf16 v[76:79], v[192:195], a[248:251], v[76:79]\n"
  "v_cvt_pk_bf16_f32 v137, v66, v67\n"
  "v_mfma_f32_16x16x32_bf16 v[76:79], v[196:199], a[252:255], v[76:79]\n"
  "s_waitcnt lgkmcnt(3)\n"
  "ds_read_b64_tr_b16 v[216:217], v231 offset:49152\n"
  "v_cvt_pk_bf16_f32 v152, v68, v69\n"
  "v_mfma_f32_16x16x32_bf16 v[80:83], v[200:203], a[224:227], v[80:83]\n"
  "v_cvt_pk_bf16_f32 v153, v70, v71\n"
  "v_mfma_f32_16x16x32_bf16 v[80:83], v[204:207], a[228:231], v[80:83]\n"
  "ds_read_b64_tr_b16 v[218:219], v231 offset:50176\n"
  "v_cvt_pk_bf16_f32 v168, v72, v73\n"
  "v_mfma_f32_16x16x32_bf16 v[84:87], v[200:203], a[232:235], v[84:87]\n"
  "v_cvt_pk_bf16_f32 v169, v74, v75\n"
  "v_mfma_f32_16x16x32_bf16 v[84:87], v[204:207], a[236:239], v[84:87]\n"
  "ds_read_b64_tr_b16 v[220:221], v231 offset:57344\n"
  "v_mov_b32 v252, 0\n"
  "v_mfma_f32_16x16x32_bf16 v[88:91], v[200:203], a[240:243], v[88:91]\n"
  "v_cvt_pk_bf16_f32 v184, v76, v77\n"
  "v_cvt_pk_bf16_f32 v185, v78, v79\n"
  "v_mfma_f32_16x16x32_bf16 v[88:91], v[204:207], a[244:247], v[88:91]\n"
  "ds_read_b64_tr_b16 v[222:223], v231 offset:58368\n"
  "v_cvt_pk_bf16_f32 v138, v80, v81\n"
  "v_mfma_f32_16x16x32_bf16 v[92:95], v[200:203], a[248:251], v[92:95]\n"
  "s_waitcnt vmcnt(6)\n"
  "v_cvt_pk_bf16_f32 v139, v82, v83\n"
  "v_mfma_f32_16x16x32_bf16 v[92:95], v[204:207], a[252:255], v[92:95]\n"
  "s_barrier\n"
  "ds_read_b128 a[128:131], v245 offset:0\n"
  "ds_read_b128 a[132:135], v245 offset:1024\n"
  "s_waitcnt lgkmcnt(5)\n"
  "v_mfma_f32_16x16x32_bf16 v[96:99], v[208:211], a[224:227], v[96:99]\n"
  "ds_read_b128 a[136:139], v245 offset:2048\n"
  "ds_read_b128 a[140:143], v245 offset:3072\n"
  "v_mfma_f32_16x16x32_bf16 v[96:99], v[212:215], a[228:231], v[96:99]\n"
  "ds_read_b128 a[144:147], v245 offset:4096\n"
  "ds_read_b128 a[148:151], v245 offset:5120\n"
  "v_mfma_f32_16x16x32_bf16 v[100:103], v[208:211], a[232:235], v[100:103]\n"
  "buffer_atomic_add v252, v249, s[48:51], s58 offen sc0 sc1\n"
  "v_mfma_f32_16x16x32_bf16 v[100:103], v[212:215], a[236:239], v[100:103]\n"
  "ds_read_b128 a[152:155], v245 offset:6144\n"
  "ds_read_b128 a[156:159], v245 offset:7168\n"
  "v_mfma_f32_16x16x32_bf16 v[104:107], v[208:211], a[240:243], v[104:107]\n"
  "ds_read_b128 a[160:163], v245 offset:8192\n"
  "v_cvt_pk_bf16_f32 v154, v84, v85\n"
  "v_mfma_f32_16x16x32_bf16 v[104:107], v[212:215], a[244:247], v[104:107]\n"
  "ds_read_b128 a[164:167], v245 offset:9216\n"
  "v_cvt_pk_bf16_f32 v155, v86, v87\n"
  "v_mfma_f32_16x16x32_bf16 v[108:111], v[208:211], a[248:251], v[108:111]\n"
  "ds_read_b128 a[168:171], v245 offset:10240\n"
  "v_cvt_pk_bf16_f32 v170, v88, v89\n"
  "v_mfma_f32_16x16x32_bf16 v[108:111], v[212:215], a[252:255], v[108:111]\n"
  "s_waitcnt lgkmcnt(9)\n"
  "ds_read_b128 a[172:175], v245 offset:11264\n"
  "v_cvt_pk_bf16_f32 v171, v90, v91\n"
  "v_mfma_f32_16x16x32_bf16 v[112:115], v[216:219], a[224:227], v[112:115]\n"
  "ds_read_b128 a[176:179], v245 offset:12288\n"
  "v_cvt_pk_bf16_f32 v186, v92, v93\n"
  "v_mfma_f32_16x16x32_bf16 v[112:115], v[220:223], a[228:231], v[112:115]\n"
  "ds_read_b128 a[180:183], v245 offset:13312\n"
  "v_cvt_pk_bf16_f32 v187, v94, v95\n"
  "v_mfma_f32_16x16x32_bf16 v[116:119], v[216:219], a[232:235], v[116:119]\n"
  "ds_read_b128 a[184:187], v245 offset:14336\n"
  "v_cvt_pk_bf16_f32 v140, v96, v97\n"
  "v_mfma_f32_16x16x32_bf16 v[116:119], v[220:223], a[236:239], v[116:119]\n"
  "mk_tp6:\n"
  "s_cbranch_execz mk_tp10\n"
  "ds_read_b128 a[188:191], v245 offset:15360\n"
  "v_cvt_pk_bf16_f32 v141, v98, v99\n"
  "v_mfma_f32_16x16x32_bf16 v[120:123], v[216:219], a[240:243], v[120:123]\n"
  "v_cvt_pk_bf16_f32 v156, v100, v101\n"
  "v_cvt_pk_bf16_f32 v157, v102, v103\n"
  "v_mfma_f32_16x16x32_bf16 v[120:123], v[220:223], a[244:247], v[120:123]\n"
  "v_cvt_pk_bf16_f32 v172, v104, v105\n"
  "v_cvt_pk_bf16_f32 v173, v106, v107\n"
  "v_mfma_f32_16x16x32_bf16 v[124:127], v[216:219], a[248:251], v[124:127]\n"
  "v_cvt_pk_bf16_f32 v188, v108, v109\n"
  "v_cvt_pk_bf16_f32 v189, v110, v111\n"
  "v_mfma_f32_16x16x32_bf16 v[124:127], v[220:223], a[252:255], v[124:127]\n"
  "v_cvt_pk_bf16_f32 v142, v112, v113\n"
  "v_cvt_pk_bf16_f32 v143, v114, v115\n"
  "v_cvt_pk_bf16_f32 v158, v116, v117\n"
  "v_cvt_pk_bf16_f32 v159, v118, v119\n"
  "v_cvt_pk_bf16_f32 v174, v120, v121\n"
  "v_cvt_pk_bf16_f32 v175, v122, v123\n"
  "s_nop 1\n"
  "v_cvt_pk_bf16_f32 v190, v124, v125\n"
  "v_cvt_pk_bf16_f32 v191, v126, v127\n"
  "s_nop 0\n"
  "v_mfma_f32_16x16x32_bf16 v[100:103], a[128:131], v[144:147], 0\n"
  "v_min_f32 v116, 0, v250\n"
  "v_cndmask_b32_e64 v112, 0, v116, s[64:65]\n"
  "v_mfma_f32_16x16x32_bf16 v[100:103], a[132:135], v[148:151], v[100:103]\n"
  "s_waitcnt lgkmcnt(2)\n"
  "v_cndmask_b32_e64 v113, 0, v116, s[66:67]\n"
  "v_cndmask_b32_e64 v114, 0, v116, s[68:69]\n"
  "v_mfma_f32_16x16x32_bf16 v[100:103], a[136:139], v[152:155], v[100:103]\n"
  "v_cndmask_b32_e64 v115, 0, v116, s[70:71]\n"
  "v_mov_b32 v224, 0\n"
  "v_mfma_f32_16x16x32_bf16 v[100:103], a[140:143], v[156:159], v[100:103]\n"
  "v_mov_b32 v225, 0\n"
  "v_mov_b32 v226, 0\n"
  "v_mfma_f32_16x16x32_bf16 v[104:107], a[128:131], v[160:163], 0\n"
  "v_mov_b32 v227, 0\n"
  "v_mov_b32 v228, 0\n"
  "v_mfma_f32_16x16x32_bf16 v[104:107], a[132:135], v[164:167], v[104:107]\n"
  "v_mov_b32 v229, 0\n"
  "v_mov_b32 v230, 0\n"
  "v_mfma_f32_16x16x32_bf16 v[104:107], a[136:139], v[168:171], v[104:107]\n"
  "v_max_f32 v101, v100, v101\n"
  "v_max3_f32 v100, v101, v102, v103\n"
  "v_mfma_f32_16x16x32_bf16 v[104:107], a[140:143], v[172:175], v[104:107]\n"
  "v_max3_f32 v101, v101, v102, v103\n"
  "v_mov_b32 v231, 0\n"
  "v_mfma_f32_16x16x32_bf16 v[108:111], a[128:131], v[176:179], 0\n"
  "v_mov_b32 v232, 0\n"
  "v_mov_b32 v233, 0\n"
  "v_mfma_f32_16x16x32_bf16 v[108:111], a[132:135], v[180:183], v[108:111]\n"
  "v_permlane16_swap_b32 v100, v101\n"
  "v_max_f32 v102, v100, v101\n"
  "v_mfma_f32_16x16x32_bf16 v[108:111], a[136:139], v[184:187], v[108:111]\n"
  "v_max_f32 v103, v100, v101\n"
  "v_max_f32 v105, v104, v105\n"
  "v_mfma_f32_16x16x32_bf16 v[108:111], a[140:143], v[188:191], v[108:111]\n"
  "v_max3_f32 v104, v105, v106, v107\n"
  "v_max3_f32 v105, v105, v106, v107\n"
  "v_mfma_f32_16x16x32_bf16 v[96:99], a[128:131], v[128:131], v[112:115]\n"
  "v_permlane32_swap_b32 v102, v103\n"
  "v_min_f32_e64 v196, -v102, -v103\n"
  "v_mfma_f32_16x16x32_bf16 v[96:99], a[132:135], v[132:135], v[96:99]\n"
  "v_min_f32_e64 v197, -v102, -v103\n"
  "v_min_f32_e64 v198, -v102, -v103\n"
  "v_mfma_f32_16x16x32_bf16 v[96:99], a[136:139], v[136:139], v[96:99]\n"
  "v_min_f32_e64 v199, -v102, -v103\n"
  "v_permlane16_swap_b32 v104, v105\n"
  "v_mfma_f32_16x16x32_bf16 v[96:99], a[140:143], v[140:143], v[96:99]\n"
  "v_max_f32 v106, v104, v105\n"
  "v_max_f32 v107, v104, v105\n"
  "v_mfma_f32_16x16x32_bf16 v[4:7], a[128:131], v[144:147], v[196:199]\n"
  "v_max_f32 v109, v108, v109\n"
  "v_max3_f32 v108, v109, v110, v111\n"
  "v_mfma_f32_16x16x32_bf16 v[4:7], a[132:135], v[148:151], v[4:7]\n"
  "v_permlane32_swap_b32 v106, v107\n"
  "v_min_f32_e64 v200, -v106, -v107\n"
  "v_mfma_f32_16x16x32_bf16 v[4:7], a[136:139], v[152:155], v[4:7]\n"
  "v_min_f32_e64 v201, -v106, -v107\n"
  "v_min_f32_e64 v202, -v106, -v107\n"
  "v_mfma_f32_16x16x32_bf16 v[4:7], a[140:143], v[156:159], v[4:7]\n"
  "v_min_f32_e64 v203, -v106, -v107\n"
  "v_max3_f32 v109, v109, v110, v111\n"
  "v_mfma_f32_16x16x32_bf16 v[20:23], a[144:147], v[144:147], v[196:199]\n"
  "s_waitcnt vmcnt(0)\n"
  "v_max_f32 v97, v96, v97\n"
  "v_max3_f32 v96, v97, v98, v99\n"
  "v_mfma_f32_16x16x32_bf16 v[20:23], a[148:151], v[148:151], v[20:23]\n"
  "v_permlane16_swap_b32 v108, v109\n"
  "v_max_f32 v110, v108, v109\n"
  "v_mfma_f32_16x16x32_bf16 v[20:23], a[152:155], v[152:155], v[20:23]\n"
  "v_max_f32 v111, v108, v109\n"
  "v_max3_f32 v97, v97, v98, v99\n"
  "v_mfma_f32_16x16x32_bf16 v[20:23], a[156:159], v[156:159], v[20:23]\n"
  "v_cmp_ne_u32 vcc, 8, v252\n"
  "v_mov_b32 v234, 0\n"
  "v_mfma_f32_16x16x32_bf16 v[36:39], a[160:163], v[144:147], v[196:199]\n"
  "s_and_b32 vcc_lo, vcc_lo, 0x1\n"
  "s_cbranch_scc1 mk_spinV\n"
  "mk_spinV_b:\n"
  "v_permlane32_swap_b32 v110, v111\n"
  "v_min_f32_e64 v204, -v110, -v111\n"
  "v_mfma_f32_16x16x32_bf16 v[36:39], a[164:167], v[148:151], v[36:39]\n"
  "s_sub_u32 s97, s96, 0xc000\n"
  "v_min_f32_e64 v205, -v110, -v111\n"
  "v_min_f32_e64 v206, -v110, -v111\n"
  "v_mfma_f32_16x16x32_bf16 v[36:39], a[168:171], v[152:155], v[36:39]\n"
  "s_add_u32 m0, s98, 0x4000\n"
  "v_min_f32_e64 v207, -v110, -v111\n"
  "v_permlane16_swap_b32 v96, v97\n"
  "v_mfma_f32_16x16x32_bf16 v[36:39], a[172:175], v[156:159], v[36:39]\n"
  "v_max_f32 v98, v96, v97\n"
  "v_max_f32 v99, v96, v97\n"
  "v_mfma_f32_16x16x32_bf16 v[52:55], a[176:179], v[144:147], v[196:199]\n"
  "buffer_load_dwordx4 v244, s[44:47], s97 offen offset:0 sc1 lds\n"
  "v_mfma_f32_16x16x32_bf16 v[52:55], a[180:183], v[148:151], v[52:55]\n"
  "s_waitcnt lgkmcnt(0)\n"
  "v_permlane32_swap_b32 v98, v99\n"
  "v_min_f32_e64 v192, -v98, -v99\n"
  "v_mfma_f32_16x16x32_bf16 v[52:55], a[184:187], v[152:155], v[52:55]\n"
  "v_min_f32_e64 v193, -v98, -v99\n"
  "v_min_f32_e64 v194, -v98, -v99\n"
  "v_mfma_f32_16x16x32_bf16 v[52:55], a[188:191], v[156:159], v[52:55]\n"
  "v_min_f32_e64 v195, -v98, -v99\n"
  "v_min_f32 v248, v250, v192\n"
  "v_mfma_f32_16x16x32_bf16 v[8:11], a[128:131], v[160:163], v[200:203]\n"
  "buffer_load_dwordx4 v244, s[44:47], s97 offen offset:1024 sc1 lds\n"
  "v_mfma_f32_16x16x32_bf16 v[8:11], a[132:135], v[164:167], v[8:11]\n"
  "buffer_load_dwordx4 v244, s[44:47], s97 offen offset:2048 sc1 lds\n"
  "v_mfma_f32_16x16x32_bf16 v[8:11], a[136:139], v[168:171], v[8:11]\n"
  "v_cndmask_b32_e64 v208, v192, v248, s[64:65]\n"
  "v_cndmask_b32_e64 v209, v192, v248, s[66:67]\n"
  "v_mfma_f32_16x16x32_bf16 v[8:11], a[140:143], v[172:175], v[8:11]\n"
  "v_cndmask_b32_e64 v210, v192, v248, s[68:69]\n"
  "v_cndmask_b32_e64 v211, v192, v248, s[70:71]\n"
  "v_mfma_f32_16x16x32_bf16 v[12:15], a[128:131], v[176:179], v[204:207]\n"
  "buffer_load_dwordx4 v244, s[44:47], s97 offen offset:3072 sc1 lds\n"
  "v_mfma_f32_16x16x32_bf16 v[12:15], a[132:135], v[180:183], v[12:15]\n"
  "s_add_u32 s96, s96, 0x8000\n"
  "v_exp_f32 v4, v4\n"
  "v_mfma_f32_16x16x32_bf16 v[12:15], a[136:139], v[184:187], v[12:15]\n"
  "s_add_u32 s98, s98, 0x8000\n"
  "v_exp_f32 v5, v5\n"
  "v_mfma_f32_16x16x32_bf16 v[12:15], a[140:143], v[188:191], v[12:15]\n"
  "s_and_b32 s98, s98, 0x1ffff\n"
  "v_exp_f32 v6, v6\n"
  "v_mfma_f32_16x16x32_bf16 v[0:3], a[128:131], v[128:131], v[208:211]\n"
  "s_mov_b32 m0, s98\n"
  "v_exp_f32 v7, v7\n"
  "v_mfma_f32_16x16x32_bf16 v[0:3], a[132:135], v[132:135], v[0:3]\n"
  "v_cvt_pk_bf16_f32 v68, v4, v5\n"
  "v_cvt_pk_bf16_f32 v69, v6, v7\n"
  "v_mfma_f32_16x16x32_bf16 v[0:3], a[136:139], v[136:139], v[0:3]\n"
  "buffer_load_dwordx4 v244, s[44:47], s96 offen offset:0 sc1 lds\n"
  "v_mfma_f32_16x16x32_bf16 v[0:3], a[140:143], v[140:143], v[0:3]\n"
  "ds_read_b128 a[128:131], v245 offset:32768\n"
  "ds_read_b128 a[132:135], v245 offset:33792\n"
  "v_mfma_f32_16x16x32_bf16 v[24:27], a[144:147], v[160:163], v[200:203]\n"
  "ds_read_b128 a[136:139], v245 offset:34816\n"
  "ds_read_b128 a[140:143], v245 offset:35840\n"
  "v_mfma_f32_16x16x32_bf16 v[24:27], a[148:151], v[164:167], v[24:27]\n"
  "v_cndmask_b32_e64 v212, v192, v248, s[72:73]\n"
  "v_cndmask_b32_e64 v213, v192, v248, s[74:75]\n"
  "v_mfma_f32_16x16x32_bf16 v[24:27], a[152:155], v[168:171], v[24:27]\n"
  "v_cndmask_b32_e64 v214, v192, v248, s[76:77]\n"
  "v_cndmask_b32_e64 v215, v192, v248, s[78:79]\n"
  "v_mfma_f32_16x16x32_bf16 v[24:27], a[156:159], v[172:175], v[24:27]\n"
  "buffer_load_dwordx4 v244, s[44:47], s96 offen offset:1024 sc1 lds\n"
  "v_mfma_f32_16x16x32_bf16 v[28:31], a[144:147], v[176:179], v[204:207]\n"
  "v_exp_f32 v20, v20\n"
  "v_mfma_f32_16x16x32_bf16 v[28:31], a[148:151], v[180:183], v[28:31]\n"
  "v_exp_f32 v21, v21\n"
  "v_mfma_f32_16x16x32_bf16 v[28:31], a[152:155], v[184:187], v[28:31]\n"
  "v_exp_f32 v22, v22\n"
  "v_mfma_f32_16x16x32_bf16 v[28:31], a[156:159], v[188:191], v[28:31]\n"
  "v_exp_f32 v23, v23\n"
  "v_mfma_f32_16x16x32_bf16 v[16:19], a[144:147], v[128:131], v[212:215]\n"
  "buffer_load_dwordx4 v244, s[44:47], s96 offen offset:2048 sc1 lds\n"
  "v_mfma_f32_16x16x32_bf16 v[16:19], a[148:151], v[132:135], v[16:19]\n"
  "v_cvt_pk_bf16_f32 v70, v20, v21\n"
  "v_cvt_pk_bf16_f32 v71, v22, v23\n"
  "v_mfma_f32_16x16x32_bf16 v[16:19], a[152:155], v[136:139], v[16:19]\n"
  "v_cndmask_b32_e64 v216, v192, v248, s[80:81]\n"
  "v_cndmask_b32_e64 v217, v192, v248, s[82:83]\n"
  "v_mfma_f32_16x16x32_bf16 v[16:19], a[156:159], v[140:143], v[16:19]\n"
  "ds_read_b128 a[144:147], v245 offset:36864\n"
  "ds_read_b128 a[148:151], v245 offset:37888\n"
  "v_mfma_f32_16x16x32_bf16 v[40:43], a[160:163], v[160:163], v[200:203]\n"
  "ds_read_b128 a[152:155], v245 offset:38912\n"
  "ds_read_b128 a[156:159], v245 offset:39936\n"
  "v_mfma_f32_16x16x32_bf16 v[40:43], a[164:167], v[164:167], v[40:43]\n"
  "v_cndmask_b32_e64 v218, v192, v248, s[84:85]\n"
  "v_cndmask_b32_e64 v219, v192, v248, s[86:87]\n"
  "v_mfma_f32_16x16x32_bf16 v[40:43], a[168:171], v[168:171], v[40:43]\n"
  "buffer_load_dwordx4 v244, s[44:47], s96 offen offset:3072 sc1 lds\n"
  "v_mfma_f32_16x16x32_bf16 v[40:43], a[172:175], v[172:175], v[40:43]\n"
  "s_sub_u32 s97, s96, 0xc000\n"
  "v_exp_f32 v36, v36\n"
  "v_mfma_f32_16x16x32_bf16 v[44:47], a[160:163], v[176:179], v[204:207]\n"
  "s_add_u32 m0, s98, 0x4000\n"
  "v_exp_f32 v37, v37\n"
  "v_mfma_f32_16x16x32_bf16 v[44:47], a[164:167], v[180:183], v[44:47]\n"
  "v_exp_f32 v38, v38\n"
  "v_mfma_f32_16x16x32_bf16 v[44:47], a[168:171], v[184:187], v[44:47]\n"
  "buffer_load_dwordx4 v244, s[44:47], s97 offen offset:0 sc1 lds\n"
  "v_mfma_f32_16x16x32_bf16 v[44:47], a[172:175], v[188:191], v[44:47]\n"
  "v_exp_f32 v39, v39\n"
  "v_mfma_f32_16x16x32_bf16 v[32:35], a[160:163], v[128:131], v[216:219]\n"
  "v_cvt_pk_bf16_f32 v84, v36, v37\n"
  "v_cvt_pk_bf16_f32 v85, v38, v39\n"
  "v_mfma_f32_16x16x32_bf16 v[32:35], a[164:167], v[132:135], v[32:35]\n"
  "v_exp_f32 v52, v52\n"
  "v_mfma_f32_16x16x32_bf16 v[32:35], a[168:171], v[136:139], v[32:35]\n"
  "v_cndmask_b32_e64 v220, v192, v248, s[88:89]\n"
  "v_cndmask_b32_e64 v221, v192, v248, s[90:91]\n"
  "v_mfma_f32_16x16x32_bf16 v[32:35], a[172:175], v[140:143], v[32:35]\n"
  "buffer_load_dwordx4 v244, s[44:47], s97 offen offset:1024 sc1 lds\n"
  "v_mfma_f32_16x16x32_bf16 v[56:59], a[176:179], v[160:163], v[200:203]\n"
  "ds_read_b128 a[160:163], v245 offset:40960\n"
  "ds_read_b128 a[164:167], v245 offset:41984\n"
  "v_mfma_f32_16x16x32_bf16 v[56:59], a[180:183], v[164:167], v[56:59]\n"
  "ds_read_b128 a[168:171], v245 offset:43008\n"
  "ds_read_b128 a[172:175], v245 offset:44032\n"
  "v_mfma_f32_16x16x32_bf16 v[56:59], a[184:187], v[168:171], v[56:59]\n"
  "v_cndmask_b32_e64 v222, v192, v248, s[92:93]\n"
  "v_cndmask_b32_e64 v223, v192, v248, s[94:95]\n"
  "v_mfma_f32_16x16x32_bf16 v[56:59], a[188:191], v[172:175], v[56:59]\n"
  "v_exp_f32 v53, v53\n"
  "v_mfma_f32_16x16x32_bf16 v[60:63], a[176:179], v[176:179], v[204:207]\n"
  "buffer_load_dwordx4 v244, s[44:47], s97 offen offset:2048 sc1 lds\n"
  "v_mfma_f32_16x16x32_bf16 v[60:63], a[180:183], v[180:183], v[60:63]\n"
  "v_exp_f32 v54, v54\n"
  "v_mfma_f32_16x16x32_bf16 v[60:63], a[184:187], v[184:187], v[60:63]\n"
  "v_exp_f32 v55, v55\n"
  "v_mfma_f32_16x16x32_bf16 v[60:63], a[188:191], v[188:191], v[60:63]\n"
  "v_cvt_pk_bf16_f32 v86, v52, v53\n"
  "v_cvt_pk_bf16_f32 v87, v54, v55\n"
  "v_mfma_f32_16x16x32_bf16 v[48:51], a[176:179], v[128:131], v[220:223]\n"
  "v_exp_f32 v8, v8\n"
  "v_mfma_f32_16x16x32_bf16 v[48:51], a[180:183], v[132:135], v[48:51]\n"
  "buffer_load_dwordx4 v244, s[44:47], s97 offen offset:3072 sc1 lds\n"
  "v_mfma_f32_16x16x32_bf16 v[48:51], a[184:187], v[136:139], v[48:51]\n"
  "s_cmp_eq_u32 s62, 0\n"
  "v_exp_f32 v9, v9\n"
  "v_mfma_f32_16x16x32_bf16 v[48:51], a[188:191], v[140:143], v[48:51]\n"
  "ds_read_b128 a[176:179], v245 offset:45056\n"
  "ds_read_b128 a[180:183], v245 offset:46080\n"
  "ds_read_b128 a[184:187], v245 offset:47104\n"
  "ds_read_b128 a[188:191], v245 offset:48128\n"
  "v_exp_f32 v10, v10\n"
  "v_exp_f32 v11, v11\n"
  "v_cvt_pk_bf16_f32 v72, v8, v9\n"
  "v_cvt_pk_bf16_f32 v73, v10, v11\n"
  "v_exp_f32 v12, v12\n"
  "v_exp_f32 v13, v13\n"
  "v_exp_f32 v14, v14\n"
  "v_exp_f32 v15, v15\n"
  "v_cvt_pk_bf16_f32 v76, v12, v13\n"
  "v_cvt_pk_bf16_f32 v77, v14, v15\n"
  "v_exp_f32 v0, v0\n"
  "v_exp_f32 v1, v1\n"
  "v_exp_f32 v2, v2\n"
  "v_exp_f32 v3, v3\n"
  "v_cvt_pk_bf16_f32 v64, v0, v1\n"
  "v_cvt_pk_bf16_f32 v65, v2, v3\n"
  "v_exp_f32 v24, v24\n"
  "v_exp_f32 v25, v25\n"
  "v_exp_f32 v26, v26\n"
  "v_exp_f32 v27, v27\n"
  "v_cvt_pk_bf16_f32 v74, v24, v25\n"
  "v_cvt_pk_bf16_f32 v75, v26, v27\n"
  "v_exp_f32 v28, v28\n"
  "v_exp_f32 v29, v29\n"
  "v_exp_f32 v30, v30\n"
  "v_exp_f32 v31, v31\n"
  "v_cvt_pk_bf16_f32 v78, v28, v29\n"
  "v_cvt_pk_bf16_f32 v79, v30, v31\n"
  "v_exp_f32 v16, v16\n"
  "v_exp_f32 v17, v17\n"
  "v_exp_f32 v18, v18\n"
  "v_exp_f32 v19, v19\n"
  "v_cvt_pk_bf16_f32 v66, v16, v17\n"
  "v_cvt_pk_bf16_f32 v67, v18, v19\n"
  "v_exp_f32 v40, v40\n"
  "v_exp_f32 v41, v41\n"
  "v_exp_f32 v42, v42\n"
  "v_exp_f32 v43, v43\n"
  "v_cvt_pk_bf16_f32 v88, v40, v41\n"
  "v_cvt_pk_bf16_f32 v89, v42, v43\n"
  "v_exp_f32 v44, v44\n"
  "v_exp_f32 v45, v45\n"
  "v_exp_f32 v46, v46\n"
  "v_exp_f32 v47, v47\n"
  "v_cvt_pk_bf16_f32 v92, v44, v45\n"
  "v_cvt_pk_bf16_f32 v93, v46, v47\n"
  "v_exp_f32 v32, v32\n"
  "v_exp_f32 v33, v33\n"
  "v_exp_f32 v34, v34\n"
  "v_exp_f32 v35, v35\n"
  "v_cvt_pk_bf16_f32 v80, v32, v33\n"
  "v_cvt_pk_bf16_f32 v81, v34, v35\n"
  "v_exp_f32 v56, v56\n"
  "v_exp_f32 v57, v57\n"
  "v_exp_f32 v58, v58\n"
  "v_exp_f32 v59, v59\n"
  "v_cvt_pk_bf16_f32 v90, v56, v57\n"
  "v_cvt_pk_bf16_f32 v91, v58, v59\n"
  "v_exp_f32 v60, v60\n"
  "v_exp_f32 v61, v61\n"
  "v_exp_f32 v62, v62\n"
  "v_exp_f32 v63, v63\n"
  "v_xor_b32 v245, 0x10000, v245\n"
  "v_cvt_pk_bf16_f32 v94, v60, v61\n"
  "v_cvt_pk_bf16_f32 v95, v62, v63\n"
  "v_mov_b32 v235, 0\n"
  "v_exp_f32 v48, v48\n"
  "v_exp_f32 v49, v49\n"
  "v_exp_f32 v50, v50\n"
  "v_exp_f32 v51, v51\n"
  "v_cvt_pk_bf16_f32 v82, v48, v49\n"
  "v_cvt_pk_bf16_f32 v83, v50, v51\n"
  "v_mov_b32 v236, 0\n"
  "v_mov_b32 v237, 0\n"
  "v_mov_b32 v238, 0\n"
  "v_mov_b32 v239, 0\n"
  "s_waitcnt vmcnt(8) lgkmcnt(0)\n"
  "s_barrier\n"
  "s_cbranch_scc1 mk_T3\n"
  "mk_LA1:\n"
  "v_mfma_f32_16x16x32_bf16 v[0:3], a[128:131], v[128:131], v[192:195]\n"
  "s_add_u32 s96, s96, 0x8000\n"
  "ds_read_b128 a[192:195], v245 offset:16384\n"
  "v_mfma_f32_16x16x32_bf16 v[0:3], a[132:135], v[132:135], v[0:3]\n"
  "s_add_u32 s98, s98, 0x8000\n"
  "ds_read_b128 a[196:199], v245 offset:17408\n"
  "v_mfma_f32_16x16x32_bf16 v[0:3], a[136:139], v[136:139], v[0:3]\n"
  "s_and_b32 s98, s98, 0x1ffff\n"
  "v_mfma_f32_16x16x32_bf16 v[0:3], a[140:143], v[140:143], v[0:3]\n"
  "s_mov_b32 m0, s98\n"
  "v_mfma_f32_16x16x32_bf16 v[4:7], a[128:131], v[144:147], v[196:199]\n"
  "buffer_load_dwordx4 v244, s[44:47], s96 offen offset:0 sc1 lds\n"
  "v_mfma_f32_16x16x32_bf16 v[4:7], a[132:135], v[148:151], v[4:7]\n"
  "ds_read_b128 a[200:203], v245 offset:18432\n"
  "v_mfma_f32_16x16x32_bf16 v[4:7], a[136:139], v[152:155], v[4:7]\n"
  "v_exp_f32 v0, v0\n"
  "v_mfma_f32_16x16x32_bf16 v[4:7], a[140:143], v[156:159], v[4:7]\n"
  "v_exp_f32 v1, v1\n"
  "v_mfma_f32_16x16x32_bf16 v[8:11], a[128:131], v[160:163], v[200:203]\n"
  "v_cvt_pk_bf16_f32 v96, v0, v1\n"
  "v_mfma_f32_16x16x32_bf16 v[8:11], a[132:135], v[164:167], v[8:11]\n"
  "v_exp_f32 v2, v2\n"
  "v_mfma_f32_16x16x32_bf16 v[8:11], a[136:139], v[168:171], v[8:11]\n"
  "v_exp_f32 v3, v3\n"
  "v_mfma_f32_16x16x32_bf16 v[8:11], a[140:143], v[172:175], v[8:11]\n"
  "ds_read_b128 a[204:207], v245 offset:19456\n"
  "v_mfma_f32_16x16x32_bf16 v[12:15], a[128:131], v[176:179], v[204:207]\n"
  "v_cvt_pk_bf16_f32 v97, v2, v3\n"
  "v_mfma_f32_16x16x32_bf16 v[12:15], a[132:135], v[180:183], v[12:15]\n"
  "v_exp_f32 v4, v4\n"
  "v_mfma_f32_16x16x32_bf16 v[12:15], a[136:139], v[184:187], v[12:15]\n"
  "v_exp_f32 v5, v5\n"
  "v_mfma_f32_16x16x32_bf16 v[12:15], a[140:143], v[188:191], v[12:15]\n"
  "ds_read_b128 a[208:211], v245 offset:20480\n"
  "v_mfma_f32_16x16x32_bf16 v[16:19], a[144:147], v[128:131], v[192:195]\n"
  "v_cvt_pk_bf16_f32 v100, v4, v5\n"
  "v_mfma_f32_16x16x32_bf16 v[16:19], a[148:151], v[132:135], v[16:19]\n"
  "v_exp_f32 v6, v6\n"
  "v_mfma_f32_16x16x32_bf16 v[16:19], a[152:155], v[136:139], v[16:19]\n"
  "v_exp_f32 v7, v7\n"
  "v_mfma_f32_16x16x32_bf16 v[16:19], a[156:159], v[140:143], v[16:19]\n"
  "ds_read_b128 a[212:215], v245 offset:21504\n"
  "v_mfma_f32_16x16x32_bf16 v[20:23], a[144:147], v[144:147], v[196:199]\n"
  "buffer_load_dwordx4 v244, s[44:47], s96 offen offset:1024 sc1 lds\n"
  "v_mfma_f32_16x16x32_bf16 v[20:23], a[148:151], v[148:151], v[20:23]\n"
  "ds_read_b128 a[216:219], v245 offset:22528\n"
  "v_cvt_pk_bf16_f32 v101, v6, v7\n"
  "v_mfma_f32_16x16x32_bf16 v[20:23], a[152:155], v[152:155], v[20:23]\n"
  "v_exp_f32 v8, v8\n"
  "v_mfma_f32_16x16x32_bf16 v[20:23], a[156:159], v[156:159], v[20:23]\n"
  "v_exp_f32 v9, v9\n"
  "v_mfma_f32_16x16x32_bf16 v[24:27], a[144:147], v[160:163], v[200:203]\n"
  "v_cvt_pk_bf16_f32 v104, v8, v9\n"
  "v_mfma_f32_16x16x32_bf16 v[24:27], a[148:151], v[164:167], v[24:27]\n"
  "v_exp_f32 v10, v10\n"
  "v_mfma_f32_16x16x32_bf16 v[24:27], a[152:155], v[168:171], v[24:27]\n"
  "v_exp_f32 v11, v11\n"
  "v_mfma_f32_16x16x32_bf16 v[24:27], a[156:159], v[172:175], v[24:27]\n"
  "ds_read_b128 a[220:223], v245 offset:23552\n"
  "v_cvt_pk_bf16_f32 v105, v10, v11\n"
  "v_mfma_f32_16x16x32_bf16 v[28:31], a[144:147], v[176:179], v[204:207]\n"
  "v_exp_f32 v12, v12\n"
  "v_mfma_f32_16x16x32_bf16 v[28:31], a[148:151], v[180:183], v[28:31]\n"
  "ds_read_b128 a[224:227], v245 offset:24576\n"
  "v_mfma_f32_16x16x32_bf16 v[28:31], a[152:155], v[184:187], v[28:31]\n"
  "v_exp_f32 v13, v13\n"
  "v_mfma_f32_16x16x32_bf16 v[28:31], a[156:159], v[188:191], v[28:31]\n"
  "v_cvt_pk_bf16_f32 v108, v12, v13\n"
  "v_mfma_f32_16x16x32_bf16 v[32:35], a[160:163], v[128:131], v[192:195]\n"
  "v_exp_f32 v14, v14\n"
  "v_mfma_f32_16x16x32_bf16 v[32:35], a[164:167], v[132:135], v[32:35]\n"
  "ds_read_b128 a[228:231], v245 offset:25600\n"
  "v_mfma_f32_16x16x32_bf16 v[32:35], a[168:171], v[136:139], v[32:35]\n"
  "v_exp_f32 v15, v15\n"
  "v_mfma_f32_16x16x32_bf16 v[32:35], a[172:175], v[140:143], v[32:35]\n"
  "v_cvt_pk_bf16_f32 v109, v14, v15\n"
  "v_mfma_f32_16x16x32_bf16 v[36:39], a[160:163], v[144:147], v[196:199]\n"
  "buffer_load_dwordx4 v244, s[44:47], s96 offen offset:2048 sc1 lds\n"
  "v_mfma_f32_16x16x32_bf16 v[36:39], a[164:167], v[148:151], v[36:39]\n"
  "v_exp_f32 v16, v16\n"
  "v_mfma_f32_16x16x32_bf16 v[36:39], a[168:171], v[152:155], v[36:39]\n"
  "v_exp_f32 v17, v17\n"
  "v_mfma_f32_16x16x32_bf16 v[36:39], a[172:175], v[156:159], v[36:39]\n"
  "ds_read_b128 a[232:235], v245 offset:26624\n"
  "v_cvt_pk_bf16_f32 v98, v16, v17\n"
  "v_mfma_f32_16x16x32_bf16 v[40:43], a[160:163], v[160:163], v[200:203]\n"
  "v_exp_f32 v18, v18\n"
  "v_mfma_f32_16x16x32_bf16 v[40:43], a[164:167], v[164:167], v[40:43]\n"
  "ds_read_b128 a[236:239], v245 offset:27648\n"
  "v_mfma_f32_16x16x32_bf16 v[40:43], a[168:171], v[168:171], v[40:43]\n"
  "v_exp_f32 v19, v19\n"
  "v_mfma_f32_16x16x32_bf16 v[40:43], a[172:175], v[172:175], v[40:43]\n"
  "v_cvt_pk_bf16_f32 v99, v18, v19\n"
  "v_mfma_f32_16x16x32_bf16 v[44:47], a[160:163], v[176:179], v[204:207]\n"
  "v_exp_f32 v20, v20\n"
  "v_mfma_f32_16x16x32_bf16 v[44:47], a[164:167], v[180:183], v[44:47]\n"
  "ds_read_b128 a[240:243], v245 offset:28672\n"
  "v_mfma_f32_16x16x32_bf16 v[44:47], a[168:171], v[184:187], v[44:47]\n"
  "v_exp_f32 v21, v21\n"
  "v_mfma_f32_16x16x32_bf16 v[44:47], a[172:175], v[188:191], v[44:47]\n"
  "v_cvt_pk_bf16_f32 v102, v20, v21\n"
  "v_mfma_f32_16x16x32_bf16 v[48:51], a[176:179], v[128:131], v[192:195]\n"
  "v_exp_f32 v22, v22\n"
  "v_mfma_f32_16x16x32_bf16 v[48:51], a[180:183], v[132:135], v[48:51]\n"
  "v_exp_f32 v23, v23\n"
  "v_mfma_f32_16x16x32_bf16 v[48:51], a[184:187], v[136:139], v[48:51]\n"
  "ds_read_b128 a[244:247], v245 offset:29696\n"
  "v_mfma_f32_16x16x32_bf16 v[48:51], a[188:191], v[140:143], v[48:51]\n"
  "buffer_load_dwordx4 v244, s[44:47], s96 offen offset:3072 sc1 lds\n"
  "v_mfma_f32_16x16x32_bf16 v[52:55], a[176:179], v[144:147], v[196:199]\n"
  "v_cvt_pk_bf16_f32 v103, v22, v23\n"
  "v_mfma_f32_16x16x32_bf16 v[52:55], a[180:183], v[148:151], v[52:55]\n"
  "v_exp_f32 v24, v24\n"
  "v_mfma_f32_16x16x32_bf16 v[52:55], a[184:187], v[152:155], v[52:55]\n"
  "v_exp_f32 v25, v25\n"
  "v_mfma_f32_16x16x32_bf16 v[52:55], a[188:191], v[156:159], v[52:55]\n"
  "ds_read_b128 a[248:251], v245 offset:30720\n"
  "v_cvt_pk_bf16_f32 v106, v24, v25\n"
  "v_mfma_f32_16x16x32_bf16 v[56:59], a[176:179], v[160:163], v[200:203]\n"
  "v_exp_f32 v26, v26\n"
  "v_mfma_f32_16x16x32_bf16 v[56:59], a[180:183], v[164:167], v[56:59]\n"
  "v_exp_f32 v27, v27\n"
  "v_mfma_f32_16x16x32_bf16 v[56:59], a[184:187], v[168:171], v[56:59]\n"
  "ds_read_b128 a[252:255], v245 offset:31744\n"
  "v_mfma_f32_16x16x32_bf16 v[56:59], a[188:191], v[172:175], v[56:59]\n"
  "v_cvt_pk_bf16_f32 v107, v26, v27\n"
  "v_mfma_f32_16x16x32_bf16 v[60:63], a[176:179], v[176:179], v[204:207]\n"
  "v_exp_f32 v28, v28\n"
  "v_mfma_f32_16x16x32_bf16 v[60:63], a[180:183], v[180:183], v[60:63]\n"
  "v_exp_f32 v29, v29\n"
  "v_mfma_f32_16x16x32_bf16 v[60:63], a[184:187], v[184:187], v[60:63]\n"
  "s_sub_u32 s97, s96, 0xc000\n"
  "ds_read_b128 a[128:131], v245 offset:0\n"
  "v_mfma_f32_16x16x32_bf16 v[60:63], a[188:191], v[188:191], v[60:63]\n"
  "s_add_u32 m0, s98, 0x4000\n"
  "v_cvt_pk_bf16_f32 v110, v28, v29\n"
  "v_mfma_f32_16x16x32_bf16 v[224:227], v[240:243], v[64:67], v[224:227]\n"
  "v_exp_f32 v30, v30\n"
  "v_mfma_f32_16x16x32_bf16 v[224:227], v[240:243], v[80:83], v[224:227]\n"
  "v_exp_f32 v31, v31\n"
  "v_mfma_f32_16x16x32_bf16 v[228:231], v[240:243], v[68:71], v[228:231]\n"
  "ds_read_b128 a[132:135], v245 offset:1024\n"
  "v_mfma_f32_16x16x32_bf16 v[228:231], v[240:243], v[84:87], v[228:231]\n"
  "buffer_load_dwordx4 v244, s[44:47], s97 offen offset:0 sc1 lds\n"
  "v_mfma_f32_16x16x32_bf16 v[232:235], v[240:243], v[72:75], v[232:235]\n"
  "v_cvt_pk_bf16_f32 v111, v30, v31\n"
  "v_mfma_f32_16x16x32_bf16 v[232:235], v[240:243], v[88:91], v[232:235]\n"
  "v_exp_f32 v32, v32\n"
  "v_mfma_f32_16x16x32_bf16 v[236:239], v[240:243], v[76:79], v[236:239]\n"
  "v_exp_f32 v33, v33\n"
  "v_mfma_f32_16x16x32_bf16 v[236:239], v[240:243], v[92:95], v[236:239]\n"
  "s_waitcnt lgkmcnt(1)\n"
  "ds_read_b128 a[136:139], v245 offset:2048\n"
  "v_cvt_pk_bf16_f32 v112, v32, v33\n"
  "v_mfma_f32_16x16x32_bf16 a[0:3], a[192:195], v[64:67], a[0:3]\n"
  "v_exp_f32 v34, v34\n"
  "v_mfma_f32_16x16x32_bf16 a[0:3], a[196:199], v[80:83], a[0:3]\n"
  "v_exp_f32 v35, v35\n"
  "v_mfma_f32_16x16x32_bf16 a[32:35], a[192:195], v[68:71], a[32:35]\n"
  "ds_read_b128 a[140:143], v245 offset:3072\n"
  "v_cvt_pk_bf16_f32 v113, v34, v35\n"
  "v_mfma_f32_16x16x32_bf16 a[32:35], a[196:199], v[84:87], a[32:35]\n"
  "v_exp_f32 v36, v36\n"
  "v_mfma_f32_16x16x32_bf16 a[64:67], a[192:195], v[72:75], a[64:67]\n"
  "v_mfma_f32_16x16x32_bf16 a[64:67], a[196:199], v[88:91], a[64:67]\n"
  "v_exp_f32 v37, v37\n"
  "v_mfma_f32_16x16x32_bf16 a[96:99], a[192:195], v[76:79], a[96:99]\n"
  "ds_read_b128 a[144:147], v245 offset:4096\n"
  "v_cvt_pk_bf16_f32 v116, v36, v37\n"
  "v_mfma_f32_16x16x32_bf16 a[96:99], a[196:199], v[92:95], a[96:99]\n"
  "v_exp_f32 v38, v38\n"
  "v_mfma_f32_16x16x32_bf16 a[4:7], a[200:203], v[64:67], a[4:7]\n"
  "v_mfma_f32_16x16x32_bf16 a[4:7], a[204:207], v[80:83], a[4:7]\n"
  "v_exp_f32 v39, v39\n"
  "v_mfma_f32_16x16x32_bf16 a[36:39], a[200:203], v[68:71], a[36:39]\n"
  "ds_read_b128 a[148:151], v245 offset:5120\n"
  "v_cvt_pk_bf16_f32 v117, v38, v39\n"
  "v_mfma_f32_16x16x32_bf16 a[36:39], a[204:207], v[84:87], a[36:39]\n"
  "buffer_load_dwordx4 v244, s[44:47], s97 offen offset:1024 sc1 lds\n"
  "v_mfma_f32_16x16x32_bf16 a[68:71], a[200:203], v[72:75], a[68:71]\n"
  "v_exp_f32 v40, v40\n"
  "v_mfma_f32_16x16x32_bf16 a[68:71], a[204:207], v[88:91], a[68:71]\n"
  "v_exp_f32 v41, v41\n"
  "v_mfma_f32_16x16x32_bf16 a[100:103], a[200:203], v[76:79], a[100:103]\n"
  "ds_read_b128 a[152:155], v245 offset:6144\n"
  "v_cvt_pk_bf16_f32 v120, v40, v41\n"
  "v_mfma_f32_16x16x32_bf16 a[100:103], a[204:207], v[92:95], a[100:103]\n"
  "v_exp_f32 v42, v42\n"
  "v_mfma_f32_16x16x32_bf16 a[8:11], a[208:211], v[64:67], a[8:11]\n"
  "v_exp_f32 v43, v43\n"
  "v_mfma_f32_16x16x32_bf16 a[8:11], a[212:215], v[80:83], a[8:11]\n"
  "ds_read_b128 a[156:159], v245 offset:7168\n"
  "v_mfma_f32_16x16x32_bf16 a[40:43], a[208:211], v[68:71], a[40:43]\n"
  "v_cvt_pk_bf16_f32 v121, v42, v43\n"
  "v_mfma_f32_16x16x32_bf16 a[40:43], a[212:215], v[84:87], a[40:43]\n"
  "v_exp_f32 v44, v44\n"
  "v_mfma_f32_16x16x32_bf16 a[72:75], a[208:211], v[72:75], a[72:75]\n"
  "v_exp_f32 v45, v45\n"
  "v_mfma_f32_16x16x32_bf16 a[72:75], a[212:215], v[88:91], a[72:75]\n"
  "ds_read_b128 a[160:163], v245 offset:8192\n"
  "v_mfma_f32_16x16x32_bf16 a[104:107], a[208:211], v[76:79], a[104:107]\n"
  "v_cvt_pk_bf16_f32 v124, v44, v45\n"
  "v_mfma_f32_16x16x32_bf16 a[104:107], a[212:215], v[92:95], a[104:107]\n"
  "v_exp_f32 v46, v46\n"
  "v_mfma_f32_16x16x32_bf16 a[12:15], a[216:219], v[64:67], a[12:15]\n"
  "v_exp_f32 v47, v47\n"
  "v_mfma_f32_16x16x32_bf16 a[12:15], a[220:223], v[80:83], a[12:15]\n"
  "ds_read_b128 a[164:167], v245 offset:9216\n"
  "v_mfma_f32_16x16x32_bf16 a[44:47], a[216:219], v[68:71], a[44:47]\n"
  "buffer_load_dwordx4 v244, s[44:47], s97 offen offset:2048 sc1 lds\n"
  "v_mfma_f32_16x16x32_bf16 a[44:47], a[220:223], v[84:87], a[44:47]\n"
  "v_cvt_pk_bf16_f32 v125, v46, v47\n"
  "v_mfma_f32_16x16x32_bf16 a[76:79], a[216:219], v[72:75], a[76:79]\n"
  "v_exp_f32 v48, v48\n"
  "v_mfma_f32_16x16x32_bf16 a[76:79], a[220:223], v[88:91], a[76:79]\n"
  "v_exp_f32 v49, v49\n"
  "v_mfma_f32_16x16x32_bf16 a[108:111], a[216:219], v[76:79], a[108:111]\n"
  "ds_read_b128 a[168:171], v245 offset:10240\n"
  "v_cvt_pk_bf16_f32 v114, v48, v49\n"
  "v_mfma_f32_16x16x32_bf16 a[108:111], a[220:223], v[92:95], a[108:111]\n"
  "v_exp_f32 v50, v50\n"
  "v_mfma_f32_16x16x32_bf16 a[16:19], a[224:227], v[64:67], a[16:19]\n"
  "v_exp_f32 v51, v51\n"
  "v_mfma_f32_16x16x32_bf16 a[16:19], a[228:231], v[80:83], a[16:19]\n"
  "ds_read_b128 a[172:175], v245 offset:11264\n"
  "v_mfma_f32_16x16x32_bf16 a[48:51], a[224:227], v[68:71], a[48:51]\n"
  "v_cvt_pk_bf16_f32 v115, v50, v51\n"
  "v_mfma_f32_16x16x32_bf16 a[48:51], a[228:231], v[84:87], a[48:51]\n"
  "v_exp_f32 v52, v52\n"
  "v_mfma_f32_16x16x32_bf16 a[80:83], a[224:227], v[72:75], a[80:83]\n"
  "v_exp_f32 v53, v53\n"
  "v_mfma_f32_16x16x32_bf16 a[80:83], a[228:231], v[88:91], a[80:83]\n"
  "ds_read_b128 a[176:179], v245 offset:12288\n"
  "v_mfma_f32_16x16x32_bf16 a[112:115], a[224:227], v[76:79], a[112:115]\n"
  "v_cvt_pk_bf16_f32 v118, v52, v53\n"
  "v_mfma_f32_16x16x32_bf16 a[112:115], a[228:231], v[92:95], a[112:115]\n"
  "v_exp_f32 v54, v54\n"
  "v_mfma_f32_16x16x32_bf16 a[20:23], a[232:235], v[64:67], a[20:23]\n"
  "v_exp_f32 v55, v55\n"
  "v_mfma_f32_16x16x32_bf16 a[20:23], a[236:239], v[80:83], a[20:23]\n"
  "ds_read_b128 a[180:183], v245 offset:13312\n"
  "v_cvt_pk_bf16_f32 v119, v54, v55\n"
  "v_mfma_f32_16x16x32_bf16 a[52:55], a[232:235], v[68:71], a[52:55]\n"
  "buffer_load_dwordx4 v244, s[44:47], s97 offen offset:3072 sc1 lds\n"
  "v_mfma_f32_16x16x32_bf16 a[52:55], a[236:239], v[84:87], a[52:55]\n"
  "v_exp_f32 v56, v56\n"
  "v_mfma_f32_16x16x32_bf16 a[84:87], a[232:235], v[72:75], a[84:87]\n"
  "v_exp_f32 v57, v57\n"
  "v_mfma_f32_16x16x32_bf16 a[84:87], a[236:239], v[88:91], a[84:87]\n"
  "ds_read_b128 a[184:187], v245 offset:14336\n"
  "v_cvt_pk_bf16_f32 v122, v56, v57\n"
  "v_mfma_f32_16x16x32_bf16 a[116:119], a[232:235], v[76:79], a[116:119]\n"
  "v_exp_f32 v58, v58\n"
  "v_mfma_f32_16x16x32_bf16 a[116:119], a[236:239], v[92:95], a[116:119]\n"
  "v_mfma_f32_16x16x32_bf16 a[24:27], a[240:243], v[64:67], a[24:27]\n"
  "v_exp_f32 v59, v59\n"
  "v_mfma_f32_16x16x32_bf16 a[24:27], a[244:247], v[80:83], a[24:27]\n"
  "ds_read_b128 a[188:191], v245 offset:15360\n"
  "v_cvt_pk_bf16_f32 v123, v58, v59\n"
  "v_mfma_f32_16x16x32_bf16 a[56:59], a[240:243], v[68:71], a[56:59]\n"
  "buffer_load_dword v253, v251, s[36:39], 0 offen\n"
  "v_mfma_f32_16x16x32_bf16 a[56:59], a[244:247], v[84:87], a[56:59]\n"
  "v_exp_f32 v60, v60\n"
  "v_mfma_f32_16x16x32_bf16 a[88:91], a[240:243], v[72:75], a[88:91]\n"
  "v_exp_f32 v61, v61\n"
  "v_mfma_f32_16x16x32_bf16 a[88:91], a[244:247], v[88:91], a[88:91]\n"
  "v_cvt_pk_bf16_f32 v126, v60, v61\n"
  "v_mfma_f32_16x16x32_bf16 a[120:123], a[240:243], v[76:79], a[120:123]\n"
  "v_exp_f32 v62, v62\n"
  "v_mfma_f32_16x16x32_bf16 a[120:123], a[244:247], v[92:95], a[120:123]\n"
  "v_mfma_f32_16x16x32_bf16 a[28:31], a[248:251], v[64:67], a[28:31]\n"
  "v_exp_f32 v63, v63\n"
  "v_mfma_f32_16x16x32_bf16 a[28:31], a[252:255], v[80:83], a[28:31]\n"
  "v_cvt_pk_bf16_f32 v127, v62, v63\n"
  "v_mfma_f32_16x16x32_bf16 a[60:63], a[248:251], v[68:71], a[60:63]\n"
  "mk_tp7:\n"
  "s_cbranch_execz mk_tp11\n"
  "v_mfma_f32_16x16x32_bf16 a[60:63], a[252:255], v[84:87], a[60:63]\n"
  "v_mfma_f32_16x16x32_bf16 a[92:95], a[248:251], v[72:75], a[92:95]\n"
  "s_cmp_eq_u32 s62, 1\n"
  "v_mfma_f32_16x16x32_bf16 a[92:95], a[252:255], v[88:91], a[92:95]\n"
  "v_mfma_f32_16x16x32_bf16 a[124:127], a[248:251], v[76:79], a[124:127]\n"
  "v_mfma_f32_16x16x32_bf16 a[124:127], a[252:255], v[92:95], a[124:127]\n"
  "s_waitcnt vmcnt(9) lgkmcnt(0)\n"
  "s_barrier\n"
  "s_cbranch_scc1 mk_fixA\n"
  "mk_LA2:\n"
  "v_mfma_f32_16x16x32_bf16 v[4:7], a[128:131], v[144:147], v[196:199]\n"
  "s_add_u32 s96, s96, 0x8000\n"
  "ds_read_b128 a[192:195], v245 offset:49152\n"
  "v_mfma_f32_16x16x32_bf16 v[4:7], a[132:135], v[148:151], v[4:7]\n"
  "s_add_u32 s98, s98, 0x8000\n"
  "ds_read_b128 a[196:199], v245 offset:50176\n"
  "v_mfma_f32_16x16x32_bf16 v[4:7], a[136:139], v[152:155], v[4:7]\n"
  "s_and_b32 s98, s98, 0x1ffff\n"
  "v_mfma_f32_16x16x32_bf16 v[4:7], a[140:143], v[156:159], v[4:7]\n"
  "s_mov_b32 m0, s98\n"
  "v_mfma_f32_16x16x32_bf16 v[8:11], a[128:131], v[160:163], v[200:203]\n"
  "buffer_load_dwordx4 v244, s[44:47], s96 offen offset:0 sc1 lds\n"
  "v_mfma_f32_16x16x32_bf16 v[8:11], a[132:135], v[164:167], v[8:11]\n"
  "ds_read_b128 a[200:203], v245 offset:51200\n"
  "v_mfma_f32_16x16x32_bf16 v[8:11], a[136:139], v[168:171], v[8:11]\n"
  "v_exp_f32 v4, v4\n"
  "v_mfma_f32_16x16x32_bf16 v[8:11], a[140:143], v[172:175], v[8:11]\n"
  "v_exp_f32 v5, v5\n"
  "v_mfma_f32_16x16x32_bf16 v[12:15], a[128:131], v[176:179], v[204:207]\n"
  "v_cvt_pk_bf16_f32 v68, v4, v5\n"
  "v_mfma_f32_16x16x32_bf16 v[12:15], a[132:135], v[180:183], v[12:15]\n"
  "v_exp_f32 v6, v6\n"
  "v_mfma_f32_16x16x32_bf16 v[12:15], a[136:139], v[184:187], v[12:15]\n"
  "v_exp_f32 v7, v7\n"
  "v_mfma_f32_16x16x32_bf16 v[12:15], a[140:143], v[188:191], v[12:15]\n"
  "ds_read_b128 a[204:207], v245 offset:52224\n"
  "v_mfma_f32_16x16x32_bf16 v[0:3], a[128:131], v[128:131], v[208:211]\n"
  "v_cvt_pk_bf16_f32 v69, v6, v7\n"
  "v_mfma_f32_16x16x32_bf16 v[0:3], a[132:135], v[132:135], v[0:3]\n"
  "v_exp_f32 v8, v8\n"
  "v_mfma_f32_16x16x32_bf16 v[0:3], a[136:139], v[136:139], v[0:3]\n"
  "v_exp_f32 v9, v9\n"
  "v_mfma_f32_16x16x32_bf16 v[0:3], a[140:143], v[140:143], v[0:3]\n"
  "ds_read_b128 a[208:211], v245 offset:53248\n"
  "v_mfma_f32_16x16x32_bf16 v[20:23], a[144:147], v[144:147], v[196:199]\n"
  "v_cvt_pk_bf16_f32 v72, v8, v9\n"
  "v_mfma_f32_16x16x32_bf16 v[20:23], a[148:151], v[148:151], v[20:23]\n"
  "s_mov_b64 s[36:37], s[10:11]\n"
  "v_exp_f32 v10, v10\n"
  "v_mfma_f32_16x16x32_bf16 v[20:23], a[152:155], v[152:155], v[20:23]\n"
  "v_exp_f32 v11, v11\n"
  "v_mfma_f32_16x16x32_bf16 v[20:23], a[156:159], v[156:159], v[20:23]\n"
  "s_mov_b64 s[10:11], s[12:13]\n"
  "ds_read_b128 a[212:215], v245 offset:54272\n"
  "v_mfma_f32_16x16x32_bf16 v[24:27], a[144:147], v[160:163], v[200:203]\n"
  "buffer_load_dwordx4 v244, s[44:47], s96 offen offset:1024 sc1 lds\n"
  "v_mfma_f32_16x16x32_bf16 v[24:27], a[148:151], v[164:167], v[24:27]\n"
  "s_lshr_b32 s14, s14, 1\n"
  "ds_read_b128 a[216:219], v245 offset:55296\n"
  "v_cvt_pk_bf16_f32 v73, v10, v11\n"
  "v_mfma_f32_16x16x32_bf16 v[24:27], a[152:155], v[168:171], v[24:27]\n"
  "v_exp_f32 v12, v12\n"
  "v_mfma_f32_16x16x32_bf16 v[24:27], a[156:159], v[172:175], v[24:27]\n"
  "s_and_b32 s38, s14, 0x2000\n"
  "v_exp_f32 v13, v13\n"
  "v_mfma_f32_16x16x32_bf16 v[28:31], a[144:147], v[176:179], v[204:207]\n"
  "v_cvt_pk_bf16_f32 v76, v12, v13\n"
  "v_mfma_f32_16x16x32_bf16 v[28:31], a[148:151], v[180:183], v[28:31]\n"
  "v_exp_f32 v14, v14\n"
  "v_mfma_f32_16x16x32_bf16 v[28:31], a[152:155], v[184:187], v[28:31]\n"
  "v_exp_f32 v15, v15\n"
  "v_mfma_f32_16x16x32_bf16 v[28:31], a[156:159], v[188:191], v[28:31]\n"
  "ds_read_b128 a[220:223], v245 offset:56320\n"
  "v_cvt_pk_bf16_f32 v77, v14, v15\n"
  "v_mfma_f32_16x16x32_bf16 v[16:19], a[144:147], v[128:131], v[212:215]\n"
  "v_exp_f32 v0, v0\n"
  "v_mfma_f32_16x16x32_bf16 v[16:19], a[148:151], v[132:135], v[16:19]\n"
  "ds_read_b128 a[224:227], v245 offset:57344\n"
  "v_mfma_f32_16x16x32_bf16 v[16:19], a[152:155], v[136:139], v[16:19]\n"
  "v_exp_f32 v1, v1\n"
  "v_mfma_f32_16x16x32_bf16 v[16:19], a[156:159], v[140:143], v[16:19]\n"
  "v_cvt_pk_bf16_f32 v64, v0, v1\n"
  "v_mfma_f32_16x16x32_bf16 v[36:39], a[160:163], v[144:147], v[196:199]\n"
  "v_exp_f32 v2, v2\n"
  "v_mfma_f32_16x16x32_bf16 v[36:39], a[164:167], v[148:151], v[36:39]\n"
  "ds_read_b128 a[228:231], v245 offset:58368\n"
  "v_mfma_f32_16x16x32_bf16 v[36:39], a[168:171], v[152:155], v[36:39]\n"
  "v_exp_f32 v3, v3\n"
  "v_mfma_f32_16x16x32_bf16 v[36:39], a[172:175], v[156:159], v[36:39]\n"
  "v_cvt_pk_bf16_f32 v65, v2, v3\n"
  "v_mfma_f32_16x16x32_bf16 v[40:43], a[160:163], v[160:163], v[200:203]\n"
  "buffer_load_dwordx4 v244, s[44:47], s96 offen offset:2048 sc1 lds\n"
  "v_mfma_f32_16x16x32_bf16 v[40:43], a[164:167], v[164:167], v[40:43]\n"
  "v_exp_f32 v20, v20\n"
  "v_mfma_f32_16x16x32_bf16 v[40:43], a[168:171], v[168:171], v[40:43]\n"
  "v_exp_f32 v21, v21\n"
  "v_mfma_f32_16x16x32_bf16 v[40:43], a[172:175], v[172:175], v[40:43]\n"
  "ds_read_b128 a[232:235], v245 offset:59392\n"
  "v_cvt_pk_bf16_f32 v70, v20, v21\n"
  "v_mfma_f32_16x16x32_bf16 v[44:47], a[160:163], v[176:179], v[204:207]\n"
  "v_exp_f32 v22, v22\n"
  "v_mfma_f32_16x16x32_bf16 v[44:47], a[164:167], v[180:183], v[44:47]\n"
  "ds_read_b128 a[236:239], v245 offset:60416\n"
  "v_mfma_f32_16x16x32_bf16 v[44:47], a[168:171], v[184:187], v[44:47]\n"
  "v_exp_f32 v23, v23\n"
  "v_mfma_f32_16x16x32_bf16 v[44:47], a[172:175], v[188:191], v[44:47]\n"
  "v_cvt_pk_bf16_f32 v71, v22, v23\n"
  "v_mfma_f32_16x16x32_bf16 v[32:35], a[160:163], v[128:131], v[216:219]\n"
  "v_exp_f32 v24, v24\n"
  "v_mfma_f32_16x16x32_bf16 v[32:35], a[164:167], v[132:135], v[32:35]\n"
  "ds_read_b128 a[240:243], v245 offset:61440\n"
  "v_mfma_f32_16x16x32_bf16 v[32:35], a[168:171], v[136:139], v[32:35]\n"
  "v_exp_f32 v25, v25\n"
  "v_mfma_f32_16x16x32_bf16 v[32:35], a[172:175], v[140:143], v[32:35]\n"
  "v_cvt_pk_bf16_f32 v74, v24, v25\n"
  "v_mfma_f32_16x16x32_bf16 v[52:55], a[176:179], v[144:147], v[196:199]\n"
  "v_exp_f32 v26, v26\n"
  "v_mfma_f32_16x16x32_bf16 v[52:55], a[180:183], v[148:151], v[52:55]\n"
  "v_exp_f32 v27, v27\n"
  "v_mfma_f32_16x16x32_bf16 v[52:55], a[184:187], v[152:155], v[52:55]\n"
  "ds_read_b128 a[244:247], v245 offset:62464\n"
  "v_mfma_f32_16x16x32_bf16 v[52:55], a[188:191], v[156:159], v[52:55]\n"
  "buffer_load_dwordx4 v244, s[44:47], s96 offen offset:3072 sc1 lds\n"
  "v_mfma_f32_16x16x32_bf16 v[56:59], a[176:179], v[160:163], v[200:203]\n"
  "v_cvt_pk_bf16_f32 v75, v26, v27\n"
  "v_mfma_f32_16x16x32_bf16 v[56:59], a[180:183], v[164:167], v[56:59]\n"
  "v_exp_f32 v28, v28\n"
  "v_mfma_f32_16x16x32_bf16 v[56:59], a[184:187], v[168:171], v[56:59]\n"
  "v_exp_f32 v29, v29\n"
  "v_mfma_f32_16x16x32_bf16 v[56:59], a[188:191], v[172:175], v[56:59]\n"
  "ds_read_b128 a[248:251], v245 offset:63488\n"
  "v_cvt_pk_bf16_f32 v78, v28, v29\n"
  "v_mfma_f32_16x16x32_bf16 v[60:63], a[176:179], v[176:179], v[204:207]\n"
  "v_exp_f32 v30, v30\n"
  "v_mfma_f32_16x16x32_bf16 v[60:63], a[180:183], v[180:183], v[60:63]\n"
  "v_exp_f32 v31, v31\n"
  "v_mfma_f32_16x16x32_bf16 v[60:63], a[184:187], v[184:187], v[60:63]\n"
  "ds_read_b128 a[252:255], v245 offset:64512\n"
  "v_mfma_f32_16x16x32_bf16 v[60:63], a[188:191], v[188:191], v[60:63]\n"
  "v_cvt_pk_bf16_f32 v79, v30, v31\n"
  "v_mfma_f32_16x16x32_bf16 v[48:51], a[176:179], v[128:131], v[220:223]\n"
  "v_exp_f32 v16, v16\n"
  "v_mfma_f32_16x16x32_bf16 v[48:51], a[180:183], v[132:135], v[48:51]\n"
  "v_exp_f32 v17, v17\n"
  "v_mfma_f32_16x16x32_bf16 v[48:51], a[184:187], v[136:139], v[48:51]\n"
  "s_sub_u32 s97, s96, 0xc000\n"
  "ds_read_b128 a[128:131], v245 offset:32768\n"
  "v_mfma_f32_16x16x32_bf16 v[48:51], a[188:191], v[140:143], v[48:51]\n"
  "s_add_u32 m0, s98, 0x4000\n"
  "v_cvt_pk_bf16_f32 v66, v16, v17\n"
  "v_mfma_f32_16x16x32_bf16 v[224:227], v[240:243], v[96:99], v[224:227]\n"
  "v_exp_f32 v18, v18\n"
  "v_mfma_f32_16x16x32_bf16 v[224:227], v[240:243], v[112:115], v[224:227]\n"
  "v_exp_f32 v19, v19\n"
  "v_mfma_f32_16x16x32_bf16 v[228:231], v[240:243], v[100:103], v[228:231]\n"
  "ds_read_b128 a[132:135], v245 offset:33792\n"
  "v_mfma_f32_16x16x32_bf16 v[228:231], v[240:243], v[116:119], v[228:231]\n"
  "buffer_load_dwordx4 v244, s[44:47], s97 offen offset:0 sc1 lds\n"
  "v_mfma_f32_16x16x32_bf16 v[232:235], v[240:243], v[104:107], v[232:235]\n"
  "v_cvt_pk_bf16_f32 v67, v18, v19\n"
  "v_mfma_f32_16x16x32_bf16 v[232:235], v[240:243], v[120:123], v[232:235]\n"
  "v_exp_f32 v36, v36\n"
  "v_mfma_f32_16x16x32_bf16 v[236:239], v[240:243], v[108:111], v[236:239]\n"
  "v_exp_f32 v37, v37\n"
  "v_mfma_f32_16x16x32_bf16 v[236:239], v[240:243], v[124:127], v[236:239]\n"
  "s_waitcnt lgkmcnt(1)\n"
  "ds_read_b128 a[136:139], v245 offset:34816\n"
  "v_cvt_pk_bf16_f32 v84, v36, v37\n"
  "v_mfma_f32_16x16x32_bf16 a[0:3], a[192:195], v[96:99], a[0:3]\n"
  "v_exp_f32 v38, v38\n"
  "v_mfma_f32_16x16x32_bf16 a[0:3], a[196:199], v[112:115], a[0:3]\n"
  "v_exp_f32 v39, v39\n"
  "v_mfma_f32_16x16x32_bf16 a[32:35], a[192:195], v[100:103], a[32:35]\n"
  "ds_read_b128 a[140:143], v245 offset:35840\n"
  "v_cvt_pk_bf16_f32 v85, v38, v39\n"
  "v_mfma_f32_16x16x32_bf16 a[32:35], a[196:199], v[116:119], a[32:35]\n"
  "v_exp_f32 v40, v40\n"
  "v_mfma_f32_16x16x32_bf16 a[64:67], a[192:195], v[104:107], a[64:67]\n"
  "v_mfma_f32_16x16x32_bf16 a[64:67], a[196:199], v[120:123], a[64:67]\n"
  "v_exp_f32 v41, v41\n"
  "v_mfma_f32_16x16x32_bf16 a[96:99], a[192:195], v[108:111], a[96:99]\n"
  "ds_read_b128 a[144:147], v245 offset:36864\n"
  "v_cvt_pk_bf16_f32 v88, v40, v41\n"
  "v_mfma_f32_16x16x32_bf16 a[96:99], a[196:199], v[124:127], a[96:99]\n"
  "v_exp_f32 v42, v42\n"
  "v_mfma_f32_16x16x32_bf16 a[4:7], a[200:203], v[96:99], a[4:7]\n"
  "v_mfma_f32_16x16x32_bf16 a[4:7], a[204:207], v[112:115], a[4:7]\n"
  "v_exp_f32 v43, v43\n"
  "v_mfma_f32_16x16x32_bf16 a[36:39], a[200:203], v[100:103], a[36:39]\n"
  "ds_read_b128 a[148:151], v245 offset:37888\n"
  "v_cvt_pk_bf16_f32 v89, v42, v43\n"
  "v_mfma_f32_16x16x32_bf16 a[36:39], a[204:207], v[116:119], a[36:39]\n"
  "buffer_load_dwordx4 v244, s[44:47], s97 offen offset:1024 sc1 lds\n"
  "v_mfma_f32_16x16x32_bf16 a[68:71], a[200:203], v[104:107], a[68:71]\n"
  "v_exp_f32 v44, v44\n"
  "v_mfma_f32_16x16x32_bf16 a[68:71], a[204:207], v[120:123], a[68:71]\n"
  "v_exp_f32 v45, v45\n"
  "v_mfma_f32_16x16x32_bf16 a[100:103], a[200:203], v[108:111], a[100:103]\n"
  "ds_read_b128 a[152:155], v245 offset:38912\n"
  "v_cvt_pk_bf16_f32 v92, v44, v45\n"
  "v_mfma_f32_16x16x32_bf16 a[100:103], a[204:207], v[124:127], a[100:103]\n"
  "v_exp_f32 v46, v46\n"
  "v_mfma_f32_16x16x32_bf16 a[8:11], a[208:211], v[96:99], a[8:11]\n"
  "v_exp_f32 v47, v47\n"
  "v_mfma_f32_16x16x32_bf16 a[8:11], a[212:215], v[112:115], a[8:11]\n"
  "ds_read_b128 a[156:159], v245 offset:39936\n"
  "v_mfma_f32_16x16x32_bf16 a[40:43], a[208:211], v[100:103], a[40:43]\n"
  "v_cvt_pk_bf16_f32 v93, v46, v47\n"
  "v_mfma_f32_16x16x32_bf16 a[40:43], a[212:215], v[116:119], a[40:43]\n"
  "v_exp_f32 v32, v32\n"
  "v_mfma_f32_16x16x32_bf16 a[72:75], a[208:211], v[104:107], a[72:75]\n"
  "v_exp_f32 v33, v33\n"
  "v_mfma_f32_16x16x32_bf16 a[72:75], a[212:215], v[120:123], a[72:75]\n"
  "ds_read_b128 a[160:163], v245 offset:40960\n"
  "v_mfma_f32_16x16x32_bf16 a[104:107], a[208:211], v[108:111], a[104:107]\n"
  "v_cvt_pk_bf16_f32 v80, v32, v33\n"
  "v_mfma_f32_16x16x32_bf16 a[104:107], a[212:215], v[124:127], a[104:107]\n"
  "v_exp_f32 v34, v34\n"
  "v_mfma_f32_16x16x32_bf16 a[12:15], a[216:219], v[96:99], a[12:15]\n"
  "v_exp_f32 v35, v35\n"
  "v_mfma_f32_16x16x32_bf16 a[12:15], a[220:223], v[112:115], a[12:15]\n"
  "ds_read_b128 a[164:167], v245 offset:41984\n"
  "v_mfma_f32_16x16x32_bf16 a[44:47], a[216:219], v[100:103], a[44:47]\n"
  "buffer_load_dwordx4 v244, s[44:47], s97 offen offset:2048 sc1 lds\n"
  "v_mfma_f32_16x16x32_bf16 a[44:47], a[220:223], v[116:119], a[44:47]\n"
  "v_cvt_pk_bf16_f32 v81, v34, v35\n"
  "v_mfma_f32_16x16x32_bf16 a[76:79], a[216:219], v[104:107], a[76:79]\n"
  "v_exp_f32 v52, v52\n"
  "v_mfma_f32_16x16x32_bf16 a[76:79], a[220:223], v[120:123], a[76:79]\n"
  "v_exp_f32 v53, v53\n"
  "v_mfma_f32_16x16x32_bf16 a[108:111], a[216:219], v[108:111], a[108:111]\n"
  "ds_read_b128 a[168:171], v245 offset:43008\n"
  "v_cvt_pk_bf16_f32 v86, v52, v53\n"
  "v_mfma_f32_16x16x32_bf16 a[108:111], a[220:223], v[124:127], a[108:111]\n"
  "v_exp_f32 v54, v54\n"
  "v_mfma_f32_16x16x32_bf16 a[16:19], a[224:227], v[96:99], a[16:19]\n"
  "v_exp_f32 v55, v55\n"
  "v_mfma_f32_16x16x32_bf16 a[16:19], a[228:231], v[112:115], a[16:19]\n"
  "ds_read_b128 a[172:175], v245 offset:44032\n"
  "v_mfma_f32_16x16x32_bf16 a[48:51], a[224:227], v[100:103], a[48:51]\n"
  "v_cvt_pk_bf16_f32 v87, v54, v55\n"
  "v_mfma_f32_16x16x32_bf16 a[48:51], a[228:231], v[116:119], a[48:51]\n"
  "v_exp_f32 v56, v56\n"
  "v_mfma_f32_16x16x32_bf16 a[80:83], a[224:227], v[104:107], a[80:83]\n"
  "v_exp_f32 v57, v57\n"
  "v_mfma_f32_16x16x32_bf16 a[80:83], a[228:231], v[120:123], a[80:83]\n"
  "ds_read_b128 a[176:179], v245 offset:45056\n"
  "v_mfma_f32_16x16x32_bf16 a[112:115], a[224:227], v[108:111], a[112:115]\n"
  "v_cvt_pk_bf16_f32 v90, v56, v57\n"
  "v_mfma_f32_16x16x32_bf16 a[112:115], a[228:231], v[124:127], a[112:115]\n"
  "v_exp_f32 v58, v58\n"
  "v_mfma_f32_16x16x32_bf16 a[20:23], a[232:235], v[96:99], a[20:23]\n"
  "v_exp_f32 v59, v59\n"
  "v_mfma_f32_16x16x32_bf16 a[20:23], a[236:239], v[112:115], a[20:23]\n"
  "ds_read_b128 a[180:183], v245 offset:46080\n"
  "v_cvt_pk_bf16_f32 v91, v58, v59\n"
  "v_mfma_f32_16x16x32_bf16 a[52:55], a[232:235], v[100:103], a[52:55]\n"
  "buffer_load_dwordx4 v244, s[44:47], s97 offen offset:3072 sc1 lds\n"
  "v_mfma_f32_16x16x32_bf16 a[52:55], a[236:239], v[116:119], a[52:55]\n"
  "v_exp_f32 v60, v60\n"
  "v_mfma_f32_16x16x32_bf16 a[84:87], a[232:235], v[104:107], a[84:87]\n"
  "v_exp_f32 v61, v61\n"
  "v_mfma_f32_16x16x32_bf16 a[84:87], a[236:239], v[120:123], a[84:87]\n"
  "ds_read_b128 a[184:187], v245 offset:47104\n"
  "v_cvt_pk_bf16_f32 v94, v60, v61\n"
  "v_mfma_f32_16x16x32_bf16 a[116:119], a[232:235], v[108:111], a[116:119]\n"
  "v_exp_f32 v62, v62\n"
  "v_mfma_f32_16x16x32_bf16 a[116:119], a[236:239], v[124:127], a[116:119]\n"
  "v_mfma_f32_16x16x32_bf16 a[24:27], a[240:243], v[96:99], a[24:27]\n"
  "v_exp_f32 v63, v63\n"
  "v_mfma_f32_16x16x32_bf16 a[24:27], a[244:247], v[112:115], a[24:27]\n"
  "ds_read_b128 a[188:191], v245 offset:48128\n"
  "v_cvt_pk_bf16_f32 v95, v62, v63\n"
  "v_mfma_f32_16x16x32_bf16 a[56:59], a[240:243], v[100:103], a[56:59]\n"
  "v_exp_f32 v48, v48\n"
  "v_mfma_f32_16x16x32_bf16 a[56:59], a[244:247], v[116:119], a[56:59]\n"
  "v_mfma_f32_16x16x32_bf16 a[88:91], a[240:243], v[104:107], a[88:91]\n"
  "v_exp_f32 v49, v49\n"
  "v_mfma_f32_16x16x32_bf16 a[88:91], a[244:247], v[120:123], a[88:91]\n"
  "v_cvt_pk_bf16_f32 v82, v48, v49\n"
  "v_xor_b32 v245, 0x10000, v245\n"
  "v_mfma_f32_16x16x32_bf16 a[120:123], a[240:243], v[108:111], a[120:123]\n"
  "v_exp_f32 v50, v50\n"
  "v_mfma_f32_16x16x32_bf16 a[120:123], a[244:247], v[124:127], a[120:123]\n"
  "v_mfma_f32_16x16x32_bf16 a[28:31], a[248:251], v[96:99], a[28:31]\n"
  "v_exp_f32 v51, v51\n"
  "v_mfma_f32_16x16x32_bf16 a[28:31], a[252:255], v[112:115], a[28:31]\n"
  "v_cvt_pk_bf16_f32 v83, v50, v51\n"
  "v_mfma_f32_16x16x32_bf16 a[60:63], a[248:251], v[100:103], a[60:63]\n"
  "v_mfma_f32_16x16x32_bf16 a[60:63], a[252:255], v[116:119], a[60:63]\n"
  "s_sub_u32 s62, s62, 1\n"
  "v_mfma_f32_16x16x32_bf16 a[92:95], a[248:251], v[104:107], a[92:95]\n"
  "s_cmp_lg_u32 s62, 0\n"
  "v_mfma_f32_16x16x32_bf16 a[92:95], a[252:255], v[120:123], a[92:95]\n"
  "v_mfma_f32_16x16x32_bf16 a[124:127], a[248:251], v[108:111], a[124:127]\n"
  "v_mfma_f32_16x16x32_bf16 a[124:127], a[252:255], v[124:127], a[124:127]\n"
  "s_waitcnt vmcnt(8) lgkmcnt(0)\n"
  "s_barrier\n"
  "s_cbranch_scc1 mk_LA1\n"
  "mk_T3:\n"
  "mk_tp8:\n"
  "s_cbranch_execz mk_tp12\n"
  "v_mfma_f32_16x16x32_bf16 v[8:11], a[128:131], v[160:163], v[200:203]\n"
  "s_mov_b32 s33, 0x6638000\n"
  "v_cndmask_b32_e64 v208, v196, v246, s[64:65]\n"
  "v_cndmask_b32_e64 v209, v196, v246, s[66:67]\n"
  "v_mfma_f32_16x16x32_bf16 v[8:11], a[132:135], v[164:167], v[8:11]\n"
  "s_add_u32 s96, s96, 0x8000\n"
  "buffer_load_dword v252, v251, s[48:51], s33 offen sc1\n"
  "v_mfma_f32_16x16x32_bf16 v[8:11], a[136:139], v[168:171], v[8:11]\n"
  "s_add_u32 s98, s98, 0x8000\n"
  "v_cndmask_b32_e64 v210, v196, v246, s[68:69]\n"
  "v_cndmask_b32_e64 v211, v196, v246, s[70:71]\n"
  "v_mfma_f32_16x16x32_bf16 v[8:11], a[140:143], v[172:175], v[8:11]\n"
  "s_and_b32 s98, s98, 0x1ffff\n"
  "v_cndmask_b32_e64 v212, v196, v246, s[72:73]\n"
  "v_cndmask_b32_e64 v213, v196, v246, s[74:75]\n"
  "v_mfma_f32_16x16x32_bf16 v[12:15], a[128:131], v[176:179], v[204:207]\n"
  "s_mov_b32 m0, s98\n"
  "v_cndmask_b32_e64 v214, v196, v246, s[76:77]\n"
  "v_cndmask_b32_e64 v215, v196, v246, s[78:79]\n"
  "v_mfma_f32_16x16x32_bf16 v[12:15], a[132:135], v[180:183], v[12:15]\n"
  "v_cndmask_b32_e64 v216, v196, v246, s[80:81]\n"
  "v_cndmask_b32_e64 v217, v196, v246, s[82:83]\n"
  "v_mfma_f32_16x16x32_bf16 v[12:15], a[136:139], v[184:187], v[12:15]\n"
  "v_cndmask_b32_e64 v218, v196, v246, s[84:85]\n"
  "v_cndmask_b32_e64 v219, v196, v246, s[86:87]\n"
  "v_mfma_f32_16x16x32_bf16 v[12:15], a[140:143], v[188:191], v[12:15]\n"
  "v_cndmask_b32_e64 v220, v196, v246, s[88:89]\n"
  "v_cndmask_b32_e64 v221, v196, v246, s[90:91]\n"
  "v_mfma_f32_16x16x32_bf16 v[4:7], a[128:131], v[144:147], v[208:211]\n"
  "v_cndmask_b32_e64 v222, v196, v246, s[92:93]\n"
  "v_cndmask_b32_e64 v223, v196, v246, s[94:95]\n"
  "v_mfma_f32_16x16x32_bf16 v[4:7], a[132:135], v[148:151], v[4:7]\n"
  "ds_read_b128 a[192:195], v245 offset:16384\n"
  "ds_read_b128 a[196:199], v245 offset:17408\n"
  "v_mfma_f32_16x16x32_bf16 v[4:7], a[136:139], v[152:155], v[4:7]\n"
  "buffer_load_dwordx4 v244, s[44:47], s96 offen offset:0 sc1 lds\n"
  "v_mfma_f32_16x16x32_bf16 v[4:7], a[140:143], v[156:159], v[4:7]\n"
  "v_exp_f32 v8, v8\n"
  "v_mfma_f32_16x16x32_bf16 v[24:27], a[144:147], v[160:163], v[200:203]\n"
  "ds_read_b128 a[200:203], v245 offset:18432\n"
  "ds_read_b128 a[204:207], v245 offset:19456\n"
  "v_mfma_f32_16x16x32_bf16 v[24:27], a[148:151], v[164:167], v[24:27]\n"
  "v_exp_f32 v9, v9\n"
  "v_mfma_f32_16x16x32_bf16 v[24:27], a[152:155], v[168:171], v[24:27]\n"
  "ds_read_b128 a[208:211], v245 offset:20480\n"
  "v_cvt_pk_bf16_f32 v104, v8, v9\n"
  "v_mfma_f32_16x16x32_bf16 v[24:27], a[156:159], v[172:175], v[24:27]\n"
  "v_exp_f32 v10, v10\n"
  "v_mfma_f32_16x16x32_bf16 v[28:31], a[144:147], v[176:179], v[204:207]\n"
  "v_exp_f32 v11, v11\n"
  "v_mfma_f32_16x16x32_bf16 v[28:31], a[148:151], v[180:183], v[28:31]\n"
  "ds_read_b128 a[212:215], v245 offset:21504\n"
  "v_cvt_pk_bf16_f32 v105, v10, v11\n"
  "v_mfma_f32_16x16x32_bf16 v[28:31], a[152:155], v[184:187], v[28:31]\n"
  "v_exp_f32 v12, v12\n"
  "v_mfma_f32_16x16x32_bf16 v[28:31], a[156:159], v[188:191], v[28:31]\n"
  "v_exp_f32 v13, v13\n"
  "v_mfma_f32_16x16x32_bf16 v[20:23], a[144:147], v[144:147], v[212:215]\n"
  "buffer_load_dwordx4 v244, s[44:47], s96 offen offset:1024 sc1 lds\n"
  "v_mfma_f32_16x16x32_bf16 v[20:23], a[148:151], v[148:151], v[20:23]\n"
  "ds_read_b128 a[216:219], v245 offset:22528\n"
  "v_cvt_pk_bf16_f32 v108, v12, v13\n"
  "v_mfma_f32_16x16x32_bf16 v[20:23], a[152:155], v[152:155], v[20:23]\n"
  "v_exp_f32 v14, v14\n"
  "v_mfma_f32_16x16x32_bf16 v[20:23], a[156:159], v[156:159], v[20:23]\n"
  "v_exp_f32 v15, v15\n"
  "v_mfma_f32_16x16x32_bf16 v[40:43], a[160:163], v[160:163], v[200:203]\n"
  "ds_read_b128 a[220:223], v245 offset:23552\n"
  "v_cvt_pk_bf16_f32 v109, v14, v15\n"
  "v_mfma_f32_16x16x32_bf16 v[40:43], a[164:167], v[164:167], v[40:43]\n"
  "v_exp_f32 v4, v4\n"
  "v_mfma_f32_16x16x32_bf16 v[40:43], a[168:171], v[168:171], v[40:43]\n"
  "v_exp_f32 v5, v5\n"
  "v_mfma_f32_16x16x32_bf16 v[40:43], a[172:175], v[172:175], v[40:43]\n"
  "ds_read_b128 a[224:227], v245 offset:24576\n"
  "v_cvt_pk_bf16_f32 v100, v4, v5\n"
  "v_mfma_f32_16x16x32_bf16 v[44:47], a[160:163], v[176:179], v[204:207]\n"
  "v_exp_f32 v6, v6\n"
  "v_mfma_f32_16x16x32_bf16 v[44:47], a[164:167], v[180:183], v[44:47]\n"
  "v_exp_f32 v7, v7\n"
  "v_mfma_f32_16x16x32_bf16 v[44:47], a[168:171], v[184:187], v[44:47]\n"
  "ds_read_b128 a[228:231], v245 offset:25600\n"
  "v_mfma_f32_16x16x32_bf16 v[44:47], a[172:175], v[188:191], v[44:47]\n"
  "v_cvt_pk_bf16_f32 v101, v6, v7\n"
  "v_mfma_f32_16x16x32_bf16 v[36:39], a[160:163], v[144:147], v[216:219]\n"
  "buffer_load_dwordx4 v244, s[44:47], s96 offen offset:2048 sc1 lds\n"
  "v_mfma_f32_16x16x32_bf16 v[36:39], a[164:167], v[148:151], v[36:39]\n"
  "v_exp_f32 v24, v24\n"
  "v_mfma_f32_16x16x32_bf16 v[36:39], a[168:171], v[152:155], v[36:39]\n"
  "ds_read_b128 a[232:235], v245 offset:26624\n"
  "v_mfma_f32_16x16x32_bf16 v[36:39], a[172:175], v[156:159], v[36:39]\n"
  "v_exp_f32 v25, v25\n"
  "v_mfma_f32_16x16x32_bf16 v[56:59], a[176:179], v[160:163], v[200:203]\n"
  "ds_read_b128 a[236:239], v245 offset:27648\n"
  "v_cvt_pk_bf16_f32 v106, v24, v25\n"
  "v_mfma_f32_16x16x32_bf16 v[56:59], a[180:183], v[164:167], v[56:59]\n"
  "v_exp_f32 v26, v26\n"
  "v_mfma_f32_16x16x32_bf16 v[56:59], a[184:187], v[168:171], v[56:59]\n"
  "v_mfma_f32_16x16x32_bf16 v[56:59], a[188:191], v[172:175], v[56:59]\n"
  "v_exp_f32 v27, v27\n"
  "v_mfma_f32_16x16x32_bf16 v[60:63], a[176:179], v[176:179], v[204:207]\n"
  "ds_read_b128 a[240:243], v245 offset:28672\n"
  "v_cvt_pk_bf16_f32 v107, v26, v27\n"
  "v_mfma_f32_16x16x32_bf16 v[60:63], a[180:183], v[180:183], v[60:63]\n"
  "v_exp_f32 v28, v28\n"
  "v_mfma_f32_16x16x32_bf16 v[60:63], a[184:187], v[184:187], v[60:63]\n"
  "v_mfma_f32_16x16x32_bf16 v[60:63], a[188:191], v[188:191], v[60:63]\n"
  "v_exp_f32 v29, v29\n"
  "v_mfma_f32_16x16x32_bf16 v[52:55], a[176:179], v[144:147], v[220:223]\n"
  "ds_read_b128 a[244:247], v245 offset:29696\n"
  "v_mfma_f32_16x16x32_bf16 v[52:55], a[180:183], v[148:151], v[52:55]\n"
  "buffer_load_dwordx4 v244, s[44:47], s96 offen offset:3072 sc1 lds\n"
  "v_mfma_f32_16x16x32_bf16 v[52:55], a[184:187], v[152:155], v[52:55]\n"
  "ds_read_b128 a[248:251], v245 offset:30720\n"
  "v_cvt_pk_bf16_f32 v110, v28, v29\n"
  "v_mfma_f32_16x16x32_bf16 v[52:55], a[188:191], v[156:159], v[52:55]\n"
  "v_exp_f32 v30, v30\n"
  "v_mfma_f32_16x16x32_bf16 v[224:227], v[240:243], v[64:67], v[224:227]\n"
  "s_cmp_eq_u32 s52, 7\n"
  "s_cselect_b32 s63, 1.0, -1.0\n"
  "v_exp_f32 v31, v31\n"
  "v_mfma_f32_16x16x32_bf16 v[224:227], v[240:243], v[80:83], v[224:227]\n"
  "v_cvt_pk_bf16_f32 v111, v30, v31\n"
  "v_mul_f32 v248, s63, v246\n"
  "v_mfma_f32_16x16x32_bf16 v[228:231], v[240:243], v[68:71], v[228:231]\n"
  "v_exp_f32 v20, v20\n"
  "v_mfma_f32_16x16x32_bf16 v[228:231], v[240:243], v[84:87], v[228:231]\n"
  "ds_read_b128 a[252:255], v245 offset:31744\n"
  "v_min_f32 v248, v248, v200\n"
  "v_mfma_f32_16x16x32_bf16 v[232:235], v[240:243], v[72:75], v[232:235]\n"
  "v_exp_f32 v21, v21\n"
  "v_mfma_f32_16x16x32_bf16 v[232:235], v[240:243], v[88:91], v[232:235]\n"
  "ds_read_b128 a[128:131], v245 offset:0\n"
  "v_cndmask_b32_e64 v208, v200, v248, s[64:65]\n"
  "v_mfma_f32_16x16x32_bf16 v[236:239], v[240:243], v[76:79], v[236:239]\n"
  "s_sub_u32 s97, s96, 0xc000\n"
  "v_cvt_pk_bf16_f32 v102, v20, v21\n"
  "v_cndmask_b32_e64 v209, v200, v248, s[66:67]\n"
  "v_mfma_f32_16x16x32_bf16 v[236:239], v[240:243], v[92:95], v[236:239]\n"
  "s_waitcnt lgkmcnt(2)\n"
  "s_add_u32 m0, s98, 0x4000\n"
  "v_exp_f32 v22, v22\n"
  "v_mfma_f32_16x16x32_bf16 a[0:3], a[192:195], v[64:67], a[0:3]\n"
  "ds_read_b128 a[132:135], v245 offset:1024\n"
  "v_cndmask_b32_e64 v210, v200, v248, s[68:69]\n"
  "v_mfma_f32_16x16x32_bf16 a[0:3], a[196:199], v[80:83], a[0:3]\n"
  "v_exp_f32 v23, v23\n"
  "v_mfma_f32_16x16x32_bf16 a[32:35], a[192:195], v[68:71], a[32:35]\n"
  "v_cvt_pk_bf16_f32 v103, v22, v23\n"
  "v_cndmask_b32_e64 v211, v200, v248, s[70:71]\n"
  "v_mfma_f32_16x16x32_bf16 a[32:35], a[196:199], v[84:87], a[32:35]\n"
  "buffer_load_dwordx4 v244, s[44:47], s97 offen offset:0 sc1 lds\n"
  "v_mfma_f32_16x16x32_bf16 a[64:67], a[192:195], v[72:75], a[64:67]\n"
  "v_exp_f32 v40, v40\n"
  "v_mfma_f32_16x16x32_bf16 a[64:67], a[196:199], v[88:91], a[64:67]\n"
  "ds_read_b128 a[136:139], v245 offset:2048\n"
  "v_cndmask_b32_e64 v212, v200, v248, s[72:73]\n"
  "v_mfma_f32_16x16x32_bf16 a[96:99], a[192:195], v[76:79], a[96:99]\n"
  "v_exp_f32 v41, v41\n"
  "v_mfma_f32_16x16x32_bf16 a[96:99], a[196:199], v[92:95], a[96:99]\n"
  "ds_read_b128 a[140:143], v245 offset:3072\n"
  "v_cvt_pk_bf16_f32 v120, v40, v41\n"
  "v_mfma_f32_16x16x32_bf16 a[4:7], a[200:203], v[64:67], a[4:7]\n"
  "v_exp_f32 v42, v42\n"
  "v_mfma_f32_16x16x32_bf16 a[4:7], a[204:207], v[80:83], a[4:7]\n"
  "v_cndmask_b32_e64 v213, v200, v248, s[74:75]\n"
  "v_cndmask_b32_e64 v214, v200, v248, s[76:77]\n"
  "v_mfma_f32_16x16x32_bf16 a[36:39], a[200:203], v[68:71], a[36:39]\n"
  "v_exp_f32 v43, v43\n"
  "v_mfma_f32_16x16x32_bf16 a[36:39], a[204:207], v[84:87], a[36:39]\n"
  "ds_read_b128 a[144:147], v245 offset:4096\n"
  "v_cvt_pk_bf16_f32 v121, v42, v43\n"
  "v_mfma_f32_16x16x32_bf16 a[68:71], a[200:203], v[72:75], a[68:71]\n"
  "v_exp_f32 v44, v44\n"
  "v_mfma_f32_16x16x32_bf16 a[68:71], a[204:207], v[88:91], a[68:71]\n"
  "v_cndmask_b32_e64 v215, v200, v248, s[78:79]\n"
  "v_cndmask_b32_e64 v216, v200, v248, s[80:81]\n"
  "v_mfma_f32_16x16x32_bf16 a[100:103], a[200:203], v[76:79], a[100:103]\n"
  "v_exp_f32 v45, v45\n"
  "v_mfma_f32_16x16x32_bf16 a[100:103], a[204:207], v[92:95], a[100:103]\n"
  "ds_read_b128 a[148:151], v245 offset:5120\n"
  "v_cndmask_b32_e64 v217, v200, v248, s[82:83]\n"
  "v_mfma_f32_16x16x32_bf16 a[8:11], a[208:211], v[64:67], a[8:11]\n"
  "v_cvt_pk_bf16_f32 v124, v44, v45\n"
  "v_cndmask_b32_e64 v218, v200, v248, s[84:85]\n"
  "v_mfma_f32_16x16x32_bf16 a[8:11], a[212:215], v[80:83], a[8:11]\n"
  "buffer_load_dwordx4 v244, s[44:47], s97 offen offset:1024 sc1 lds\n"
  "v_mfma_f32_16x16x32_bf16 a[40:43], a[208:211], v[68:71], a[40:43]\n"
  "v_exp_f32 v46, v46\n"
  "v_mfma_f32_16x16x32_bf16 a[40:43], a[212:215], v[84:87], a[40:43]\n"
  "ds_read_b128 a[152:155], v245 offset:6144\n"
  "v_cndmask_b32_e64 v219, v200, v248, s[86:87]\n"
  "v_mfma_f32_16x16x32_bf16 a[72:75], a[208:211], v[72:75], a[72:75]\n"
  "v_exp_f32 v47, v47\n"
  "v_mfma_f32_16x16x32_bf16 a[72:75], a[212:215], v[88:91], a[72:75]\n"
  "ds_read_b128 a[156:159], v245 offset:7168\n"
  "v_cvt_pk_bf16_f32 v125, v46, v47\n"
  "v_mfma_f32_16x16x32_bf16 a[104:107], a[208:211], v[76:79], a[104:107]\n"
  "v_exp_f32 v36, v36\n"
  "v_mfma_f32_16x16x32_bf16 a[104:107], a[212:215], v[92:95], a[104:107]\n"
  "v_exp_f32 v37, v37\n"
  "v_mfma_f32_16x16x32_bf16 a[12:15], a[216:219], v[64:67], a[12:15]\n"
  "v_cndmask_b32_e64 v220, v200, v248, s[88:89]\n"
  "v_cndmask_b32_e64 v221, v200, v248, s[90:91]\n"
  "v_mfma_f32_16x16x32_bf16 a[12:15], a[220:223], v[80:83], a[12:15]\n"
  "ds_read_b128 a[160:163], v245 offset:8192\n"
  "v_cvt_pk_bf16_f32 v116, v36, v37\n"
  "v_mfma_f32_16x16x32_bf16 a[44:47], a[216:219], v[68:71], a[44:47]\n"
  "v_exp_f32 v38, v38\n"
  "v_mfma_f32_16x16x32_bf16 a[44:47], a[220:223], v[84:87], a[44:47]\n"
  "v_cndmask_b32_e64 v222, v200, v248, s[92:93]\n"
  "v_cndmask_b32_e64 v223, v200, v248, s[94:95]\n"
  "v_mfma_f32_16x16x32_bf16 a[76:79], a[216:219], v[72:75], a[76:79]\n"
  "v_exp_f32 v39, v39\n"
  "v_mfma_f32_16x16x32_bf16 a[76:79], a[220:223], v[88:91], a[76:79]\n"
  "ds_read_b128 a[164:167], v245 offset:9216\n"
  "v_cvt_pk_bf16_f32 v117, v38, v39\n"
  "v_mfma_f32_16x16x32_bf16 a[108:111], a[216:219], v[76:79], a[108:111]\n"
  "buffer_load_dwordx4 v244, s[44:47], s97 offen offset:2048 sc1 lds\n"
  "v_mfma_f32_16x16x32_bf16 a[108:111], a[220:223], v[92:95], a[108:111]\n"
  "v_exp_f32 v56, v56\n"
  "v_mfma_f32_16x16x32_bf16 a[16:19], a[224:227], v[64:67], a[16:19]\n"
  "ds_read_b128 a[168:171], v245 offset:10240\n"
  "v_cndmask_b32_e64 v0, v204, v246, s[64:65]\n"
  "v_mfma_f32_16x16x32_bf16 a[16:19], a[228:231], v[80:83], a[16:19]\n"
  "v_exp_f32 v57, v57\n"
  "v_mfma_f32_16x16x32_bf16 a[48:51], a[224:227], v[68:71], a[48:51]\n"
  "v_cvt_pk_bf16_f32 v122, v56, v57\n"
  "v_cndmask_b32_e64 v1, v204, v246, s[66:67]\n"
  "v_mfma_f32_16x16x32_bf16 a[48:51], a[228:231], v[84:87], a[48:51]\n"
  "v_exp_f32 v58, v58\n"
  "v_mfma_f32_16x16x32_bf16 a[80:83], a[224:227], v[72:75], a[80:83]\n"
  "ds_read_b128 a[172:175], v245 offset:11264\n"
  "v_cndmask_b32_e64 v2, v204, v246, s[68:69]\n"
  "v_mfma_f32_16x16x32_bf16 a[80:83], a[228:231], v[88:91], a[80:83]\n"
  "v_exp_f32 v59, v59\n"
  "v_mfma_f32_16x16x32_bf16 a[112:115], a[224:227], v[76:79], a[112:115]\n"
  "v_cndmask_b32_e64 v3, v204, v246, s[70:71]\n"
  "v_cndmask_b32_e64 v16, v204, v246, s[72:73]\n"
  "v_mfma_f32_16x16x32_bf16 a[112:115], a[228:231], v[92:95], a[112:115]\n"
  "ds_read_b128 a[176:179], v245 offset:12288\n"
  "v_cvt_pk_bf16_f32 v123, v58, v59\n"
  "v_mfma_f32_16x16x32_bf16 a[20:23], a[232:235], v[64:67], a[20:23]\n"
  "v_exp_f32 v60, v60\n"
  "v_mfma_f32_16x16x32_bf16 a[20:23], a[236:239], v[80:83], a[20:23]\n"
  "v_cndmask_b32_e64 v17, v204, v246, s[74:75]\n"
  "v_cndmask_b32_e64 v18, v204, v246, s[76:77]\n"
  "v_mfma_f32_16x16x32_bf16 a[52:55], a[232:235], v[68:71], a[52:55]\n"
  "v_exp_f32 v61, v61\n"
  "v_mfma_f32_16x16x32_bf16 a[52:55], a[236:239], v[84:87], a[52:55]\n"
  "ds_read_b128 a[180:183], v245 offset:13312\n"
  "v_cvt_pk_bf16_f32 v126, v60, v61\n"
  "v_mfma_f32_16x16x32_bf16 a[84:87], a[232:235], v[72:75], a[84:87]\n"
  "buffer_load_dwordx4 v244, s[44:47], s97 offen offset:3072 sc1 lds\n"
  "v_mfma_f32_16x16x32_bf16 a[84:87], a[236:239], v[88:91], a[84:87]\n"
  "v_exp_f32 v62, v62\n"
  "v_mfma_f32_16x16x32_bf16 a[116:119], a[232:235], v[76:79], a[116:119]\n"
  "v_exp_f32 v63, v63\n"
  "v_mfma_f32_16x16x32_bf16 a[116:119], a[236:239], v[92:95], a[116:119]\n"
  "ds_read_b128 a[184:187], v245 offset:14336\n"
  "v_cndmask_b32_e64 v19, v204, v246, s[78:79]\n"
  "v_mfma_f32_16x16x32_bf16 a[24:27], a[240:243], v[64:67], a[24:27]\n"
  "v_cvt_pk_bf16_f32 v127, v62, v63\n"
  "v_cndmask_b32_e64 v32, v204, v246, s[80:81]\n"
  "v_mfma_f32_16x16x32_bf16 a[24:27], a[244:247], v[80:83], a[24:27]\n"
  "v_exp_f32 v52, v52\n"
  "v_mfma_f32_16x16x32_bf16 a[56:59], a[240:243], v[68:71], a[56:59]\n"
  "ds_read_b128 a[188:191], v245 offset:15360\n"
  "v_cndmask_b32_e64 v33, v204, v246, s[82:83]\n"
  "v_mfma_f32_16x16x32_bf16 a[56:59], a[244:247], v[84:87], a[56:59]\n"
  "v_exp_f32 v53, v53\n"
  "v_mfma_f32_16x16x32_bf16 a[88:91], a[240:243], v[72:75], a[88:91]\n"
  "v_cvt_pk_bf16_f32 v118, v52, v53\n"
  "v_cndmask_b32_e64 v34, v204, v246, s[84:85]\n"
  "v_mfma_f32_16x16x32_bf16 a[88:91], a[244:247], v[88:91], a[88:91]\n"
  "v_exp_f32 v54, v54\n"
  "v_mfma_f32_16x16x32_bf16 a[120:123], a[240:243], v[76:79], a[120:123]\n"
  "mk_tp9:\n"
  "s_cbranch_execz mk_tp13\n"
  "v_cndmask_b32_e64 v35, v204, v246, s[86:87]\n"
  "v_cndmask_b32_e64 v48, v204, v246, s[88:89]\n"
  "v_mfma_f32_16x16x32_bf16 a[120:123], a[244:247], v[92:95], a[120:123]\n"
  "v_exp_f32 v55, v55\n"
  "v_mfma_f32_16x16x32_bf16 a[28:31], a[248:251], v[64:67], a[28:31]\n"
  "s_waitcnt lgkmcnt(1)\n"
  "v_cndmask_b32_e64 v49, v204, v246, s[90:91]\n"
  "v_cndmask_b32_e64 v50, v204, v246, s[92:93]\n"
  "v_mfma_f32_16x16x32_bf16 a[28:31], a[252:255], v[80:83], a[28:31]\n"
  "v_cvt_pk_bf16_f32 v119, v54, v55\n"
  "v_cndmask_b32_e64 v51, v204, v246, s[94:95]\n"
  "v_mfma_f32_16x16x32_bf16 a[60:63], a[248:251], v[68:71], a[60:63]\n"
  "v_mfma_f32_16x16x32_bf16 a[60:63], a[252:255], v[84:87], a[60:63]\n"
  "v_mfma_f32_16x16x32_bf16 a[92:95], a[248:251], v[72:75], a[92:95]\n"
  "v_mfma_f32_16x16x32_bf16 a[92:95], a[252:255], v[88:91], a[92:95]\n"
  "v_mfma_f32_16x16x32_bf16 a[124:127], a[248:251], v[76:79], a[124:127]\n"
  "v_mfma_f32_16x16x32_bf16 a[124:127], a[252:255], v[92:95], a[124:127]\n"
  "s_waitcnt vmcnt(8) lgkmcnt(0)\n"
  "s_barrier\n"
  "v_mfma_f32_16x16x32_bf16 v[12:15], a[128:131], v[176:179], v[204:207]\n"
  "s_add_u32 s96, s96, 0x8000\n"
  "v_and_b32 v36, 0xff, v252\n"
  "v_cmp_ne_u32 vcc, 8, v36\n"
  "v_mfma_f32_16x16x32_bf16 v[12:15], a[132:135], v[180:183], v[12:15]\n"
  "s_and_b32 vcc_lo, vcc_lo, 0xfffff\n"
  "s_cbranch_scc1 mk_spinP\n"
  "mk_spinP_b:\n"
  "ds_read_b128 a[192:195], v245 offset:49152\n"
  "v_cmp_lt_u32 vcc, 0x4ff, v252\n"
  "v_mfma_f32_16x16x32_bf16 v[12:15], a[136:139], v[184:187], v[12:15]\n"
  "s_mov_b32 s33, vcc_lo\n"
  "ds_read_b128 a[196:199], v245 offset:50176\n"
  "ds_read_b128 a[200:203], v245 offset:51200\n"
  "v_mfma_f32_16x16x32_bf16 v[12:15], a[140:143], v[188:191], v[12:15]\n"
  "s_add_u32 s98, s98, 0x8000\n"
  "v_lshrrev_b32 v253, 8, v244\n"
  "v_lshlrev_b32 v250, 5, v253\n"
  "v_mfma_f32_16x16x32_bf16 v[8:11], a[128:131], v[160:163], v[208:211]\n"
  "s_and_b32 s98, s98, 0x1ffff\n"
  "v_and_b32 v249, 0xf0, v244\n"
  "v_xor_b32 v249, v249, v250\n"
  "v_mfma_f32_16x16x32_bf16 v[8:11], a[132:135], v[164:167], v[8:11]\n"
  "s_mov_b32 m0, s98\n"
  "ds_read_b128 a[204:207], v245 offset:52224\n"
  "v_lshl_add_u32 v249, v253, 10, v249\n"
  "v_mfma_f32_16x16x32_bf16 v[8:11], a[136:139], v[168:171], v[8:11]\n"
  "s_lshr_b32 s61, s53, 2\n"
  "buffer_load_dwordx4 v244, s[44:47], s96 offen offset:0 sc1 lds\n"
  "v_mfma_f32_16x16x32_bf16 v[8:11], a[140:143], v[172:175], v[8:11]\n"
  "s_lshl_b32 s61, s61, 19\n"
  "v_exp_f32 v12, v12\n"
  "v_mfma_f32_16x16x32_bf16 v[28:31], a[144:147], v[176:179], v[204:207]\n"
  "s_and_b32 s59, s53, 3\n"
  "v_exp_f32 v13, v13\n"
  "v_mfma_f32_16x16x32_bf16 v[28:31], a[148:151], v[180:183], v[28:31]\n"
  "s_lshl_b32 s59, s59, 8\n"
  "ds_read_b128 a[208:211], v245 offset:53248\n"
  "v_cvt_pk_bf16_f32 v76, v12, v13\n"
  "v_mfma_f32_16x16x32_bf16 v[28:31], a[152:155], v[184:187], v[28:31]\n"
  "s_add_u32 s61, s61, s59\n"
  "buffer_load_dwordx4 v244, s[44:47], s96 offen offset:1024 sc1 lds\n"
  "v_mfma_f32_16x16x32_bf16 v[28:31], a[156:159], v[188:191], v[28:31]\n"
  "s_add_u32 s36, s18, s61\n"
  "s_addc_u32 s37, s19, 0\n"
  "v_exp_f32 v14, v14\n"
  "v_mfma_f32_16x16x32_bf16 v[24:27], a[144:147], v[160:163], v[212:215]\n"
  "s_sub_u32 s38, 0x400000, s61\n"
  "ds_read_b128 a[212:215], v245 offset:54272\n"
  "ds_read_b128 a[216:219], v245 offset:55296\n"
  "v_mfma_f32_16x16x32_bf16 v[24:27], a[148:151], v[164:167], v[24:27]\n"
  "s_bfe_i32 s35, s53, 0x10004\n"
  "v_exp_f32 v15, v15\n"
  "v_mfma_f32_16x16x32_bf16 v[24:27], a[152:155], v[168:171], v[24:27]\n"
  "s_xor_b32 s35, s35, s33\n"
  "ds_read_b128 a[220:223], v245 offset:56320\n"
  "v_cvt_pk_bf16_f32 v77, v14, v15\n"
  "v_mfma_f32_16x16x32_bf16 v[24:27], a[156:159], v[172:175], v[24:27]\n"
  "s_lshr_b32 s63, s40, 4\n"
  "s_lshr_b32 s63, s35, s63\n"
  "buffer_load_dwordx4 v244, s[44:47], s96 offen offset:2048 sc1 lds\n"
  "v_mfma_f32_16x16x32_bf16 v[44:47], a[160:163], v[176:179], v[204:207]\n"
  "s_and_b32 s63, s63, 1\n"
  "v_exp_f32 v8, v8\n"
  "v_mfma_f32_16x16x32_bf16 v[44:47], a[164:167], v[180:183], v[44:47]\n"
  "s_lshl4_add_u32 s40, s40, s63\n"
  "v_exp_f32 v9, v9\n"
  "v_mfma_f32_16x16x32_bf16 v[44:47], a[168:171], v[184:187], v[44:47]\n"
  "s_lshl_b32 s40, s40, 13\n"
  "ds_read_b128 a[224:227], v245 offset:57344\n"
  "v_cvt_pk_bf16_f32 v72, v8, v9\n"
  "v_mfma_f32_16x16x32_bf16 v[44:47], a[172:175], v[188:191], v[44:47]\n"
  "s_add_u32 s40, s40, 0x4000000\n"
  "v_exp_f32 v10, v10\n"
  "v_mfma_f32_16x16x32_bf16 v[40:43], a[160:163], v[160:163], v[216:219]\n"
  "s_lshr_b32 s63, s41, 4\n"
  "s_lshr_b32 s63, s35, s63\n"
  "buffer_load_dwordx4 v244, s[44:47], s96 offen offset:3072 sc1 lds\n"
  "v_mfma_f32_16x16x32_bf16 v[40:43], a[164:167], v[164:167], v[40:43]\n"
  "s_sub_u32 s97, s96, 0xc000\n"
  "ds_read_b128 a[228:231], v245 offset:58368\n"
  "v_mov_b32 v253, %1\n"
  "v_mfma_f32_16x16x32_bf16 v[40:43], a[168:171], v[168:171], v[40:43]\n"
  "s_add_u32 m0, s98, 0x4000\n"
  "v_exp_f32 v11, v11\n"
  "v_mfma_f32_16x16x32_bf16 v[40:43], a[172:175], v[172:175], v[40:43]\n"
  "s_and_b32 s63, s63, 1\n"
  "ds_read_b128 a[232:235], v245 offset:59392\n"
  "v_cvt_pk_bf16_f32 v73, v10, v11\n"
  "v_mfma_f32_16x16x32_bf16 v[60:63], a[176:179], v[176:179], v[204:207]\n"
  "s_lshl4_add_u32 s41, s41, s63\n"
  "v_exp_f32 v28, v28\n"
  "v_mfma_f32_16x16x32_bf16 v[60:63], a[180:183], v[180:183], v[60:63]\n"
  "s_lshl_b32 s41, s41, 13\n"
  "ds_read_b128 a[236:239], v245 offset:60416\n"
  "v_lshl_add_u32 v249, v253, 14, v249\n"
  "v_mfma_f32_16x16x32_bf16 v[60:63], a[184:187], v[184:187], v[60:63]\n"
  "s_add_u32 s41, s41, 0x4000000\n"
  "buffer_load_dwordx4 v244, s[44:47], s97 offen offset:0 sc1 lds\n"
  "v_mfma_f32_16x16x32_bf16 v[60:63], a[188:191], v[188:191], v[60:63]\n"
  "s_lshr_b32 s63, s42, 4\n"
  "s_lshr_b32 s63, s35, s63\n"
  "v_exp_f32 v29, v29\n"
  "v_mfma_f32_16x16x32_bf16 v[56:59], a[176:179], v[160:163], v[220:223]\n"
  "s_and_b32 s63, s63, 1\n"
  "ds_read_b128 a[240:243], v245 offset:61440\n"
  "v_cvt_pk_bf16_f32 v78, v28, v29\n"
  "v_mfma_f32_16x16x32_bf16 v[56:59], a[180:183], v[164:167], v[56:59]\n"
  "s_lshl4_add_u32 s42, s42, s63\n"
  "v_exp_f32 v30, v30\n"
  "v_mfma_f32_16x16x32_bf16 v[56:59], a[184:187], v[168:171], v[56:59]\n"
  "s_lshl_b32 s42, s42, 13\n"
  "ds_read_b128 a[244:247], v245 offset:62464\n"
  "v_xor_b32 v250, 0x80, v249\n"
  "v_mfma_f32_16x16x32_bf16 v[56:59], a[188:191], v[172:175], v[56:59]\n"
  "s_add_u32 s42, s42, 0x4000000\n"
  "v_exp_f32 v31, v31\n"
  "v_mfma_f32_16x16x32_bf16 v[228:231], v[240:243], v[100:103], v[228:231]\n"
  "s_lshr_b32 s63, s43, 4\n"
  "s_lshr_b32 s63, s35, s63\n"
  "buffer_load_dwordx4 v244, s[44:47], s97 offen offset:1024 sc1 lds\n"
  "v_mfma_f32_16x16x32_bf16 v[228:231], v[240:243], v[116:119], v[228:231]\n"
  "s_and_b32 s63, s63, 1\n"
  "ds_read_b128 a[248:251], v245 offset:63488\n"
  "v_cvt_pk_bf16_f32 v79, v30, v31\n"
  "v_mfma_f32_16x16x32_bf16 v[232:235], v[240:243], v[104:107], v[232:235]\n"
  "s_lshl4_add_u32 s43, s43, s63\n"
  "v_exp_f32 v24, v24\n"
  "v_mfma_f32_16x16x32_bf16 v[232:235], v[240:243], v[120:123], v[232:235]\n"
  "s_lshl_b32 s43, s43, 13\n"
  "ds_read_b128 a[252:255], v245 offset:64512\n"
  "v_add_u32 v250, 3072, v250\n"
  "v_mfma_f32_16x16x32_bf16 v[236:239], v[240:243], v[108:111], v[236:239]\n"
  "s_add_u32 s43, s43, 0x4000000\n"
  "v_exp_f32 v25, v25\n"
  "v_mfma_f32_16x16x32_bf16 v[236:239], v[240:243], v[124:127], v[236:239]\n"
  "s_waitcnt lgkmcnt(3)\n"
  "s_lshl_b32 s11, s52, 5\n"
  "buffer_load_dwordx4 v244, s[44:47], s97 offen offset:2048 sc1 lds\n"
  "v_mfma_f32_16x16x32_bf16 a[32:35], a[192:195], v[100:103], a[32:35]\n"
  "s_and_b32 s63, s53, 28\n"
  "ds_read_b128 a[128:131], v245 offset:32768\n"
  "v_cvt_pk_bf16_f32 v74, v24, v25\n"
  "v_mfma_f32_16x16x32_bf16 a[32:35], a[196:199], v[116:119], a[32:35]\n"
  "s_add_u32 s11, s11, s63\n"
  "v_exp_f32 v26, v26\n"
  "v_mfma_f32_16x16x32_bf16 a[64:67], a[192:195], v[104:107], a[64:67]\n"
  "s_add_u32 s11, s11, %1\n"
  "ds_read_b128 a[132:135], v245 offset:33792\n"
  "v_add_u32 v251, 6144, v249\n"
  "v_mfma_f32_16x16x32_bf16 a[64:67], a[196:199], v[120:123], a[64:67]\n"
  "s_lshl_b32 s11, s11, 8\n"
  "v_exp_f32 v27, v27\n"
  "v_mfma_f32_16x16x32_bf16 a[96:99], a[192:195], v[108:111], a[96:99]\n"
  "s_lshl_b32 s63, s11, 1\n"
  "buffer_load_dwordx4 v244, s[44:47], s97 offen offset:3072 sc1 lds\n"
  "v_mfma_f32_16x16x32_bf16 a[96:99], a[196:199], v[124:127], a[96:99]\n"
  "s_xor_b32 s63, s63, s11\n"
  "ds_read_b128 a[136:139], v245 offset:34816\n"
  "v_cvt_pk_bf16_f32 v75, v26, v27\n"
  "v_mfma_f32_16x16x32_bf16 a[36:39], a[200:203], v[100:103], a[36:39]\n"
  "s_bfe_i32 s63, s63, 0x1000d\n"
  "v_exp_f32 v44, v44\n"
  "v_mfma_f32_16x16x32_bf16 a[36:39], a[204:207], v[116:119], a[36:39]\n"
  "s_and_b32 s63, s63, 0x3000\n"
  "ds_read_b128 a[140:143], v245 offset:35840\n"
  "v_add_u32 v252, 6144, v250\n"
  "v_mfma_f32_16x16x32_bf16 a[68:71], a[200:203], v[104:107], a[68:71]\n"
  "s_add_u32 s55, s53, 0\n"
  "v_exp_f32 v45, v45\n"
  "v_mfma_f32_16x16x32_bf16 a[68:71], a[204:207], v[120:123], a[68:71]\n"
  "s_and_b32 s55, s55, 3\n"
  "ds_read_b128 a[144:147], v245 offset:36864\n"
  "v_cvt_pk_bf16_f32 v92, v44, v45\n"
  "v_mfma_f32_16x16x32_bf16 a[100:103], a[200:203], v[108:111], a[100:103]\n"
  "s_lshl_b32 s55, s55, 17\n"
  "v_exp_f32 v46, v46\n"
  "v_mfma_f32_16x16x32_bf16 a[100:103], a[204:207], v[124:127], a[100:103]\n"
  "s_add_u32 m0, s57, 0x8000\n"
  "v_exp_f32 v47, v47\n"
  "v_mfma_f32_16x16x32_bf16 a[40:43], a[208:211], v[100:103], a[40:43]\n"
  "s_xor_b32 s11, s11, s63\n"
  "ds_read_b128 a[148:151], v245 offset:37888\n"
  "ds_read_b128 a[152:155], v245 offset:38912\n"
  "v_mfma_f32_16x16x32_bf16 a[40:43], a[212:215], v[116:119], a[40:43]\n"
  "s_lshr_b32 s63, s33, 6\n"
  "v_cvt_pk_bf16_f32 v93, v46, v47\n"
  "v_cmp_eq_u32 vcc, 0, v244\n"
  "v_mfma_f32_16x16x32_bf16 a[72:75], a[208:211], v[104:107], a[72:75]\n"
  "s_and_b32 s63, s63, 0x2000\n"
  "buffer_load_dwordx4 v249, s[36:39], s55 offen offset:0 lds\n"
  "v_mfma_f32_16x16x32_bf16 a[72:75], a[212:215], v[120:123], a[72:75]\n"
  "s_xor_b32 s11, s11, s63\n"
  "v_exp_f32 v40, v40\n"
  "v_mfma_f32_16x16x32_bf16 a[104:107], a[208:211], v[108:111], a[104:107]\n"
  "s_add_u32 s11, s11, 0x6604000\n"
  "v_exp_f32 v41, v41\n"
  "v_mfma_f32_16x16x32_bf16 a[104:107], a[212:215], v[124:127], a[104:107]\n"
  "s_getpc_b64 s[12:13]\n"
  "mk_pc2:\n"
  "s_add_u32 s12, s12, mk_T1-mk_pc2\n"
  "s_addc_u32 s13, s13, 0\n"
  "buffer_load_dwordx4 v250, s[36:39], s55 offen offset:1024 lds\n"
  "v_mfma_f32_16x16x32_bf16 a[44:47], a[216:219], v[100:103], a[44:47]\n"
  "s_mov_b32 s14, mk_end-mk_T1\n"
  "ds_read_b128 a[156:159], v245 offset:39936\n"
  "v_cvt_pk_bf16_f32 v88, v40, v41\n"
  "v_mfma_f32_16x16x32_bf16 a[44:47], a[220:223], v[116:119], a[44:47]\n"
  "s_mov_b32 s15, 0x20000\n"
  "v_exp_f32 v42, v42\n"
  "v_mfma_f32_16x16x32_bf16 a[76:79], a[216:219], v[104:107], a[76:79]\n"
  "s_lshr_b32 s63, %2, 3\n"
  "ds_read_b128 a[160:163], v245 offset:40960\n"
  "ds_read_b128 a[164:167], v245 offset:41984\n"
  "v_mfma_f32_16x16x32_bf16 a[76:79], a[220:223], v[120:123], a[76:79]\n"
  "s_sub_u32 s63, s63, 8\n"
  "s_and_b32 s63, s63, 31\n"
  "buffer_load_dwordx4 v251, s[36:39], s55 offen offset:2048 lds\n"
  "v_mfma_f32_16x16x32_bf16 a[108:111], a[216:219], v[108:111], a[108:111]\n"
  "s_lshl2_add_u32 s63, s63, %1\n"
  "v_exp_f32 v43, v43\n"
  "v_mfma_f32_16x16x32_bf16 a[108:111], a[220:223], v[124:127], a[108:111]\n"
  "s_lshl_b32 s63, s63, 12\n"
  "buffer_load_dwordx4 v252, s[36:39], s55 offen offset:3072 lds\n"
  "v_mfma_f32_16x16x32_bf16 a[48:51], a[224:227], v[100:103], a[48:51]\n"
  "s_add_u32 s55, s53, 0\n"
  "ds_read_b128 a[168:171], v245 offset:43008\n"
  "v_cvt_pk_bf16_f32 v89, v42, v43\n"
  "v_mfma_f32_16x16x32_bf16 a[48:51], a[228:231], v[116:119], a[48:51]\n"
  "s_and_b32 s55, s55, 3\n"
  "v_exp_f32 v60, v60\n"
  "v_mfma_f32_16x16x32_bf16 a[80:83], a[224:227], v[104:107], a[80:83]\n"
  "s_lshl_b32 s55, s55, 17\n"
  "v_exp_f32 v61, v61\n"
  "v_mfma_f32_16x16x32_bf16 a[80:83], a[228:231], v[120:123], a[80:83]\n"
  "s_add_u32 s55, s55, 0x10000\n"
  "ds_read_b128 a[172:175], v245 offset:44032\n"
  "v_cvt_pk_bf16_f32 v94, v60, v61\n"
  "v_mfma_f32_16x16x32_bf16 a[112:115], a[224:227], v[108:111], a[112:115]\n"
  "s_add_u32 m0, s57, 0xc000\n"
  "v_exp_f32 v62, v62\n"
  "v_mfma_f32_16x16x32_bf16 a[112:115], a[228:231], v[124:127], a[112:115]\n"
  "ds_read_b128 a[176:179], v245 offset:45056\n"
  "ds_read_b128 a[180:183], v245 offset:46080\n"
  "v_mfma_f32_16x16x32_bf16 a[52:55], a[232:235], v[100:103], a[52:55]\n"
  "v_exp_f32 v63, v63\n"
  "v_mfma_f32_16x16x32_bf16 a[52:55], a[236:239], v[116:119], a[52:55]\n"
  "buffer_load_dwordx4 v249, s[36:39], s55 offen offset:0 lds\n"
  "v_mfma_f32_16x16x32_bf16 a[84:87], a[232:235], v[104:107], a[84:87]\n"
  "ds_read_b128 a[184:187], v245 offset:47104\n"
  "v_cvt_pk_bf16_f32 v95, v62, v63\n"
  "v_mfma_f32_16x16x32_bf16 a[84:87], a[236:239], v[120:123], a[84:87]\n"
  "v_exp_f32 v56, v56\n"
  "v_mfma_f32_16x16x32_bf16 a[116:119], a[232:235], v[108:111], a[116:119]\n"
  "buffer_load_dwordx4 v250, s[36:39], s55 offen offset:1024 lds\n"
  "v_mfma_f32_16x16x32_bf16 a[116:119], a[236:239], v[124:127], a[116:119]\n"
  "v_exp_f32 v57, v57\n"
  "v_mfma_f32_16x16x32_bf16 a[56:59], a[240:243], v[100:103], a[56:59]\n"
  "s_waitcnt lgkmcnt(3)\n"
  "ds_read_b128 a[188:191], v245 offset:48128\n"
  "v_cvt_pk_bf16_f32 v90, v56, v57\n"
  "v_mfma_f32_16x16x32_bf16 a[56:59], a[244:247], v[116:119], a[56:59]\n"
  "buffer_load_dwordx4 v251, s[36:39], s55 offen offset:2048 lds\n"
  "v_mfma_f32_16x16x32_bf16 a[88:91], a[240:243], v[104:107], a[88:91]\n"
  "v_exp_f32 v58, v58\n"
  "v_mfma_f32_16x16x32_bf16 a[88:91], a[244:247], v[120:123], a[88:91]\n"
  "v_xor_b32 v245, 0x10000, v245\n"
  "v_mov_b32 v253, 0x7fff0000\n"
  "v_mfma_f32_16x16x32_bf16 a[120:123], a[240:243], v[108:111], a[120:123]\n"
  "buffer_load_dwordx4 v252, s[36:39], s55 offen offset:3072 lds\n"
  "v_mfma_f32_16x16x32_bf16 a[120:123], a[244:247], v[124:127], a[120:123]\n"
  "v_exp_f32 v59, v59\n"
  "v_mfma_f32_16x16x32_bf16 a[60:63], a[248:251], v[100:103], a[60:63]\n"
  "v_cvt_pk_bf16_f32 v91, v58, v59\n"
  "v_mov_b32 v248, 1\n"
  "v_mfma_f32_16x16x32_bf16 a[60:63], a[252:255], v[116:119], a[60:63]\n"
  "s_lshl_b32 s62, s52, 1\n"
  "v_cndmask_b32 v253, v253, v244, vcc\n"
  "v_lshl_add_u32 v37, v244, 2, s63\n"
  "v_mfma_f32_16x16x32_bf16 a[92:95], a[248:251], v[104:107], a[92:95]\n"
  "s_sub_u32 s62, 14, s62\n"
  "v_mfma_f32_16x16x32_bf16 a[92:95], a[252:255], v[120:123], a[92:95]\n"
  "v_mfma_f32_16x16x32_bf16 a[124:127], a[248:251], v[108:111], a[124:127]\n"
  "v_mfma_f32_16x16x32_bf16 a[124:127], a[252:255], v[124:127], a[124:127]\n"
  "s_waitcnt vmcnt(16) lgkmcnt(0)\n"
  "s_barrier\n"
  "v_min_f32 v192, v224, v228\n"
  "v_max_f32 v193, v224, v228\n"
  "v_cmp_u_f32_e64 s[16:17], v224, v228\n"
  "v_cmp_nle_f32 vcc, 0x2b800000, v192\n"
  "s_or_b64 s[16:17], s[16:17], vcc\n"
  "v_cmp_nge_f32 vcc, 0x5d800000, v193\n"
  "s_or_b64 s[16:17], s[16:17], vcc\n"
  "s_cselect_b32 s10, 1, 0\n"
  "s_cbranch_scc1 mk_skipA\n"
  "v_rcp_f32 v226, v224\n"
  "v_rcp_f32 v230, v228\n"
  "s_and_b32 s61, s53, 14\n"
  "s_and_b32 s63, s53, 15\n"
  "s_add_u32 s61, s61, s63\n"
  "s_lshl_b32 s61, s61, 12\n"
  "s_add_u32 s59, s40, s61\n"
  "v_accvgpr_read_b32 v192, a0\n"
  "v_accvgpr_read_b32 v193, a1\n"
  "v_accvgpr_read_b32 v194, a2\n"
  "v_accvgpr_read_b32 v195, a3\n"
  "v_accvgpr_read_b32 v196, a4\n"
  "v_accvgpr_read_b32 v197, a5\n"
  "v_accvgpr_read_b32 v198, a6\n"
  "v_accvgpr_read_b32 v199, a7\n"
  "v_pk_mul_f32 v[192:193], v[192:193], v[226:227] op_sel_hi:[1,0]\n"
  "v_pk_mul_f32 v[194:195], v[194:195], v[226:227] op_sel_hi:[1,0]\n"
  "v_pk_mul_f32 v[196:197], v[196:197], v[226:227] op_sel_hi:[1,0]\n"
  "v_pk_mul_f32 v[198:199], v[198:199], v[226:227] op_sel_hi:[1,0]\n"
  "v_cvt_pk_bf16_f32 v128, v192, v193\n"
  "v_cvt_pk_bf16_f32 v129, v194, v195\n"
  "v_cvt_pk_bf16_f32 v130, v196, v197\n"
  "v_cvt_pk_bf16_f32 v131, v198, v199\n"
  "buffer_store_dwordx4 v[128:131], v244, s[48:51], s59 offen offset:0 sc1\n"
  "v_accvgpr_read_b32 v4, a8\n"
  "v_accvgpr_read_b32 v5, a9\n"
  "v_accvgpr_read_b32 v6, a10\n"
  "v_accvgpr_read_b32 v7, a11\n"
  "v_accvgpr_read_b32 v20, a12\n"
  "v_accvgpr_read_b32 v21, a13\n"
  "v_accvgpr_read_b32 v22, a14\n"
  "v_accvgpr_read_b32 v23, a15\n"
  "v_pk_mul_f32 v[4:5], v[4:5], v[226:227] op_sel_hi:[1,0]\n"
  "v_pk_mul_f32 v[6:7], v[6:7], v[226:227] op_sel_hi:[1,0]\n"
  "v_pk_mul_f32 v[20:21], v[20:21], v[226:227] op_sel_hi:[1,0]\n"
  "v_pk_mul_f32 v[22:23], v[22:23], v[226:227] op_sel_hi:[1,0]\n"
  "v_cvt_pk_bf16_f32 v132, v4, v5\n"
  "v_cvt_pk_bf16_f32 v133, v6, v7\n"
  "v_cvt_pk_bf16_f32 v134, v20, v21\n"
  "v_cvt_pk_bf16_f32 v135, v22, v23\n"
  "buffer_store_dwordx4 v[132:135], v244, s[48:51], s59 offen offset:1024 sc1\n"
  "v_accvgpr_read_b32 v192, a16\n"
  "v_accvgpr_read_b32 v193, a17\n"
  "v_accvgpr_read_b32 v194, a18\n"
  "v_accvgpr_read_b32 v195, a19\n"
  "v_accvgpr_read_b32 v196, a20\n"
  "v_accvgpr_read_b32 v197, a21\n"
  "v_accvgpr_read_b32 v198, a22\n"
  "v_accvgpr_read_b32 v199, a23\n"
  "v_pk_mul_f32 v[192:193], v[192:193], v[226:227] op_sel_hi:[1,0]\n"
  "v_pk_mul_f32 v[194:195], v[194:195], v[226:227] op_sel_hi:[1,0]\n"
  "v_pk_mul_f32 v[196:197], v[196:197], v[226:227] op_sel_hi:[1,0]\n"
  "v_pk_mul_f32 v[198:199], v[198:199], v[226:227] op_sel_hi:[1,0]\n"
  "v_cvt_pk_bf16_f32 v136, v192, v193\n"
  "v_cvt_pk_bf16_f32 v137, v194, v195\n"
  "v_cvt_pk_bf16_f32 v138, v196, v197\n"
  "v_cvt_pk_bf16_f32 v139, v198, v199\n"
  "buffer_store_dwordx4 v[136:139], v244, s[48:51], s59 offen offset:2048 sc1\n"
  "v_accvgpr_read_b32 v4, a24\n"
  "v_accvgpr_read_b32 v5, a25\n"
  "v_accvgpr_read_b32 v6, a26\n"
  "v_accvgpr_read_b32 v7, a27\n"
  "v_accvgpr_read_b32 v20, a28\n"
  "v_accvgpr_read_b32 v21, a29\n"
  "v_accvgpr_read_b32 v22, a30\n"
  "v_accvgpr_read_b32 v23, a31\n"
  "v_pk_mul_f32 v[4:5], v[4:5], v[226:227] op_sel_hi:[1,0]\n"
  "v_pk_mul_f32 v[6:7], v[6:7], v[226:227] op_sel_hi:[1,0]\n"
  "v_pk_mul_f32 v[20:21], v[20:21], v[226:227] op_sel_hi:[1,0]\n"
  "v_pk_mul_f32 v[22:23], v[22:23], v[226:227] op_sel_hi:[1,0]\n"
  "v_cvt_pk_bf16_f32 v140, v4, v5\n"
  "v_cvt_pk_bf16_f32 v141, v6, v7\n"
  "v_cvt_pk_bf16_f32 v142, v20, v21\n"
  "v_cvt_pk_bf16_f32 v143, v22, v23\n"
  "buffer_store_dwordx4 v[140:143], v244, s[48:51], s59 offen offset:3072 sc1\n"
  "s_add_u32 s59, s41, s61\n"
  "v_accvgpr_read_b32 v192, a32\n"
  "v_accvgpr_read_b32 v193, a33\n"
  "v_accvgpr_read_b32 v194, a34\n"
  "v_accvgpr_read_b32 v195, a35\n"
  "v_accvgpr_read_b32 v196, a36\n"
  "v_accvgpr_read_b32 v197, a37\n"
  "v_accvgpr_read_b32 v198, a38\n"
  "v_accvgpr_read_b32 v199, a39\n"
  "v_pk_mul_f32 v[192:193], v[192:193], v[230:231] op_sel_hi:[1,0]\n"
  "v_pk_mul_f32 v[194:195], v[194:195], v[230:231] op_sel_hi:[1,0]\n"
  "v_pk_mul_f32 v[196:197], v[196:197], v[230:231] op_sel_hi:[1,0]\n"
  "v_pk_mul_f32 v[198:199], v[198:199], v[230:231] op_sel_hi:[1,0]\n"
  "v_cvt_pk_bf16_f32 v144, v192, v193\n"
  "v_cvt_pk_bf16_f32 v145, v194, v195\n"
  "v_cvt_pk_bf16_f32 v146, v196, v197\n"
  "v_cvt_pk_bf16_f32 v147, v198, v199\n"
  "buffer_store_dwordx4 v[144:147], v244, s[48:51], s59 offen offset:0 sc1\n"
  "v_accvgpr_read_b32 v4, a40\n"
  "v_accvgpr_read_b32 v5, a41\n"
  "v_accvgpr_read_b32 v6, a42\n"
  "v_accvgpr_read_b32 v7, a43\n"
  "v_accvgpr_read_b32 v20, a44\n"
  "v_accvgpr_read_b32 v21, a45\n"
  "v_accvgpr_read_b32 v22, a46\n"
  "v_accvgpr_read_b32 v23, a47\n"
  "v_pk_mul_f32 v[4:5], v[4:5], v[230:231] op_sel_hi:[1,0]\n"
  "v_pk_mul_f32 v[6:7], v[6:7], v[230:231] op_sel_hi:[1,0]\n"
  "v_pk_mul_f32 v[20:21], v[20:21], v[230:231] op_sel_hi:[1,0]\n"
  "v_pk_mul_f32 v[22:23], v[22:23], v[230:231] op_sel_hi:[1,0]\n"
  "v_cvt_pk_bf16_f32 v148, v4, v5\n"
  "v_cvt_pk_bf16_f32 v149, v6, v7\n"
  "v_cvt_pk_bf16_f32 v150, v20, v21\n"
  "v_cvt_pk_bf16_f32 v151, v22, v23\n"
  "buffer_store_dwordx4 v[148:151], v244, s[48:51], s59 offen offset:1024 sc1\n"
  "v_accvgpr_read_b32 v192, a48\n"
  "v_accvgpr_read_b32 v193, a49\n"
  "v_accvgpr_read_b32 v194, a50\n"
  "v_accvgpr_read_b32 v195, a51\n"
  "v_accvgpr_read_b32 v196, a52\n"
  "v_accvgpr_read_b32 v197, a53\n"
  "v_accvgpr_read_b32 v198, a54\n"
  "v_accvgpr_read_b32 v199, a55\n"
  "v_pk_mul_f32 v[192:193], v[192:193], v[230:231] op_sel_hi:[1,0]\n"
  "v_pk_mul_f32 v[194:195], v[194:195], v[230:231] op_sel_hi:[1,0]\n"
  "v_pk_mul_f32 v[196:197], v[196:197], v[230:231] op_sel_hi:[1,0]\n"
  "v_pk_mul_f32 v[198:199], v[198:199], v[230:231] op_sel_hi:[1,0]\n"
  "v_cvt_pk_bf16_f32 v152, v192, v193\n"
  "v_cvt_pk_bf16_f32 v153, v194, v195\n"
  "v_cvt_pk_bf16_f32 v154, v196, v197\n"
  "v_cvt_pk_bf16_f32 v155, v198, v199\n"
  "buffer_store_dwordx4 v[152:155], v244, s[48:51], s59 offen offset:2048 sc1\n"
  "v_accvgpr_read_b32 v4, a56\n"
  "v_accvgpr_read_b32 v5, a57\n"
  "v_accvgpr_read_b32 v6, a58\n"
  "v_accvgpr_read_b32 v7, a59\n"
  "v_accvgpr_read_b32 v20, a60\n"
  "v_accvgpr_read_b32 v21, a61\n"
  "v_accvgpr_read_b32 v22, a62\n"
  "v_accvgpr_read_b32 v23, a63\n"
  "v_pk_mul_f32 v[4:5], v[4:5], v[230:231] op_sel_hi:[1,0]\n"
  "v_pk_mul_f32 v[6:7], v[6:7], v[230:231] op_sel_hi:[1,0]\n"
  "v_pk_mul_f32 v[20:21], v[20:21], v[230:231] op_sel_hi:[1,0]\n"
  "v_pk_mul_f32 v[22:23], v[22:23], v[230:231] op_sel_hi:[1,0]\n"
  "v_cvt_pk_bf16_f32 v156, v4, v5\n"
  "v_cvt_pk_bf16_f32 v157, v6, v7\n"
  "v_cvt_pk_bf16_f32 v158, v20, v21\n"
  "v_cvt_pk_bf16_f32 v159, v22, v23\n"
  "buffer_store_dwordx4 v[156:159], v244, s[48:51], s59 offen offset:3072 sc1\n"
  "mk_skipA:\n"
  "s_cmp_eq_u32 s62, 0\n"
  "s_cbranch_scc1 mk_T1\n"
  "mk_LB1:\n"
  "v_mfma_f32_16x16x32_bf16 v[8:11], a[128:131], v[160:163], v[200:203]\n"
  "s_add_u32 s96, s96, 0x8000\n"
  "ds_read_b128 a[192:195], v245 offset:16384\n"
  "ds_read_b128 a[196:199], v245 offset:17408\n"
  "v_mfma_f32_16x16x32_bf16 v[8:11], a[132:135], v[164:167], v[8:11]\n"
  "s_add_u32 s98, s98, 0x8000\n"
  "ds_read_b128 a[200:203], v245 offset:18432\n"
  "ds_read_b128 a[204:207], v245 offset:19456\n"
  "v_mfma_f32_16x16x32_bf16 v[8:11], a[136:139], v[168:171], v[8:11]\n"
  "s_and_b32 s98, s98, 0x1ffff\n"
  "ds_read_b128 a[208:211], v245 offset:20480\n"
  "ds_read_b128 a[212:215], v245 offset:21504\n"
  "v_mfma_f32_16x16x32_bf16 v[8:11], a[140:143], v[172:175], v[8:11]\n"
  "s_mov_b32 m0, s98\n"
  "v_mfma_f32_16x16x32_bf16 v[12:15], a[128:131], v[176:179], v[204:207]\n"
  "buffer_load_dwordx4 v244, s[44:47], s96 offen offset:0 sc1 lds\n"
  "v_mfma_f32_16x16x32_bf16 v[12:15], a[132:135], v[180:183], v[12:15]\n"
  "ds_read_b128 a[216:219], v245 offset:22528\n"
  "v_mfma_f32_16x16x32_bf16 v[12:15], a[136:139], v[184:187], v[12:15]\n"
  "v_exp_f32 v8, v8\n"
  "v_mfma_f32_16x16x32_bf16 v[12:15], a[140:143], v[188:191], v[12:15]\n"
  "v_exp_f32 v9, v9\n"
  "v_mfma_f32_16x16x32_bf16 v[24:27], a[144:147], v[160:163], v[200:203]\n"
  "ds_read_b128 a[220:223], v245 offset:23552\n"
  "v_cvt_pk_bf16_f32 v104, v8, v9\n"
  "v_mfma_f32_16x16x32_bf16 v[24:27], a[148:151], v[164:167], v[24:27]\n"
  "v_exp_f32 v10, v10\n"
  "v_mfma_f32_16x16x32_bf16 v[24:27], a[152:155], v[168:171], v[24:27]\n"
  "v_exp_f32 v11, v11\n"
  "v_mfma_f32_16x16x32_bf16 v[24:27], a[156:159], v[172:175], v[24:27]\n"
  "buffer_load_dwordx4 v244, s[44:47], s96 offen offset:1024 sc1 lds\n"
  "v_mfma_f32_16x16x32_bf16 v[28:31], a[144:147], v[176:179], v[204:207]\n"
  "ds_read_b128 a[224:227], v245 offset:24576\n"
  "v_cvt_pk_bf16_f32 v105, v10, v11\n"
  "v_mfma_f32_16x16x32_bf16 v[28:31], a[148:151], v[180:183], v[28:31]\n"
  "v_exp_f32 v12, v12\n"
  "v_mfma_f32_16x16x32_bf16 v[28:31], a[152:155], v[184:187], v[28:31]\n"
  "v_exp_f32 v13, v13\n"
  "v_mfma_f32_16x16x32_bf16 v[28:31], a[156:159], v[188:191], v[28:31]\n"
  "ds_read_b128 a[228:231], v245 offset:25600\n"
  "v_cvt_pk_bf16_f32 v108, v12, v13\n"
  "v_mfma_f32_16x16x32_bf16 v[40:43], a[160:163], v[160:163], v[200:203]\n"
  "v_exp_f32 v14, v14\n"
  "v_mfma_f32_16x16x32_bf16 v[40:43], a[164:167], v[164:167], v[40:43]\n"
  "ds_read_b128 a[232:235], v245 offset:26624\n"
  "ds_read_b128 a[236:239], v245 offset:27648\n"
  "v_mfma_f32_16x16x32_bf16 v[40:43], a[168:171], v[168:171], v[40:43]\n"
  "buffer_load_dwordx4 v244, s[44:47], s96 offen offset:2048 sc1 lds\n"
  "v_mfma_f32_16x16x32_bf16 v[40:43], a[172:175], v[172:175], v[40:43]\n"
  "v_exp_f32 v15, v15\n"
  "v_mfma_f32_16x16x32_bf16 v[44:47], a[160:163], v[176:179], v[204:207]\n"
  "ds_read_b128 a[240:243], v245 offset:28672\n"
  "v_cvt_pk_bf16_f32 v109, v14, v15\n"
  "v_mfma_f32_16x16x32_bf16 v[44:47], a[164:167], v[180:183], v[44:47]\n"
  "v_exp_f32 v24, v24\n"
  "v_mfma_f32_16x16x32_bf16 v[44:47], a[168:171], v[184:187], v[44:47]\n"
  "v_exp_f32 v25, v25\n"
  "v_mfma_f32_16x16x32_bf16 v[44:47], a[172:175], v[188:191], v[44:47]\n"
  "ds_read_b128 a[244:247], v245 offset:29696\n"
  "v_cvt_pk_bf16_f32 v106, v24, v25\n"
  "v_mfma_f32_16x16x32_bf16 v[56:59], a[176:179], v[160:163], v[200:203]\n"
  "v_exp_f32 v26, v26\n"
  "v_mfma_f32_16x16x32_bf16 v[56:59], a[180:183], v[164:167], v[56:59]\n"
  "ds_read_b128 a[248:251], v245 offset:30720\n"
  "ds_read_b128 a[252:255], v245 offset:31744\n"
  "v_mfma_f32_16x16x32_bf16 v[56:59], a[184:187], v[168:171], v[56:59]\n"
  "v_exp_f32 v27, v27\n"
  "v_mfma_f32_16x16x32_bf16 v[56:59], a[188:191], v[172:175], v[56:59]\n"
  "buffer_load_dwordx4 v244, s[44:47], s96 offen offset:3072 sc1 lds\n"
  "v_mfma_f32_16x16x32_bf16 v[60:63], a[176:179], v[176:179], v[204:207]\n"
  "s_sub_u32 s97, s96, 0xc000\n"
  "ds_read_b128 a[128:131], v245 offset:0\n"
  "v_cvt_pk_bf16_f32 v107, v26, v27\n"
  "v_mfma_f32_16x16x32_bf16 v[60:63], a[180:183], v[180:183], v[60:63]\n"
  "s_add_u32 m0, s98, 0x4000\n"
  "v_exp_f32 v28, v28\n"
  "v_mfma_f32_16x16x32_bf16 v[60:63], a[184:187], v[184:187], v[60:63]\n"
  "v_exp_f32 v29, v29\n"
  "v_mfma_f32_16x16x32_bf16 v[60:63], a[188:191], v[188:191], v[60:63]\n"
  "ds_read_b128 a[132:135], v245 offset:1024\n"
  "v_cvt_pk_bf16_f32 v110, v28, v29\n"
  "v_mfma_f32_16x16x32_bf16 v[232:235], v[240:243], v[72:75], v[232:235]\n"
  "v_exp_f32 v30, v30\n"
  "v_mfma_f32_16x16x32_bf16 v[232:235], v[240:243], v[88:91], v[232:235]\n"
  "ds_read_b128 a[136:139], v245 offset:2048\n"
  "ds_read_b128 a[140:143], v245 offset:3072\n"
  "v_mfma_f32_16x16x32_bf16 v[236:239], v[240:243], v[76:79], v[236:239]\n"
  "v_exp_f32 v31, v31\n"
  "v_mfma_f32_16x16x32_bf16 v[236:239], v[240:243], v[92:95], v[236:239]\n"
  "s_waitcnt lgkmcnt(4)\n"
  "buffer_load_dwordx4 v244, s[44:47], s97 offen offset:0 sc1 lds\n"
  "v_mfma_f32_16x16x32_bf16 a[64:67], a[192:195], v[72:75], a[64:67]\n"
  "ds_read_b128 a[144:147], v245 offset:4096\n"
  "v_cvt_pk_bf16_f32 v111, v30, v31\n"
  "v_mfma_f32_16x16x32_bf16 a[64:67], a[196:199], v[88:91], a[64:67]\n"
  "v_exp_f32 v40, v40\n"
  "v_mfma_f32_16x16x32_bf16 a[96:99], a[192:195], v[76:79], a[96:99]\n"
  "ds_read_b128 a[148:151], v245 offset:5120\n"
  "ds_read_b128 a[152:155], v245 offset:6144\n"
  "v_mfma_f32_16x16x32_bf16 a[96:99], a[196:199], v[92:95], a[96:99]\n"
  "v_exp_f32 v41, v41\n"
  "v_mfma_f32_16x16x32_bf16 a[68:71], a[200:203], v[72:75], a[68:71]\n"
  "ds_read_b128 a[156:159], v245 offset:7168\n"
  "v_cvt_pk_bf16_f32 v120, v40, v41\n"
  "v_mfma_f32_16x16x32_bf16 a[68:71], a[204:207], v[88:91], a[68:71]\n"
  "v_exp_f32 v42, v42\n"
  "v_mfma_f32_16x16x32_bf16 a[100:103], a[200:203], v[76:79], a[100:103]\n"
  "v_exp_f32 v43, v43\n"
  "v_mfma_f32_16x16x32_bf16 a[100:103], a[204:207], v[92:95], a[100:103]\n"
  "buffer_load_dwordx4 v244, s[44:47], s97 offen offset:1024 sc1 lds\n"
  "v_mfma_f32_16x16x32_bf16 a[72:75], a[208:211], v[72:75], a[72:75]\n"
  "ds_read_b128 a[160:163], v245 offset:8192\n"
  "v_cvt_pk_bf16_f32 v121, v42, v43\n"
  "v_mfma_f32_16x16x32_bf16 a[72:75], a[212:215], v[88:91], a[72:75]\n"
  "v_exp_f32 v44, v44\n"
  "v_mfma_f32_16x16x32_bf16 a[104:107], a[208:211], v[76:79], a[104:107]\n"
  "ds_read_b128 a[164:167], v245 offset:9216\n"
  "ds_read_b128 a[168:171], v245 offset:10240\n"
  "v_mfma_f32_16x16x32_bf16 a[104:107], a[212:215], v[92:95], a[104:107]\n"
  "v_exp_f32 v45, v45\n"
  "v_mfma_f32_16x16x32_bf16 a[76:79], a[216:219], v[72:75], a[76:79]\n"
  "ds_read_b128 a[172:175], v245 offset:11264\n"
  "v_cvt_pk_bf16_f32 v124, v44, v45\n"
  "v_mfma_f32_16x16x32_bf16 a[76:79], a[220:223], v[88:91], a[76:79]\n"
  "v_exp_f32 v46, v46\n"
  "v_mfma_f32_16x16x32_bf16 a[108:111], a[216:219], v[76:79], a[108:111]\n"
  "mk_tp10:\n"
  "s_cbranch_execz mk_tp14\n"
  "v_exp_f32 v47, v47\n"
  "v_mfma_f32_16x16x32_bf16 a[108:111], a[220:223], v[92:95], a[108:111]\n"
  "buffer_load_dwordx4 v244, s[44:47], s97 offen offset:2048 sc1 lds\n"
  "v_mfma_f32_16x16x32_bf16 a[80:83], a[224:227], v[72:75], a[80:83]\n"
  "ds_read_b128 a[176:179], v245 offset:12288\n"
  "v_cvt_pk_bf16_f32 v125, v46, v47\n"
  "v_mfma_f32_16x16x32_bf16 a[80:83], a[228:231], v[88:91], a[80:83]\n"
  "v_exp_f32 v56, v56\n"
  "v_mfma_f32_16x16x32_bf16 a[112:115], a[224:227], v[76:79], a[112:115]\n"
  "ds_read_b128 a[180:183], v245 offset:13312\n"
  "ds_read_b128 a[184:187], v245 offset:14336\n"
  "v_mfma_f32_16x16x32_bf16 a[112:115], a[228:231], v[92:95], a[112:115]\n"
  "v_exp_f32 v57, v57\n"
  "v_mfma_f32_16x16x32_bf16 a[84:87], a[232:235], v[72:75], a[84:87]\n"
  "ds_read_b128 a[188:191], v245 offset:15360\n"
  "v_cvt_pk_bf16_f32 v122, v56, v57\n"
  "v_mfma_f32_16x16x32_bf16 a[84:87], a[236:239], v[88:91], a[84:87]\n"
  "v_exp_f32 v58, v58\n"
  "v_mfma_f32_16x16x32_bf16 a[116:119], a[232:235], v[76:79], a[116:119]\n"
  "v_exp_f32 v59, v59\n"
  "v_mfma_f32_16x16x32_bf16 a[116:119], a[236:239], v[92:95], a[116:119]\n"
  "buffer_load_dwordx4 v244, s[44:47], s97 offen offset:3072 sc1 lds\n"
  "v_mfma_f32_16x16x32_bf16 a[88:91], a[240:243], v[72:75], a[88:91]\n"
  "v_cvt_pk_bf16_f32 v123, v58, v59\n"
  "v_mfma_f32_16x16x32_bf16 a[88:91], a[244:247], v[88:91], a[88:91]\n"
  "v_exp_f32 v60, v60\n"
  "v_mfma_f32_16x16x32_bf16 a[120:123], a[240:243], v[76:79], a[120:123]\n"
  "v_exp_f32 v61, v61\n"
  "v_mfma_f32_16x16x32_bf16 a[120:123], a[244:247], v[92:95], a[120:123]\n"
  "v_cvt_pk_bf16_f32 v126, v60, v61\n"
  "v_mfma_f32_16x16x32_bf16 a[92:95], a[248:251], v[72:75], a[92:95]\n"
  "s_cmp_eq_u32 s62, 1\n"
  "v_exp_f32 v62, v62\n"
  "v_mfma_f32_16x16x32_bf16 a[92:95], a[252:255], v[88:91], a[92:95]\n"
  "v_exp_f32 v63, v63\n"
  "v_mfma_f32_16x16x32_bf16 a[124:127], a[248:251], v[76:79], a[124:127]\n"
  "v_cvt_pk_bf16_f32 v127, v62, v63\n"
  "v_mfma_f32_16x16x32_bf16 a[124:127], a[252:255], v[92:95], a[124:127]\n"
  "s_waitcnt vmcnt(8) lgkmcnt(0)\n"
  "s_barrier\n"
  "s_cbranch_scc1 mk_fixB\n"
  "mk_LB2:\n"
  "v_mfma_f32_16x16x32_bf16 v[12:15], a[128:131], v[176:179], v[204:207]\n"
  "s_add_u32 s96, s96, 0x8000\n"
  "ds_read_b128 a[192:195], v245 offset:49152\n"
  "ds_read_b128 a[196:199], v245 offset:50176\n"
  "v_mfma_f32_16x16x32_bf16 v[12:15], a[132:135], v[180:183], v[12:15]\n"
  "s_add_u32 s98, s98, 0x8000\n"
  "ds_read_b128 a[200:203], v245 offset:51200\n"
  "ds_read_b128 a[204:207], v245 offset:52224\n"
  "v_mfma_f32_16x16x32_bf16 v[12:15], a[136:139], v[184:187], v[12:15]\n"
  "s_and_b32 s98, s98, 0x1ffff\n"
  "ds_read_b128 a[208:211], v245 offset:53248\n"
  "ds_read_b128 a[212:215], v245 offset:54272\n"
  "v_mfma_f32_16x16x32_bf16 v[12:15], a[140:143], v[188:191], v[12:15]\n"
  "s_mov_b32 m0, s98\n"
  "v_mfma_f32_16x16x32_bf16 v[8:11], a[128:131], v[160:163], v[208:211]\n"
  "buffer_load_dwordx4 v244, s[44:47], s96 offen offset:0 sc1 lds\n"
  "v_mfma_f32_16x16x32_bf16 v[8:11], a[132:135], v[164:167], v[8:11]\n"
  "ds_read_b128 a[216:219], v245 offset:55296\n"
  "v_mfma_f32_16x16x32_bf16 v[8:11], a[136:139], v[168:171], v[8:11]\n"
  "v_exp_f32 v12, v12\n"
  "v_mfma_f32_16x16x32_bf16 v[8:11], a[140:143], v[172:175], v[8:11]\n"
  "v_exp_f32 v13, v13\n"
  "v_mfma_f32_16x16x32_bf16 v[28:31], a[144:147], v[176:179], v[204:207]\n"
  "ds_read_b128 a[220:223], v245 offset:56320\n"
  "v_cvt_pk_bf16_f32 v76, v12, v13\n"
  "v_mfma_f32_16x16x32_bf16 v[28:31], a[148:151], v[180:183], v[28:31]\n"
  "v_exp_f32 v14, v14\n"
  "v_mfma_f32_16x16x32_bf16 v[28:31], a[152:155], v[184:187], v[28:31]\n"
  "v_exp_f32 v15, v15\n"
  "v_mfma_f32_16x16x32_bf16 v[28:31], a[156:159], v[188:191], v[28:31]\n"
  "buffer_load_dwordx4 v244, s[44:47], s96 offen offset:1024 sc1 lds\n"
  "v_mfma_f32_16x16x32_bf16 v[24:27], a[144:147], v[160:163], v[212:215]\n"
  "ds_read_b128 a[224:227], v245 offset:57344\n"
  "v_cvt_pk_bf16_f32 v77, v14, v15\n"
  "v_mfma_f32_16x16x32_bf16 v[24:27], a[148:151], v[164:167], v[24:27]\n"
  "v_exp_f32 v8, v8\n"
  "v_mfma_f32_16x16x32_bf16 v[24:27], a[152:155], v[168:171], v[24:27]\n"
  "v_exp_f32 v9, v9\n"
  "v_mfma_f32_16x16x32_bf16 v[24:27], a[156:159], v[172:175], v[24:27]\n"
  "ds_read_b128 a[228:231], v245 offset:58368\n"
  "v_cvt_pk_bf16_f32 v72, v8, v9\n"
  "v_mfma_f32_16x16x32_bf16 v[44:47], a[160:163], v[176:179], v[204:207]\n"
  "v_exp_f32 v10, v10\n"
  "v_mfma_f32_16x16x32_bf16 v[44:47], a[164:167], v[180:183], v[44:47]\n"
  "ds_read_b128 a[232:235], v245 offset:59392\n"
  "ds_read_b128 a[236:239], v245 offset:60416\n"
  "v_mfma_f32_16x16x32_bf16 v[44:47], a[168:171], v[184:187], v[44:47]\n"
  "buffer_load_dwordx4 v244, s[44:47], s96 offen offset:2048 sc1 lds\n"
  "v_mfma_f32_16x16x32_bf16 v[44:47], a[172:175], v[188:191], v[44:47]\n"
  "v_exp_f32 v11, v11\n"
  "v_mfma_f32_16x16x32_bf16 v[40:43], a[160:163], v[160:163], v[216:219]\n"
  "ds_read_b128 a[240:243], v245 offset:61440\n"
  "v_cvt_pk_bf16_f32 v73, v10, v11\n"
  "v_mfma_f32_16x16x32_bf16 v[40:43], a[164:167], v[164:167], v[40:43]\n"
  "v_exp_f32 v28, v28\n"
  "v_mfma_f32_16x16x32_bf16 v[40:43], a[168:171], v[168:171], v[40:43]\n"
  "v_exp_f32 v29, v29\n"
  "v_mfma_f32_16x16x32_bf16 v[40:43], a[172:175], v[172:175], v[40:43]\n"
  "ds_read_b128 a[244:247], v245 offset:62464\n"
  "v_cvt_pk_bf16_f32 v78, v28, v29\n"
  "v_mfma_f32_16x16x32_bf16 v[60:63], a[176:179], v[176:179], v[204:207]\n"
  "v_exp_f32 v30, v30\n"
  "v_mfma_f32_16x16x32_bf16 v[60:63], a[180:183], v[180:183], v[60:63]\n"
  "ds_read_b128 a[248:251], v245 offset:63488\n"
  "ds_read_b128 a[252:255], v245 offset:64512\n"
  "v_mfma_f32_16x16x32_bf16 v[60:63], a[184:187], v[184:187], v[60:63]\n"
  "v_exp_f32 v31, v31\n"
  "v_mfma_f32_16x16x32_bf16 v[60:63], a[188:191], v[188:191], v[60:63]\n"
  "buffer_load_dwordx4 v244, s[44:47], s96 offen offset:3072 sc1 lds\n"
  "v_mfma_f32_16x16x32_bf16 v[56:59], a[176:179], v[160:163], v[220:223]\n"
  "s_sub_u32 s97, s96, 0xc000\n"
  "ds_read_b128 a[128:131], v245 offset:32768\n"
  "v_cvt_pk_bf16_f32 v79, v30, v31\n"
  "v_mfma_f32_16x16x32_bf16 v[56:59], a[180:183], v[164:167], v[56:59]\n"
  "s_add_u32 m0, s98, 0x4000\n"
  "v_exp_f32 v24, v24\n"
  "v_mfma_f32_16x16x32_bf16 v[56:59], a[184:187], v[168:171], v[56:59]\n"
  "v_exp_f32 v25, v25\n"
  "v_mfma_f32_16x16x32_bf16 v[56:59], a[188:191], v[172:175], v[56:59]\n"
  "ds_read_b128 a[132:135], v245 offset:33792\n"
  "v_cvt_pk_bf16_f32 v74, v24, v25\n"
  "v_mfma_f32_16x16x32_bf16 v[232:235], v[240:243], v[104:107], v[232:235]\n"
  "v_exp_f32 v26, v26\n"
  "v_mfma_f32_16x16x32_bf16 v[232:235], v[240:243], v[120:123], v[232:235]\n"
  "ds_read_b128 a[136:139], v245 offset:34816\n"
  "ds_read_b128 a[140:143], v245 offset:35840\n"
  "v_mfma_f32_16x16x32_bf16 v[236:239], v[240:243], v[108:111], v[236:239]\n"
  "v_exp_f32 v27, v27\n"
  "v_mfma_f32_16x16x32_bf16 v[236:239], v[240:243], v[124:127], v[236:239]\n"
  "s_waitcnt lgkmcnt(4)\n"
  "buffer_load_dwordx4 v244, s[44:47], s97 offen offset:0 sc1 lds\n"
  "v_mfma_f32_16x16x32_bf16 a[64:67], a[192:195], v[104:107], a[64:67]\n"
  "ds_read_b128 a[144:147], v245 offset:36864\n"
  "v_cvt_pk_bf16_f32 v75, v26, v27\n"
  "v_mfma_f32_16x16x32_bf16 a[64:67], a[196:199], v[120:123], a[64:67]\n"
  "v_exp_f32 v44, v44\n"
  "v_mfma_f32_16x16x32_bf16 a[96:99], a[192:195], v[108:111], a[96:99]\n"
  "ds_read_b128 a[148:151], v245 offset:37888\n"
  "ds_read_b128 a[152:155], v245 offset:38912\n"
  "v_mfma_f32_16x16x32_bf16 a[96:99], a[196:199], v[124:127], a[96:99]\n"
  "v_exp_f32 v45, v45\n"
  "v_mfma_f32_16x16x32_bf16 a[68:71], a[200:203], v[104:107], a[68:71]\n"
  "ds_read_b128 a[156:159], v245 offset:39936\n"
  "v_cvt_pk_bf16_f32 v92, v44, v45\n"
  "v_mfma_f32_16x16x32_bf16 a[68:71], a[204:207], v[120:123], a[68:71]\n"
  "v_exp_f32 v46, v46\n"
  "v_mfma_f32_16x16x32_bf16 a[100:103], a[200:203], v[108:111], a[100:103]\n"
  "v_exp_f32 v47, v47\n"
  "v_mfma_f32_16x16x32_bf16 a[100:103], a[204:207], v[124:127], a[100:103]\n"
  "buffer_load_dwordx4 v244, s[44:47], s97 offen offset:1024 sc1 lds\n"
  "v_mfma_f32_16x16x32_bf16 a[72:75], a[208:211], v[104:107], a[72:75]\n"
  "ds_read_b128 a[160:163], v245 offset:40960\n"
  "v_cvt_pk_bf16_f32 v93, v46, v47\n"
  "v_mfma_f32_16x16x32_bf16 a[72:75], a[212:215], v[120:123], a[72:75]\n"
  "v_exp_f32 v40, v40\n"
  "v_mfma_f32_16x16x32_bf16 a[104:107], a[208:211], v[108:111], a[104:107]\n"
  "ds_read_b128 a[164:167], v245 offset:41984\n"
  "ds_read_b128 a[168:171], v245 offset:43008\n"
  "v_mfma_f32_16x16x32_bf16 a[104:107], a[212:215], v[124:127], a[104:107]\n"
  "v_exp_f32 v41, v41\n"
  "v_mfma_f32_16x16x32_bf16 a[76:79], a[216:219], v[104:107], a[76:79]\n"
  "ds_read_b128 a[172:175], v245 offset:44032\n"
  "v_cvt_pk_bf16_f32 v88, v40, v41\n"
  "v_mfma_f32_16x16x32_bf16 a[76:79], a[220:223], v[120:123], a[76:79]\n"
  "v_exp_f32 v42, v42\n"
  "v_mfma_f32_16x16x32_bf16 a[108:111], a[216:219], v[108:111], a[108:111]\n"
  "v_exp_f32 v43, v43\n"
  "v_mfma_f32_16x16x32_bf16 a[108:111], a[220:223], v[124:127], a[108:111]\n"
  "buffer_load_dwordx4 v244, s[44:47], s97 offen offset:2048 sc1 lds\n"
  "v_mfma_f32_16x16x32_bf16 a[80:83], a[224:227], v[104:107], a[80:83]\n"
  "ds_read_b128 a[176:179], v245 offset:45056\n"
  "v_cvt_pk_bf16_f32 v89, v42, v43\n"
  "v_mfma_f32_16x16x32_bf16 a[80:83], a[228:231], v[120:123], a[80:83]\n"
  "v_exp_f32 v60, v60\n"
  "v_mfma_f32_16x16x32_bf16 a[112:115], a[224:227], v[108:111], a[112:115]\n"
  "ds_read_b128 a[180:183], v245 offset:46080\n"
  "ds_read_b128 a[184:187], v245 offset:47104\n"
  "v_mfma_f32_16x16x32_bf16 a[112:115], a[228:231], v[124:127], a[112:115]\n"
  "v_exp_f32 v61, v61\n"
  "v_mfma_f32_16x16x32_bf16 a[84:87], a[232:235], v[104:107], a[84:87]\n"
  "ds_read_b128 a[188:191], v245 offset:48128\n"
  "v_cvt_pk_bf16_f32 v94, v60, v61\n"
  "v_mfma_f32_16x16x32_bf16 a[84:87], a[236:239], v[120:123], a[84:87]\n"
  "v_exp_f32 v62, v62\n"
  "v_mfma_f32_16x16x32_bf16 a[116:119], a[232:235], v[108:111], a[116:119]\n"
  "v_exp_f32 v63, v63\n"
  "v_mfma_f32_16x16x32_bf16 a[116:119], a[236:239], v[124:127], a[116:119]\n"
  "buffer_load_dwordx4 v244, s[44:47], s97 offen offset:3072 sc1 lds\n"
  "v_mfma_f32_16x16x32_bf16 a[88:91], a[240:243], v[104:107], a[88:91]\n"
  "v_xor_b32 v245, 0x10000, v245\n"
  "v_cvt_pk_bf16_f32 v95, v62, v63\n"
  "v_mfma_f32_16x16x32_bf16 a[88:91], a[244:247], v[120:123], a[88:91]\n"
  "v_exp_f32 v56, v56\n"
  "v_mfma_f32_16x16x32_bf16 a[120:123], a[240:243], v[108:111], a[120:123]\n"
  "v_exp_f32 v57, v57\n"
  "v_mfma_f32_16x16x32_bf16 a[120:123], a[244:247], v[124:127], a[120:123]\n"
  "s_sub_u32 s62, s62, 1\n"
  "v_cvt_pk_bf16_f32 v90, v56, v57\n"
  "v_mfma_f32_16x16x32_bf16 a[92:95], a[248:251], v[104:107], a[92:95]\n"
  "s_cmp_lg_u32 s62, 0\n"
  "v_exp_f32 v58, v58\n"
  "v_mfma_f32_16x16x32_bf16 a[92:95], a[252:255], v[120:123], a[92:95]\n"
  "v_exp_f32 v59, v59\n"
  "v_mfma_f32_16x16x32_bf16 a[124:127], a[248:251], v[108:111], a[124:127]\n"
  "v_cvt_pk_bf16_f32 v91, v58, v59\n"
  "v_mfma_f32_16x16x32_bf16 a[124:127], a[252:255], v[124:127], a[124:127]\n"
  "s_waitcnt vmcnt(8) lgkmcnt(0)\n"
  "s_barrier\n"
  "s_cbranch_scc1 mk_LB1\n"
  "mk_T1:\n"
  "mk_tp11:\n"
  "s_cbranch_execz mk_tp15\n"
  "v_mfma_f32_16x16x32_bf16 v[12:15], a[128:131], v[176:179], v[0:3]\n"
  "s_add_u32 s55, s53, 1\n"
  "ds_read_b128 a[192:195], v245 offset:16384\n"
  "ds_read_b128 a[196:199], v245 offset:17408\n"
  "v_mfma_f32_16x16x32_bf16 v[12:15], a[132:135], v[180:183], v[12:15]\n"
  "s_and_b32 s55, s55, 3\n"
  "v_bfe_u32 v192, v244, 6, 4\n"
  "v_bfe_u32 v193, v244, 1, 5\n"
  "v_mfma_f32_16x16x32_bf16 v[12:15], a[136:139], v[184:187], v[12:15]\n"
  "s_lshl_b32 s55, s55, 17\n"
  "v_lshl_add_u32 v193, v192, 8, v193\n"
  "v_mfma_f32_16x16x32_bf16 v[12:15], a[140:143], v[188:191], v[12:15]\n"
  "s_sub_u32 m0, s57, 0x18000\n"
  "ds_read_b128 a[200:203], v245 offset:18432\n"
  "v_add_u32 v193, 0x20000, v193\n"
  "v_mfma_f32_16x16x32_bf16 v[28:31], a[144:147], v[176:179], v[16:19]\n"
  "buffer_load_dwordx4 v249, s[36:39], s55 offen offset:0 lds\n"
  "v_mfma_f32_16x16x32_bf16 v[28:31], a[148:151], v[180:183], v[28:31]\n"
  "v_and_b32 v192, 7, v192\n"
  "v_mfma_f32_16x16x32_bf16 v[28:31], a[152:155], v[184:187], v[28:31]\n"
  "v_exp_f32 v12, v12\n"
  "v_mfma_f32_16x16x32_bf16 v[28:31], a[156:159], v[188:191], v[28:31]\n"
  "buffer_load_dwordx4 v250, s[36:39], s55 offen offset:1024 lds\n"
  "v_mfma_f32_16x16x32_bf16 v[44:47], a[160:163], v[176:179], v[32:35]\n"
  "ds_read_b128 a[204:207], v245 offset:19456\n"
  "ds_read_b128 a[208:211], v245 offset:20480\n"
  "v_mfma_f32_16x16x32_bf16 v[44:47], a[164:167], v[180:183], v[44:47]\n"
  "v_exp_f32 v13, v13\n"
  "v_mfma_f32_16x16x32_bf16 v[44:47], a[168:171], v[184:187], v[44:47]\n"
  "buffer_load_dwordx4 v251, s[36:39], s55 offen offset:2048 lds\n"
  "v_mfma_f32_16x16x32_bf16 v[44:47], a[172:175], v[188:191], v[44:47]\n"
  "ds_read_b128 a[212:215], v245 offset:21504\n"
  "v_cvt_pk_bf16_f32 v108, v12, v13\n"
  "v_mfma_f32_16x16x32_bf16 v[60:63], a[176:179], v[176:179], v[48:51]\n"
  "v_exp_f32 v14, v14\n"
  "v_mfma_f32_16x16x32_bf16 v[60:63], a[180:183], v[180:183], v[60:63]\n"
  "buffer_load_dwordx4 v252, s[36:39], s55 offen offset:3072 lds\n"
  "v_mfma_f32_16x16x32_bf16 v[60:63], a[184:187], v[184:187], v[60:63]\n"
  "s_add_u32 s55, s53, 1\n"
  "v_exp_f32 v15, v15\n"
  "v_mfma_f32_16x16x32_bf16 v[60:63], a[188:191], v[188:191], v[60:63]\n"
  "s_and_b32 s55, s55, 3\n"
  "ds_read_b128 a[216:219], v245 offset:22528\n"
  "v_cvt_pk_bf16_f32 v109, v14, v15\n"
  "v_mfma_f32_16x16x32_bf16 v[232:235], v[240:243], v[72:75], v[232:235]\n"
  "s_lshl_b32 s55, s55, 17\n"
  "v_exp_f32 v28, v28\n"
  "v_mfma_f32_16x16x32_bf16 v[232:235], v[240:243], v[88:91], v[232:235]\n"
  "s_add_u32 s55, s55, 0x10000\n"
  "v_exp_f32 v29, v29\n"
  "v_mfma_f32_16x16x32_bf16 v[236:239], v[240:243], v[76:79], v[236:239]\n"
  "s_sub_u32 m0, s57, 0x10000\n"
  "ds_read_b128 a[220:223], v245 offset:23552\n"
  "v_xor_b32 v194, 0, v192\n"
  "v_mfma_f32_16x16x32_bf16 v[236:239], v[240:243], v[92:95], v[236:239]\n"
  "s_waitcnt lgkmcnt(2)\n"
  "buffer_load_dwordx4 v249, s[36:39], s55 offen offset:0 lds\n"
  "v_mfma_f32_16x16x32_bf16 a[64:67], a[192:195], v[72:75], a[64:67]\n"
  "ds_read_b128 a[224:227], v245 offset:24576\n"
  "v_cvt_pk_bf16_f32 v110, v28, v29\n"
  "v_mfma_f32_16x16x32_bf16 a[64:67], a[196:199], v[88:91], a[64:67]\n"
  "v_exp_f32 v30, v30\n"
  "v_mfma_f32_16x16x32_bf16 a[96:99], a[192:195], v[76:79], a[96:99]\n"
  "buffer_load_dwordx4 v250, s[36:39], s55 offen offset:1024 lds\n"
  "v_mfma_f32_16x16x32_bf16 a[96:99], a[196:199], v[92:95], a[96:99]\n"
  "v_exp_f32 v31, v31\n"
  "v_mfma_f32_16x16x32_bf16 a[68:71], a[200:203], v[72:75], a[68:71]\n"
  "ds_read_b128 a[228:231], v245 offset:25600\n"
  "v_cvt_pk_bf16_f32 v111, v30, v31\n"
  "v_mfma_f32_16x16x32_bf16 a[68:71], a[204:207], v[88:91], a[68:71]\n"
  "buffer_load_dwordx4 v251, s[36:39], s55 offen offset:2048 lds\n"
  "v_mfma_f32_16x16x32_bf16 a[100:103], a[200:203], v[76:79], a[100:103]\n"
  "v_exp_f32 v44, v44\n"
  "v_mfma_f32_16x16x32_bf16 a[100:103], a[204:207], v[92:95], a[100:103]\n"
  "ds_read_b128 a[232:235], v245 offset:26624\n"
  "ds_read_b128 a[236:239], v245 offset:27648\n"
  "v_mfma_f32_16x16x32_bf16 a[72:75], a[208:211], v[72:75], a[72:75]\n"
  "buffer_load_dwordx4 v252, s[36:39], s55 offen offset:3072 lds\n"
  "v_mfma_f32_16x16x32_bf16 a[72:75], a[212:215], v[88:91], a[72:75]\n"
  "s_cmp_eq_u32 s10, 0\n"
  "s_cselect_b32 s59, s11, 0x7fff0000\n"
  "v_exp_f32 v45, v45\n"
  "v_mfma_f32_16x16x32_bf16 a[104:107], a[208:211], v[76:79], a[104:107]\n"
  "s_waitcnt vmcnt(8)\n"
  "buffer_atomic_add v248, v253, s[48:51], s59 offen sc1\n"
  "v_mfma_f32_16x16x32_bf16 a[104:107], a[212:215], v[92:95], a[104:107]\n"
  "s_waitcnt lgkmcnt(3)\n"
  "v_cvt_pk_bf16_f32 v124, v44, v45\n"
  "v_mov_b32 v39, 0\n"
  "v_mfma_f32_16x16x32_bf16 a[76:79], a[216:219], v[72:75], a[76:79]\n"
  "v_exp_f32 v46, v46\n"
  "v_mfma_f32_16x16x32_bf16 a[76:79], a[220:223], v[88:91], a[76:79]\n"
  "buffer_atomic_add v39, v253, s[48:51], s11 offen sc0 sc1\n"
  "v_mfma_f32_16x16x32_bf16 a[108:111], a[216:219], v[76:79], a[108:111]\n"
  "s_add_u32 s55, s53, 2\n"
  "ds_read_b128 a[240:243], v245 offset:28672\n"
  "ds_read_b128 a[244:247], v245 offset:29696\n"
  "v_mfma_f32_16x16x32_bf16 a[108:111], a[220:223], v[92:95], a[108:111]\n"
  "s_and_b32 s55, s55, 3\n"
  "v_exp_f32 v47, v47\n"
  "v_mfma_f32_16x16x32_bf16 a[80:83], a[224:227], v[72:75], a[80:83]\n"
  "s_waitcnt lgkmcnt(2)\n"
  "s_lshl_b32 s55, s55, 17\n"
  "ds_read_b128 a[248:251], v245 offset:30720\n"
  "v_cvt_pk_bf16_f32 v125, v46, v47\n"
  "v_mfma_f32_16x16x32_bf16 a[80:83], a[228:231], v[88:91], a[80:83]\n"
  "s_sub_u32 m0, s57, 0x8000\n"
  "v_exp_f32 v60, v60\n"
  "v_mfma_f32_16x16x32_bf16 a[112:115], a[224:227], v[76:79], a[112:115]\n"
  "ds_read_b128 a[252:255], v245 offset:31744\n"
  "v_lshl_add_u32 v224, v194, 5, v193\n"
  "v_mfma_f32_16x16x32_bf16 a[112:115], a[228:231], v[92:95], a[112:115]\n"
  "buffer_load_dwordx4 v249, s[36:39], s55 offen offset:0 lds\n"
  "v_mfma_f32_16x16x32_bf16 a[84:87], a[232:235], v[72:75], a[84:87]\n"
  "v_exp_f32 v61, v61\n"
  "v_mfma_f32_16x16x32_bf16 a[84:87], a[236:239], v[88:91], a[84:87]\n"
  "v_cvt_pk_bf16_f32 v126, v60, v61\n"
  "v_xor_b32 v194, 1, v192\n"
  "v_mfma_f32_16x16x32_bf16 a[116:119], a[232:235], v[76:79], a[116:119]\n"
  "buffer_load_dwordx4 v250, s[36:39], s55 offen offset:1024 lds\n"
  "v_mfma_f32_16x16x32_bf16 a[116:119], a[236:239], v[92:95], a[116:119]\n"
  "s_waitcnt lgkmcnt(2)\n"
  "v_exp_f32 v62, v62\n"
  "v_mfma_f32_16x16x32_bf16 a[88:91], a[240:243], v[72:75], a[88:91]\n"
  "buffer_load_dwordx4 v251, s[36:39], s55 offen offset:2048 lds\n"
  "v_mfma_f32_16x16x32_bf16 a[88:91], a[244:247], v[88:91], a[88:91]\n"
  "v_exp_f32 v63, v63\n"
  "v_mfma_f32_16x16x32_bf16 a[120:123], a[240:243], v[76:79], a[120:123]\n"
  "v_cvt_pk_bf16_f32 v127, v62, v63\n"
  "v_lshl_add_u32 v225, v194, 5, v193\n"
  "v_mfma_f32_16x16x32_bf16 a[120:123], a[244:247], v[92:95], a[120:123]\n"
  "s_waitcnt lgkmcnt(0)\n"
  "buffer_load_dwordx4 v252, s[36:39], s55 offen offset:3072 lds\n"
  "v_mfma_f32_16x16x32_bf16 a[92:95], a[248:251], v[72:75], a[92:95]\n"
  "v_xor_b32 v194, 2, v192\n"
  "v_lshl_add_u32 v226, v194, 5, v193\n"
  "v_mfma_f32_16x16x32_bf16 a[92:95], a[252:255], v[88:91], a[92:95]\n"
  "v_xor_b32 v194, 3, v192\n"
  "v_lshl_add_u32 v227, v194, 5, v193\n"
  "v_mfma_f32_16x16x32_bf16 a[124:127], a[248:251], v[76:79], a[124:127]\n"
  "v_xor_b32 v194, 4, v192\n"
  "v_lshl_add_u32 v228, v194, 5, v193\n"
  "v_mfma_f32_16x16x32_bf16 a[124:127], a[252:255], v[92:95], a[124:127]\n"
  "s_waitcnt lgkmcnt(0)\n"
  "s_barrier\n"
  "v_xor_b32 v194, 5, v192\n"
  "v_lshl_add_u32 v229, v194, 5, v193\n"
  "v_xor_b32 v194, 6, v192\n"
  "v_lshl_add_u32 v230, v194, 5, v193\n"
  "v_xor_b32 v194, 7, v192\n"
  "v_lshl_add_u32 v231, v194, 5, v193\n"
  "v_mfma_f32_16x16x32_bf16 v[236:239], v[240:243], v[108:111], v[236:239]\n"
  "ds_read_b128 a[192:195], v245 offset:49152\n"
  "ds_read_b128 a[196:199], v245 offset:50176\n"
  "v_mfma_f32_16x16x32_bf16 v[236:239], v[240:243], v[124:127], v[236:239]\n"
  "s_waitcnt lgkmcnt(1)\n"
  "ds_read_b128 a[200:203], v245 offset:51200\n"
  "ds_read_b128 a[204:207], v245 offset:52224\n"
  "v_mfma_f32_16x16x32_bf16 a[96:99], a[192:195], v[108:111], a[96:99]\n"
  "s_waitcnt lgkmcnt(2)\n"
  "ds_read_b128 a[208:211], v245 offset:53248\n"
  "ds_read_b128 a[212:215], v245 offset:54272\n"
  "v_mfma_f32_16x16x32_bf16 a[96:99], a[196:199], v[124:127], a[96:99]\n"
  "s_waitcnt lgkmcnt(3)\n"
  "ds_read_b128 a[216:219], v245 offset:55296\n"
  "ds_read_b128 a[220:223], v245 offset:56320\n"
  "v_mfma_f32_16x16x32_bf16 a[100:103], a[200:203], v[108:111], a[100:103]\n"
  "s_waitcnt lgkmcnt(4)\n"
  "ds_read_b128 a[224:227], v245 offset:57344\n"
  "ds_read_b128 a[228:231], v245 offset:58368\n"
  "v_mfma_f32_16x16x32_bf16 a[100:103], a[204:207], v[124:127], a[100:103]\n"
  "s_waitcnt lgkmcnt(5)\n"
  "ds_read_b128 a[232:235], v245 offset:59392\n"
  "ds_read_b128 a[236:239], v245 offset:60416\n"
  "v_mfma_f32_16x16x32_bf16 a[104:107], a[208:211], v[108:111], a[104:107]\n"
  "s_waitcnt lgkmcnt(6)\n"
  "ds_read_b128 a[240:243], v245 offset:61440\n"
  "ds_read_b128 a[244:247], v245 offset:62464\n"
  "v_mfma_f32_16x16x32_bf16 a[104:107], a[212:215], v[124:127], a[104:107]\n"
  "s_waitcnt lgkmcnt(7)\n"
  "ds_read_b128 a[248:251], v245 offset:63488\n"
  "ds_read_b128 a[252:255], v245 offset:64512\n"
  "v_mfma_f32_16x16x32_bf16 a[108:111], a[216:219], v[108:111], a[108:111]\n"
  "s_waitcnt lgkmcnt(8)\n"
  "v_mfma_f32_16x16x32_bf16 a[108:111], a[220:223], v[124:127], a[108:111]\n"
  "s_waitcnt lgkmcnt(7)\n"
  "v_mfma_f32_16x16x32_bf16 a[112:115], a[224:227], v[108:111], a[112:115]\n"
  "s_waitcnt lgkmcnt(6)\n"
  "v_mfma_f32_16x16x32_bf16 a[112:115], a[228:231], v[124:127], a[112:115]\n"
  "s_waitcnt lgkmcnt(5)\n"
  "v_mfma_f32_16x16x32_bf16 a[116:119], a[232:235], v[108:111], a[116:119]\n"
  "s_waitcnt lgkmcnt(4)\n"
  "v_mfma_f32_16x16x32_bf16 a[116:119], a[236:239], v[124:127], a[116:119]\n"
  "s_waitcnt lgkmcnt(3)\n"
  "v_mfma_f32_16x16x32_bf16 a[120:123], a[240:243], v[108:111], a[120:123]\n"
  "s_waitcnt lgkmcnt(2)\n"
  "v_mfma_f32_16x16x32_bf16 a[120:123], a[244:247], v[124:127], a[120:123]\n"
  "s_waitcnt lgkmcnt(0)\n"
  "v_mfma_f32_16x16x32_bf16 a[124:127], a[248:251], v[108:111], a[124:127]\n"
  "v_mfma_f32_16x16x32_bf16 a[124:127], a[252:255], v[124:127], a[124:127]\n"
  "s_nop 0\n"
  "v_min_f32 v0, v232, v236\n"
  "v_max_f32 v1, v232, v236\n"
  "v_cmp_u_f32_e64 s[16:17], v232, v236\n"
  "v_cmp_nle_f32 vcc, 0x2b800000, v0\n"
  "s_or_b64 s[16:17], s[16:17], vcc\n"
  "v_cmp_nge_f32 vcc, 0x5d800000, v1\n"
  "s_or_b64 s[16:17], s[16:17], vcc\n"
  "s_cbranch_scc1 mk_slow\n"
  "s_bitcmp1_b32 s10, 0\n"
  "s_cbranch_scc1 mk_slow\n"
  "mk_norm:\n"
  "s_waitcnt vmcnt(4)\n"
  "v_cmp_ne_u32 vcc, 4, v39\n"
  "s_and_b32 vcc_lo, vcc_lo, 0x1\n"
  "s_cbranch_scc1 mk_spin3a\n"
  "mk_spin3a_b:\n"
  "s_barrier\n"
  "s_add_u32 s61, s53, 1\n"
  "s_and_b32 s61, s61, 3\n"
  "s_and_b32 s59, s53, 12\n"
  "s_add_u32 s61, s61, s59\n"
  "s_and_b32 s59, s61, 14\n"
  "s_add_u32 s61, s61, s59\n"
  "s_lshl_b32 s62, s61, 12\n"
  "s_add_u32 s56, s40, s62\n"
  "buffer_load_dwordx4 a[128:131], v244, s[48:51], s56 offen offset:0 sc1\n"
  "buffer_load_dwordx4 a[132:135], v244, s[48:51], s56 offen offset:1024 sc1\n"
  "s_add_u32 s56, s41, s62\n"
  "buffer_load_dwordx4 a[136:139], v244, s[48:51], s56 offen offset:0 sc1\n"
  "buffer_load_dwordx4 a[140:143], v244, s[48:51], s56 offen offset:1024 sc1\n"
  "s_add_u32 s56, s40, s62\n"
  "buffer_load_dwordx4 a[144:147], v244, s[48:51], s56 offen offset:2048 sc1\n"
  "buffer_load_dwordx4 a[148:151], v244, s[48:51], s56 offen offset:3072 sc1\n"
  "s_add_u32 s56, s41, s62\n"
  "buffer_load_dwordx4 a[152:155], v244, s[48:51], s56 offen offset:2048 sc1\n"
  "buffer_load_dwordx4 a[156:159], v244, s[48:51], s56 offen offset:3072 sc1\n"
  "ds_read_b64_tr_b16 v[192:193], v224 offset:0\n"
  "ds_read_b64_tr_b16 v[194:195], v224 offset:4096\n"
  "ds_read_b64_tr_b16 v[196:197], v224 offset:8192\n"
  "ds_read_b64_tr_b16 v[198:199], v224 offset:12288\n"
  "ds_read_b64_tr_b16 v[200:201], v225 offset:0\n"
  "ds_read_b64_tr_b16 v[202:203], v225 offset:4096\n"
  "ds_read_b64_tr_b16 v[204:205], v225 offset:8192\n"
  "ds_read_b64_tr_b16 v[206:207], v225 offset:12288\n"
  "v_rcp_f32 v234, v232\n"
  "v_rcp_f32 v238, v236\n"
  "s_and_b32 s61, s53, 14\n"
  "s_and_b32 s63, s53, 15\n"
  "s_add_u32 s61, s61, s63\n"
  "s_lshl_b32 s61, s61, 12\n"
  "s_add_u32 s16, s42, s61\n"
  "v_accvgpr_read_b32 v0, a64\n"
  "v_accvgpr_read_b32 v1, a65\n"
  "v_accvgpr_read_b32 v2, a66\n"
  "v_accvgpr_read_b32 v3, a67\n"
  "v_accvgpr_read_b32 v4, a68\n"
  "v_accvgpr_read_b32 v5, a69\n"
  "v_accvgpr_read_b32 v6, a70\n"
  "v_accvgpr_read_b32 v7, a71\n"
  "v_pk_mul_f32 v[0:1], v[0:1], v[234:235] op_sel_hi:[1,0]\n"
  "v_pk_mul_f32 v[2:3], v[2:3], v[234:235] op_sel_hi:[1,0]\n"
  "v_pk_mul_f32 v[4:5], v[4:5], v[234:235] op_sel_hi:[1,0]\n"
  "v_pk_mul_f32 v[6:7], v[6:7], v[234:235] op_sel_hi:[1,0]\n"
  "v_cvt_pk_bf16_f32 v160, v0, v1\n"
  "v_cvt_pk_bf16_f32 v161, v2, v3\n"
  "v_cvt_pk_bf16_f32 v162, v4, v5\n"
  "v_cvt_pk_bf16_f32 v163, v6, v7\n"
  "buffer_store_dwordx4 v[160:163], v244, s[48:51], s16 offen offset:0 sc1\n"
  "v_accvgpr_read_b32 v8, a72\n"
  "v_accvgpr_read_b32 v9, a73\n"
  "v_accvgpr_read_b32 v10, a74\n"
  "v_accvgpr_read_b32 v11, a75\n"
  "v_accvgpr_read_b32 v12, a76\n"
  "v_accvgpr_read_b32 v13, a77\n"
  "v_accvgpr_read_b32 v14, a78\n"
  "v_accvgpr_read_b32 v15, a79\n"
  "v_pk_mul_f32 v[8:9], v[8:9], v[234:235] op_sel_hi:[1,0]\n"
  "v_pk_mul_f32 v[10:11], v[10:11], v[234:235] op_sel_hi:[1,0]\n"
  "v_pk_mul_f32 v[12:13], v[12:13], v[234:235] op_sel_hi:[1,0]\n"
  "v_pk_mul_f32 v[14:15], v[14:15], v[234:235] op_sel_hi:[1,0]\n"
  "v_cvt_pk_bf16_f32 v164, v8, v9\n"
  "v_cvt_pk_bf16_f32 v165, v10, v11\n"
  "v_cvt_pk_bf16_f32 v166, v12, v13\n"
  "v_cvt_pk_bf16_f32 v167, v14, v15\n"
  "buffer_store_dwordx4 v[164:167], v244, s[48:51], s16 offen offset:1024 sc1\n"
  "v_accvgpr_read_b32 v0, a80\n"
  "v_accvgpr_read_b32 v1, a81\n"
  "v_accvgpr_read_b32 v2, a82\n"
  "v_accvgpr_read_b32 v3, a83\n"
  "v_accvgpr_read_b32 v4, a84\n"
  "v_accvgpr_read_b32 v5, a85\n"
  "v_accvgpr_read_b32 v6, a86\n"
  "v_accvgpr_read_b32 v7, a87\n"
  "v_pk_mul_f32 v[0:1], v[0:1], v[234:235] op_sel_hi:[1,0]\n"
  "v_pk_mul_f32 v[2:3], v[2:3], v[234:235] op_sel_hi:[1,0]\n"
  "v_pk_mul_f32 v[4:5], v[4:5], v[234:235] op_sel_hi:[1,0]\n"
  "v_pk_mul_f32 v[6:7], v[6:7], v[234:235] op_sel_hi:[1,0]\n"
  "v_cvt_pk_bf16_f32 v168, v0, v1\n"
  "v_cvt_pk_bf16_f32 v169, v2, v3\n"
  "v_cvt_pk_bf16_f32 v170, v4, v5\n"
  "v_cvt_pk_bf16_f32 v171, v6, v7\n"
  "buffer_store_dwordx4 v[168:171], v244, s[48:51], s16 offen offset:2048 sc1\n"
  "v_accvgpr_read_b32 v8, a88\n"
  "v_accvgpr_read_b32 v9, a89\n"
  "v_accvgpr_read_b32 v10, a90\n"
  "v_accvgpr_read_b32 v11, a91\n"
  "v_accvgpr_read_b32 v12, a92\n"
  "v_accvgpr_read_b32 v13, a93\n"
  "v_accvgpr_read_b32 v14, a94\n"
  "v_accvgpr_read_b32 v15, a95\n"
  "v_pk_mul_f32 v[8:9], v[8:9], v[234:235] op_sel_hi:[1,0]\n"
  "v_pk_mul_f32 v[10:11], v[10:11], v[234:235] op_sel_hi:[1,0]\n"
  "v_pk_mul_f32 v[12:13], v[12:13], v[234:235] op_sel_hi:[1,0]\n"
  "v_pk_mul_f32 v[14:15], v[14:15], v[234:235] op_sel_hi:[1,0]\n"
  "v_cvt_pk_bf16_f32 v172, v8, v9\n"
  "v_cvt_pk_bf16_f32 v173, v10, v11\n"
  "v_cvt_pk_bf16_f32 v174, v12, v13\n"
  "v_cvt_pk_bf16_f32 v175, v14, v15\n"
  "buffer_store_dwordx4 v[172:175], v244, s[48:51], s16 offen offset:3072 sc1\n"
  "s_add_u32 s17, s43, s61\n"
  "v_accvgpr_read_b32 v0, a96\n"
  "v_accvgpr_read_b32 v1, a97\n"
  "v_accvgpr_read_b32 v2, a98\n"
  "v_accvgpr_read_b32 v3, a99\n"
  "v_accvgpr_read_b32 v4, a100\n"
  "v_accvgpr_read_b32 v5, a101\n"
  "v_accvgpr_read_b32 v6, a102\n"
  "v_accvgpr_read_b32 v7, a103\n"
  "v_pk_mul_f32 v[0:1], v[0:1], v[238:239] op_sel_hi:[1,0]\n"
  "v_pk_mul_f32 v[2:3], v[2:3], v[238:239] op_sel_hi:[1,0]\n"
  "v_pk_mul_f32 v[4:5], v[4:5], v[238:239] op_sel_hi:[1,0]\n"
  "v_pk_mul_f32 v[6:7], v[6:7], v[238:239] op_sel_hi:[1,0]\n"
  "v_cvt_pk_bf16_f32 v176, v0, v1\n"
  "v_cvt_pk_bf16_f32 v177, v2, v3\n"
  "v_cvt_pk_bf16_f32 v178, v4, v5\n"
  "v_cvt_pk_bf16_f32 v179, v6, v7\n"
  "v_accvgpr_read_b32 v8, a104\n"
  "v_accvgpr_read_b32 v9, a105\n"
  "v_accvgpr_read_b32 v10, a106\n"
  "v_accvgpr_read_b32 v11, a107\n"
  "v_accvgpr_read_b32 v12, a108\n"
  "v_accvgpr_read_b32 v13, a109\n"
  "v_accvgpr_read_b32 v14, a110\n"
  "v_accvgpr_read_b32 v15, a111\n"
  "v_pk_mul_f32 v[8:9], v[8:9], v[238:239] op_sel_hi:[1,0]\n"
  "v_pk_mul_f32 v[10:11], v[10:11], v[238:239] op_sel_hi:[1,0]\n"
  "v_pk_mul_f32 v[12:13], v[12:13], v[238:239] op_sel_hi:[1,0]\n"
  "v_pk_mul_f32 v[14:15], v[14:15], v[238:239] op_sel_hi:[1,0]\n"
  "v_cvt_pk_bf16_f32 v180, v8, v9\n"
  "v_cvt_pk_bf16_f32 v181, v10, v11\n"
  "v_cvt_pk_bf16_f32 v182, v12, v13\n"
  "v_cvt_pk_bf16_f32 v183, v14, v15\n"
  "v_accvgpr_read_b32 v0, a112\n"
  "v_accvgpr_read_b32 v1, a113\n"
  "v_accvgpr_read_b32 v2, a114\n"
  "v_accvgpr_read_b32 v3, a115\n"
  "v_accvgpr_read_b32 v4, a116\n"
  "v_accvgpr_read_b32 v5, a117\n"
  "v_accvgpr_read_b32 v6, a118\n"
  "v_accvgpr_read_b32 v7, a119\n"
  "v_pk_mul_f32 v[0:1], v[0:1], v[238:239] op_sel_hi:[1,0]\n"
  "v_pk_mul_f32 v[2:3], v[2:3], v[238:239] op_sel_hi:[1,0]\n"
  "v_pk_mul_f32 v[4:5], v[4:5], v[238:239] op_sel_hi:[1,0]\n"
  "v_pk_mul_f32 v[6:7], v[6:7], v[238:239] op_sel_hi:[1,0]\n"
  "v_cvt_pk_bf16_f32 v184, v0, v1\n"
  "v_cvt_pk_bf16_f32 v185, v2, v3\n"
  "v_cvt_pk_bf16_f32 v186, v4, v5\n"
  "v_cvt_pk_bf16_f32 v187, v6, v7\n"
  "v_accvgpr_read_b32 v8, a120\n"
  "v_accvgpr_read_b32 v9, a121\n"
  "v_accvgpr_read_b32 v10, a122\n"
  "v_accvgpr_read_b32 v11, a123\n"
  "v_accvgpr_read_b32 v12, a124\n"
  "v_accvgpr_read_b32 v13, a125\n"
  "v_accvgpr_read_b32 v14, a126\n"
  "v_accvgpr_read_b32 v15, a127\n"
  "v_pk_mul_f32 v[8:9], v[8:9], v[238:239] op_sel_hi:[1,0]\n"
  "v_pk_mul_f32 v[10:11], v[10:11], v[238:239] op_sel_hi:[1,0]\n"
  "v_pk_mul_f32 v[12:13], v[12:13], v[238:239] op_sel_hi:[1,0]\n"
  "v_pk_mul_f32 v[14:15], v[14:15], v[238:239] op_sel_hi:[1,0]\n"
  "mk_tp12:\n"
  "s_cbranch_execz mk_tp16\n"
  "v_cvt_pk_bf16_f32 v188, v8, v9\n"
  "v_cvt_pk_bf16_f32 v189, v10, v11\n"
  "v_cvt_pk_bf16_f32 v190, v12, v13\n"
  "v_cvt_pk_bf16_f32 v191, v14, v15\n"
  "s_add_u32 s97, s11, 128\n"
  "s_nop 0\n"
  "s_waitcnt lgkmcnt(6)\n"
  "v_mfma_f32_16x16x32_bf16 v[0:3], v[128:131], v[192:195], 0\n"
  "s_waitcnt lgkmcnt(4)\n"
  "s_lshl_b32 s12, %1, 14\n"
  "ds_read_b64_tr_b16 v[208:209], v226 offset:0\n"
  "ds_read_b64_tr_b16 v[210:211], v226 offset:4096\n"
  "v_mfma_f32_16x16x32_bf16 v[0:3], v[132:135], v[196:199], v[0:3]\n"
  "s_add_u32 s12, s12, 0x18000\n"
  "v_mfma_f32_16x16x32_bf16 v[4:7], v[144:147], v[192:195], 0\n"
  "s_mul_i32 s13, %1, 24576\n"
  "buffer_store_dwordx4 v[176:179], v244, s[48:51], s17 offen offset:0 sc1\n"
  "v_mfma_f32_16x16x32_bf16 v[4:7], v[148:151], v[196:199], v[4:7]\n"
  "s_sub_u32 s54, 15, s52\n"
  "ds_read_b64_tr_b16 v[212:213], v226 offset:8192\n"
  "v_mfma_f32_16x16x32_bf16 v[8:11], v[160:163], v[192:195], 0\n"
  "s_xor_b32 s10, s53, s52\n"
  "ds_read_b64_tr_b16 v[214:215], v226 offset:12288\n"
  "v_lshrrev_b32 v239, 4, v244\n"
  "v_mfma_f32_16x16x32_bf16 v[8:11], v[164:167], v[196:199], v[8:11]\n"
  "s_bitcmp0_b32 s10, 0\n"
  "s_cselect_b32 s54, s52, s54\n"
  "v_lshrrev_b32 v240, 8, v244\n"
  "v_and_b32 v241, 0xf0, v244\n"
  "v_mfma_f32_16x16x32_bf16 v[12:15], v[176:179], v[192:195], 0\n"
  "s_lshr_b32 s60, s53, 2\n"
  "ds_read_b64_tr_b16 v[216:217], v227 offset:0\n"
  "v_lshl_add_u32 v234, v240, 17, v241\n"
  "v_mfma_f32_16x16x32_bf16 v[12:15], v[180:183], v[196:199], v[12:15]\n"
  "s_waitcnt lgkmcnt(7)\n"
  "s_and_b32 s60, s60, 4\n"
  "v_lshl_add_u32 v235, v240, 14, v241\n"
  "v_and_b32 v242, 1, v240\n"
  "v_mfma_f32_16x16x32_bf16 v[16:19], v[128:131], v[200:203], 0\n"
  "s_waitcnt lgkmcnt(5)\n"
  "s_and_b32 s90, s53, 3\n"
  "ds_read_b64_tr_b16 v[218:219], v227 offset:4096\n"
  "v_lshrrev_b32 v245, 1, v240\n"
  "v_mfma_f32_16x16x32_bf16 v[16:19], v[132:135], v[204:207], v[16:19]\n"
  "s_add_u32 s60, s60, s90\n"
  "v_lshl_add_u32 v245, v242, 1, v245\n"
  "v_lshl_add_u32 v236, v245, 10, v241\n"
  "v_mfma_f32_16x16x32_bf16 v[20:23], v[144:147], v[200:203], 0\n"
  "s_lshr_b32 s58, s53, 1\n"
  "ds_read_b64_tr_b16 v[220:221], v227 offset:8192\n"
  "v_lshlrev_b32 v237, 4, v242\n"
  "v_mfma_f32_16x16x32_bf16 v[20:23], v[148:151], v[204:207], v[20:23]\n"
  "s_lshl_b32 s90, s58, 12\n"
  "v_add_u32 v237, 0x4000, v237\n"
  "v_mfma_f32_16x16x32_bf16 v[24:27], v[160:163], v[200:203], 0\n"
  "s_lshl_b32 s91, s54, 4\n"
  "buffer_store_dwordx4 v[180:183], v244, s[48:51], s17 offen offset:1024 sc1\n"
  "v_mfma_f32_16x16x32_bf16 v[24:27], v[164:167], v[204:207], v[24:27]\n"
  "s_add_u32 s58, s58, s91\n"
  "ds_read_b64_tr_b16 v[222:223], v227 offset:12288\n"
  "v_mfma_f32_16x16x32_bf16 v[28:31], v[176:179], v[200:203], 0\n"
  "s_lshl_b32 s91, s54, 17\n"
  "ds_read_b64_tr_b16 a[224:225], v228 offset:0\n"
  "v_mfma_f32_16x16x32_bf16 v[28:31], v[180:183], v[204:207], v[28:31]\n"
  "s_waitcnt lgkmcnt(6)\n"
  "s_add_u32 s90, s90, s91\n"
  "v_mfma_f32_16x16x32_bf16 v[32:35], v[128:131], v[208:211], 0\n"
  "s_waitcnt lgkmcnt(5)\n"
  "s_lshl_b32 s91, %1, 8\n"
  "ds_read_b64_tr_b16 a[226:227], v228 offset:4096\n"
  "v_mfma_f32_16x16x32_bf16 v[32:35], v[132:135], v[212:215], v[32:35]\n"
  "s_add_u32 s55, s53, 2\n"
  "v_mfma_f32_16x16x32_bf16 v[36:39], v[144:147], v[208:211], 0\n"
  "s_and_b32 s55, s55, 3\n"
  "ds_read_b64_tr_b16 a[228:229], v228 offset:8192\n"
  "v_mfma_f32_16x16x32_bf16 v[36:39], v[148:151], v[212:215], v[36:39]\n"
  "s_lshl_b32 s55, s55, 17\n"
  "v_mfma_f32_16x16x32_bf16 v[40:43], v[160:163], v[208:211], 0\n"
  "s_add_u32 s55, s55, 0x10000\n"
  "ds_read_b64_tr_b16 a[230:231], v228 offset:12288\n"
  "v_mfma_f32_16x16x32_bf16 v[40:43], v[164:167], v[212:215], v[40:43]\n"
  "s_sub_u32 m0, s57, 0x4000\n"
  "v_mfma_f32_16x16x32_bf16 v[44:47], v[176:179], v[208:211], 0\n"
  "s_add_u32 s83, s90, s91\n"
  "buffer_store_dwordx4 v[184:187], v244, s[48:51], s17 offen offset:2048 sc1\n"
  "v_mfma_f32_16x16x32_bf16 v[44:47], v[180:183], v[212:215], v[44:47]\n"
  "s_waitcnt lgkmcnt(5)\n"
  "s_mov_b64 s[44:45], s[8:9]\n"
  "ds_read_b64_tr_b16 a[232:233], v229 offset:0\n"
  "v_mfma_f32_16x16x32_bf16 v[48:51], v[128:131], v[216:219], 0\n"
  "s_waitcnt lgkmcnt(5)\n"
  "s_mov_b32 s46, 0x200000\n"
  "ds_read_b64_tr_b16 a[234:235], v229 offset:4096\n"
  "v_mfma_f32_16x16x32_bf16 v[48:51], v[132:135], v[220:223], v[48:51]\n"
  "s_bfe_u32 s90, s53, 0x20002\n"
  "buffer_load_dwordx4 v249, s[36:39], s55 offen offset:0 lds\n"
  "v_mfma_f32_16x16x32_bf16 v[52:55], v[144:147], v[216:219], 0\n"
  "s_mul_i32 s91, s90, 7\n"
  "ds_read_b64_tr_b16 a[236:237], v229 offset:8192\n"
  "v_mfma_f32_16x16x32_bf16 v[52:55], v[148:151], v[220:223], v[52:55]\n"
  "s_add_u32 s14, s52, 7\n"
  "v_mfma_f32_16x16x32_bf16 v[56:59], v[160:163], v[216:219], 0\n"
  "s_and_b32 s14, s14, 7\n"
  "ds_read_b64_tr_b16 a[238:239], v229 offset:12288\n"
  "v_mfma_f32_16x16x32_bf16 v[56:59], v[164:167], v[220:223], v[56:59]\n"
  "s_add_u32 s14, s91, s14\n"
  "v_mfma_f32_16x16x32_bf16 v[60:63], v[176:179], v[216:219], 0\n"
  "s_lshl_b32 s90, s90, 3\n"
  "buffer_load_dwordx4 v250, s[36:39], s55 offen offset:1024 lds\n"
  "v_mfma_f32_16x16x32_bf16 v[60:63], v[180:183], v[220:223], v[60:63]\n"
  "s_waitcnt lgkmcnt(5)\n"
  "s_lshr_b32 s91, s24, 7\n"
  "ds_read_b64_tr_b16 a[240:241], v230 offset:0\n"
  "v_mfma_f32_16x16x32_bf16 v[64:67], v[128:131], a[224:227], 0\n"
  "s_waitcnt lgkmcnt(5)\n"
  "s_sub_u32 s91, s52, s91\n"
  "buffer_store_dwordx4 v[188:191], v244, s[48:51], s17 offen offset:3072 sc1\n"
  "v_mfma_f32_16x16x32_bf16 v[64:67], v[132:135], a[228:231], v[64:67]\n"
  "s_add_u32 s91, s91, 11\n"
  "ds_read_b64_tr_b16 a[242:243], v230 offset:4096\n"
  "v_mfma_f32_16x16x32_bf16 v[68:71], v[144:147], a[224:227], 0\n"
  "s_and_b32 s91, s91, 7\n"
  "buffer_load_dwordx4 v251, s[36:39], s55 offen offset:2048 lds\n"
  "v_mfma_f32_16x16x32_bf16 v[68:71], v[148:151], a[228:231], v[68:71]\n"
  "s_add_u32 s98, s90, s91\n"
  "ds_read_b64_tr_b16 a[244:245], v230 offset:8192\n"
  "v_mfma_f32_16x16x32_bf16 v[72:75], v[160:163], a[224:227], 0\n"
  "s_lshl_b32 s90, s14, 7\n"
  "ds_read_b64_tr_b16 a[246:247], v230 offset:12288\n"
  "v_mfma_f32_16x16x32_bf16 v[72:75], v[164:167], a[228:231], v[72:75]\n"
  "s_add_u32 s64, s20, s90\n"
  "s_addc_u32 s65, s21, 0\n"
  "v_mfma_f32_16x16x32_bf16 v[76:79], v[176:179], a[224:227], 0\n"
  "s_sub_u32 s66, 0x700000, s90\n"
  "ds_read_b64_tr_b16 a[248:249], v231 offset:0\n"
  "v_mfma_f32_16x16x32_bf16 v[76:79], v[180:183], a[228:231], v[76:79]\n"
  "s_waitcnt lgkmcnt(6)\n"
  "s_cmp_eq_u32 s52, 0\n"
  "s_cselect_b32 s66, 0, s66\n"
  "buffer_load_dwordx4 v252, s[36:39], s55 offen offset:3072 lds\n"
  "v_mfma_f32_16x16x32_bf16 v[80:83], v[128:131], a[232:235], 0\n"
  "s_waitcnt lgkmcnt(5)\n"
  "s_add_u32 s61, s53, 2\n"
  "s_and_b32 s61, s61, 3\n"
  "ds_read_b64_tr_b16 a[250:251], v231 offset:4096\n"
  "v_mfma_f32_16x16x32_bf16 v[80:83], v[132:135], a[236:239], v[80:83]\n"
  "s_and_b32 s59, s53, 12\n"
  "s_add_u32 s61, s61, s59\n"
  "v_mfma_f32_16x16x32_bf16 v[84:87], v[144:147], a[232:235], 0\n"
  "s_and_b32 s59, s61, 14\n"
  "s_add_u32 s61, s61, s59\n"
  "ds_read_b64_tr_b16 a[252:253], v231 offset:8192\n"
  "v_mfma_f32_16x16x32_bf16 v[84:87], v[148:151], a[236:239], v[84:87]\n"
  "s_lshl_b32 s62, s61, 12\n"
  "v_mfma_f32_16x16x32_bf16 v[88:91], v[160:163], a[232:235], 0\n"
  "s_add_u32 s56, s40, s62\n"
  "buffer_load_dwordx4 a[160:163], v244, s[48:51], s56 offen offset:0 sc1\n"
  "v_mfma_f32_16x16x32_bf16 v[88:91], v[164:167], a[236:239], v[88:91]\n"
  "s_mov_b32 s67, 0x20000\n"
  "ds_read_b64_tr_b16 a[254:255], v231 offset:12288\n"
  "v_mfma_f32_16x16x32_bf16 v[92:95], v[176:179], a[232:235], 0\n"
  "s_mul_i32 s89, %1, 458752\n"
  "ds_read_b64_tr_b16 v[192:193], v224 offset:16384\n"
  "v_mfma_f32_16x16x32_bf16 v[92:95], v[180:183], a[236:239], v[92:95]\n"
  "s_waitcnt lgkmcnt(6)\n"
  "s_and_b32 s91, s10, 1\n"
  "v_mfma_f32_16x16x32_bf16 v[96:99], v[128:131], a[240:243], 0\n"
  "s_waitcnt lgkmcnt(5)\n"
  "s_mul_i32 s90, s91, 1835008\n"
  "buffer_load_dwordx4 a[164:167], v244, s[48:51], s56 offen offset:1024 sc1\n"
  "v_mfma_f32_16x16x32_bf16 v[96:99], v[132:135], a[244:247], v[96:99]\n"
  "s_add_u32 s56, s41, s62\n"
  "ds_read_b64_tr_b16 v[194:195], v224 offset:20480\n"
  "v_mfma_f32_16x16x32_bf16 v[100:103], v[144:147], a[240:243], 0\n"
  "s_sub_u32 s11, s89, s90\n"
  "ds_read_b64_tr_b16 v[196:197], v224 offset:24576\n"
  "v_mfma_f32_16x16x32_bf16 v[100:103], v[148:151], a[244:247], v[100:103]\n"
  "s_add_u32 s89, s89, s90\n"
  "v_mfma_f32_16x16x32_bf16 v[104:107], v[160:163], a[240:243], 0\n"
  "s_and_b32 s90, s53, 28\n"
  "buffer_load_dwordx4 a[168:171], v244, s[48:51], s56 offen offset:0 sc1\n"
  "v_mfma_f32_16x16x32_bf16 v[104:107], v[164:167], a[244:247], v[104:107]\n"
  "s_add_u32 s90, s90, s53\n"
  "ds_read_b64_tr_b16 v[198:199], v224 offset:28672\n"
  "v_mfma_f32_16x16x32_bf16 v[108:111], v[176:179], a[240:243], 0\n"
  "s_lshl_b32 s90, s90, 11\n"
  "ds_read_b64_tr_b16 v[200:201], v225 offset:16384\n"
  "v_mfma_f32_16x16x32_bf16 v[108:111], v[180:183], a[244:247], v[108:111]\n"
  "s_waitcnt lgkmcnt(6)\n"
  "s_and_b32 s91, %1, 1\n"
  "v_mfma_f32_16x16x32_bf16 v[112:115], v[128:131], a[248:251], 0\n"
  "s_waitcnt lgkmcnt(5)\n"
  "s_lshl_b32 s91, s91, 19\n"
  "buffer_load_dwordx4 a[172:175], v244, s[48:51], s56 offen offset:1024 sc1\n"
  "v_mfma_f32_16x16x32_bf16 v[112:115], v[132:135], a[252:255], v[112:115]\n"
  "s_add_u32 s55, s53, 3\n"
  "ds_read_b64_tr_b16 v[202:203], v225 offset:20480\n"
  "v_mfma_f32_16x16x32_bf16 v[116:119], v[144:147], a[248:251], 0\n"
  "s_and_b32 s55, s55, 3\n"
  "ds_read_b64_tr_b16 v[204:205], v225 offset:24576\n"
  "v_mfma_f32_16x16x32_bf16 v[116:119], v[148:151], a[252:255], v[116:119]\n"
  "s_lshl_b32 s55, s55, 17\n"
  "v_mfma_f32_16x16x32_bf16 v[120:123], v[160:163], a[248:251], 0\n"
  "s_sub_u32 m0, s57, 0xc000\n"
  "ds_read_b64_tr_b16 v[206:207], v225 offset:28672\n"
  "v_mfma_f32_16x16x32_bf16 v[120:123], v[164:167], a[252:255], v[120:123]\n"
  "s_add_u32 s90, s90, s91\n"
  "v_mfma_f32_16x16x32_bf16 v[124:127], v[176:179], a[248:251], 0\n"
  "s_add_u32 s90, s90, 0x5000000\n"
  "buffer_load_dwordx4 v249, s[36:39], s55 offen offset:0 lds\n"
  "v_mfma_f32_16x16x32_bf16 v[124:127], v[180:183], a[252:255], v[124:127]\n"
  "s_waitcnt lgkmcnt(5)\n"
  "s_and_b32 s91, %1, 2\n"
  "ds_read_b64_tr_b16 v[208:209], v226 offset:16384\n"
  "v_mfma_f32_16x16x32_bf16 v[0:3], v[136:139], v[192:195], v[0:3]\n"
  "s_waitcnt lgkmcnt(5)\n"
  "s_lshl_b32 s91, s91, 12\n"
  "ds_read_b64_tr_b16 v[210:211], v226 offset:20480\n"
  "v_mfma_f32_16x16x32_bf16 v[0:3], v[140:143], v[196:199], v[0:3]\n"
  "s_add_u32 s90, s90, s91\n"
  "v_mfma_f32_16x16x32_bf16 v[4:7], v[152:155], v[192:195], v[4:7]\n"
  "s_lshr_b32 s91, s52, 1\n"
  "ds_read_b64_tr_b16 v[212:213], v226 offset:24576\n"
  "v_mfma_f32_16x16x32_bf16 v[4:7], v[156:159], v[196:199], v[4:7]\n"
  "s_lshr_b32 s76, s33, s91\n"
  "buffer_load_dwordx4 v250, s[36:39], s55 offen offset:1024 lds\n"
  "v_mfma_f32_16x16x32_bf16 v[8:11], v[168:171], v[192:195], v[8:11]\n"
  "s_lshl_b32 s77, s33, s91\n"
  "ds_read_b64_tr_b16 v[214:215], v226 offset:28672\n"
  "v_mfma_f32_16x16x32_bf16 v[8:11], v[172:175], v[196:199], v[8:11]\n"
  "s_lshl_b32 s76, s76, 5\n"
  "v_mfma_f32_16x16x32_bf16 v[12:15], v[184:187], v[192:195], v[12:15]\n"
  "s_lshr_b32 s77, s77, 2\n"
  "ds_read_b64_tr_b16 v[216:217], v227 offset:16384\n"
  "v_mfma_f32_16x16x32_bf16 v[12:15], v[188:191], v[196:199], v[12:15]\n"
  "s_waitcnt lgkmcnt(6)\n"
  "s_and_b32 s76, s76, 0x2000\n"
  "buffer_load_dwordx4 v251, s[36:39], s55 offen offset:2048 lds\n"
  "v_mfma_f32_16x16x32_bf16 v[16:19], v[136:139], v[200:203], v[16:19]\n"
  "s_waitcnt lgkmcnt(5)\n"
  "s_and_b32 s77, s77, 0x2000\n"
  "ds_read_b64_tr_b16 v[218:219], v227 offset:20480\n"
  "v_mfma_f32_16x16x32_bf16 v[16:19], v[140:143], v[204:207], v[16:19]\n"
  "s_xor_b32 s76, s76, s90\n"
  "v_mfma_f32_16x16x32_bf16 v[20:23], v[152:155], v[200:203], v[20:23]\n"
  "s_xor_b32 s77, s77, s90\n"
  "ds_read_b64_tr_b16 v[220:221], v227 offset:24576\n"
  "v_mfma_f32_16x16x32_bf16 v[20:23], v[156:159], v[204:207], v[20:23]\n"
  "s_lshl_b32 s91, s52, 20\n"
  "v_mfma_f32_16x16x32_bf16 v[24:27], v[168:171], v[200:203], v[24:27]\n"
  "s_add_u32 s76, s76, s91\n"
  "buffer_load_dwordx4 v252, s[36:39], s55 offen offset:3072 lds\n"
  "v_mfma_f32_16x16x32_bf16 v[24:27], v[172:175], v[204:207], v[24:27]\n"
  "s_add_u32 s55, s53, 3\n"
  "ds_read_b64_tr_b16 v[222:223], v227 offset:28672\n"
  "v_mfma_f32_16x16x32_bf16 v[28:31], v[184:187], v[200:203], v[28:31]\n"
  "s_and_b32 s55, s55, 3\n"
  "ds_read_b64_tr_b16 a[224:225], v228 offset:16384\n"
  "v_mfma_f32_16x16x32_bf16 v[28:31], v[188:191], v[204:207], v[28:31]\n"
  "s_waitcnt lgkmcnt(6)\n"
  "s_lshl_b32 s55, s55, 17\n"
  "v_mfma_f32_16x16x32_bf16 v[32:35], v[136:139], v[208:211], v[32:35]\n"
  "s_waitcnt lgkmcnt(5)\n"
  "s_add_u32 s55, s55, 0x10000\n"
  "ds_read_b64_tr_b16 a[226:227], v228 offset:20480\n"
  "v_mfma_f32_16x16x32_bf16 v[32:35], v[140:143], v[212:215], v[32:35]\n"
  "s_sub_u32 m0, s57, 0x14000\n"
  "v_mfma_f32_16x16x32_bf16 v[36:39], v[152:155], v[208:211], v[36:39]\n"
  "s_sub_u32 s77, s77, s91\n"
  "buffer_load_dwordx4 v249, s[36:39], s55 offen offset:0 lds\n"
  "v_mfma_f32_16x16x32_bf16 v[36:39], v[156:159], v[212:215], v[36:39]\n"
  "s_add_u32 s77, s77, 0xf00000\n"
  "ds_read_b64_tr_b16 a[228:229], v228 offset:24576\n"
  "v_mfma_f32_16x16x32_bf16 v[40:43], v[168:171], v[208:211], v[40:43]\n"
  "s_lshl_b32 s78, s52, 2\n"
  "ds_read_b64_tr_b16 a[230:231], v228 offset:28672\n"
  "v_mfma_f32_16x16x32_bf16 v[40:43], v[172:175], v[212:215], v[40:43]\n"
  "s_add_u32 s78, s78, %1\n"
  "v_mfma_f32_16x16x32_bf16 v[44:47], v[184:187], v[208:211], v[44:47]\n"
  "s_lshl_b32 s78, s78, 2\n"
  "buffer_load_dwordx4 v250, s[36:39], s55 offen offset:1024 lds\n"
  "v_mfma_f32_16x16x32_bf16 v[44:47], v[188:191], v[212:215], v[44:47]\n"
  "s_waitcnt lgkmcnt(5)\n"
  "s_and_b32 s63, s53, 3\n"
  "ds_read_b64_tr_b16 a[232:233], v229 offset:16384\n"
  "v_mfma_f32_16x16x32_bf16 v[48:51], v[136:139], v[216:219], v[48:51]\n"
  "s_waitcnt lgkmcnt(5)\n"
  "s_add_u32 s78, s78, s63\n"
  "ds_read_b64_tr_b16 a[234:235], v229 offset:20480\n"
  "v_mfma_f32_16x16x32_bf16 v[48:51], v[140:143], v[220:223], v[48:51]\n"
  "s_lshl_b32 s78, s78, 8\n"
  "v_mfma_f32_16x16x32_bf16 v[52:55], v[152:155], v[216:219], v[52:55]\n"
  "s_lshl_b32 s63, s78, 2\n"
  "buffer_load_dwordx4 v251, s[36:39], s55 offen offset:2048 lds\n"
  "v_mfma_f32_16x16x32_bf16 v[52:55], v[156:159], v[220:223], v[52:55]\n"
  "s_xor_b32 s63, s63, s78\n"
  "ds_read_b64_tr_b16 a[236:237], v229 offset:24576\n"
  "v_mfma_f32_16x16x32_bf16 v[56:59], v[168:171], v[216:219], v[56:59]\n"
  "s_bfe_i32 s63, s63, 0x1000d\n"
  "ds_read_b64_tr_b16 a[238:239], v229 offset:28672\n"
  "v_mfma_f32_16x16x32_bf16 v[56:59], v[172:175], v[220:223], v[56:59]\n"
  "s_and_b32 s63, s63, 0x2800\n"
  "v_mfma_f32_16x16x32_bf16 v[60:63], v[184:187], v[216:219], v[60:63]\n"
  "s_xor_b32 s78, s78, s63\n"
  "ds_read_b64_tr_b16 a[240:241], v230 offset:16384\n"
  "v_mfma_f32_16x16x32_bf16 v[60:63], v[188:191], v[220:223], v[60:63]\n"
  "s_waitcnt lgkmcnt(6)\n"
  "s_lshr_b32 s63, s33, 6\n"
  "buffer_load_dwordx4 v252, s[36:39], s55 offen offset:3072 lds\n"
  "v_mfma_f32_16x16x32_bf16 v[64:67], v[136:139], a[224:227], v[64:67]\n"
  "s_waitcnt lgkmcnt(5)\n"
  "s_add_u32 s56, s40, s62\n"
  "ds_read_b64_tr_b16 a[242:243], v230 offset:20480\n"
  "v_mfma_f32_16x16x32_bf16 v[64:67], v[140:143], a[228:231], v[64:67]\n"
  "s_and_b32 s63, s63, 0x2000\n"
  "v_mfma_f32_16x16x32_bf16 v[68:71], v[152:155], a[224:227], v[68:71]\n"
  "s_xor_b32 s78, s78, s63\n"
  "ds_read_b64_tr_b16 a[244:245], v230 offset:24576\n"
  "v_mfma_f32_16x16x32_bf16 v[68:71], v[156:159], a[228:231], v[68:71]\n"
  "s_add_u32 s78, s78, 0x6614000\n"
  "buffer_load_dwordx4 a[176:179], v244, s[48:51], s56 offen offset:2048 sc1\n"
  "v_mfma_f32_16x16x32_bf16 v[72:75], v[168:171], a[224:227], v[72:75]\n"
  "s_waitcnt vmcnt(15)\n"
  "buffer_atomic_add v248, v253, s[48:51], s97 offen sc1\n"
  "v_mfma_f32_16x16x32_bf16 v[72:75], v[172:175], a[228:231], v[72:75]\n"
  "s_add_u32 s79, s78, 128\n"
  "ds_read_b64_tr_b16 a[246:247], v230 offset:28672\n"
  "v_mfma_f32_16x16x32_bf16 v[76:79], v[184:187], a[224:227], v[76:79]\n"
  "s_lshl_b32 s80, s52, 4\n"
  "ds_read_b64_tr_b16 a[248:249], v231 offset:16384\n"
  "v_mfma_f32_16x16x32_bf16 v[76:79], v[188:191], a[228:231], v[76:79]\n"
  "s_waitcnt lgkmcnt(6)\n"
  "s_and_b32 s90, s58, 12\n"
  "v_mfma_f32_16x16x32_bf16 v[80:83], v[136:139], a[232:235], v[80:83]\n"
  "s_waitcnt lgkmcnt(5)\n"
  "s_add_u32 s80, s80, s90\n"
  "buffer_load_dwordx4 a[180:183], v244, s[48:51], s56 offen offset:3072 sc1\n"
  "v_mfma_f32_16x16x32_bf16 v[80:83], v[140:143], a[236:239], v[80:83]\n"
  "s_add_u32 s56, s41, s62\n"
  "ds_read_b64_tr_b16 a[250:251], v231 offset:20480\n"
  "v_mfma_f32_16x16x32_bf16 v[84:87], v[152:155], a[232:235], v[84:87]\n"
  "s_add_u32 s80, s80, %1\n"
  "ds_read_b64_tr_b16 a[252:253], v231 offset:24576\n"
  "v_mfma_f32_16x16x32_bf16 v[84:87], v[156:159], a[236:239], v[84:87]\n"
  "s_lshl_b32 s80, s80, 1\n"
  "v_mfma_f32_16x16x32_bf16 v[88:91], v[168:171], a[232:235], v[88:91]\n"
  "s_and_b32 s90, s10, 1\n"
  "buffer_load_dwordx4 a[184:187], v244, s[48:51], s56 offen offset:2048 sc1\n"
  "v_mfma_f32_16x16x32_bf16 v[88:91], v[172:175], a[236:239], v[88:91]\n"
  "s_add_u32 s80, s80, s90\n"
  "ds_read_b64_tr_b16 a[254:255], v231 offset:28672\n"
  "v_add_u32 v224, 0xfffe0000, v224\n"
  "v_mfma_f32_16x16x32_bf16 v[92:95], v[184:187], a[232:235], v[92:95]\n"
  "s_lshl_b32 s80, s80, 7\n"
  "ds_read_b64_tr_b16 v[192:193], v224 offset:0\n"
  "ds_read_b64_tr_b16 v[194:195], v224 offset:4096\n"
  "v_mfma_f32_16x16x32_bf16 v[92:95], v[188:191], a[236:239], v[92:95]\n"
  "s_waitcnt lgkmcnt(7)\n"
  "s_lshl_b32 s63, s80, 2\n"
  "ds_read_b64_tr_b16 v[196:197], v224 offset:8192\n"
  "ds_read_b64_tr_b16 v[198:199], v224 offset:12288\n"
  "v_mfma_f32_16x16x32_bf16 v[96:99], v[136:139], a[240:243], v[96:99]\n"
  "s_waitcnt lgkmcnt(8)\n"
  "s_xor_b32 s63, s63, s80\n"
  "v_add_u32 v225, 0xfffe0000, v225\n"
  "v_mfma_f32_16x16x32_bf16 v[96:99], v[140:143], a[244:247], v[96:99]\n"
  "s_bfe_i32 s63, s63, 0x1000d\n"
  "ds_read_b64_tr_b16 v[200:201], v225 offset:0\n"
  "ds_read_b64_tr_b16 v[202:203], v225 offset:4096\n"
  "v_mfma_f32_16x16x32_bf16 v[100:103], v[152:155], a[240:243], v[100:103]\n"
  "s_and_b32 s63, s63, 0x2800\n"
  "buffer_load_dwordx4 a[188:191], v244, s[48:51], s56 offen offset:3072 sc1\n"
  "v_mfma_f32_16x16x32_bf16 v[100:103], v[156:159], a[244:247], v[100:103]\n"
  "s_add_u32 s61, s53, 3\n"
  "s_and_b32 s61, s61, 3\n"
  "ds_read_b64_tr_b16 v[204:205], v225 offset:8192\n"
  "ds_read_b64_tr_b16 v[206:207], v225 offset:12288\n"
  "v_mfma_f32_16x16x32_bf16 v[104:107], v[168:171], a[240:243], v[104:107]\n"
  "s_and_b32 s59, s53, 12\n"
  "s_add_u32 s61, s61, s59\n"
  "v_add_u32 v226, 0xfffe0000, v226\n"
  "v_mfma_f32_16x16x32_bf16 v[104:107], v[172:175], a[244:247], v[104:107]\n"
  "s_and_b32 s59, s61, 14\n"
  "s_add_u32 s61, s61, s59\n"
  "ds_read_b64_tr_b16 v[208:209], v226 offset:0\n"
  "ds_read_b64_tr_b16 v[210:211], v226 offset:4096\n"
  "v_mfma_f32_16x16x32_bf16 v[108:111], v[184:187], a[240:243], v[108:111]\n"
  "s_lshl_b32 s62, s61, 12\n"
  "ds_read_b64_tr_b16 v[212:213], v226 offset:8192\n"
  "ds_read_b64_tr_b16 v[214:215], v226 offset:12288\n"
  "v_mfma_f32_16x16x32_bf16 v[108:111], v[188:191], a[244:247], v[108:111]\n"
  "s_waitcnt lgkmcnt(13)\n"
  "s_add_u32 s56, s40, s62\n"
  "buffer_load_dwordx4 a[192:195], v244, s[48:51], s56 offen offset:0 sc1\n"
  "v_mfma_f32_16x16x32_bf16 v[112:115], v[136:139], a[248:251], v[112:115]\n"
  "s_waitcnt lgkmcnt(12)\n"
  "s_xor_b32 s80, s80, s63\n"
  "v_add_u32 v227, 0xfffe0000, v227\n"
  "v_mfma_f32_16x16x32_bf16 v[112:115], v[140:143], a[252:255], v[112:115]\n"
  "s_lshr_b32 s63, s33, 6\n"
  "ds_read_b64_tr_b16 v[216:217], v227 offset:0\n"
  "ds_read_b64_tr_b16 v[218:219], v227 offset:4096\n"
  "v_mfma_f32_16x16x32_bf16 v[116:119], v[152:155], a[248:251], v[116:119]\n"
  "s_and_b32 s63, s63, 0x2000\n"
  "ds_read_b64_tr_b16 v[220:221], v227 offset:8192\n"
  "ds_read_b64_tr_b16 v[222:223], v227 offset:12288\n"
  "v_mfma_f32_16x16x32_bf16 v[116:119], v[156:159], a[252:255], v[116:119]\n"
  "s_xor_b32 s80, s80, s63\n"
  "buffer_load_dwordx4 a[196:199], v244, s[48:51], s56 offen offset:1024 sc1\n"
  "v_mfma_f32_16x16x32_bf16 v[120:123], v[168:171], a[248:251], v[120:123]\n"
  "s_add_u32 s56, s41, s62\n"
  "v_add_u32 v228, 0xfffe0000, v228\n"
  "v_mfma_f32_16x16x32_bf16 v[120:123], v[172:175], a[252:255], v[120:123]\n"
  "s_add_u32 s80, s80, 0x6614000\n"
  "ds_read_b64_tr_b16 a[224:225], v228 offset:0\n"
  "ds_read_b64_tr_b16 a[226:227], v228 offset:4096\n"
  "v_mfma_f32_16x16x32_bf16 v[124:127], v[184:187], a[248:251], v[124:127]\n"
  "s_lshl_b32 s81, s54, 3\n"
  "ds_read_b64_tr_b16 a[228:229], v228 offset:8192\n"
  "ds_read_b64_tr_b16 a[230:231], v228 offset:12288\n"
  "v_mfma_f32_16x16x32_bf16 v[124:127], v[188:191], a[252:255], v[124:127]\n"
  "s_waitcnt lgkmcnt(12)\n"
  "s_waitcnt vmcnt(35)\n"
  "buffer_load_dwordx4 a[200:203], v244, s[48:51], s56 offen offset:0 sc1\n"
  "v_mfma_f32_16x16x32_bf16 v[0:3], a[128:131], v[192:195], v[0:3]\n"
  "s_and_b32 s90, s58, 7\n"
  "v_add_u32 v229, 0xfffe0000, v229\n"
  "v_mfma_f32_16x16x32_bf16 v[0:3], a[132:135], v[196:199], v[0:3]\n"
  "s_add_u32 s81, s81, s90\n"
  "ds_read_b64_tr_b16 a[232:233], v229 offset:0\n"
  "ds_read_b64_tr_b16 a[234:235], v229 offset:4096\n"
  "v_mfma_f32_16x16x32_bf16 v[4:7], a[136:139], v[192:195], v[4:7]\n"
  "s_lshl_b32 s81, s81, 17\n"
  "ds_read_b64_tr_b16 a[236:237], v229 offset:8192\n"
  "ds_read_b64_tr_b16 a[238:239], v229 offset:12288\n"
  "v_mfma_f32_16x16x32_bf16 v[4:7], a[140:143], v[196:199], v[4:7]\n"
  "s_lshr_b32 s90, s54, 1\n"
  "buffer_load_dwordx4 a[204:207], v244, s[48:51], s56 offen offset:1024 sc1\n"
  "v_mfma_f32_16x16x32_bf16 v[16:19], a[128:131], v[200:203], v[16:19]\n"
  "s_add_u32 s56, s40, s62\n"
  "v_add_u32 v230, 0xfffe0000, v230\n"
  "v_lshrrev_b32 v241, 3, v239\n"
  "v_mfma_f32_16x16x32_bf16 v[16:19], a[132:135], v[204:207], v[16:19]\n"
  "s_lshr_b32 s90, s35, s90\n"
  "ds_read_b64_tr_b16 a[240:241], v230 offset:0\n"
  "ds_read_b64_tr_b16 a[242:243], v230 offset:4096\n"
  "v_mfma_f32_16x16x32_bf16 v[20:23], a[136:139], v[200:203], v[20:23]\n"
  "s_lshl_b32 s90, s90, 5\n"
  "ds_read_b64_tr_b16 a[244:245], v230 offset:8192\n"
  "ds_read_b64_tr_b16 a[246:247], v230 offset:12288\n"
  "v_mfma_f32_16x16x32_bf16 v[20:23], a[140:143], v[204:207], v[20:23]\n"
  "s_waitcnt lgkmcnt(12)\n"
  "s_and_b32 s90, s90, 0x2000\n"
  "v_add_u32 v231, 0xfffe0000, v231\n"
  "v_and_b32 v242, 7, v239\n"
  "v_mfma_f32_16x16x32_bf16 v[32:35], a[128:131], v[208:211], v[32:35]\n"
  "s_add_u32 s81, s81, s90\n"
  "buffer_load_dwordx4 a[208:211], v244, s[48:51], s56 offen offset:2048 sc1\n"
  "v_mfma_f32_16x16x32_bf16 v[32:35], a[132:135], v[212:215], v[32:35]\n"
  "s_lshl_b32 s90, %1, 11\n"
  "ds_read_b64_tr_b16 a[248:249], v231 offset:0\n"
  "ds_read_b64_tr_b16 a[250:251], v231 offset:4096\n"
  "v_mfma_f32_16x16x32_bf16 v[36:39], a[136:139], v[208:211], v[36:39]\n"
  "s_add_u32 s81, s81, s90\n"
  "ds_read_b64_tr_b16 a[252:253], v231 offset:8192\n"
  "ds_read_b64_tr_b16 a[254:255], v231 offset:12288\n"
  "v_mfma_f32_16x16x32_bf16 v[36:39], a[140:143], v[212:215], v[36:39]\n"
  "s_add_u32 s81, s81, 0x5000000\n"
  "ds_read_b64_tr_b16 v[192:193], v224 offset:32768\n"
  "ds_read_b64_tr_b16 v[194:195], v224 offset:36864\n"
  "v_mfma_f32_16x16x32_bf16 v[48:51], a[128:131], v[216:219], v[48:51]\n"
  "s_add_u32 s82, s81, 0x10000\n"
  "buffer_load_dwordx4 a[212:215], v244, s[48:51], s56 offen offset:3072 sc1\n"
  "v_mfma_f32_16x16x32_bf16 v[48:51], a[132:135], v[220:223], v[48:51]\n"
  "s_add_u32 s56, s41, s62\n"
  "ds_read_b64_tr_b16 v[196:197], v224 offset:40960\n"
  "ds_read_b64_tr_b16 v[198:199], v224 offset:45056\n"
  "v_mfma_f32_16x16x32_bf16 v[52:55], a[136:139], v[216:219], v[52:55]\n"
  "s_bitcmp0_b32 s10, 0\n"
  "s_cselect_b32 s75, 1, 0\n"
  "s_cselect_b32 s8, s81, 0x7fff0000\n"
  "ds_read_b64_tr_b16 v[200:201], v225 offset:32768\n"
  "v_lshlrev_b32 v245, 1, v240\n"
  "v_mfma_f32_16x16x32_bf16 v[52:55], a[140:143], v[220:223], v[52:55]\n"
  "s_waitcnt lgkmcnt(13)\n"
  "s_add_u32 s9, s8, 0x8000\n"
  "s_add_u32 s9, s9, 0x8000\n"
  "ds_read_b64_tr_b16 v[202:203], v225 offset:36864\n"
  "v_xor_b32 v242, v242, v245\n"
  "v_mfma_f32_16x16x32_bf16 v[64:67], a[128:131], a[224:227], v[64:67]\n"
  "s_lshr_b32 s84, s58, 2\n"
  "buffer_load_dwordx4 a[216:219], v244, s[48:51], s56 offen offset:2048 sc1\n"
  "v_mfma_f32_16x16x32_bf16 v[64:67], a[132:135], a[228:231], v[64:67]\n"
  "s_lshl_b32 s84, s84, 15\n"
  "ds_read_b64_tr_b16 v[204:205], v225 offset:40960\n"
  "ds_read_b64_tr_b16 v[206:207], v225 offset:45056\n"
  "v_mfma_f32_16x16x32_bf16 v[68:71], a[136:139], a[224:227], v[68:71]\n"
  "s_and_b32 s90, %1, 1\n"
  "ds_read_b64_tr_b16 v[208:209], v226 offset:32768\n"
  "v_mov_b32 v233, 0\n"
  "v_mfma_f32_16x16x32_bf16 v[68:71], a[140:143], a[228:231], v[68:71]\n"
  "s_lshl_b32 s90, s90, 14\n"
  "ds_read_b64_tr_b16 v[210:211], v226 offset:36864\n"
  "v_lshlrev_b32 v242, 4, v242\n"
  "v_mfma_f32_16x16x32_bf16 v[80:83], a[128:131], a[232:235], v[80:83]\n"
  "s_add_u32 s84, s84, s90\n"
  "buffer_load_dwordx4 a[220:223], v244, s[48:51], s56 offen offset:3072 sc1\n"
  "v_mfma_f32_16x16x32_bf16 v[80:83], a[132:135], a[236:239], v[80:83]\n"
  "s_and_b32 s90, %1, 2\n"
  "ds_read_b64_tr_b16 v[212:213], v226 offset:40960\n"
  "ds_read_b64_tr_b16 v[214:215], v226 offset:45056\n"
  "v_mfma_f32_16x16x32_bf16 v[84:87], a[136:139], a[232:235], v[84:87]\n"
  "s_add_u32 s59, s89, 0x0\n"
  "buffer_atomic_add v233, v253, s[48:51], s97 offen sc0 sc1\n"
  "v_mfma_f32_16x16x32_bf16 v[84:87], a[140:143], a[236:239], v[84:87]\n"
  "s_waitcnt lgkmcnt(10)\n"
  "s_add_u32 m0, s12, 0\n"
  "ds_read_b64_tr_b16 v[216:217], v227 offset:32768\n"
  "ds_read_b64_tr_b16 v[218:219], v227 offset:36864\n"
  "v_mfma_f32_16x16x32_bf16 v[96:99], a[128:131], a[240:243], v[96:99]\n"
  "s_lshl_b32 s90, s90, 12\n"
  "ds_read_b64_tr_b16 v[220:221], v227 offset:40960\n"
  "v_mul_u32_u24 v245, 3584, v241\n"
  "v_mfma_f32_16x16x32_bf16 v[96:99], a[132:135], a[244:247], v[96:99]\n"
  "s_add_u32 s84, s84, s90\n"
  "ds_read_b64_tr_b16 v[222:223], v227 offset:45056\n"
  "v_add_u32 v184, v245, v242\n"
  "v_mfma_f32_16x16x32_bf16 v[100:103], a[136:139], a[240:243], v[100:103]\n"
  "s_and_b32 s90, s58, 3\n"
  "ds_read_b64_tr_b16 a[224:225], v228 offset:32768\n"
  "v_lshl_add_u32 v238, v241, 12, v242\n"
  "v_mfma_f32_16x16x32_bf16 v[100:103], a[140:143], a[244:247], v[100:103]\n"
  "s_lshl_b32 s90, s90, 8\n"
  "ds_read_b64_tr_b16 a[226:227], v228 offset:36864\n"
  "v_add_u32 v185, 0x6c00, v184\n"
  "v_mfma_f32_16x16x32_bf16 v[112:115], a[128:131], a[248:251], v[112:115]\n"
  "s_add_u32 s84, s84, s90\n"
  "ds_read_b64_tr_b16 a[228:229], v228 offset:40960\n"
  "v_add_u32 v186, 0xd800, v184\n"
  "v_mfma_f32_16x16x32_bf16 v[112:115], a[132:135], a[252:255], v[112:115]\n"
  "s_lshl_b32 s85, %1, 4\n"
  "ds_read_b64_tr_b16 a[230:231], v228 offset:45056\n"
  "v_add_u32 v187, 0x14400, v184\n"
  "v_mfma_f32_16x16x32_bf16 v[116:119], a[136:139], a[248:251], v[116:119]\n"
  "s_add_u32 s85, s85, s54\n"
  "ds_read_b64_tr_b16 a[232:233], v229 offset:32768\n"
  "v_bfe_u32 v245, v244, 6, 4\n"
  "v_mfma_f32_16x16x32_bf16 v[116:119], a[140:143], a[252:255], v[116:119]\n"
  "s_waitcnt vmcnt(38)\n"
  "ds_read_b64_tr_b16 a[234:235], v229 offset:36864\n"
  "v_bfe_u32 v246, v245, 1, 2\n"
  "v_mfma_f32_16x16x32_bf16 v[0:3], a[144:147], v[192:195], v[0:3]\n"
  "s_waitcnt lgkmcnt(12)\n"
  "s_lshl_b32 s85, s85, 10\n"
  "ds_read_b64_tr_b16 a[236:237], v229 offset:40960\n"
  "v_lshlrev_b32 v245, 7, v245\n"
  "v_mfma_f32_16x16x32_bf16 v[0:3], a[148:151], v[196:199], v[0:3]\n"
  "s_lshr_b32 s63, s85, 2\n"
  "ds_read_b64_tr_b16 a[238:239], v229 offset:45056\n"
  "v_and_b32 v242, 3, v239\n"
  "v_mfma_f32_16x16x32_bf16 v[4:7], a[152:155], v[192:195], v[4:7]\n"
  "s_xor_b32 s63, s63, s85\n"
  "ds_read_b64_tr_b16 a[240:241], v230 offset:32768\n"
  "v_lshl_add_u32 v245, v242, 3, v245\n"
  "v_mfma_f32_16x16x32_bf16 v[4:7], a[156:159], v[196:199], v[4:7]\n"
  "s_bfe_i32 s63, s63, 0x1000d\n"
  "ds_read_b64_tr_b16 a[242:243], v230 offset:36864\n"
  "v_add_u32 v245, s12, v245\n"
  "v_mfma_f32_16x16x32_bf16 v[16:19], a[144:147], v[200:203], v[16:19]\n"
  "s_and_b32 s63, s63, 0xa000\n"
  "ds_read_b64_tr_b16 a[244:245], v230 offset:40960\n"
  "v_xor_b32 v242, 0, v246\n"
  "v_mfma_f32_16x16x32_bf16 v[16:19], a[148:151], v[204:207], v[16:19]\n"
  "s_xor_b32 s85, s85, s63\n"
  "ds_read_b64_tr_b16 a[246:247], v230 offset:45056\n"
  "v_lshl_add_u32 v188, v242, 5, v245\n"
  "v_mfma_f32_16x16x32_bf16 v[20:23], a[152:155], v[200:203], v[20:23]\n"
  "s_lshr_b32 s63, s33, 6\n"
  "ds_read_b64_tr_b16 a[248:249], v231 offset:32768\n"
  "v_xor_b32 v242, 1, v246\n"
  "v_mfma_f32_16x16x32_bf16 v[20:23], a[156:159], v[204:207], v[20:23]\n"
  "s_and_b32 s63, s63, 0x2000\n"
  "ds_read_b64_tr_b16 a[250:251], v231 offset:36864\n"
  "v_lshl_add_u32 v189, v242, 5, v245\n"
  "v_mfma_f32_16x16x32_bf16 v[32:35], a[144:147], v[208:211], v[32:35]\n"
  "s_waitcnt lgkmcnt(11)\n"
  "s_xor_b32 s85, s85, s63\n"
  "ds_read_b64_tr_b16 a[252:253], v231 offset:40960\n"
  "v_xor_b32 v242, 2, v246\n"
  "v_mfma_f32_16x16x32_bf16 v[32:35], a[148:151], v[212:215], v[32:35]\n"
  "s_waitcnt vmcnt(14)\n"
  "ds_read_b64_tr_b16 a[254:255], v231 offset:45056\n"
  "v_add_u32 v224, 0x8000, v224\n"
  "v_mfma_f32_16x16x32_bf16 v[36:39], a[152:155], v[208:211], v[36:39]\n"
  "s_barrier\n"
  "ds_read_b64_tr_b16 v[192:193], v224 offset:32768\n"
  "v_lshl_add_u32 v190, v242, 5, v245\n"
  "v_mfma_f32_16x16x32_bf16 v[36:39], a[156:159], v[212:215], v[36:39]\n"
  "s_add_u32 s85, s85, 0x661c000\n"
  "buffer_load_dwordx4 v184, s[64:67], s59 offen offset:0 lds\n"
  "v_mfma_f32_16x16x32_bf16 v[48:51], a[144:147], v[216:219], v[48:51]\n"
  "s_lshr_b32 s86, s60, 1\n"
  "buffer_load_dwordx4 v185, s[64:67], s59 offen offset:1024 lds\n"
  "v_mfma_f32_16x16x32_bf16 v[48:51], a[148:151], v[220:223], v[48:51]\n"
  "s_lshl_b32 s86, s86, 4\n"
  "buffer_load_dwordx4 v186, s[64:67], s59 offen offset:2048 lds\n"
  "v_mfma_f32_16x16x32_bf16 v[52:55], a[152:155], v[216:219], v[52:55]\n"
  "s_add_u32 s86, s86, %1\n"
  "buffer_load_dwordx4 v187, s[64:67], s59 offen offset:3072 lds\n"
  "v_mfma_f32_16x16x32_bf16 v[52:55], a[156:159], v[220:223], v[52:55]\n"
  "s_add_u32 s59, s89, 0x1c000\n"
  "ds_read_b64_tr_b16 v[194:195], v224 offset:36864\n"
  "ds_read_b64_tr_b16 v[196:197], v224 offset:40960\n"
  "v_mfma_f32_16x16x32_bf16 v[64:67], a[144:147], a[224:227], v[64:67]\n"
  "s_waitcnt lgkmcnt(8)\n"
  "s_add_u32 m0, s12, 4096\n"
  "mk_tp13:\n"
  "s_cbranch_execz mk_tp17\n"
  "ds_read_b64_tr_b16 v[198:199], v224 offset:45056\n"
  "v_add_u32 v225, 0x8000, v225\n"
  "v_mfma_f32_16x16x32_bf16 v[64:67], a[148:151], a[228:231], v[64:67]\n"
  "s_lshl_b32 s86, s86, 1\n"
  "buffer_load_dwordx4 v184, s[64:67], s59 offen offset:0 lds\n"
  "v_mfma_f32_16x16x32_bf16 v[68:71], a[152:155], a[224:227], v[68:71]\n"
  "s_and_b32 s90, s53, 1\n"
  "ds_read_b64_tr_b16 v[200:201], v225 offset:32768\n"
  "ds_read_b64_tr_b16 v[202:203], v225 offset:36864\n"
  "v_mfma_f32_16x16x32_bf16 v[68:71], a[156:159], a[228:231], v[68:71]\n"
  "s_add_u32 s86, s86, s90\n"
  "ds_read_b64_tr_b16 v[204:205], v225 offset:40960\n"
  "ds_read_b64_tr_b16 v[206:207], v225 offset:45056\n"
  "v_mfma_f32_16x16x32_bf16 v[80:83], a[144:147], a[232:235], v[80:83]\n"
  "s_lshl_b32 s86, s86, 2\n"
  "buffer_load_dwordx4 v185, s[64:67], s59 offen offset:1024 lds\n"
  "v_mfma_f32_16x16x32_bf16 v[80:83], a[148:151], a[236:239], v[80:83]\n"
  "s_bfe_u32 s90, s53, 0x20002\n"
  "v_add_u32 v226, 0x8000, v226\n"
  "v_xor_b32 v242, 3, v246\n"
  "v_mfma_f32_16x16x32_bf16 v[84:87], a[152:155], a[232:235], v[84:87]\n"
  "s_add_u32 s86, s86, s90\n"
  "ds_read_b64_tr_b16 v[208:209], v226 offset:32768\n"
  "ds_read_b64_tr_b16 v[210:211], v226 offset:36864\n"
  "v_mfma_f32_16x16x32_bf16 v[84:87], a[156:159], a[236:239], v[84:87]\n"
  "s_lshl_b32 s86, s86, 7\n"
  "ds_read_b64_tr_b16 v[212:213], v226 offset:40960\n"
  "ds_read_b64_tr_b16 v[214:215], v226 offset:45056\n"
  "v_mfma_f32_16x16x32_bf16 v[96:99], a[144:147], a[240:243], v[96:99]\n"
  "s_waitcnt lgkmcnt(11)\n"
  "s_lshr_b32 s63, s86, 2\n"
  "buffer_load_dwordx4 v186, s[64:67], s59 offen offset:2048 lds\n"
  "v_mfma_f32_16x16x32_bf16 v[96:99], a[148:151], a[244:247], v[96:99]\n"
  "s_xor_b32 s63, s63, s86\n"
  "v_add_u32 v227, 0x8000, v227\n"
  "v_lshl_add_u32 v191, v242, 5, v245\n"
  "v_mfma_f32_16x16x32_bf16 v[100:103], a[152:155], a[240:243], v[100:103]\n"
  "s_bfe_i32 s63, s63, 0x1000d\n"
  "ds_read_b64_tr_b16 v[216:217], v227 offset:32768\n"
  "ds_read_b64_tr_b16 v[218:219], v227 offset:36864\n"
  "v_mfma_f32_16x16x32_bf16 v[100:103], a[156:159], a[244:247], v[100:103]\n"
  "s_and_b32 s63, s63, 0xa000\n"
  "ds_read_b64_tr_b16 v[220:221], v227 offset:40960\n"
  "ds_read_b64_tr_b16 v[222:223], v227 offset:45056\n"
  "v_mfma_f32_16x16x32_bf16 v[112:115], a[144:147], a[248:251], v[112:115]\n"
  "s_xor_b32 s86, s86, s63\n"
  "buffer_load_dwordx4 v187, s[64:67], s59 offen offset:3072 lds\n"
  "v_mfma_f32_16x16x32_bf16 v[112:115], a[148:151], a[252:255], v[112:115]\n"
  "s_lshr_b32 s63, s33, 6\n"
  "v_add_u32 v228, 0x8000, v228\n"
  "v_and_b32 v243, 3, v239\n"
  "v_mfma_f32_16x16x32_bf16 v[116:119], a[152:155], a[248:251], v[116:119]\n"
  "s_and_b32 s63, s63, 0x2000\n"
  "ds_read_b64_tr_b16 a[224:225], v228 offset:32768\n"
  "ds_read_b64_tr_b16 a[226:227], v228 offset:36864\n"
  "v_mfma_f32_16x16x32_bf16 v[116:119], a[156:159], a[252:255], v[116:119]\n"
  "s_waitcnt lgkmcnt(10)\n"
  "s_waitcnt vmcnt(30)\n"
  "ds_read_b64_tr_b16 a[228:229], v228 offset:40960\n"
  "ds_read_b64_tr_b16 a[230:231], v228 offset:45056\n"
  "v_mfma_f32_16x16x32_bf16 v[0:3], a[160:163], v[192:195], v[0:3]\n"
  "s_xor_b32 s86, s86, s63\n"
  "v_add_u32 v229, 0x8000, v229\n"
  "v_lshlrev_b32 v243, 10, v243\n"
  "v_mfma_f32_16x16x32_bf16 v[0:3], a[164:167], v[196:199], v[0:3]\n"
  "s_add_u32 s86, s86, 0x661c000\n"
  "ds_read_b64_tr_b16 a[232:233], v229 offset:32768\n"
  "ds_read_b64_tr_b16 a[234:235], v229 offset:36864\n"
  "v_mfma_f32_16x16x32_bf16 v[4:7], a[168:171], v[192:195], v[4:7]\n"
  "s_add_u32 s87, s86, 32768\n"
  "ds_read_b64_tr_b16 a[236:237], v229 offset:40960\n"
  "ds_read_b64_tr_b16 a[238:239], v229 offset:45056\n"
  "v_mfma_f32_16x16x32_bf16 v[4:7], a[172:175], v[196:199], v[4:7]\n"
  "s_lshl_b32 s94, s60, 2\n"
  "v_add_u32 v230, 0x8000, v230\n"
  "v_bfe_u32 v242, v239, 2, 1\n"
  "v_mfma_f32_16x16x32_bf16 v[16:19], a[160:163], v[200:203], v[16:19]\n"
  "s_add_u32 s94, s94, %1\n"
  "ds_read_b64_tr_b16 a[240:241], v230 offset:32768\n"
  "ds_read_b64_tr_b16 a[242:243], v230 offset:36864\n"
  "v_mfma_f32_16x16x32_bf16 v[16:19], a[164:167], v[204:207], v[16:19]\n"
  "s_lshl_b32 s92, %1, 10\n"
  "ds_read_b64_tr_b16 a[244:245], v230 offset:40960\n"
  "ds_read_b64_tr_b16 a[246:247], v230 offset:45056\n"
  "v_mfma_f32_16x16x32_bf16 v[20:23], a[168:171], v[200:203], v[20:23]\n"
  "s_lshl_b32 s94, s94, 3\n"
  "v_add_u32 v231, 0x8000, v231\n"
  "v_lshl_add_u32 v243, v242, 16, v243\n"
  "v_mfma_f32_16x16x32_bf16 v[20:23], a[172:175], v[204:207], v[20:23]\n"
  "s_waitcnt lgkmcnt(12)\n"
  "s_add_u32 s94, s94, s90\n"
  "ds_read_b64_tr_b16 a[248:249], v231 offset:32768\n"
  "ds_read_b64_tr_b16 a[250:251], v231 offset:36864\n"
  "v_mfma_f32_16x16x32_bf16 v[32:35], a[160:163], v[208:211], v[32:35]\n"
  "s_lshl_b32 s94, s94, 7\n"
  "ds_read_b64_tr_b16 a[252:253], v231 offset:40960\n"
  "v_bfe_u32 v242, v239, 3, 1\n"
  "v_mfma_f32_16x16x32_bf16 v[32:35], a[164:167], v[212:215], v[32:35]\n"
  "s_lshr_b32 s63, s94, 1\n"
  "ds_read_b64_tr_b16 a[254:255], v231 offset:45056\n"
  "v_lshl_add_u32 v243, v242, 5, v243\n"
  "v_mfma_f32_16x16x32_bf16 v[36:39], a[168:171], v[208:211], v[36:39]\n"
  "s_xor_b32 s63, s63, s94\n"
  "ds_read_b64_tr_b16 v[192:193], v224 offset:49152\n"
  "v_bfe_u32 v242, v239, 4, 1\n"
  "v_mfma_f32_16x16x32_bf16 v[36:39], a[172:175], v[212:215], v[36:39]\n"
  "s_bfe_i32 s63, s63, 0x1000d\n"
  "ds_read_b64_tr_b16 v[194:195], v224 offset:53248\n"
  "v_lshl_add_u32 v243, v242, 4, v243\n"
  "v_mfma_f32_16x16x32_bf16 v[48:51], a[160:163], v[216:219], v[48:51]\n"
  "s_and_b32 s63, s63, 0x6000\n"
  "ds_read_b64_tr_b16 v[196:197], v224 offset:57344\n"
  "v_lshrrev_b32 v242, 5, v239\n"
  "v_mfma_f32_16x16x32_bf16 v[48:51], a[164:167], v[220:223], v[48:51]\n"
  "s_waitcnt vmcnt(8)\n"
  "ds_read_b64_tr_b16 v[198:199], v224 offset:61440\n"
  "v_cmp_ne_u32 vcc, 4, v233\n"
  "v_mfma_f32_16x16x32_bf16 v[52:55], a[168:171], v[216:219], v[52:55]\n"
  "s_and_b32 vcc_lo, vcc_lo, 0x1\n"
  "s_cbranch_scc1 mk_spin3b\n"
  "mk_spin3b_b:\n"
  "ds_read_b64_tr_b16 v[200:201], v224 offset:16384\n"
  "v_lshl_add_u32 v243, v242, 6, v243\n"
  "v_mfma_f32_16x16x32_bf16 v[52:55], a[172:175], v[220:223], v[52:55]\n"
  "s_waitcnt lgkmcnt(13)\n"
  "s_add_u32 s61, s53, 1\n"
  "s_and_b32 s61, s61, 3\n"
  "ds_read_b64_tr_b16 v[202:203], v224 offset:20480\n"
  "v_and_b32 v241, 15, v239\n"
  "v_mfma_f32_16x16x32_bf16 v[64:67], a[160:163], a[224:227], v[64:67]\n"
  "s_and_b32 s59, s53, 12\n"
  "s_add_u32 s61, s61, s59\n"
  "ds_read_b64_tr_b16 v[204:205], v224 offset:24576\n"
  "v_and_b32 v245, 1, v241\n"
  "v_mfma_f32_16x16x32_bf16 v[64:67], a[164:167], a[228:231], v[64:67]\n"
  "s_and_b32 s59, s61, 14\n"
  "s_add_u32 s61, s61, s59\n"
  "ds_read_b64_tr_b16 v[206:207], v224 offset:28672\n"
  "v_add_u32 v224, 0xffff8000, v224\n"
  "v_mfma_f32_16x16x32_bf16 v[68:71], a[168:171], a[224:227], v[68:71]\n"
  "s_lshl_b32 s62, s61, 12\n"
  "ds_read_b64_tr_b16 v[208:209], v224 offset:16384\n"
  "v_lshlrev_b32 v245, 4, v245\n"
  "v_mfma_f32_16x16x32_bf16 v[68:71], a[172:175], a[228:231], v[68:71]\n"
  "s_add_u32 s56, s42, s62\n"
  "buffer_load_dwordx4 a[128:131], v244, s[48:51], s56 offen offset:0 sc1\n"
  "v_mfma_f32_16x16x32_bf16 v[80:83], a[160:163], a[232:235], v[80:83]\n"
  "s_xor_b32 s94, s94, s63\n"
  "ds_read_b64_tr_b16 v[210:211], v224 offset:20480\n"
  "ds_read_b64_tr_b16 v[212:213], v224 offset:24576\n"
  "v_mfma_f32_16x16x32_bf16 v[80:83], a[164:167], a[236:239], v[80:83]\n"
  "s_lshr_b32 s63, s33, 6\n"
  "ds_read_b64_tr_b16 v[214:215], v224 offset:28672\n"
  "v_mov_b32 v246, 0x3f80\n"
  "v_mfma_f32_16x16x32_bf16 v[84:87], a[168:171], a[232:235], v[84:87]\n"
  "s_and_b32 s63, s63, 0x2000\n"
  "ds_read_b64_tr_b16 v[216:217], v225 offset:49152\n"
  "v_lshlrev_b32 v245, v245, v246\n"
  "v_mfma_f32_16x16x32_bf16 v[84:87], a[172:175], a[236:239], v[84:87]\n"
  "s_waitcnt lgkmcnt(11)\n"
  "s_xor_b32 s94, s94, s63\n"
  "ds_read_b64_tr_b16 v[218:219], v225 offset:53248\n"
  "v_lshrrev_b32 v246, 1, v241\n"
  "v_mfma_f32_16x16x32_bf16 v[96:99], a[160:163], a[240:243], v[96:99]\n"
  "s_add_u32 s94, s94, 0x662c000\n"
  "buffer_load_dwordx4 a[132:135], v244, s[48:51], s56 offen offset:1024 sc1\n"
  "v_mfma_f32_16x16x32_bf16 v[96:99], a[164:167], a[244:247], v[96:99]\n"
  "s_add_u32 s56, s43, s62\n"
  "ds_read_b64_tr_b16 v[220:221], v225 offset:57344\n"
  "ds_read_b64_tr_b16 v[222:223], v225 offset:61440\n"
  "v_mfma_f32_16x16x32_bf16 v[100:103], a[168:171], a[240:243], v[100:103]\n"
  "s_lshl_b32 s90, s14, 16\n"
  "ds_read_b64_tr_b16 a[224:225], v225 offset:16384\n"
  "v_cmp_eq_u32_e64 s[16:17], 0, v246\n"
  "v_mfma_f32_16x16x32_bf16 v[100:103], a[172:175], a[244:247], v[100:103]\n"
  "s_add_u32 s92, s92, s90\n"
  "ds_read_b64_tr_b16 a[226:227], v225 offset:20480\n"
  "v_mfma_f32_16x16x32_bf16 v[112:115], a[160:163], a[248:251], v[112:115]\n"
  "s_add_u32 s92, s92, 0x6400000\n"
  "buffer_load_dwordx4 a[136:139], v244, s[48:51], s56 offen offset:0 sc1\n"
  "v_mfma_f32_16x16x32_bf16 v[112:115], a[164:167], a[252:255], v[112:115]\n"
  "s_and_b32 s91, s53, 2\n"
  "ds_read_b64_tr_b16 a[228:229], v225 offset:24576\n"
  "ds_read_b64_tr_b16 a[230:231], v225 offset:28672\n"
  "v_mfma_f32_16x16x32_bf16 v[116:119], a[168:171], a[248:251], v[116:119]\n"
  "s_and_b32 s90, s53, 3\n"
  "v_add_u32 v225, 0xffff8000, v225\n"
  "v_cndmask_b32_e64 v160, 0, v245, s[16:17]\n"
  "v_mfma_f32_16x16x32_bf16 v[116:119], a[172:175], a[252:255], v[116:119]\n"
  "s_waitcnt vmcnt(20)\n"
  "ds_read_b64_tr_b16 a[232:233], v225 offset:16384\n"
  "ds_read_b64_tr_b16 a[234:235], v225 offset:20480\n"
  "v_mfma_f32_16x16x32_bf16 v[0:3], a[176:179], v[192:195], v[0:3]\n"
  "s_waitcnt lgkmcnt(11)\n"
  "s_add_u32 s91, s91, s90\n"
  "buffer_load_dwordx4 a[140:143], v244, s[48:51], s56 offen offset:1024 sc1\n"
  "v_mfma_f32_16x16x32_bf16 v[0:3], a[180:183], v[196:199], v[0:3]\n"
  "s_add_u32 s56, s42, s62\n"
  "ds_read_b64_tr_b16 a[236:237], v225 offset:24576\n"
  "ds_read_b64_tr_b16 a[238:239], v225 offset:28672\n"
  "v_mfma_f32_16x16x32_bf16 v[4:7], a[184:187], v[192:195], v[4:7]\n"
  "s_lshl_b32 s91, s91, 12\n"
  "ds_read_b64_tr_b16 a[240:241], v226 offset:49152\n"
  "v_cmp_eq_u32_e64 s[16:17], 1, v246\n"
  "v_mfma_f32_16x16x32_bf16 v[4:7], a[188:191], v[196:199], v[4:7]\n"
  "s_waitcnt vmcnt(17)\n"
  "ds_read_b64_tr_b16 a[242:243], v226 offset:53248\n"
  "v_mfma_f32_16x16x32_bf16 v[0:3], a[192:195], v[200:203], v[0:3]\n"
  "s_add_u32 s92, s92, s91\n"
  "ds_read_b64_tr_b16 a[244:245], v226 offset:57344\n"
  "v_cndmask_b32_e64 v161, 0, v245, s[16:17]\n"
  "v_mfma_f32_16x16x32_bf16 v[0:3], a[196:199], v[204:207], v[0:3]\n"
  "s_lshl_b32 s88, %1, 17\n"
  "buffer_load_dwordx4 a[144:147], v244, s[48:51], s56 offen offset:2048 sc1\n"
  "v_mfma_f32_16x16x32_bf16 v[4:7], a[200:203], v[200:203], v[4:7]\n"
  "s_add_u32 s88, s88, s91\n"
  "ds_read_b64_tr_b16 a[246:247], v226 offset:61440\n"
  "ds_read_b64_tr_b16 a[248:249], v226 offset:16384\n"
  "v_mfma_f32_16x16x32_bf16 v[4:7], a[204:207], v[204:207], v[4:7]\n"
  "s_waitcnt vmcnt(14)\n"
  "ds_read_b64_tr_b16 a[250:251], v226 offset:20480\n"
  "v_cmp_eq_u32_e64 s[16:17], 2, v246\n"
  "v_mfma_f32_16x16x32_bf16 v[0:3], a[208:211], v[208:211], v[0:3]\n"
  "s_waitcnt lgkmcnt(12)\n"
  "s_add_u32 s88, s88, 0x6000000\n"
  "ds_read_b64_tr_b16 a[252:253], v226 offset:24576\n"
  "v_mfma_f32_16x16x32_bf16 v[0:3], a[212:215], v[212:215], v[0:3]\n"
  "s_mul_i32 s95, %1, 458752\n"
  "ds_read_b64_tr_b16 a[254:255], v226 offset:28672\n"
  "v_add_u32 v226, 0xffff8000, v226\n"
  "v_mfma_f32_16x16x32_bf16 v[4:7], a[216:219], v[208:211], v[4:7]\n"
  "s_add_u32 s95, s95, s91\n"
  "buffer_load_dwordx4 a[148:151], v244, s[48:51], s56 offen offset:3072 sc1\n"
  "v_mfma_f32_16x16x32_bf16 v[4:7], a[220:223], v[212:215], v[4:7]\n"
  "s_add_u32 s56, s43, s62\n"
  "ds_read_b64_tr_b16 v[192:193], v226 offset:16384\n"
  "ds_read_b64_tr_b16 v[194:195], v226 offset:20480\n"
  "v_mfma_f32_16x16x32_bf16 v[16:19], a[176:179], v[216:219], v[16:19]\n"
  "s_add_u32 s95, s95, 0x6400000\n"
  "ds_read_b64_tr_b16 v[196:197], v226 offset:24576\n"
  "v_cndmask_b32_e64 v162, 0, v245, s[16:17]\n"
  "v_mfma_f32_16x16x32_bf16 v[16:19], a[180:183], v[220:223], v[16:19]\n"
  "s_lshr_b32 s72, s98, 1\n"
  "v_cvt_pk_bf16_f32 v128, v0, v1\n"
  "v_cvt_pk_bf16_f32 v129, v2, v3\n"
  "v_mfma_f32_16x16x32_bf16 v[20:23], a[184:187], v[216:219], v[20:23]\n"
  "s_lshl_b32 s72, s72, 2\n"
  "buffer_load_dwordx4 a[152:155], v244, s[48:51], s56 offen offset:2048 sc1\n"
  "v_mfma_f32_16x16x32_bf16 v[20:23], a[188:191], v[220:223], v[20:23]\n"
  "s_add_u32 s72, s72, %1\n"
  "ds_read_b64_tr_b16 v[198:199], v226 offset:28672\n"
  "v_cvt_pk_bf16_f32 v130, v4, v5\n"
  "v_mfma_f32_16x16x32_bf16 v[16:19], a[192:195], a[224:227], v[16:19]\n"
  "s_waitcnt lgkmcnt(9)\n"
  "s_lshl_b32 s72, s72, 15\n"
  "ds_read_b64_tr_b16 v[200:201], v227 offset:49152\n"
  "v_cvt_pk_bf16_f32 v131, v6, v7\n"
  "v_mfma_f32_16x16x32_bf16 v[16:19], a[196:199], a[228:231], v[16:19]\n"
  "s_add_u32 s72, s72, s91\n"
  "ds_read_b64_tr_b16 v[202:203], v227 offset:53248\n"
  "ds_read_b64_tr_b16 v[204:205], v227 offset:57344\n"
  "v_mfma_f32_16x16x32_bf16 v[20:23], a[200:203], a[224:227], v[20:23]\n"
  "s_add_u32 s72, s72, 0x6200000\n"
  "buffer_store_dwordx4 v[128:131], v234, s[48:51], s76 offen offset:0 sc1\n"
  "v_mfma_f32_16x16x32_bf16 v[20:23], a[204:207], a[228:231], v[20:23]\n"
  "s_lshr_b32 s90, s35, 3\n"
  "buffer_load_dwordx4 a[156:159], v244, s[48:51], s56 offen offset:3072 sc1\n"
  "v_mfma_f32_16x16x32_bf16 v[16:19], a[208:211], a[232:235], v[16:19]\n"
  "s_add_u32 s61, s53, 2\n"
  "s_and_b32 s61, s61, 3\n"
  "ds_read_b64_tr_b16 v[206:207], v227 offset:61440\n"
  "ds_read_b64_tr_b16 v[208:209], v227 offset:16384\n"
  "v_mfma_f32_16x16x32_bf16 v[16:19], a[212:215], a[236:239], v[16:19]\n"
  "s_and_b32 s59, s53, 12\n"
  "s_add_u32 s61, s61, s59\n"
  "ds_read_b64_tr_b16 v[210:211], v227 offset:20480\n"
  "ds_read_b64_tr_b16 v[212:213], v227 offset:24576\n"
  "v_mfma_f32_16x16x32_bf16 v[20:23], a[216:219], a[232:235], v[20:23]\n"
  "s_and_b32 s59, s61, 14\n"
  "s_add_u32 s61, s61, s59\n"
  "ds_read_b64_tr_b16 v[214:215], v227 offset:28672\n"
  "v_add_u32 v227, 0xffff8000, v227\n"
  "v_mfma_f32_16x16x32_bf16 v[20:23], a[220:223], a[236:239], v[20:23]\n"
  "s_lshl_b32 s62, s61, 12\n"
  "ds_read_b64_tr_b16 v[216:217], v227 offset:16384\n"
  "ds_read_b64_tr_b16 v[218:219], v227 offset:20480\n"
  "v_mfma_f32_16x16x32_bf16 v[32:35], a[176:179], a[240:243], v[32:35]\n"
  "s_waitcnt lgkmcnt(11)\n"
  "s_add_u32 s56, s42, s62\n"
  "buffer_load_dwordx4 a[0:3], v244, s[48:51], s56 offen offset:0 sc1\n"
  "v_mfma_f32_16x16x32_bf16 v[32:35], a[180:183], a[244:247], v[32:35]\n"
  "s_and_b32 s90, s90, 0x2000\n"
  "v_cvt_pk_bf16_f32 v132, v16, v17\n"
  "v_cvt_pk_bf16_f32 v133, v18, v19\n"
  "v_mfma_f32_16x16x32_bf16 v[36:39], a[184:187], a[240:243], v[36:39]\n"
  "s_add_u32 s88, s88, s90\n"
  "ds_read_b64_tr_b16 v[220:221], v227 offset:24576\n"
  "ds_read_b64_tr_b16 v[222:223], v227 offset:28672\n"
  "v_mfma_f32_16x16x32_bf16 v[36:39], a[188:191], a[244:247], v[36:39]\n"
  "s_lshr_b32 s90, s35, 4\n"
  "v_cvt_pk_bf16_f32 v134, v20, v21\n"
  "v_cvt_pk_bf16_f32 v135, v22, v23\n"
  "v_mfma_f32_16x16x32_bf16 v[32:35], a[192:195], a[248:251], v[32:35]\n"
  "s_and_b32 s90, s90, 0x2000\n"
  "ds_read_b64_tr_b16 a[224:225], v228 offset:49152\n"
  "ds_read_b64_tr_b16 a[226:227], v228 offset:53248\n"
  "v_mfma_f32_16x16x32_bf16 v[32:35], a[196:199], a[252:255], v[32:35]\n"
  "s_add_u32 s72, s72, s90\n"
  "buffer_load_dwordx4 a[4:7], v244, s[48:51], s56 offen offset:1024 sc1\n"
  "v_mfma_f32_16x16x32_bf16 v[36:39], a[200:203], a[248:251], v[36:39]\n"
  "s_add_u32 s56, s43, s62\n"
  "ds_read_b64_tr_b16 a[228:229], v228 offset:57344\n"
  "ds_read_b64_tr_b16 a[230:231], v228 offset:61440\n"
  "v_mfma_f32_16x16x32_bf16 v[36:39], a[204:207], a[252:255], v[36:39]\n"
  "s_lshr_b32 s90, s35, 5\n"
  "buffer_store_dwordx4 v[132:135], v234, s[48:51], s76 offen offset:256 sc1\n"
  "v_mfma_f32_16x16x32_bf16 v[32:35], a[208:211], v[192:195], v[32:35]\n"
  "s_waitcnt lgkmcnt(11)\n"
  "s_and_b32 s90, s90, 0x2000\n"
  "ds_read_b64_tr_b16 a[232:233], v228 offset:16384\n"
  "ds_read_b64_tr_b16 a[234:235], v228 offset:20480\n"
  "v_mfma_f32_16x16x32_bf16 v[32:35], a[212:215], v[196:199], v[32:35]\n"
  "s_add_u32 s92, s92, s90\n"
  "buffer_load_dwordx4 a[8:11], v244, s[48:51], s56 offen offset:0 sc1\n"
  "v_mfma_f32_16x16x32_bf16 v[36:39], a[216:219], v[192:195], v[36:39]\n"
  "s_add_u32 s95, s95, s90\n"
  "ds_read_b64_tr_b16 a[236:237], v228 offset:24576\n"
  "ds_read_b64_tr_b16 a[238:239], v228 offset:28672\n"
  "v_mfma_f32_16x16x32_bf16 v[36:39], a[220:223], v[196:199], v[36:39]\n"
  "s_lshl_b32 s93, s60, 2\n"
  "v_add_u32 v228, 0xffff8000, v228\n"
  "v_cmp_eq_u32_e64 s[16:17], 3, v246\n"
  "v_mfma_f32_16x16x32_bf16 v[48:51], a[176:179], v[200:203], v[48:51]\n"
  "s_bfe_u32 s63, s53, 0x20002\n"
  "ds_read_b64_tr_b16 a[240:241], v228 offset:16384\n"
  "ds_read_b64_tr_b16 a[242:243], v228 offset:20480\n"
  "v_mfma_f32_16x16x32_bf16 v[48:51], a[180:183], v[204:207], v[48:51]\n"
  "s_add_u32 s93, s93, s63\n"
  "buffer_load_dwordx4 a[12:15], v244, s[48:51], s56 offen offset:1024 sc1\n"
  "v_mfma_f32_16x16x32_bf16 v[52:55], a[184:187], v[200:203], v[52:55]\n"
  "s_add_u32 s56, s42, s62\n"
  "v_cvt_pk_bf16_f32 v136, v32, v33\n"
  "v_cvt_pk_bf16_f32 v137, v34, v35\n"
  "v_mfma_f32_16x16x32_bf16 v[52:55], a[188:191], v[204:207], v[52:55]\n"
  "s_waitcnt lgkmcnt(10)\n"
  "s_lshl_b32 s93, s93, 1\n"
  "ds_read_b64_tr_b16 a[244:245], v228 offset:24576\n"
  "ds_read_b64_tr_b16 a[246:247], v228 offset:28672\n"
  "v_mfma_f32_16x16x32_bf16 v[48:51], a[192:195], v[208:211], v[48:51]\n"
  "s_and_b32 s63, %1, 1\n"
  "v_cvt_pk_bf16_f32 v138, v36, v37\n"
  "v_cvt_pk_bf16_f32 v139, v38, v39\n"
  "v_mfma_f32_16x16x32_bf16 v[48:51], a[196:199], v[212:215], v[48:51]\n"
  "s_add_u32 s93, s93, s63\n"
  "ds_read_b64_tr_b16 a[248:249], v229 offset:49152\n"
  "ds_read_b64_tr_b16 a[250:251], v229 offset:53248\n"
  "v_mfma_f32_16x16x32_bf16 v[52:55], a[200:203], v[208:211], v[52:55]\n"
  "s_lshl_b32 s93, s93, 9\n"
  "buffer_load_dwordx4 a[16:19], v244, s[48:51], s56 offen offset:2048 sc1\n"
  "v_mfma_f32_16x16x32_bf16 v[52:55], a[204:207], v[212:215], v[52:55]\n"
  "s_lshr_b32 s63, s93, 1\n"
  "ds_read_b64_tr_b16 a[252:253], v229 offset:57344\n"
  "ds_read_b64_tr_b16 a[254:255], v229 offset:61440\n"
  "v_mfma_f32_16x16x32_bf16 v[48:51], a[208:211], v[216:219], v[48:51]\n"
  "s_xor_b32 s63, s63, s93\n"
  "ds_read_b64_tr_b16 v[192:193], v229 offset:16384\n"
  "ds_read_b64_tr_b16 v[194:195], v229 offset:20480\n"
  "v_mfma_f32_16x16x32_bf16 v[48:51], a[212:215], v[220:223], v[48:51]\n"
  "s_bfe_i32 s63, s63, 0x1000d\n"
  "buffer_store_dwordx4 v[136:139], v234, s[48:51], s76 offen offset:512 sc1\n"
  "v_mfma_f32_16x16x32_bf16 v[52:55], a[216:219], v[216:219], v[52:55]\n"
  "s_and_b32 s63, s63, 0x6000\n"
  "ds_read_b64_tr_b16 v[196:197], v229 offset:24576\n"
  "ds_read_b64_tr_b16 v[198:199], v229 offset:28672\n"
  "v_mfma_f32_16x16x32_bf16 v[52:55], a[220:223], v[220:223], v[52:55]\n"
  "s_waitcnt lgkmcnt(12)\n"
  "s_xor_b32 s93, s93, s63\n"
  "buffer_load_dwordx4 a[20:23], v244, s[48:51], s56 offen offset:3072 sc1\n"
  "v_mfma_f32_16x16x32_bf16 v[64:67], a[176:179], a[224:227], v[64:67]\n"
  "s_add_u32 s56, s43, s62\n"
  "v_add_u32 v229, 0xffff8000, v229\n"
  "v_cndmask_b32_e64 v163, 0, v245, s[16:17]\n"
  "v_mfma_f32_16x16x32_bf16 v[64:67], a[180:183], a[228:231], v[64:67]\n"
  "s_lshr_b32 s63, s33, 6\n"
  "ds_read_b64_tr_b16 v[200:201], v229 offset:16384\n"
  "ds_read_b64_tr_b16 v[202:203], v229 offset:20480\n"
  "v_mfma_f32_16x16x32_bf16 v[68:71], a[184:187], a[224:227], v[68:71]\n"
  "s_and_b32 s63, s63, 0x2000\n"
  "v_cvt_pk_bf16_f32 v140, v48, v49\n"
  "v_cvt_pk_bf16_f32 v141, v50, v51\n"
  "v_mfma_f32_16x16x32_bf16 v[68:71], a[188:191], a[228:231], v[68:71]\n"
  "s_xor_b32 s93, s93, s63\n"
  "ds_read_b64_tr_b16 v[204:205], v229 offset:24576\n"
  "ds_read_b64_tr_b16 v[206:207], v229 offset:28672\n"
  "v_mfma_f32_16x16x32_bf16 v[64:67], a[192:195], a[232:235], v[64:67]\n"
  "s_add_u32 s93, s93, 0x662c000\n"
  "buffer_load_dwordx4 a[24:27], v244, s[48:51], s56 offen offset:2048 sc1\n"
  "v_mfma_f32_16x16x32_bf16 v[64:67], a[196:199], a[236:239], v[64:67]\n"
  "s_lshl_b32 s90, s98, 7\n"
  "v_cvt_pk_bf16_f32 v142, v52, v53\n"
  "v_cvt_pk_bf16_f32 v143, v54, v55\n"
  "v_mfma_f32_16x16x32_bf16 v[68:71], a[200:203], a[232:235], v[68:71]\n"
  "s_add_u32 s68, s24, s90\n"
  "s_addc_u32 s69, s25, 0\n"
  "ds_read_b64_tr_b16 v[208:209], v230 offset:49152\n"
  "ds_read_b64_tr_b16 v[210:211], v230 offset:53248\n"
  "v_mfma_f32_16x16x32_bf16 v[68:71], a[204:207], a[236:239], v[68:71]\n"
  "s_waitcnt lgkmcnt(10)\n"
  "s_sub_u32 s70, 0x700000, s90\n"
  "ds_read_b64_tr_b16 v[212:213], v230 offset:57344\n"
  "ds_read_b64_tr_b16 v[214:215], v230 offset:61440\n"
  "v_mfma_f32_16x16x32_bf16 v[64:67], a[208:211], a[240:243], v[64:67]\n"
  "s_mov_b32 s71, 0x20000\n"
  "buffer_load_dwordx4 a[28:31], v244, s[48:51], s56 offen offset:3072 sc1\n"
  "v_mfma_f32_16x16x32_bf16 v[64:67], a[212:215], a[244:247], v[64:67]\n"
  "s_add_u32 s61, s53, 3\n"
  "s_and_b32 s61, s61, 3\n"
  "ds_read_b64_tr_b16 v[216:217], v230 offset:16384\n"
  "ds_read_b64_tr_b16 v[218:219], v230 offset:20480\n"
  "v_mfma_f32_16x16x32_bf16 v[68:71], a[216:219], a[240:243], v[68:71]\n"
  "s_and_b32 s59, s53, 12\n"
  "s_add_u32 s61, s61, s59\n"
  "ds_read_b64_tr_b16 v[220:221], v230 offset:24576\n"
  "ds_read_b64_tr_b16 v[222:223], v230 offset:28672\n"
  "v_mfma_f32_16x16x32_bf16 v[68:71], a[220:223], a[244:247], v[68:71]\n"
  "s_and_b32 s59, s61, 14\n"
  "s_add_u32 s61, s61, s59\n"
  "v_add_u32 v230, 0xffff8000, v230\n"
  "v_subrev_u32 v242, 8, v241\n"
  "v_mfma_f32_16x16x32_bf16 v[80:83], a[176:179], a[248:251], v[80:83]\n"
  "s_lshl_b32 s62, s61, 12\n"
  "buffer_store_dwordx4 v[140:143], v234, s[48:51], s76 offen offset:768 sc1\n"
  "v_mfma_f32_16x16x32_bf16 v[80:83], a[180:183], a[252:255], v[80:83]\n"
  "s_add_u32 s56, s42, s62\n"
  "buffer_load_dwordx4 a[32:35], v244, s[48:51], s56 offen offset:0 sc1\n"
  "v_mfma_f32_16x16x32_bf16 v[84:87], a[184:187], a[248:251], v[84:87]\n"
  "s_mul_i32 s15, %1, 1835008\n"
  "ds_read_b64_tr_b16 a[224:225], v230 offset:16384\n"
  "ds_read_b64_tr_b16 a[226:227], v230 offset:20480\n"
  "v_mfma_f32_16x16x32_bf16 v[84:87], a[188:191], a[252:255], v[84:87]\n"
  "s_waitcnt lgkmcnt(10)\n"
  "s_lshl_b32 s73, s98, 2\n"
  "v_cvt_pk_bf16_f32 v144, v64, v65\n"
  "v_cvt_pk_bf16_f32 v145, v66, v67\n"
  "v_mfma_f32_16x16x32_bf16 v[80:83], a[192:195], v[192:195], v[80:83]\n"
  "s_add_u32 s73, s73, %1\n"
  "ds_read_b64_tr_b16 a[228:229], v230 offset:24576\n"
  "ds_read_b64_tr_b16 a[230:231], v230 offset:28672\n"
  "v_mfma_f32_16x16x32_bf16 v[80:83], a[196:199], v[196:199], v[80:83]\n"
  "s_lshl_b32 s74, s73, 14\n"
  "v_cvt_pk_bf16_f32 v146, v68, v69\n"
  "v_cvt_pk_bf16_f32 v147, v70, v71\n"
  "v_mfma_f32_16x16x32_bf16 v[84:87], a[200:203], v[192:195], v[84:87]\n"
  "s_lshl_b32 s90, s60, 7\n"
  "ds_read_b64_tr_b16 a[232:233], v231 offset:49152\n"
  "ds_read_b64_tr_b16 a[234:235], v231 offset:53248\n"
  "v_mfma_f32_16x16x32_bf16 v[84:87], a[204:207], v[196:199], v[84:87]\n"
  "s_add_u32 s74, s74, s90\n"
  "buffer_load_dwordx4 a[36:39], v244, s[48:51], s56 offen offset:1024 sc1\n"
  "v_mfma_f32_16x16x32_bf16 v[80:83], a[208:211], v[200:203], v[80:83]\n"
  "s_add_u32 s56, s43, s62\n"
  "ds_read_b64_tr_b16 a[236:237], v231 offset:57344\n"
  "ds_read_b64_tr_b16 a[238:239], v231 offset:61440\n"
  "v_mfma_f32_16x16x32_bf16 v[80:83], a[212:215], v[204:207], v[80:83]\n"
  "s_and_b32 s91, s10, 1\n"
  "ds_read_b64_tr_b16 a[240:241], v231 offset:16384\n"
  "ds_read_b64_tr_b16 a[242:243], v231 offset:20480\n"
  "v_mfma_f32_16x16x32_bf16 v[84:87], a[216:219], v[200:203], v[84:87]\n"
  "s_lshl_b32 s90, s91, 19\n"
  "buffer_load_dwordx4 a[40:43], v244, s[48:51], s56 offen offset:0 sc1\n"
  "v_mfma_f32_16x16x32_bf16 v[84:87], a[220:223], v[204:207], v[84:87]\n"
  "s_waitcnt lgkmcnt(10)\n"
  "s_sub_u32 s96, s88, s90\n"
  "ds_read_b64_tr_b16 a[244:245], v231 offset:24576\n"
  "ds_read_b64_tr_b16 a[246:247], v231 offset:28672\n"
  "v_mfma_f32_16x16x32_bf16 v[96:99], a[176:179], v[208:211], v[96:99]\n"
  "s_add_u32 s88, s88, s90\n"
  "v_add_u32 v231, 0xffff8000, v231\n"
  "v_and_b32 v245, 1, v242\n"
  "v_mfma_f32_16x16x32_bf16 v[96:99], a[180:183], v[212:215], v[96:99]\n"
  "s_mul_i32 s90, s91, 917504\n"
  "ds_read_b64_tr_b16 a[248:249], v231 offset:16384\n"
  "ds_read_b64_tr_b16 a[250:251], v231 offset:20480\n"
  "v_mfma_f32_16x16x32_bf16 v[100:103], a[184:187], v[208:211], v[100:103]\n"
  "s_sub_u32 s18, s15, s90\n"
  "buffer_load_dwordx4 a[44:47], v244, s[48:51], s56 offen offset:1024 sc1\n"
  "v_mfma_f32_16x16x32_bf16 v[100:103], a[188:191], v[212:215], v[100:103]\n"
  "s_add_u32 s56, s42, s62\n"
  "buffer_store_dwordx4 v[144:147], v234, s[48:51], s76 offen offset:1024 sc1\n"
  "v_mfma_f32_16x16x32_bf16 v[96:99], a[192:195], v[216:219], v[96:99]\n"
  "s_add_u32 s15, s15, s90\n"
  "v_cvt_pk_bf16_f32 v148, v80, v81\n"
  "v_cvt_pk_bf16_f32 v149, v82, v83\n"
  "v_mfma_f32_16x16x32_bf16 v[96:99], a[196:199], v[220:223], v[96:99]\n"
  "s_mul_i32 s90, s91, 229376\n"
  "ds_read_b64_tr_b16 a[252:253], v231 offset:24576\n"
  "ds_read_b64_tr_b16 a[254:255], v231 offset:28672\n"
  "v_mfma_f32_16x16x32_bf16 v[100:103], a[200:203], v[216:219], v[100:103]\n"
  "s_sub_u32 s19, s95, s90\n"
  "v_cvt_pk_bf16_f32 v150, v84, v85\n"
  "v_cvt_pk_bf16_f32 v151, v86, v87\n"
  "v_mfma_f32_16x16x32_bf16 v[100:103], a[204:207], v[220:223], v[100:103]\n"
  "s_waitcnt lgkmcnt(10)\n"
  "s_add_u32 s95, s95, s90\n"
  "ds_read_b64_tr_b16 v[192:193], v224 offset:0\n"
  "ds_read_b64_tr_b16 v[194:195], v224 offset:4096\n"
  "v_mfma_f32_16x16x32_bf16 v[96:99], a[208:211], a[224:227], v[96:99]\n"
  "buffer_load_dwordx4 a[48:51], v244, s[48:51], s56 offen offset:2048 sc1\n"
  "v_mfma_f32_16x16x32_bf16 v[96:99], a[212:215], a[228:231], v[96:99]\n"
  "ds_read_b64_tr_b16 v[196:197], v224 offset:8192\n"
  "ds_read_b64_tr_b16 v[198:199], v224 offset:12288\n"
  "v_mfma_f32_16x16x32_bf16 v[100:103], a[216:219], a[224:227], v[100:103]\n"
  "ds_read_b64_tr_b16 v[200:201], v225 offset:0\n"
  "ds_read_b64_tr_b16 v[202:203], v225 offset:4096\n"
  "v_mfma_f32_16x16x32_bf16 v[100:103], a[220:223], a[228:231], v[100:103]\n"
  "ds_read_b64_tr_b16 v[204:205], v225 offset:8192\n"
  "ds_read_b64_tr_b16 v[206:207], v225 offset:12288\n"
  "v_mfma_f32_16x16x32_bf16 v[112:115], a[176:179], a[232:235], v[112:115]\n"
  "s_waitcnt lgkmcnt(12)\n"
  "buffer_load_dwordx4 a[52:55], v244, s[48:51], s56 offen offset:3072 sc1\n"
  "v_mfma_f32_16x16x32_bf16 v[112:115], a[180:183], a[236:239], v[112:115]\n"
  "s_add_u32 s56, s43, s62\n"
  "ds_read_b64_tr_b16 v[208:209], v226 offset:0\n"
  "ds_read_b64_tr_b16 v[210:211], v226 offset:4096\n"
  "v_mfma_f32_16x16x32_bf16 v[116:119], a[184:187], a[232:235], v[116:119]\n"
  "v_cvt_pk_bf16_f32 v152, v96, v97\n"
  "v_cvt_pk_bf16_f32 v153, v98, v99\n"
  "v_mfma_f32_16x16x32_bf16 v[116:119], a[188:191], a[236:239], v[116:119]\n"
  "ds_read_b64_tr_b16 v[212:213], v226 offset:8192\n"
  "ds_read_b64_tr_b16 v[214:215], v226 offset:12288\n"
  "v_mfma_f32_16x16x32_bf16 v[112:115], a[192:195], a[240:243], v[112:115]\n"
  "buffer_load_dwordx4 a[56:59], v244, s[48:51], s56 offen offset:2048 sc1\n"
  "v_mfma_f32_16x16x32_bf16 v[112:115], a[196:199], a[244:247], v[112:115]\n"
  "buffer_store_dwordx4 v[148:151], v234, s[48:51], s76 offen offset:1280 sc1\n"
  "v_mfma_f32_16x16x32_bf16 v[116:119], a[200:203], a[240:243], v[116:119]\n"
  "v_cvt_pk_bf16_f32 v154, v100, v101\n"
  "v_cvt_pk_bf16_f32 v155, v102, v103\n"
  "v_mfma_f32_16x16x32_bf16 v[116:119], a[204:207], a[244:247], v[116:119]\n"
  "s_waitcnt lgkmcnt(10)\n"
  "ds_read_b64_tr_b16 v[216:217], v227 offset:0\n"
  "ds_read_b64_tr_b16 v[218:219], v227 offset:4096\n"
  "v_mfma_f32_16x16x32_bf16 v[112:115], a[208:211], a[248:251], v[112:115]\n"
  "ds_read_b64_tr_b16 v[220:221], v227 offset:8192\n"
  "ds_read_b64_tr_b16 v[222:223], v227 offset:12288\n"
  "v_mfma_f32_16x16x32_bf16 v[112:115], a[212:215], a[252:255], v[112:115]\n"
  "buffer_load_dwordx4 a[60:63], v244, s[48:51], s56 offen offset:3072 sc1\n"
  "v_mfma_f32_16x16x32_bf16 v[116:119], a[216:219], a[248:251], v[116:119]\n"
  "s_add_u32 s59, s89, 0x38000\n"
  "ds_read_b64_tr_b16 a[224:225], v228 offset:0\n"
  "ds_read_b64_tr_b16 a[226:227], v228 offset:4096\n"
  "v_mfma_f32_16x16x32_bf16 v[116:119], a[220:223], a[252:255], v[116:119]\n"
  "s_waitcnt vmcnt(26)\n"
  "ds_read_b64_tr_b16 a[228:229], v228 offset:8192\n"
  "ds_read_b64_tr_b16 a[230:231], v228 offset:12288\n"
  "v_mfma_f32_16x16x32_bf16 v[8:11], a[128:131], v[192:195], v[8:11]\n"
  "s_waitcnt lgkmcnt(12)\n"
  "s_add_u32 m0, s12, 8192\n"
  "ds_read_b64_tr_b16 a[232:233], v229 offset:0\n"
  "ds_read_b64_tr_b16 a[234:235], v229 offset:4096\n"
  "v_mfma_f32_16x16x32_bf16 v[8:11], a[132:135], v[196:199], v[8:11]\n"
  "buffer_load_dwordx4 v184, s[64:67], s59 offen offset:0 lds\n"
  "v_mfma_f32_16x16x32_bf16 v[12:15], a[136:139], v[192:195], v[12:15]\n"
  "v_cvt_pk_bf16_f32 v156, v112, v113\n"
  "v_cvt_pk_bf16_f32 v157, v114, v115\n"
  "v_mfma_f32_16x16x32_bf16 v[12:15], a[140:143], v[196:199], v[12:15]\n"
  "ds_read_b64_tr_b16 a[236:237], v229 offset:8192\n"
  "ds_read_b64_tr_b16 a[238:239], v229 offset:12288\n"
  "v_mfma_f32_16x16x32_bf16 v[24:27], a[128:131], v[200:203], v[24:27]\n"
  "v_cvt_pk_bf16_f32 v158, v116, v117\n"
  "v_cvt_pk_bf16_f32 v159, v118, v119\n"
  "v_mfma_f32_16x16x32_bf16 v[24:27], a[132:135], v[204:207], v[24:27]\n"
  "ds_read_b64_tr_b16 a[240:241], v230 offset:0\n"
  "ds_read_b64_tr_b16 a[242:243], v230 offset:4096\n"
  "v_mfma_f32_16x16x32_bf16 v[28:31], a[136:139], v[200:203], v[28:31]\n"
  "buffer_load_dwordx4 v185, s[64:67], s59 offen offset:1024 lds\n"
  "v_mfma_f32_16x16x32_bf16 v[28:31], a[140:143], v[204:207], v[28:31]\n"
  "s_waitcnt lgkmcnt(12)\n"
  "buffer_store_dwordx4 v[152:155], v234, s[48:51], s76 offen offset:1536 sc1\n"
  "v_mfma_f32_16x16x32_bf16 v[40:43], a[128:131], v[208:211], v[40:43]\n"
  "mk_tp14:\n"
  "s_cbranch_execz mk_tp18\n"
  "ds_read_b64_tr_b16 a[244:245], v230 offset:8192\n"
  "ds_read_b64_tr_b16 a[246:247], v230 offset:12288\n"
  "v_mfma_f32_16x16x32_bf16 v[40:43], a[132:135], v[212:215], v[40:43]\n"
  "ds_read_b64_tr_b16 a[248:249], v231 offset:0\n"
  "ds_read_b64_tr_b16 a[250:251], v231 offset:4096\n"
  "v_mfma_f32_16x16x32_bf16 v[44:47], a[136:139], v[208:211], v[44:47]\n"
  "buffer_load_dwordx4 v186, s[64:67], s59 offen offset:2048 lds\n"
  "v_mfma_f32_16x16x32_bf16 v[44:47], a[140:143], v[212:215], v[44:47]\n"
  "ds_read_b64_tr_b16 a[252:253], v231 offset:8192\n"
  "ds_read_b64_tr_b16 a[254:255], v231 offset:12288\n"
  "v_mfma_f32_16x16x32_bf16 v[56:59], a[128:131], v[216:219], v[56:59]\n"
  "s_waitcnt lgkmcnt(10)\n"
  "ds_read_b64_tr_b16 v[192:193], v224 offset:32768\n"
  "ds_read_b64_tr_b16 v[194:195], v224 offset:36864\n"
  "v_mfma_f32_16x16x32_bf16 v[56:59], a[132:135], v[220:223], v[56:59]\n"
  "ds_read_b64_tr_b16 v[196:197], v224 offset:40960\n"
  "ds_read_b64_tr_b16 v[198:199], v224 offset:45056\n"
  "v_mfma_f32_16x16x32_bf16 v[60:63], a[136:139], v[216:219], v[60:63]\n"
  "buffer_load_dwordx4 v187, s[64:67], s59 offen offset:3072 lds\n"
  "v_mfma_f32_16x16x32_bf16 v[60:63], a[140:143], v[220:223], v[60:63]\n"
  "s_add_u32 s59, s89, 0x54000\n"
  "v_add_u32 v224, 0x8000, v224\n"
  "v_lshlrev_b32 v245, 4, v245\n"
  "v_mfma_f32_16x16x32_bf16 v[72:75], a[128:131], a[224:227], v[72:75]\n"
  "s_add_u32 m0, s12, 12288\n"
  "ds_read_b64_tr_b16 v[200:201], v224 offset:32768\n"
  "ds_read_b64_tr_b16 v[202:203], v224 offset:36864\n"
  "v_mfma_f32_16x16x32_bf16 v[72:75], a[132:135], a[228:231], v[72:75]\n"
  "ds_read_b64_tr_b16 v[204:205], v224 offset:40960\n"
  "ds_read_b64_tr_b16 v[206:207], v224 offset:45056\n"
  "v_mfma_f32_16x16x32_bf16 v[76:79], a[136:139], a[224:227], v[76:79]\n"
  "ds_read_b64_tr_b16 v[208:209], v224 offset:49152\n"
  "v_mov_b32 v246, 0x3f80\n"
  "v_mfma_f32_16x16x32_bf16 v[76:79], a[140:143], a[228:231], v[76:79]\n"
  "buffer_store_dwordx4 v[156:159], v234, s[48:51], s76 offen offset:1792 sc1\n"
  "v_mfma_f32_16x16x32_bf16 v[88:91], a[128:131], a[232:235], v[88:91]\n"
  "s_waitcnt lgkmcnt(13)\n"
  "buffer_load_dwordx4 v184, s[64:67], s59 offen offset:0 lds\n"
  "v_mfma_f32_16x16x32_bf16 v[88:91], a[132:135], a[236:239], v[88:91]\n"
  "ds_read_b64_tr_b16 v[210:211], v224 offset:53248\n"
  "ds_read_b64_tr_b16 v[212:213], v224 offset:57344\n"
  "v_mfma_f32_16x16x32_bf16 v[92:95], a[136:139], a[232:235], v[92:95]\n"
  "ds_read_b64_tr_b16 v[214:215], v224 offset:61440\n"
  "ds_read_b64_tr_b16 v[216:217], v224 offset:16384\n"
  "v_mfma_f32_16x16x32_bf16 v[92:95], a[140:143], a[236:239], v[92:95]\n"
  "ds_read_b64_tr_b16 v[218:219], v224 offset:20480\n"
  "v_lshlrev_b32 v245, v245, v246\n"
  "v_mfma_f32_16x16x32_bf16 v[104:107], a[128:131], a[240:243], v[104:107]\n"
  "buffer_load_dwordx4 v185, s[64:67], s59 offen offset:1024 lds\n"
  "v_mfma_f32_16x16x32_bf16 v[104:107], a[132:135], a[244:247], v[104:107]\n"
  "ds_read_b64_tr_b16 v[220:221], v224 offset:24576\n"
  "ds_read_b64_tr_b16 v[222:223], v224 offset:28672\n"
  "v_mfma_f32_16x16x32_bf16 v[108:111], a[136:139], a[240:243], v[108:111]\n"
  "v_add_u32 v224, 0xffff8000, v224\n"
  "v_lshrrev_b32 v246, 1, v242\n"
  "v_mfma_f32_16x16x32_bf16 v[108:111], a[140:143], a[244:247], v[108:111]\n"
  "s_waitcnt lgkmcnt(12)\n"
  "ds_read_b64_tr_b16 a[224:225], v224 offset:16384\n"
  "ds_read_b64_tr_b16 a[226:227], v224 offset:20480\n"
  "v_mfma_f32_16x16x32_bf16 v[120:123], a[128:131], a[248:251], v[120:123]\n"
  "ds_read_b64_tr_b16 a[228:229], v224 offset:24576\n"
  "v_cmp_eq_u32_e64 s[16:17], 0, v246\n"
  "v_mfma_f32_16x16x32_bf16 v[120:123], a[132:135], a[252:255], v[120:123]\n"
  "buffer_load_dwordx4 v186, s[64:67], s59 offen offset:2048 lds\n"
  "v_mfma_f32_16x16x32_bf16 v[124:127], a[136:139], a[248:251], v[124:127]\n"
  "ds_read_b64_tr_b16 a[230:231], v224 offset:28672\n"
  "ds_read_b64_tr_b16 a[232:233], v225 offset:32768\n"
  "v_mfma_f32_16x16x32_bf16 v[124:127], a[140:143], a[252:255], v[124:127]\n"
  "s_waitcnt vmcnt(30)\n"
  "ds_read_b64_tr_b16 a[234:235], v225 offset:36864\n"
  "v_cndmask_b32_e64 v164, 0, v245, s[16:17]\n"
  "v_mfma_f32_16x16x32_bf16 v[8:11], a[144:147], v[192:195], v[8:11]\n"
  "ds_read_b64_tr_b16 a[236:237], v225 offset:40960\n"
  "v_cmp_eq_u32_e64 s[16:17], 1, v246\n"
  "v_mfma_f32_16x16x32_bf16 v[8:11], a[148:151], v[196:199], v[8:11]\n"
  "ds_read_b64_tr_b16 a[238:239], v225 offset:45056\n"
  "v_add_u32 v225, 0x8000, v225\n"
  "v_mfma_f32_16x16x32_bf16 v[12:15], a[152:155], v[192:195], v[12:15]\n"
  "buffer_load_dwordx4 v187, s[64:67], s59 offen offset:3072 lds\n"
  "v_mfma_f32_16x16x32_bf16 v[12:15], a[156:159], v[196:199], v[12:15]\n"
  "s_waitcnt lgkmcnt(10)\n"
  "s_waitcnt vmcnt(26)\n"
  "ds_read_b64_tr_b16 a[240:241], v225 offset:32768\n"
  "ds_read_b64_tr_b16 a[242:243], v225 offset:36864\n"
  "v_mfma_f32_16x16x32_bf16 v[8:11], a[0:3], v[200:203], v[8:11]\n"
  "ds_read_b64_tr_b16 a[244:245], v225 offset:40960\n"
  "v_cndmask_b32_e64 v165, 0, v245, s[16:17]\n"
  "v_mfma_f32_16x16x32_bf16 v[8:11], a[4:7], v[204:207], v[8:11]\n"
  "ds_read_b64_tr_b16 a[246:247], v225 offset:45056\n"
  "v_cmp_eq_u32_e64 s[16:17], 2, v246\n"
  "v_mfma_f32_16x16x32_bf16 v[12:15], a[8:11], v[200:203], v[12:15]\n"
  "ds_read_b64_tr_b16 a[248:249], v225 offset:49152\n"
  "v_mfma_f32_16x16x32_bf16 v[12:15], a[12:15], v[204:207], v[12:15]\n"
  "s_waitcnt vmcnt(21)\n"
  "ds_read_b64_tr_b16 a[250:251], v225 offset:53248\n"
  "v_cndmask_b32_e64 v166, 0, v245, s[16:17]\n"
  "v_mfma_f32_16x16x32_bf16 v[8:11], a[16:19], v[208:211], v[8:11]\n"
  "ds_read_b64_tr_b16 a[252:253], v225 offset:57344\n"
  "v_cmp_eq_u32_e64 s[16:17], 3, v246\n"
  "v_mfma_f32_16x16x32_bf16 v[8:11], a[20:23], v[212:215], v[8:11]\n"
  "ds_read_b64_tr_b16 a[254:255], v225 offset:61440\n"
  "v_mfma_f32_16x16x32_bf16 v[12:15], a[24:27], v[208:211], v[12:15]\n"
  "ds_read_b64_tr_b16 v[192:193], v225 offset:16384\n"
  "v_cndmask_b32_e64 v167, 0, v245, s[16:17]\n"
  "v_mfma_f32_16x16x32_bf16 v[12:15], a[28:31], v[212:215], v[12:15]\n"
  "s_waitcnt vmcnt(16)\n"
  "ds_read_b64_tr_b16 v[194:195], v225 offset:20480\n"
  "v_lshlrev_b32 v242, 3, v240\n"
  "v_mfma_f32_16x16x32_bf16 v[8:11], a[32:35], v[216:219], v[8:11]\n"
  "s_waitcnt lgkmcnt(11)\n"
  "ds_read_b64_tr_b16 v[196:197], v225 offset:24576\n"
  "v_sub_u32 v242, v241, v242\n"
  "v_mfma_f32_16x16x32_bf16 v[8:11], a[36:39], v[220:223], v[8:11]\n"
  "ds_read_b64_tr_b16 v[198:199], v225 offset:28672\n"
  "v_add_u32 v225, 0xffff8000, v225\n"
  "v_mfma_f32_16x16x32_bf16 v[12:15], a[40:43], v[216:219], v[12:15]\n"
  "ds_read_b64_tr_b16 v[200:201], v225 offset:16384\n"
  "v_and_b32 v245, 1, v242\n"
  "v_mfma_f32_16x16x32_bf16 v[12:15], a[44:47], v[220:223], v[12:15]\n"
  "s_waitcnt vmcnt(10)\n"
  "ds_read_b64_tr_b16 v[202:203], v225 offset:20480\n"
  "v_lshlrev_b32 v245, 4, v245\n"
  "v_mfma_f32_16x16x32_bf16 v[8:11], a[48:51], a[224:227], v[8:11]\n"
  "ds_read_b64_tr_b16 v[204:205], v225 offset:24576\n"
  "v_mov_b32 v246, 0x3f80\n"
  "v_mfma_f32_16x16x32_bf16 v[8:11], a[52:55], a[228:231], v[8:11]\n"
  "ds_read_b64_tr_b16 v[206:207], v225 offset:28672\n"
  "v_lshlrev_b32 v245, v245, v246\n"
  "v_mfma_f32_16x16x32_bf16 v[12:15], a[56:59], a[224:227], v[12:15]\n"
  "ds_read_b64_tr_b16 v[208:209], v226 offset:32768\n"
  "v_lshrrev_b32 v246, 1, v242\n"
  "v_mfma_f32_16x16x32_bf16 v[12:15], a[60:63], a[228:231], v[12:15]\n"
  "ds_read_b64_tr_b16 v[210:211], v226 offset:36864\n"
  "v_cmp_eq_u32_e64 s[16:17], 0, v246\n"
  "v_mfma_f32_16x16x32_bf16 v[24:27], a[144:147], a[232:235], v[24:27]\n"
  "s_waitcnt lgkmcnt(11)\n"
  "ds_read_b64_tr_b16 v[212:213], v226 offset:40960\n"
  "v_mfma_f32_16x16x32_bf16 v[24:27], a[148:151], a[236:239], v[24:27]\n"
  "v_cvt_pk_bf16_f32 v128, v8, v9\n"
  "v_cvt_pk_bf16_f32 v129, v10, v11\n"
  "v_mfma_f32_16x16x32_bf16 v[28:31], a[152:155], a[232:235], v[28:31]\n"
  "ds_read_b64_tr_b16 v[214:215], v226 offset:45056\n"
  "v_add_u32 v226, 0x8000, v226\n"
  "v_mfma_f32_16x16x32_bf16 v[28:31], a[156:159], a[236:239], v[28:31]\n"
  "v_cvt_pk_bf16_f32 v130, v12, v13\n"
  "v_cvt_pk_bf16_f32 v131, v14, v15\n"
  "v_mfma_f32_16x16x32_bf16 v[24:27], a[0:3], a[240:243], v[24:27]\n"
  "buffer_store_dwordx4 v[128:131], v234, s[48:51], s77 offen offset:0 sc1\n"
  "v_mfma_f32_16x16x32_bf16 v[24:27], a[4:7], a[244:247], v[24:27]\n"
  "ds_read_b64_tr_b16 v[216:217], v226 offset:32768\n"
  "ds_read_b64_tr_b16 v[218:219], v226 offset:36864\n"
  "v_mfma_f32_16x16x32_bf16 v[28:31], a[8:11], a[240:243], v[28:31]\n"
  "ds_read_b64_tr_b16 v[220:221], v226 offset:40960\n"
  "ds_read_b64_tr_b16 v[222:223], v226 offset:45056\n"
  "v_mfma_f32_16x16x32_bf16 v[28:31], a[12:15], a[244:247], v[28:31]\n"
  "ds_read_b64_tr_b16 a[224:225], v226 offset:49152\n"
  "ds_read_b64_tr_b16 a[226:227], v226 offset:53248\n"
  "v_mfma_f32_16x16x32_bf16 v[24:27], a[16:19], a[248:251], v[24:27]\n"
  "s_waitcnt lgkmcnt(11)\n"
  "ds_read_b64_tr_b16 a[228:229], v226 offset:57344\n"
  "v_cndmask_b32_e64 v168, 0, v245, s[16:17]\n"
  "v_mfma_f32_16x16x32_bf16 v[24:27], a[20:23], a[252:255], v[24:27]\n"
  "ds_read_b64_tr_b16 a[230:231], v226 offset:61440\n"
  "v_cmp_eq_u32_e64 s[16:17], 1, v246\n"
  "v_mfma_f32_16x16x32_bf16 v[28:31], a[24:27], a[248:251], v[28:31]\n"
  "ds_read_b64_tr_b16 a[232:233], v226 offset:16384\n"
  "v_mfma_f32_16x16x32_bf16 v[28:31], a[28:31], a[252:255], v[28:31]\n"
  "ds_read_b64_tr_b16 a[234:235], v226 offset:20480\n"
  "v_cndmask_b32_e64 v169, 0, v245, s[16:17]\n"
  "v_mfma_f32_16x16x32_bf16 v[24:27], a[32:35], v[192:195], v[24:27]\n"
  "ds_read_b64_tr_b16 a[236:237], v226 offset:24576\n"
  "v_cmp_eq_u32_e64 s[16:17], 2, v246\n"
  "v_mfma_f32_16x16x32_bf16 v[24:27], a[36:39], v[196:199], v[24:27]\n"
  "s_waitcnt vmcnt(5)\n"
  "buffer_atomic_add v248, v253, s[48:51], s78 offen sc1\n"
  "v_mfma_f32_16x16x32_bf16 v[28:31], a[40:43], v[192:195], v[28:31]\n"
  "ds_read_b64_tr_b16 a[238:239], v226 offset:28672\n"
  "v_add_u32 v226, 0xffff8000, v226\n"
  "v_mfma_f32_16x16x32_bf16 v[28:31], a[44:47], v[196:199], v[28:31]\n"
  "ds_read_b64_tr_b16 a[240:241], v226 offset:16384\n"
  "ds_read_b64_tr_b16 a[242:243], v226 offset:20480\n"
  "v_mfma_f32_16x16x32_bf16 v[24:27], a[48:51], v[200:203], v[24:27]\n"
  "s_waitcnt lgkmcnt(14)\n"
  "buffer_load_dwordx4 v[176:179], v243, s[44:47], s83 offen offset:0\n"
  "v_mfma_f32_16x16x32_bf16 v[24:27], a[52:55], v[204:207], v[24:27]\n"
  "ds_read_b64_tr_b16 a[244:245], v226 offset:24576\n"
  "ds_read_b64_tr_b16 a[246:247], v226 offset:28672\n"
  "v_mfma_f32_16x16x32_bf16 v[28:31], a[56:59], v[200:203], v[28:31]\n"
  "ds_read_b64_tr_b16 a[248:249], v227 offset:32768\n"
  "v_cndmask_b32_e64 v170, 0, v245, s[16:17]\n"
  "v_mfma_f32_16x16x32_bf16 v[28:31], a[60:63], v[204:207], v[28:31]\n"
  "ds_read_b64_tr_b16 a[250:251], v227 offset:36864\n"
  "v_cmp_eq_u32_e64 s[16:17], 3, v246\n"
  "v_mfma_f32_16x16x32_bf16 v[40:43], a[144:147], v[208:211], v[40:43]\n"
  "ds_read_b64_tr_b16 a[252:253], v227 offset:40960\n"
  "v_mfma_f32_16x16x32_bf16 v[40:43], a[148:151], v[212:215], v[40:43]\n"
  "buffer_load_dwordx4 v[180:183], v243, s[44:47], s83 offen offset:128\n"
  "v_mfma_f32_16x16x32_bf16 v[44:47], a[152:155], v[208:211], v[44:47]\n"
  "v_cvt_pk_bf16_f32 v132, v24, v25\n"
  "v_cvt_pk_bf16_f32 v133, v26, v27\n"
  "v_mfma_f32_16x16x32_bf16 v[44:47], a[156:159], v[212:215], v[44:47]\n"
  "s_waitcnt lgkmcnt(9)\n"
  "ds_read_b64_tr_b16 a[254:255], v227 offset:45056\n"
  "v_add_u32 v227, 0x8000, v227\n"
  "v_mfma_f32_16x16x32_bf16 v[40:43], a[0:3], v[216:219], v[40:43]\n"
  "v_cvt_pk_bf16_f32 v134, v28, v29\n"
  "v_cvt_pk_bf16_f32 v135, v30, v31\n"
  "v_mfma_f32_16x16x32_bf16 v[40:43], a[4:7], v[220:223], v[40:43]\n"
  "buffer_store_dwordx4 v[132:135], v234, s[48:51], s77 offen offset:256 sc1\n"
  "v_mfma_f32_16x16x32_bf16 v[44:47], a[8:11], v[216:219], v[44:47]\n"
  "ds_read_b64_tr_b16 v[192:193], v227 offset:32768\n"
  "ds_read_b64_tr_b16 v[194:195], v227 offset:36864\n"
  "v_mfma_f32_16x16x32_bf16 v[44:47], a[12:15], v[220:223], v[44:47]\n"
  "ds_read_b64_tr_b16 v[196:197], v227 offset:40960\n"
  "ds_read_b64_tr_b16 v[198:199], v227 offset:45056\n"
  "v_mfma_f32_16x16x32_bf16 v[40:43], a[16:19], a[224:227], v[40:43]\n"
  "ds_read_b64_tr_b16 v[200:201], v227 offset:49152\n"
  "ds_read_b64_tr_b16 v[202:203], v227 offset:53248\n"
  "v_mfma_f32_16x16x32_bf16 v[40:43], a[20:23], a[228:231], v[40:43]\n"
  "ds_read_b64_tr_b16 v[204:205], v227 offset:57344\n"
  "ds_read_b64_tr_b16 v[206:207], v227 offset:61440\n"
  "v_mfma_f32_16x16x32_bf16 v[44:47], a[24:27], a[224:227], v[44:47]\n"
  "ds_read_b64_tr_b16 v[208:209], v227 offset:16384\n"
  "v_cndmask_b32_e64 v171, 0, v245, s[16:17]\n"
  "v_mfma_f32_16x16x32_bf16 v[44:47], a[28:31], a[228:231], v[44:47]\n"
  "ds_read_b64_tr_b16 v[210:211], v227 offset:20480\n"
  "v_add_u32 v242, 16, v242\n"
  "v_mfma_f32_16x16x32_bf16 v[40:43], a[32:35], a[232:235], v[40:43]\n"
  "s_waitcnt lgkmcnt(11)\n"
  "ds_read_b64_tr_b16 v[212:213], v227 offset:24576\n"
  "v_and_b32 v245, 1, v242\n"
  "v_mfma_f32_16x16x32_bf16 v[40:43], a[36:39], a[236:239], v[40:43]\n"
  "ds_read_b64_tr_b16 v[214:215], v227 offset:28672\n"
  "v_add_u32 v227, 0xffff8000, v227\n"
  "v_mfma_f32_16x16x32_bf16 v[44:47], a[40:43], a[232:235], v[44:47]\n"
  "ds_read_b64_tr_b16 v[216:217], v227 offset:16384\n"
  "v_lshlrev_b32 v245, 4, v245\n"
  "v_mfma_f32_16x16x32_bf16 v[44:47], a[44:47], a[236:239], v[44:47]\n"
  "ds_read_b64_tr_b16 v[218:219], v227 offset:20480\n"
  "v_mov_b32 v246, 0x3f80\n"
  "v_mfma_f32_16x16x32_bf16 v[40:43], a[48:51], a[240:243], v[40:43]\n"
  "ds_read_b64_tr_b16 v[220:221], v227 offset:24576\n"
  "v_lshlrev_b32 v245, v245, v246\n"
  "v_mfma_f32_16x16x32_bf16 v[40:43], a[52:55], a[244:247], v[40:43]\n"
  "ds_read_b64_tr_b16 v[222:223], v227 offset:28672\n"
  "v_lshrrev_b32 v246, 1, v242\n"
  "v_mfma_f32_16x16x32_bf16 v[44:47], a[56:59], a[240:243], v[44:47]\n"
  "ds_read_b64_tr_b16 a[224:225], v228 offset:32768\n"
  "v_cmp_eq_u32_e64 s[16:17], 0, v246\n"
  "v_mfma_f32_16x16x32_bf16 v[44:47], a[60:63], a[244:247], v[44:47]\n"
  "ds_read_b64_tr_b16 a[226:227], v228 offset:36864\n"
  "v_mfma_f32_16x16x32_bf16 v[56:59], a[144:147], a[248:251], v[56:59]\n"
  "s_waitcnt lgkmcnt(12)\n"
  "ds_read_b64_tr_b16 a[228:229], v228 offset:40960\n"
  "v_cndmask_b32_e64 v172, 0, v245, s[16:17]\n"
  "v_mfma_f32_16x16x32_bf16 v[56:59], a[148:151], a[252:255], v[56:59]\n"
  "v_cvt_pk_bf16_f32 v136, v40, v41\n"
  "v_cvt_pk_bf16_f32 v137, v42, v43\n"
  "v_mfma_f32_16x16x32_bf16 v[60:63], a[152:155], a[248:251], v[60:63]\n"
  "ds_read_b64_tr_b16 a[230:231], v228 offset:45056\n"
  "v_add_u32 v228, 0x8000, v228\n"
  "v_mfma_f32_16x16x32_bf16 v[60:63], a[156:159], a[252:255], v[60:63]\n"
  "v_cvt_pk_bf16_f32 v138, v44, v45\n"
  "v_cvt_pk_bf16_f32 v139, v46, v47\n"
  "v_mfma_f32_16x16x32_bf16 v[56:59], a[0:3], v[192:195], v[56:59]\n"
  "buffer_store_dwordx4 v[136:139], v234, s[48:51], s77 offen offset:512 sc1\n"
  "v_mfma_f32_16x16x32_bf16 v[56:59], a[4:7], v[196:199], v[56:59]\n"
  "ds_read_b64_tr_b16 a[232:233], v228 offset:32768\n"
  "ds_read_b64_tr_b16 a[234:235], v228 offset:36864\n"
  "v_mfma_f32_16x16x32_bf16 v[60:63], a[8:11], v[192:195], v[60:63]\n"
  "ds_read_b64_tr_b16 a[236:237], v228 offset:40960\n"
  "ds_read_b64_tr_b16 a[238:239], v228 offset:45056\n"
  "v_mfma_f32_16x16x32_bf16 v[60:63], a[12:15], v[196:199], v[60:63]\n"
  "ds_read_b64_tr_b16 a[240:241], v228 offset:49152\n"
  "v_mov_b32 v233, 0\n"
  "v_mfma_f32_16x16x32_bf16 v[56:59], a[16:19], v[200:203], v[56:59]\n"
  "s_waitcnt lgkmcnt(10)\n"
  "buffer_atomic_add v233, v253, s[48:51], s80 offen sc0 sc1\n"
  "v_mfma_f32_16x16x32_bf16 v[56:59], a[20:23], v[204:207], v[56:59]\n"
  "ds_read_b64_tr_b16 a[242:243], v228 offset:53248\n"
  "ds_read_b64_tr_b16 a[244:245], v228 offset:57344\n"
  "v_mfma_f32_16x16x32_bf16 v[60:63], a[24:27], v[200:203], v[60:63]\n"
  "ds_read_b64_tr_b16 a[246:247], v228 offset:61440\n"
  "ds_read_b64_tr_b16 a[248:249], v228 offset:16384\n"
  "v_mfma_f32_16x16x32_bf16 v[60:63], a[28:31], v[204:207], v[60:63]\n"
  "ds_read_b64_tr_b16 a[250:251], v228 offset:20480\n"
  "v_cmp_eq_u32_e64 s[16:17], 1, v246\n"
  "v_mfma_f32_16x16x32_bf16 v[56:59], a[32:35], v[208:211], v[56:59]\n"
  "ds_read_b64_tr_b16 a[252:253], v228 offset:24576\n"
  "v_mfma_f32_16x16x32_bf16 v[56:59], a[36:39], v[212:215], v[56:59]\n"
  "ds_read_b64_tr_b16 a[254:255], v228 offset:28672\n"
  "v_add_u32 v228, 0xffff8000, v228\n"
  "v_mfma_f32_16x16x32_bf16 v[60:63], a[40:43], v[208:211], v[60:63]\n"
  "ds_read_b64_tr_b16 v[192:193], v228 offset:16384\n"
  "v_cndmask_b32_e64 v173, 0, v245, s[16:17]\n"
  "v_mfma_f32_16x16x32_bf16 v[60:63], a[44:47], v[212:215], v[60:63]\n"
  "ds_read_b64_tr_b16 v[194:195], v228 offset:20480\n"
  "v_cmp_eq_u32_e64 s[16:17], 2, v246\n"
  "v_mfma_f32_16x16x32_bf16 v[56:59], a[48:51], v[216:219], v[56:59]\n"
  "s_waitcnt lgkmcnt(14)\n"
  "ds_read_b64_tr_b16 v[196:197], v228 offset:24576\n"
  "v_mfma_f32_16x16x32_bf16 v[56:59], a[52:55], v[220:223], v[56:59]\n"
  "ds_read_b64_tr_b16 v[198:199], v228 offset:28672\n"
  "v_cndmask_b32_e64 v174, 0, v245, s[16:17]\n"
  "v_mfma_f32_16x16x32_bf16 v[60:63], a[56:59], v[216:219], v[60:63]\n"
  "ds_read_b64_tr_b16 v[200:201], v229 offset:32768\n"
  "v_cmp_eq_u32_e64 s[16:17], 3, v246\n"
  "v_mfma_f32_16x16x32_bf16 v[60:63], a[60:63], v[220:223], v[60:63]\n"
  "ds_read_b64_tr_b16 v[202:203], v229 offset:36864\n"
  "v_mfma_f32_16x16x32_bf16 v[72:75], a[144:147], a[224:227], v[72:75]\n"
  "ds_read_b64_tr_b16 v[204:205], v229 offset:40960\n"
  "v_cndmask_b32_e64 v175, 0, v245, s[16:17]\n"
  "v_mfma_f32_16x16x32_bf16 v[72:75], a[148:151], a[228:231], v[72:75]\n"
  "v_cvt_pk_bf16_f32 v140, v56, v57\n"
  "v_cvt_pk_bf16_f32 v141, v58, v59\n"
  "v_mfma_f32_16x16x32_bf16 v[76:79], a[152:155], a[224:227], v[76:79]\n"
  "ds_read_b64_tr_b16 v[206:207], v229 offset:45056\n"
  "v_add_u32 v229, 0x8000, v229\n"
  "v_mfma_f32_16x16x32_bf16 v[76:79], a[156:159], a[228:231], v[76:79]\n"
  "s_waitcnt lgkmcnt(10)\n"
  "v_cvt_pk_bf16_f32 v142, v60, v61\n"
  "v_cvt_pk_bf16_f32 v143, v62, v63\n"
  "v_mfma_f32_16x16x32_bf16 v[72:75], a[0:3], a[232:235], v[72:75]\n"
  "buffer_store_dwordx4 v[140:143], v234, s[48:51], s77 offen offset:768 sc1\n"
  "v_mfma_f32_16x16x32_bf16 v[72:75], a[4:7], a[236:239], v[72:75]\n"
  "ds_read_b64_tr_b16 v[208:209], v229 offset:32768\n"
  "ds_read_b64_tr_b16 v[210:211], v229 offset:36864\n"
  "v_mfma_f32_16x16x32_bf16 v[76:79], a[8:11], a[232:235], v[76:79]\n"
  "ds_read_b64_tr_b16 v[212:213], v229 offset:40960\n"
  "ds_read_b64_tr_b16 v[214:215], v229 offset:45056\n"
  "v_mfma_f32_16x16x32_bf16 v[76:79], a[12:15], a[236:239], v[76:79]\n"
  "ds_read_b64_tr_b16 v[216:217], v229 offset:49152\n"
  "ds_read_b64_tr_b16 v[218:219], v229 offset:53248\n"
  "v_mfma_f32_16x16x32_bf16 v[72:75], a[16:19], a[240:243], v[72:75]\n"
  "ds_read_b64_tr_b16 v[220:221], v229 offset:57344\n"
  "v_mfma_f32_16x16x32_bf16 v[72:75], a[20:23], a[244:247], v[72:75]\n"
  "ds_read_b64_tr_b16 v[222:223], v229 offset:61440\n"
  "v_mfma_f32_16x16x32_bf16 v[76:79], a[24:27], a[240:243], v[76:79]\n"
  "ds_read_b64_tr_b16 a[224:225], v229 offset:16384\n"
  "v_mfma_f32_16x16x32_bf16 v[76:79], a[28:31], a[244:247], v[76:79]\n"
  "ds_read_b64_tr_b16 a[226:227], v229 offset:20480\n"
  "v_mfma_f32_16x16x32_bf16 v[72:75], a[32:35], a[248:251], v[72:75]\n"
  "s_waitcnt lgkmcnt(11)\n"
  "ds_read_b64_tr_b16 a[228:229], v229 offset:24576\n"
  "v_mfma_f32_16x16x32_bf16 v[72:75], a[36:39], a[252:255], v[72:75]\n"
  "ds_read_b64_tr_b16 a[230:231], v229 offset:28672\n"
  "v_add_u32 v229, 0xffff8000, v229\n"
  "v_mfma_f32_16x16x32_bf16 v[76:79], a[40:43], a[248:251], v[76:79]\n"
  "ds_read_b64_tr_b16 a[232:233], v229 offset:16384\n"
  "v_mfma_f32_16x16x32_bf16 v[76:79], a[44:47], a[252:255], v[76:79]\n"
  "ds_read_b64_tr_b16 a[234:235], v229 offset:20480\n"
  "v_mfma_f32_16x16x32_bf16 v[72:75], a[48:51], v[192:195], v[72:75]\n"
  "ds_read_b64_tr_b16 a[236:237], v229 offset:24576\n"
  "v_mfma_f32_16x16x32_bf16 v[72:75], a[52:55], v[196:199], v[72:75]\n"
  "ds_read_b64_tr_b16 a[238:239], v229 offset:28672\n"
  "v_mfma_f32_16x16x32_bf16 v[76:79], a[56:59], v[192:195], v[76:79]\n"
  "s_waitcnt vmcnt(1)\n"
  "ds_read_b64_tr_b16 a[240:241], v230 offset:32768\n"
  "v_cmp_ne_u32 vcc, 8, v233\n"
  "v_mfma_f32_16x16x32_bf16 v[76:79], a[60:63], v[196:199], v[76:79]\n"
  "s_and_b32 vcc_lo, vcc_lo, s75\n"
  "s_cbranch_scc1 mk_spin4\n"
  "mk_spin4_b:\n"
  "ds_read_b64_tr_b16 a[242:243], v230 offset:36864\n"
  "v_mfma_f32_16x16x32_bf16 v[88:91], a[144:147], v[200:203], v[88:91]\n"
  "s_waitcnt lgkmcnt(11)\n"
  "ds_read_b64_tr_b16 a[244:245], v230 offset:40960\n"
  "v_mfma_f32_16x16x32_bf16 v[88:91], a[148:151], v[204:207], v[88:91]\n"
  "buffer_load_dwordx4 v[0:3], v235, s[48:51], s8 offen offset:0 sc1\n"
  "v_mfma_f32_16x16x32_bf16 v[92:95], a[152:155], v[200:203], v[92:95]\n"
  "v_cvt_pk_bf16_f32 v144, v72, v73\n"
  "v_cvt_pk_bf16_f32 v145, v74, v75\n"
  "v_mfma_f32_16x16x32_bf16 v[92:95], a[156:159], v[204:207], v[92:95]\n"
  "ds_read_b64_tr_b16 a[246:247], v230 offset:45056\n"
  "v_add_u32 v230, 0x8000, v230\n"
  "v_mfma_f32_16x16x32_bf16 v[88:91], a[0:3], v[208:211], v[88:91]\n"
  "v_cvt_pk_bf16_f32 v146, v76, v77\n"
  "v_cvt_pk_bf16_f32 v147, v78, v79\n"
  "v_mfma_f32_16x16x32_bf16 v[88:91], a[4:7], v[212:215], v[88:91]\n"
  "buffer_store_dwordx4 v[144:147], v234, s[48:51], s77 offen offset:1024 sc1\n"
  "v_mfma_f32_16x16x32_bf16 v[92:95], a[8:11], v[208:211], v[92:95]\n"
  "ds_read_b64_tr_b16 a[248:249], v230 offset:32768\n"
  "ds_read_b64_tr_b16 a[250:251], v230 offset:36864\n"
  "v_mfma_f32_16x16x32_bf16 v[92:95], a[12:15], v[212:215], v[92:95]\n"
  "buffer_load_dwordx4 v[4:7], v235, s[48:51], s9 offen offset:0 sc1\n"
  "v_mfma_f32_16x16x32_bf16 v[88:91], a[16:19], v[216:219], v[88:91]\n"
  "s_waitcnt lgkmcnt(7)\n"
  "ds_read_b64_tr_b16 a[252:253], v230 offset:40960\n"
  "ds_read_b64_tr_b16 a[254:255], v230 offset:45056\n"
  "v_mfma_f32_16x16x32_bf16 v[88:91], a[20:23], v[220:223], v[88:91]\n"
  "buffer_load_dwordx4 v[16:19], v235, s[48:51], s8 offen offset:256 sc1\n"
  "v_mfma_f32_16x16x32_bf16 v[92:95], a[24:27], v[216:219], v[92:95]\n"
  "ds_read_b64_tr_b16 v[192:193], v230 offset:49152\n"
  "ds_read_b64_tr_b16 v[194:195], v230 offset:53248\n"
  "v_mfma_f32_16x16x32_bf16 v[92:95], a[28:31], v[220:223], v[92:95]\n"
  "buffer_load_dwordx4 v[20:23], v235, s[48:51], s9 offen offset:256 sc1\n"
  "v_mfma_f32_16x16x32_bf16 v[88:91], a[32:35], a[224:227], v[88:91]\n"
  "ds_read_b64_tr_b16 v[196:197], v230 offset:57344\n"
  "ds_read_b64_tr_b16 v[198:199], v230 offset:61440\n"
  "v_mfma_f32_16x16x32_bf16 v[88:91], a[36:39], a[228:231], v[88:91]\n"
  "buffer_load_dwordx4 v[32:35], v235, s[48:51], s8 offen offset:512 sc1\n"
  "v_mfma_f32_16x16x32_bf16 v[92:95], a[40:43], a[224:227], v[92:95]\n"
  "ds_read_b64_tr_b16 v[200:201], v230 offset:16384\n"
  "ds_read_b64_tr_b16 v[202:203], v230 offset:20480\n"
  "v_mfma_f32_16x16x32_bf16 v[92:95], a[44:47], a[228:231], v[92:95]\n"
  "ds_read_b64_tr_b16 v[204:205], v230 offset:24576\n"
  "ds_read_b64_tr_b16 v[206:207], v230 offset:28672\n"
  "v_mfma_f32_16x16x32_bf16 v[88:91], a[48:51], a[232:235], v[88:91]\n"
  "s_waitcnt lgkmcnt(12)\n"
  "buffer_load_dwordx4 v[36:39], v235, s[48:51], s9 offen offset:512 sc1\n"
  "v_mfma_f32_16x16x32_bf16 v[88:91], a[52:55], a[236:239], v[88:91]\n"
  "v_add_u32 v230, 0xffff8000, v230\n"
  "v_mfma_f32_16x16x32_bf16 v[92:95], a[56:59], a[232:235], v[92:95]\n"
  "ds_read_b64_tr_b16 v[208:209], v230 offset:16384\n"
  "ds_read_b64_tr_b16 v[210:211], v230 offset:20480\n"
  "v_mfma_f32_16x16x32_bf16 v[92:95], a[60:63], a[236:239], v[92:95]\n"
  "buffer_load_dwordx4 v[48:51], v235, s[48:51], s8 offen offset:768 sc1\n"
  "v_mfma_f32_16x16x32_bf16 v[104:107], a[144:147], a[240:243], v[104:107]\n"
  "ds_read_b64_tr_b16 v[212:213], v230 offset:24576\n"
  "ds_read_b64_tr_b16 v[214:215], v230 offset:28672\n"
  "v_mfma_f32_16x16x32_bf16 v[104:107], a[148:151], a[244:247], v[104:107]\n"
  "buffer_load_dwordx4 v[52:55], v235, s[48:51], s9 offen offset:768 sc1\n"
  "v_mfma_f32_16x16x32_bf16 v[108:111], a[152:155], a[240:243], v[108:111]\n"
  "ds_read_b64_tr_b16 v[216:217], v231 offset:32768\n"
  "ds_read_b64_tr_b16 v[218:219], v231 offset:36864\n"
  "v_mfma_f32_16x16x32_bf16 v[108:111], a[156:159], a[244:247], v[108:111]\n"
  "s_waitcnt lgkmcnt(12)\n"
  "buffer_load_dwordx4 v[64:67], v235, s[48:51], s8 offen offset:1024 sc1\n"
  "v_mfma_f32_16x16x32_bf16 v[104:107], a[0:3], a[248:251], v[104:107]\n"
  "v_cvt_pk_bf16_f32 v148, v88, v89\n"
  "v_cvt_pk_bf16_f32 v149, v90, v91\n"
  "v_mfma_f32_16x16x32_bf16 v[104:107], a[4:7], a[252:255], v[104:107]\n"
  "ds_read_b64_tr_b16 v[220:221], v231 offset:40960\n"
  "ds_read_b64_tr_b16 v[222:223], v231 offset:45056\n"
  "v_mfma_f32_16x16x32_bf16 v[108:111], a[8:11], a[248:251], v[108:111]\n"
  "v_add_u32 v231, 0x8000, v231\n"
  "v_cvt_pk_bf16_f32 v150, v92, v93\n"
  "v_mfma_f32_16x16x32_bf16 v[108:111], a[12:15], a[252:255], v[108:111]\n"
  "ds_read_b64_tr_b16 a[224:225], v231 offset:32768\n"
  "v_cvt_pk_bf16_f32 v151, v94, v95\n"
  "v_mfma_f32_16x16x32_bf16 v[104:107], a[16:19], v[192:195], v[104:107]\n"
  "s_waitcnt lgkmcnt(9)\n"
  "buffer_store_dwordx4 v[148:151], v234, s[48:51], s77 offen offset:1280 sc1\n"
  "v_mfma_f32_16x16x32_bf16 v[104:107], a[20:23], v[196:199], v[104:107]\n"
  "buffer_load_dwordx4 v[68:71], v235, s[48:51], s9 offen offset:1024 sc1\n"
  "v_mfma_f32_16x16x32_bf16 v[108:111], a[24:27], v[192:195], v[108:111]\n"
  "ds_read_b64_tr_b16 a[226:227], v231 offset:36864\n"
  "ds_read_b64_tr_b16 a[228:229], v231 offset:40960\n"
  "v_mfma_f32_16x16x32_bf16 v[108:111], a[28:31], v[196:199], v[108:111]\n"
  "buffer_load_dwordx4 v[80:83], v235, s[48:51], s8 offen offset:1280 sc1\n"
  "v_mfma_f32_16x16x32_bf16 v[104:107], a[32:35], v[200:203], v[104:107]\n"
  "ds_read_b64_tr_b16 a[230:231], v231 offset:45056\n"
  "ds_read_b64_tr_b16 a[232:233], v231 offset:49152\n"
  "v_mfma_f32_16x16x32_bf16 v[104:107], a[36:39], v[204:207], v[104:107]\n"
  "buffer_load_dwordx4 v[84:87], v235, s[48:51], s9 offen offset:1280 sc1\n"
  "v_mfma_f32_16x16x32_bf16 v[108:111], a[40:43], v[200:203], v[108:111]\n"
  "ds_read_b64_tr_b16 a[234:235], v231 offset:53248\n"
  "ds_read_b64_tr_b16 a[236:237], v231 offset:57344\n"
  "v_mfma_f32_16x16x32_bf16 v[108:111], a[44:47], v[204:207], v[108:111]\n"
  "s_waitcnt lgkmcnt(9)\n"
  "ds_read_b64_tr_b16 a[238:239], v231 offset:61440\n"
  "ds_read_b64_tr_b16 a[240:241], v231 offset:16384\n"
  "v_mfma_f32_16x16x32_bf16 v[104:107], a[48:51], v[208:211], v[104:107]\n"
  "buffer_load_dwordx4 v[96:99], v235, s[48:51], s8 offen offset:1536 sc1\n"
  "v_mfma_f32_16x16x32_bf16 v[104:107], a[52:55], v[212:215], v[104:107]\n"
  "ds_read_b64_tr_b16 a[242:243], v231 offset:20480\n"
  "ds_read_b64_tr_b16 a[244:245], v231 offset:24576\n"
  "v_mfma_f32_16x16x32_bf16 v[108:111], a[56:59], v[208:211], v[108:111]\n"
  "buffer_load_dwordx4 v[100:103], v235, s[48:51], s9 offen offset:1536 sc1\n"
  "v_mfma_f32_16x16x32_bf16 v[108:111], a[60:63], v[212:215], v[108:111]\n"
  "ds_read_b64_tr_b16 a[246:247], v231 offset:28672\n"
  "v_add_u32 v231, 0xffff8000, v231\n"
  "v_mfma_f32_16x16x32_bf16 v[120:123], a[144:147], v[216:219], v[120:123]\n"
  "s_waitcnt lgkmcnt(11)\n"
  "ds_read_b64_tr_b16 a[248:249], v231 offset:16384\n"
  "ds_read_b64_tr_b16 a[250:251], v231 offset:20480\n"
  "v_mfma_f32_16x16x32_bf16 v[120:123], a[148:151], v[220:223], v[120:123]\n"
  "buffer_load_dwordx4 v[112:115], v235, s[48:51], s8 offen offset:1792 sc1\n"
  "v_mfma_f32_16x16x32_bf16 v[124:127], a[152:155], v[216:219], v[124:127]\n"
  "ds_read_b64_tr_b16 a[252:253], v231 offset:24576\n"
  "ds_read_b64_tr_b16 a[254:255], v231 offset:28672\n"
  "v_mfma_f32_16x16x32_bf16 v[124:127], a[156:159], v[220:223], v[124:127]\n"
  "s_waitcnt lgkmcnt(13)\n"
  "buffer_load_dwordx4 v[116:119], v235, s[48:51], s9 offen offset:1792 sc1\n"
  "v_mfma_f32_16x16x32_bf16 v[120:123], a[0:3], a[224:227], v[120:123]\n"
  "s_waitcnt lgkmcnt(11)\n"
  "v_cvt_pk_bf16_f32 v152, v104, v105\n"
  "v_cvt_pk_bf16_f32 v153, v106, v107\n"
  "v_mfma_f32_16x16x32_bf16 v[120:123], a[4:7], a[228:231], v[120:123]\n"
  "v_cvt_pk_bf16_f32 v154, v108, v109\n"
  "v_cvt_pk_bf16_f32 v155, v110, v111\n"
  "v_mfma_f32_16x16x32_bf16 v[124:127], a[8:11], a[224:227], v[124:127]\n"
  "buffer_store_dwordx4 v[152:155], v234, s[48:51], s77 offen offset:1536 sc1\n"
  "v_mfma_f32_16x16x32_bf16 v[124:127], a[12:15], a[228:231], v[124:127]\n"
  "s_waitcnt lgkmcnt(7)\n"
  "v_mfma_f32_16x16x32_bf16 v[120:123], a[16:19], a[232:235], v[120:123]\n"
  "v_mfma_f32_16x16x32_bf16 v[120:123], a[20:23], a[236:239], v[120:123]\n"
  "v_mfma_f32_16x16x32_bf16 v[124:127], a[24:27], a[232:235], v[124:127]\n"
  "v_mfma_f32_16x16x32_bf16 v[124:127], a[28:31], a[236:239], v[124:127]\n"
  "s_waitcnt lgkmcnt(4)\n"
  "v_mfma_f32_16x16x32_bf16 v[120:123], a[32:35], a[240:243], v[120:123]\n"
  "v_mfma_f32_16x16x32_bf16 v[120:123], a[36:39], a[244:247], v[120:123]\n"
  "v_mfma_f32_16x16x32_bf16 v[124:127], a[40:43], a[240:243], v[124:127]\n"
  "v_mfma_f32_16x16x32_bf16 v[124:127], a[44:47], a[244:247], v[124:127]\n"
  "s_waitcnt lgkmcnt(0)\n"
  "v_mfma_f32_16x16x32_bf16 v[120:123], a[48:51], a[248:251], v[120:123]\n"
  "s_waitcnt lgkmcnt(0)\n"
  "s_barrier\n"
  "v_mfma_f32_16x16x32_bf16 v[120:123], a[52:55], a[252:255], v[120:123]\n"
  "v_mfma_f32_16x16x32_bf16 v[124:127], a[56:59], a[248:251], v[124:127]\n"
  "v_mfma_f32_16x16x32_bf16 v[124:127], a[60:63], a[252:255], v[124:127]\n"
  "s_nop 1\n"
  "s_nop 1\n"
  "s_nop 1\n"
  "v_cvt_pk_bf16_f32 v156, v120, v121\n"
  "v_cvt_pk_bf16_f32 v157, v122, v123\n"
  "s_nop 1\n"
  "v_cvt_pk_bf16_f32 v158, v124, v125\n"
  "v_cvt_pk_bf16_f32 v159, v126, v127\n"
  "buffer_store_dwordx4 v[156:159], v234, s[48:51], s77 offen offset:1792 sc1\n"
  "s_nop 0\n"
  "s_waitcnt vmcnt(28)\n"
  "ds_read_b64_tr_b16 a[64:65], v188 offset:0\n"
  "ds_read_b64_tr_b16 a[66:67], v188 offset:8192\n"
  "ds_read_b64_tr_b16 a[68:69], v189 offset:0\n"
  "ds_read_b64_tr_b16 a[70:71], v189 offset:8192\n"
  "ds_read_b64_tr_b16 a[72:73], v190 offset:0\n"
  "ds_read_b64_tr_b16 a[74:75], v190 offset:8192\n"
  "ds_read_b64_tr_b16 a[76:77], v191 offset:0\n"
  "ds_read_b64_tr_b16 a[78:79], v191 offset:8192\n"
  "ds_read_b64_tr_b16 a[80:81], v188 offset:2048\n"
  "ds_read_b64_tr_b16 a[82:83], v188 offset:10240\n"
  "ds_read_b64_tr_b16 a[84:85], v189 offset:2048\n"
  "ds_read_b64_tr_b16 a[86:87], v189 offset:10240\n"
  "ds_read_b64_tr_b16 a[88:89], v190 offset:2048\n"
  "ds_read_b64_tr_b16 a[90:91], v190 offset:10240\n"
  "ds_read_b64_tr_b16 a[92:93], v191 offset:2048\n"
  "ds_read_b64_tr_b16 a[94:95], v191 offset:10240\n"
  "ds_read_b64_tr_b16 a[96:97], v188 offset:4096\n"
  "ds_read_b64_tr_b16 a[98:99], v188 offset:12288\n"
  "ds_read_b64_tr_b16 a[100:101], v189 offset:4096\n"
  "ds_read_b64_tr_b16 a[102:103], v189 offset:12288\n"
  "ds_read_b64_tr_b16 a[104:105], v190 offset:4096\n"
  "ds_read_b64_tr_b16 a[106:107], v190 offset:12288\n"
  "ds_read_b64_tr_b16 a[108:109], v191 offset:4096\n"
  "ds_read_b64_tr_b16 a[110:111], v191 offset:12288\n"
  "ds_read_b64_tr_b16 a[112:113], v188 offset:6144\n"
  "ds_read_b64_tr_b16 a[114:115], v188 offset:14336\n"
  "mk_tp15:\n"
  "s_cbranch_execz mk_tp19\n"
  "ds_read_b64_tr_b16 a[116:117], v189 offset:6144\n"
  "ds_read_b64_tr_b16 a[118:119], v189 offset:14336\n"
  "ds_read_b64_tr_b16 a[120:121], v190 offset:6144\n"
  "ds_read_b64_tr_b16 a[122:123], v190 offset:14336\n"
  "ds_read_b64_tr_b16 a[124:125], v191 offset:6144\n"
  "ds_read_b64_tr_b16 a[126:127], v191 offset:14336\n"
  "v_cmp_gt_u32 vcc, 8, v239\n"
  "v_mov_b32 v228, 0x7fff0000\n"
  "v_lshlrev_b32 v242, 7, v239\n"
  "v_cndmask_b32 v228, v228, v242, vcc\n"
  "v_cmp_gt_u32 vcc, 2, v239\n"
  "v_mov_b32 v229, 0x7fff0000\n"
  "v_lshlrev_b32 v242, 12, v239\n"
  "v_cndmask_b32 v229, v229, v242, vcc\n"
  "s_sub_u32 s61, s13, s12\n"
  "v_add_u32 v224, s61, v188\n"
  "v_add_u32 v225, s61, v189\n"
  "v_add_u32 v226, s61, v190\n"
  "v_add_u32 v227, s61, v191\n"
  "s_xor_b32 s61, %1, 0\n"
  "s_lshl_b32 s61, s61, 10\n"
  "v_add_u32 v249, s61, v244\n"
  "s_xor_b32 s61, %1, 1\n"
  "s_lshl_b32 s61, s61, 10\n"
  "v_add_u32 v250, s61, v244\n"
  "s_xor_b32 s61, %1, 2\n"
  "s_lshl_b32 s61, s61, 10\n"
  "v_add_u32 v251, s61, v244\n"
  "s_xor_b32 s61, %1, 3\n"
  "s_lshl_b32 s61, s61, 10\n"
  "v_add_u32 v252, s61, v244\n"
  "s_mov_b64 s[44:45], s[22:23]\n"
  "s_mov_b32 s46, 3584\n"
  "s_lshl_b32 s62, s14, 7\n"
  "v_lshlrev_b32 v243, 3, v240\n"
  "s_waitcnt lgkmcnt(0)\n"
  "s_add_u32 s59, s11, 0x1c0000\n"
  "s_add_u32 m0, s12, 0\n"
  "s_nop 0\n"
  "buffer_load_dwordx4 v184, s[64:67], s59 offen offset:0 lds\n"
  "buffer_load_dwordx4 v185, s[64:67], s59 offen offset:1024 lds\n"
  "buffer_load_dwordx4 v186, s[64:67], s59 offen offset:2048 lds\n"
  "buffer_load_dwordx4 v187, s[64:67], s59 offen offset:3072 lds\n"
  "s_bitcmp1_b32 s10, 0\n"
  "s_cbranch_scc1 mk_ownB\n"
  "mk_ownA:\n"
  "s_nop 0\n"
  "s_nop 0\n"
  "v_mfma_f32_16x16x32_bf16 v[8:11], v[176:179], v[168:171], 0\n"
  "v_mfma_f32_16x16x32_bf16 v[24:27], v[176:179], v[172:175], 0\n"
  "v_mfma_f32_16x16x32_bf16 v[40:43], v[180:183], v[168:171], 0\n"
  "v_mfma_f32_16x16x32_bf16 v[56:59], v[180:183], v[172:175], 0\n"
  "s_waitcnt vmcnt(19)\n"
  "v_mfma_f32_16x16x32_bf16 v[8:11], v[160:163], v[0:3], v[8:11]\n"
  "v_mfma_f32_16x16x32_bf16 v[8:11], v[160:163], v[4:7], v[8:11]\n"
  "v_mfma_f32_16x16x32_bf16 v[8:11], v[164:167], v[16:19], v[8:11]\n"
  "v_mfma_f32_16x16x32_bf16 v[8:11], v[164:167], v[20:23], v[8:11]\n"
  "s_waitcnt vmcnt(15)\n"
  "v_mfma_f32_16x16x32_bf16 v[24:27], v[160:163], v[32:35], v[24:27]\n"
  "v_mfma_f32_16x16x32_bf16 v[24:27], v[160:163], v[36:39], v[24:27]\n"
  "v_mfma_f32_16x16x32_bf16 v[24:27], v[164:167], v[48:51], v[24:27]\n"
  "v_mfma_f32_16x16x32_bf16 v[24:27], v[164:167], v[52:55], v[24:27]\n"
  "s_waitcnt vmcnt(10)\n"
  "v_mfma_f32_16x16x32_bf16 v[40:43], v[160:163], v[64:67], v[40:43]\n"
  "v_mfma_f32_16x16x32_bf16 v[40:43], v[160:163], v[68:71], v[40:43]\n"
  "v_mfma_f32_16x16x32_bf16 v[40:43], v[164:167], v[80:83], v[40:43]\n"
  "v_mfma_f32_16x16x32_bf16 v[40:43], v[164:167], v[84:87], v[40:43]\n"
  "s_waitcnt vmcnt(6)\n"
  "v_mfma_f32_16x16x32_bf16 v[56:59], v[160:163], v[96:99], v[56:59]\n"
  "v_mfma_f32_16x16x32_bf16 v[56:59], v[160:163], v[100:103], v[56:59]\n"
  "v_mfma_f32_16x16x32_bf16 v[56:59], v[164:167], v[112:115], v[56:59]\n"
  "v_mfma_f32_16x16x32_bf16 v[56:59], v[164:167], v[116:119], v[56:59]\n"
  "s_nop 0\n"
  "s_waitcnt vmcnt(4)\n"
  "buffer_atomic_add v248, v253, s[48:51], s79 offen sc1\n"
  "s_add_u32 s59, s11, 0x1dc000\n"
  "s_add_u32 m0, s12, 4096\n"
  "s_nop 0\n"
  "buffer_load_dwordx4 v184, s[64:67], s59 offen offset:0 lds\n"
  "v_pk_add_f32 v[192:193], v[8:9], v[24:25]\n"
  "v_pk_add_f32 v[194:195], v[10:11], v[26:27]\n"
  "v_pk_add_f32 v[196:197], v[40:41], v[56:57]\n"
  "v_pk_add_f32 v[198:199], v[42:43], v[58:59]\n"
  "v_pk_add_f32 v[192:193], v[192:193], v[196:197]\n"
  "v_pk_add_f32 v[194:195], v[194:195], v[198:199]\n"
  "v_cvt_pk_bf16_f32 v208, v8, v9\n"
  "v_cvt_pk_bf16_f32 v209, v10, v11\n"
  "v_cvt_pk_bf16_f32 v210, v24, v25\n"
  "v_cvt_pk_bf16_f32 v211, v26, v27\n"
  "v_cvt_pk_bf16_f32 v212, v40, v41\n"
  "v_cvt_pk_bf16_f32 v213, v42, v43\n"
  "v_cvt_pk_bf16_f32 v214, v56, v57\n"
  "v_cvt_pk_bf16_f32 v215, v58, v59\n"
  "v_add_f32_dpp v192, v192, v192 row_ror:8 row_mask:0xf bank_mask:0xf\n"
  "v_add_f32_dpp v193, v193, v193 row_ror:8 row_mask:0xf bank_mask:0xf\n"
  "buffer_load_dwordx4 v185, s[64:67], s59 offen offset:1024 lds\n"
  "v_add_f32_dpp v194, v194, v194 row_ror:8 row_mask:0xf bank_mask:0xf\n"
  "v_add_f32_dpp v195, v195, v195 row_ror:8 row_mask:0xf bank_mask:0xf\n"
  "v_add_f32_dpp v192, v192, v192 row_ror:4 row_mask:0xf bank_mask:0xf\n"
  "v_add_f32_dpp v193, v193, v193 row_ror:4 row_mask:0xf bank_mask:0xf\n"
  "v_add_f32_dpp v194, v194, v194 row_ror:4 row_mask:0xf bank_mask:0xf\n"
  "v_add_f32_dpp v195, v195, v195 row_ror:4 row_mask:0xf bank_mask:0xf\n"
  "v_add_f32_dpp v192, v192, v192 row_ror:2 row_mask:0xf bank_mask:0xf\n"
  "v_add_f32_dpp v193, v193, v193 row_ror:2 row_mask:0xf bank_mask:0xf\n"
  "v_add_f32_dpp v194, v194, v194 row_ror:2 row_mask:0xf bank_mask:0xf\n"
  "v_add_f32_dpp v195, v195, v195 row_ror:2 row_mask:0xf bank_mask:0xf\n"
  "v_add_f32_dpp v192, v192, v192 row_ror:1 row_mask:0xf bank_mask:0xf\n"
  "v_add_f32_dpp v193, v193, v193 row_ror:1 row_mask:0xf bank_mask:0xf\n"
  "v_add_f32_dpp v194, v194, v194 row_ror:1 row_mask:0xf bank_mask:0xf\n"
  "v_add_f32_dpp v195, v195, v195 row_ror:1 row_mask:0xf bank_mask:0xf\n"
  "v_mov_b32 v196, v192\n"
  "v_mov_b32 v197, v193\n"
  "buffer_load_dwordx4 v186, s[64:67], s59 offen offset:2048 lds\n"
  "v_mov_b32 v198, v194\n"
  "v_mov_b32 v199, v195\n"
  "v_permlane32_swap_b32 v192, v196\n"
  "v_permlane32_swap_b32 v193, v197\n"
  "v_permlane32_swap_b32 v194, v198\n"
  "v_permlane32_swap_b32 v195, v199\n"
  "v_pk_add_f32 v[192:193], v[192:193], v[196:197]\n"
  "v_pk_add_f32 v[194:195], v[194:195], v[198:199]\n"
  "v_mul_f32 v192, 0x3c000000, v192\n"
  "v_mul_f32 v193, 0x3c000000, v193\n"
  "v_mul_f32 v194, 0x3c000000, v194\n"
  "v_mul_f32 v195, 0x3c000000, v195\n"
  "v_pk_add_f32 v[8:9], v[8:9], v[192:193] neg_lo:[0,1] neg_hi:[0,1]\n"
  "v_pk_add_f32 v[10:11], v[10:11], v[194:195] neg_lo:[0,1] neg_hi:[0,1]\n"
  "v_pk_add_f32 v[24:25], v[24:25], v[192:193] neg_lo:[0,1] neg_hi:[0,1]\n"
  "v_pk_add_f32 v[26:27], v[26:27], v[194:195] neg_lo:[0,1] neg_hi:[0,1]\n"
  "buffer_load_dwordx4 v187, s[64:67], s59 offen offset:3072 lds\n"
  "v_pk_add_f32 v[40:41], v[40:41], v[192:193] neg_lo:[0,1] neg_hi:[0,1]\n"
  "v_pk_add_f32 v[42:43], v[42:43], v[194:195] neg_lo:[0,1] neg_hi:[0,1]\n"
  "v_pk_add_f32 v[56:57], v[56:57], v[192:193] neg_lo:[0,1] neg_hi:[0,1]\n"
  "v_pk_add_f32 v[58:59], v[58:59], v[194:195] neg_lo:[0,1] neg_hi:[0,1]\n"
  "v_pk_mul_f32 v[200:201], v[8:9], v[8:9]\n"
  "v_pk_mul_f32 v[202:203], v[10:11], v[10:11]\n"
  "v_pk_fma_f32 v[200:201], v[24:25], v[24:25], v[200:201]\n"
  "v_pk_fma_f32 v[202:203], v[26:27], v[26:27], v[202:203]\n"
  "v_pk_fma_f32 v[200:201], v[40:41], v[40:41], v[200:201]\n"
  "v_pk_fma_f32 v[202:203], v[42:43], v[42:43], v[202:203]\n"
  "v_pk_fma_f32 v[200:201], v[56:57], v[56:57], v[200:201]\n"
  "v_pk_fma_f32 v[202:203], v[58:59], v[58:59], v[202:203]\n"
  "s_nop 0\n"
  "v_add_f32_dpp v200, v200, v200 row_ror:8 row_mask:0xf bank_mask:0xf\n"
  "v_add_f32_dpp v201, v201, v201 row_ror:8 row_mask:0xf bank_mask:0xf\n"
  "v_add_f32_dpp v202, v202, v202 row_ror:8 row_mask:0xf bank_mask:0xf\n"
  "v_add_f32_dpp v203, v203, v203 row_ror:8 row_mask:0xf bank_mask:0xf\n"
  "s_add_u32 s59, s11, 0x1f8000\n"
  "s_add_u32 m0, s12, 8192\n"
  "s_nop 0\n"
  "buffer_load_dwordx4 v184, s[64:67], s59 offen offset:0 lds\n"
  "v_add_f32_dpp v200, v200, v200 row_ror:4 row_mask:0xf bank_mask:0xf\n"
  "v_add_f32_dpp v201, v201, v201 row_ror:4 row_mask:0xf bank_mask:0xf\n"
  "v_add_f32_dpp v202, v202, v202 row_ror:4 row_mask:0xf bank_mask:0xf\n"
  "v_add_f32_dpp v203, v203, v203 row_ror:4 row_mask:0xf bank_mask:0xf\n"
  "v_add_f32_dpp v200, v200, v200 row_ror:2 row_mask:0xf bank_mask:0xf\n"
  "v_add_f32_dpp v201, v201, v201 row_ror:2 row_mask:0xf bank_mask:0xf\n"
  "v_add_f32_dpp v202, v202, v202 row_ror:2 row_mask:0xf bank_mask:0xf\n"
  "v_add_f32_dpp v203, v203, v203 row_ror:2 row_mask:0xf bank_mask:0xf\n"
  "v_add_f32_dpp v200, v200, v200 row_ror:1 row_mask:0xf bank_mask:0xf\n"
  "v_add_f32_dpp v201, v201, v201 row_ror:1 row_mask:0xf bank_mask:0xf\n"
  "v_add_f32_dpp v202, v202, v202 row_ror:1 row_mask:0xf bank_mask:0xf\n"
  "v_add_f32_dpp v203, v203, v203 row_ror:1 row_mask:0xf bank_mask:0xf\n"
  "v_mov_b32 v196, v200\n"
  "v_mov_b32 v197, v201\n"
  "v_mov_b32 v198, v202\n"
  "v_mov_b32 v199, v203\n"
  "buffer_load_dwordx4 v185, s[64:67], s59 offen offset:1024 lds\n"
  "v_permlane32_swap_b32 v200, v196\n"
  "v_permlane32_swap_b32 v201, v197\n"
  "v_permlane32_swap_b32 v202, v198\n"
  "v_permlane32_swap_b32 v203, v199\n"
  "v_pk_add_f32 v[200:201], v[200:201], v[196:197]\n"
  "v_pk_add_f32 v[202:203], v[202:203], v[198:199]\n"
  "s_lshl_b32 s63, %1, 6\n"
  "v_add_u32 v196, s63, v237\n"
  "s_mov_b64 exec, 0x10001\n"
  "ds_write_b128 v196, v[192:195]\n"
  "ds_write_b128 v196, v[200:203] offset:32\n"
  "s_mov_b64 exec, -1\n"
  "s_lshr_b32 s56, s33, 4\n"
  "s_and_b32 s56, s56, 0x2000\n"
  "s_xor_b32 s56, s56, s84\n"
  "s_add_u32 s56, s56, 0x6200000\n"
  "s_add_u32 s55, s56, 0x1000\n"
  "v_permlane16_swap_b32 v208, v210\n"
  "buffer_load_dwordx4 v186, s[64:67], s59 offen offset:2048 lds\n"
  "v_permlane16_swap_b32 v209, v211\n"
  "v_permlane16_swap_b32 v212, v214\n"
  "v_permlane16_swap_b32 v213, v215\n"
  "s_nop 0\n"
  "s_waitcnt lgkmcnt(0)\n"
  "s_barrier\n"
  "ds_read_b128 v[128:131], v237 offset:0\n"
  "ds_read_b128 v[132:135], v237 offset:32\n"
  "ds_read_b128 v[136:139], v237 offset:64\n"
  "ds_read_b128 v[140:143], v237 offset:96\n"
  "ds_read_b128 v[144:147], v237 offset:128\n"
  "ds_read_b128 v[148:151], v237 offset:160\n"
  "ds_read_b128 v[152:155], v237 offset:192\n"
  "ds_read_b128 v[156:159], v237 offset:224\n"
  "buffer_load_dwordx4 v187, s[64:67], s59 offen offset:3072 lds\n"
  "buffer_store_dwordx4 v[208:211], v236, s[48:51], s56 offen sc1\n"
  "buffer_store_dwordx4 v[212:215], v236, s[48:51], s55 offen sc1\n"
  "s_lshr_b32 s56, s33, 3\n"
  "s_and_b32 s56, s56, 0x2000\n"
  "s_xor_b32 s56, s56, s84\n"
  "s_add_u32 s56, s56, 0x6000000\n"
  "s_add_u32 s55, s56, 0x1000\n"
  "s_waitcnt lgkmcnt(5)\n"
  "v_pk_add_f32 v[196:197], v[128:129], v[136:137]\n"
  "v_pk_add_f32 v[198:199], v[130:131], v[138:139]\n"
  "s_waitcnt lgkmcnt(1)\n"
  "v_pk_add_f32 v[200:201], v[144:145], v[152:153]\n"
  "v_pk_add_f32 v[202:203], v[146:147], v[154:155]\n"
  "v_pk_add_f32 v[196:197], v[196:197], v[200:201]\n"
  "v_pk_add_f32 v[198:199], v[198:199], v[202:203]\n"
  "v_mul_f32 v196, 0x3e800000, v196\n"
  "v_mul_f32 v197, 0x3e800000, v197\n"
  "v_mul_f32 v198, 0x3e800000, v198\n"
  "s_add_u32 s59, s11, 0x214000\n"
  "s_add_u32 m0, s12, 12288\n"
  "s_nop 0\n"
  "buffer_load_dwordx4 v184, s[64:67], s59 offen offset:0 lds\n"
  "v_mul_f32 v199, 0x3e800000, v199\n"
  "v_pk_add_f32 v[200:201], v[132:133], v[140:141]\n"
  "v_pk_add_f32 v[202:203], v[134:135], v[142:143]\n"
  "s_waitcnt lgkmcnt(0)\n"
  "v_pk_add_f32 v[204:205], v[148:149], v[156:157]\n"
  "v_pk_add_f32 v[206:207], v[150:151], v[158:159]\n"
  "v_pk_add_f32 v[200:201], v[200:201], v[204:205]\n"
  "v_pk_add_f32 v[202:203], v[202:203], v[206:207]\n"
  "v_pk_add_f32 v[128:129], v[128:129], v[196:197] neg_lo:[0,1] neg_hi:[0,1]\n"
  "v_pk_add_f32 v[130:131], v[130:131], v[198:199] neg_lo:[0,1] neg_hi:[0,1]\n"
  "v_pk_add_f32 v[136:137], v[136:137], v[196:197] neg_lo:[0,1] neg_hi:[0,1]\n"
  "v_pk_add_f32 v[138:139], v[138:139], v[198:199] neg_lo:[0,1] neg_hi:[0,1]\n"
  "v_pk_add_f32 v[144:145], v[144:145], v[196:197] neg_lo:[0,1] neg_hi:[0,1]\n"
  "v_pk_add_f32 v[146:147], v[146:147], v[198:199] neg_lo:[0,1] neg_hi:[0,1]\n"
  "v_pk_add_f32 v[152:153], v[152:153], v[196:197] neg_lo:[0,1] neg_hi:[0,1]\n"
  "v_pk_add_f32 v[154:155], v[154:155], v[198:199] neg_lo:[0,1] neg_hi:[0,1]\n"
  "v_pk_mul_f32 v[204:205], v[128:129], v[128:129]\n"
  "buffer_load_dwordx4 v185, s[64:67], s59 offen offset:1024 lds\n"
  "v_pk_mul_f32 v[206:207], v[130:131], v[130:131]\n"
  "v_pk_fma_f32 v[204:205], v[136:137], v[136:137], v[204:205]\n"
  "v_pk_fma_f32 v[206:207], v[138:139], v[138:139], v[206:207]\n"
  "v_pk_fma_f32 v[204:205], v[144:145], v[144:145], v[204:205]\n"
  "v_pk_fma_f32 v[206:207], v[146:147], v[146:147], v[206:207]\n"
  "v_pk_fma_f32 v[204:205], v[152:153], v[152:153], v[204:205]\n"
  "v_pk_fma_f32 v[206:207], v[154:155], v[154:155], v[206:207]\n"
  "v_pk_add_f32 v[192:193], v[192:193], v[196:197] neg_lo:[0,1] neg_hi:[0,1]\n"
  "v_pk_add_f32 v[194:195], v[194:195], v[198:199] neg_lo:[0,1] neg_hi:[0,1]\n"
  "v_fmac_f32 v200, 0x43000000, v204\n"
  "v_fmac_f32 v201, 0x43000000, v205\n"
  "v_fmac_f32 v202, 0x43000000, v206\n"
  "v_fmac_f32 v203, 0x43000000, v207\n"
  "v_mul_f32 v200, 0x3b000000, v200\n"
  "v_mul_f32 v201, 0x3b000000, v201\n"
  "v_mul_f32 v202, 0x3b000000, v202\n"
  "buffer_load_dwordx4 v186, s[64:67], s59 offen offset:2048 lds\n"
  "v_mul_f32 v203, 0x3b000000, v203\n"
  "v_add_f32 v196, 0x3727c5ac, v200\n"
  "v_add_f32 v197, 0x3727c5ac, v201\n"
  "v_add_f32 v198, 0x3727c5ac, v202\n"
  "v_add_f32 v199, 0x3727c5ac, v203\n"
  "v_rsq_f32 v196, v196\n"
  "v_rsq_f32 v197, v197\n"
  "v_rsq_f32 v198, v198\n"
  "v_rsq_f32 v199, v199\n"
  "v_pk_add_f32 v[8:9], v[8:9], v[192:193]\n"
  "v_pk_add_f32 v[10:11], v[10:11], v[194:195]\n"
  "v_pk_add_f32 v[24:25], v[24:25], v[192:193]\n"
  "v_pk_add_f32 v[26:27], v[26:27], v[194:195]\n"
  "v_pk_add_f32 v[40:41], v[40:41], v[192:193]\n"
  "v_pk_add_f32 v[42:43], v[42:43], v[194:195]\n"
  "v_pk_add_f32 v[56:57], v[56:57], v[192:193]\n"
  "buffer_load_dwordx4 v187, s[64:67], s59 offen offset:3072 lds\n"
  "v_pk_add_f32 v[58:59], v[58:59], v[194:195]\n"
  "v_pk_mul_f32 v[200:201], v[200:201], v[196:197]\n"
  "v_pk_mul_f32 v[202:203], v[202:203], v[198:199]\n"
  "v_pk_mul_f32 v[200:201], v[200:201], v[196:197]\n"
  "v_pk_mul_f32 v[202:203], v[202:203], v[198:199]\n"
  "v_add_f32 v200, 0x3727c5ac, v200\n"
  "v_add_f32 v201, 0x3727c5ac, v201\n"
  "v_add_f32 v202, 0x3727c5ac, v202\n"
  "v_add_f32 v203, 0x3727c5ac, v203\n"
  "v_rsq_f32 v200, v200\n"
  "v_rsq_f32 v201, v201\n"
  "v_rsq_f32 v202, v202\n"
  "v_rsq_f32 v203, v203\n"
  "v_pk_mul_f32 v[196:197], v[196:197], v[200:201]\n"
  "v_pk_mul_f32 v[198:199], v[198:199], v[202:203]\n"
  "v_pk_mul_f32 v[8:9], v[8:9], v[196:197]\n"
  "v_pk_mul_f32 v[10:11], v[10:11], v[198:199]\n"
  "v_pk_mul_f32 v[24:25], v[24:25], v[196:197]\n"
  "v_pk_mul_f32 v[26:27], v[26:27], v[198:199]\n"
  "v_pk_mul_f32 v[40:41], v[40:41], v[196:197]\n"
  "v_pk_mul_f32 v[42:43], v[42:43], v[198:199]\n"
  "v_pk_mul_f32 v[56:57], v[56:57], v[196:197]\n"
  "v_pk_mul_f32 v[58:59], v[58:59], v[198:199]\n"
  "v_cvt_pk_bf16_f32 v216, v8, v9\n"
  "v_cvt_pk_bf16_f32 v217, v10, v11\n"
  "v_cvt_pk_bf16_f32 v218, v24, v25\n"
  "v_cvt_pk_bf16_f32 v219, v26, v27\n"
  "v_cvt_pk_bf16_f32 v220, v40, v41\n"
  "v_cvt_pk_bf16_f32 v221, v42, v43\n"
  "v_cvt_pk_bf16_f32 v222, v56, v57\n"
  "v_cvt_pk_bf16_f32 v223, v58, v59\n"
  "v_permlane16_swap_b32 v216, v218\n"
  "v_permlane16_swap_b32 v217, v219\n"
  "v_permlane16_swap_b32 v220, v222\n"
  "v_permlane16_swap_b32 v221, v223\n"
  "buffer_store_dwordx4 v[216:219], v236, s[48:51], s56 offen sc1\n"
  "buffer_store_dwordx4 v[220:223], v236, s[48:51], s55 offen sc1\n"
  "s_nop 0\n"
  "s_waitcnt vmcnt(0)\n"
  "buffer_atomic_add v248, v228, s[48:51], s85 offen sc1\n"
  "s_mov_b32 s85, 0x7fff0000\n"
  "ds_read_b64_tr_b16 a[0:1], v188 offset:0\n"
  "ds_read_b64_tr_b16 a[2:3], v188 offset:8192\n"
  "ds_read_b64_tr_b16 a[4:5], v189 offset:0\n"
  "ds_read_b64_tr_b16 a[6:7], v189 offset:8192\n"
  "ds_read_b64_tr_b16 a[8:9], v190 offset:0\n"
  "ds_read_b64_tr_b16 a[10:11], v190 offset:8192\n"
  "ds_read_b64_tr_b16 a[12:13], v191 offset:0\n"
  "ds_read_b64_tr_b16 a[14:15], v191 offset:8192\n"
  "ds_read_b64_tr_b16 a[16:17], v188 offset:2048\n"
  "ds_read_b64_tr_b16 a[18:19], v188 offset:10240\n"
  "ds_read_b64_tr_b16 a[20:21], v189 offset:2048\n"
  "ds_read_b64_tr_b16 a[22:23], v189 offset:10240\n"
  "ds_read_b64_tr_b16 a[24:25], v190 offset:2048\n"
  "ds_read_b64_tr_b16 a[26:27], v190 offset:10240\n"
  "ds_read_b64_tr_b16 a[28:29], v191 offset:2048\n"
  "ds_read_b64_tr_b16 a[30:31], v191 offset:10240\n"
  "ds_read_b64_tr_b16 a[32:33], v188 offset:4096\n"
  "ds_read_b64_tr_b16 a[34:35], v188 offset:12288\n"
  "ds_read_b64_tr_b16 a[36:37], v189 offset:4096\n"
  "ds_read_b64_tr_b16 a[38:39], v189 offset:12288\n"
  "ds_read_b64_tr_b16 a[40:41], v190 offset:4096\n"
  "ds_read_b64_tr_b16 a[42:43], v190 offset:12288\n"
  "ds_read_b64_tr_b16 a[44:45], v191 offset:4096\n"
  "ds_read_b64_tr_b16 a[46:47], v191 offset:12288\n"
  "ds_read_b64_tr_b16 a[48:49], v188 offset:6144\n"
  "ds_read_b64_tr_b16 a[50:51], v188 offset:14336\n"
  "ds_read_b64_tr_b16 a[52:53], v189 offset:6144\n"
  "ds_read_b64_tr_b16 a[54:55], v189 offset:14336\n"
  "ds_read_b64_tr_b16 a[56:57], v190 offset:6144\n"
  "ds_read_b64_tr_b16 a[58:59], v190 offset:14336\n"
  "ds_read_b64_tr_b16 a[60:61], v191 offset:6144\n"
  "ds_read_b64_tr_b16 a[62:63], v191 offset:14336\n"
  "s_add_u32 s59, s89, 0x380000\n"
  "s_add_u32 m0, s13, 0\n"
  "s_nop 0\n"
  "s_waitcnt vmcnt(8)\n"
  "buffer_load_dwordx4 v184, s[64:67], s59 offen offset:0 lds\n"
  "s_waitcnt vmcnt(8)\n"
  "buffer_load_dwordx4 v185, s[64:67], s59 offen offset:1024 lds\n"
  "s_waitcnt vmcnt(8)\n"
  "buffer_load_dwordx4 v186, s[64:67], s59 offen offset:2048 lds\n"
  "s_waitcnt vmcnt(8)\n"
  "buffer_load_dwordx4 v187, s[64:67], s59 offen offset:3072 lds\n"
  "s_add_u32 s59, s89, 0x39c000\n"
  "s_add_u32 m0, s13, 4096\n"
  "s_nop 0\n"
  "s_waitcnt vmcnt(8)\n"
  "buffer_load_dwordx4 v184, s[64:67], s59 offen offset:0 lds\n"
  "s_waitcnt vmcnt(8)\n"
  "buffer_load_dwordx4 v185, s[64:67], s59 offen offset:1024 lds\n"
  "s_waitcnt vmcnt(8)\n"
  "buffer_load_dwordx4 v186, s[64:67], s59 offen offset:2048 lds\n"
  "s_waitcnt vmcnt(8)\n"
  "buffer_load_dwordx4 v187, s[64:67], s59 offen offset:3072 lds\n"
  "s_add_u32 s59, s89, 0x3b8000\n"
  "s_add_u32 m0, s13, 8192\n"
  "s_nop 0\n"
  "s_waitcnt vmcnt(8)\n"
  "buffer_load_dwordx4 v184, s[64:67], s59 offen offset:0 lds\n"
  "s_waitcnt vmcnt(8)\n"
  "buffer_load_dwordx4 v185, s[64:67], s59 offen offset:1024 lds\n"
  "s_waitcnt vmcnt(8)\n"
  "buffer_load_dwordx4 v186, s[64:67], s59 offen offset:2048 lds\n"
  "s_waitcnt vmcnt(8)\n"
  "buffer_load_dwordx4 v187, s[64:67], s59 offen offset:3072 lds\n"
  "s_add_u32 s59, s89, 0x3d4000\n"
  "s_add_u32 m0, s13, 12288\n"
  "s_nop 0\n"
  "s_waitcnt vmcnt(8)\n"
  "buffer_load_dwordx4 v184, s[64:67], s59 offen offset:0 lds\n"
  "s_waitcnt vmcnt(8)\n"
  "buffer_load_dwordx4 v185, s[64:67], s59 offen offset:1024 lds\n"
  "s_waitcnt vmcnt(8)\n"
  "buffer_load_dwordx4 v186, s[64:67], s59 offen offset:2048 lds\n"
  "s_waitcnt vmcnt(8)\n"
  "buffer_load_dwordx4 v187, s[64:67], s59 offen offset:3072 lds\n"
  "s_waitcnt lgkmcnt(0)\n"
  "s_add_u32 s59, s11, 0x540000\n"
  "s_add_u32 m0, s12, 0\n"
  "s_nop 0\n"
  "s_waitcnt vmcnt(8)\n"
  "buffer_load_dwordx4 v184, s[64:67], s59 offen offset:0 lds\n"
  "s_waitcnt vmcnt(8)\n"
  "buffer_load_dwordx4 v185, s[64:67], s59 offen offset:1024 lds\n"
  "buffer_load_dwordx2 v[160:161], v243, s[44:47], s62 offen offset:0\n"
  "buffer_load_dwordx2 v[162:163], v243, s[44:47], s62 offen offset:32\n"
  "buffer_load_dwordx2 v[164:165], v243, s[44:47], s62 offen offset:64\n"
  "buffer_load_dwordx2 v[166:167], v243, s[44:47], s62 offen offset:96\n"
  "v_mov_b32 v233, 0\n"
  "buffer_atomic_add v233, v229, s[48:51], s86 offen sc0 sc1\n"
  "s_waitcnt vmcnt(0)\n"
  "v_cmp_ne_u32 vcc, 16, v233\n"
  "s_and_b32 vcc_lo, vcc_lo, 0x3\n"
  "s_cbranch_scc1 mk_spin5c\n"
  "mk_spin5c_b:\n"
  "s_add_u32 s61, s88, 0x0\n"
  "buffer_load_dwordx4 a[128:131], v249, s[48:51], s61 offen sc1\n"
  "buffer_load_dwordx4 a[132:135], v250, s[48:51], s61 offen sc1\n"
  "buffer_load_dwordx4 a[136:139], v251, s[48:51], s61 offen sc1\n"
  "buffer_load_dwordx4 a[140:143], v252, s[48:51], s61 offen sc1\n"
  "s_waitcnt vmcnt(0)\n"
  "s_branch mk_join\n"
  "mk_ownB:\n"
  "mk_tp16:\n"
  "s_cbranch_execz mk_tp20\n"
  "s_waitcnt vmcnt(4)\n"
  "buffer_atomic_add v248, v253, s[48:51], s79 offen sc1\n"
  "s_nop 0\n"
  "s_add_u32 s59, s11, 0x1dc000\n"
  "s_add_u32 m0, s12, 4096\n"
  "s_nop 0\n"
  "s_waitcnt vmcnt(8)\n"
  "buffer_load_dwordx4 v184, s[64:67], s59 offen offset:0 lds\n"
  "s_waitcnt vmcnt(8)\n"
  "buffer_load_dwordx4 v185, s[64:67], s59 offen offset:1024 lds\n"
  "s_waitcnt vmcnt(8)\n"
  "buffer_load_dwordx4 v186, s[64:67], s59 offen offset:2048 lds\n"
  "s_waitcnt vmcnt(8)\n"
  "buffer_load_dwordx4 v187, s[64:67], s59 offen offset:3072 lds\n"
  "s_add_u32 s59, s11, 0x1f8000\n"
  "s_add_u32 m0, s12, 8192\n"
  "s_nop 0\n"
  "s_waitcnt vmcnt(8)\n"
  "buffer_load_dwordx4 v184, s[64:67], s59 offen offset:0 lds\n"
  "s_waitcnt vmcnt(8)\n"
  "buffer_load_dwordx4 v185, s[64:67], s59 offen offset:1024 lds\n"
  "s_waitcnt vmcnt(8)\n"
  "buffer_load_dwordx4 v186, s[64:67], s59 offen offset:2048 lds\n"
  "s_waitcnt vmcnt(8)\n"
  "buffer_load_dwordx4 v187, s[64:67], s59 offen offset:3072 lds\n"
  "s_add_u32 s59, s11, 0x214000\n"
  "s_add_u32 m0, s12, 12288\n"
  "s_nop 0\n"
  "s_waitcnt vmcnt(8)\n"
  "buffer_load_dwordx4 v184, s[64:67], s59 offen offset:0 lds\n"
  "s_waitcnt vmcnt(8)\n"
  "buffer_load_dwordx4 v185, s[64:67], s59 offen offset:1024 lds\n"
  "s_waitcnt vmcnt(8)\n"
  "buffer_load_dwordx4 v186, s[64:67], s59 offen offset:2048 lds\n"
  "s_waitcnt vmcnt(8)\n"
  "buffer_load_dwordx4 v187, s[64:67], s59 offen offset:3072 lds\n"
  "v_mov_b32 v233, 0\n"
  "buffer_atomic_add v233, v253, s[48:51], s80 offen sc0 sc1\n"
  "s_add_u32 s59, s89, 0x380000\n"
  "s_add_u32 m0, s13, 0\n"
  "s_nop 0\n"
  "s_waitcnt vmcnt(8)\n"
  "buffer_load_dwordx4 v184, s[64:67], s59 offen offset:0 lds\n"
  "s_waitcnt vmcnt(8)\n"
  "buffer_load_dwordx4 v185, s[64:67], s59 offen offset:1024 lds\n"
  "s_waitcnt vmcnt(8)\n"
  "buffer_load_dwordx4 v186, s[64:67], s59 offen offset:2048 lds\n"
  "s_waitcnt vmcnt(8)\n"
  "buffer_load_dwordx4 v187, s[64:67], s59 offen offset:3072 lds\n"
  "s_waitcnt vmcnt(4)\n"
  "v_cmp_ne_u32 vcc, 8, v233\n"
  "s_and_b32 vcc_lo, vcc_lo, 0x1\n"
  "s_cbranch_scc1 mk_spin4b\n"
  "mk_spin4b_b:\n"
  "s_nop 0\n"
  "buffer_load_dwordx4 v[0:3], v235, s[48:51], s81 offen offset:0 sc1\n"
  "buffer_load_dwordx4 v[4:7], v235, s[48:51], s82 offen offset:0 sc1\n"
  "buffer_load_dwordx4 v[16:19], v235, s[48:51], s81 offen offset:256 sc1\n"
  "buffer_load_dwordx4 v[20:23], v235, s[48:51], s82 offen offset:256 sc1\n"
  "buffer_load_dwordx4 v[32:35], v235, s[48:51], s81 offen offset:512 sc1\n"
  "buffer_load_dwordx4 v[36:39], v235, s[48:51], s82 offen offset:512 sc1\n"
  "buffer_load_dwordx4 v[48:51], v235, s[48:51], s81 offen offset:768 sc1\n"
  "buffer_load_dwordx4 v[52:55], v235, s[48:51], s82 offen offset:768 sc1\n"
  "buffer_load_dwordx4 v[64:67], v235, s[48:51], s81 offen offset:1024 sc1\n"
  "buffer_load_dwordx4 v[68:71], v235, s[48:51], s82 offen offset:1024 sc1\n"
  "buffer_load_dwordx4 v[80:83], v235, s[48:51], s81 offen offset:1280 sc1\n"
  "buffer_load_dwordx4 v[84:87], v235, s[48:51], s82 offen offset:1280 sc1\n"
  "buffer_load_dwordx4 v[96:99], v235, s[48:51], s81 offen offset:1536 sc1\n"
  "buffer_load_dwordx4 v[100:103], v235, s[48:51], s82 offen offset:1536 sc1\n"
  "buffer_load_dwordx4 v[112:115], v235, s[48:51], s81 offen offset:1792 sc1\n"
  "buffer_load_dwordx4 v[116:119], v235, s[48:51], s82 offen offset:1792 sc1\n"
  "ds_read_b64_tr_b16 a[0:1], v188 offset:0\n"
  "ds_read_b64_tr_b16 a[2:3], v188 offset:8192\n"
  "ds_read_b64_tr_b16 a[4:5], v189 offset:0\n"
  "ds_read_b64_tr_b16 a[6:7], v189 offset:8192\n"
  "ds_read_b64_tr_b16 a[8:9], v190 offset:0\n"
  "ds_read_b64_tr_b16 a[10:11], v190 offset:8192\n"
  "ds_read_b64_tr_b16 a[12:13], v191 offset:0\n"
  "ds_read_b64_tr_b16 a[14:15], v191 offset:8192\n"
  "ds_read_b64_tr_b16 a[16:17], v188 offset:2048\n"
  "ds_read_b64_tr_b16 a[18:19], v188 offset:10240\n"
  "ds_read_b64_tr_b16 a[20:21], v189 offset:2048\n"
  "ds_read_b64_tr_b16 a[22:23], v189 offset:10240\n"
  "ds_read_b64_tr_b16 a[24:25], v190 offset:2048\n"
  "ds_read_b64_tr_b16 a[26:27], v190 offset:10240\n"
  "ds_read_b64_tr_b16 a[28:29], v191 offset:2048\n"
  "ds_read_b64_tr_b16 a[30:31], v191 offset:10240\n"
  "ds_read_b64_tr_b16 a[32:33], v188 offset:4096\n"
  "ds_read_b64_tr_b16 a[34:35], v188 offset:12288\n"
  "ds_read_b64_tr_b16 a[36:37], v189 offset:4096\n"
  "ds_read_b64_tr_b16 a[38:39], v189 offset:12288\n"
  "ds_read_b64_tr_b16 a[40:41], v190 offset:4096\n"
  "ds_read_b64_tr_b16 a[42:43], v190 offset:12288\n"
  "ds_read_b64_tr_b16 a[44:45], v191 offset:4096\n"
  "ds_read_b64_tr_b16 a[46:47], v191 offset:12288\n"
  "ds_read_b64_tr_b16 a[48:49], v188 offset:6144\n"
  "ds_read_b64_tr_b16 a[50:51], v188 offset:14336\n"
  "ds_read_b64_tr_b16 a[52:53], v189 offset:6144\n"
  "ds_read_b64_tr_b16 a[54:55], v189 offset:14336\n"
  "ds_read_b64_tr_b16 a[56:57], v190 offset:6144\n"
  "ds_read_b64_tr_b16 a[58:59], v190 offset:14336\n"
  "ds_read_b64_tr_b16 a[60:61], v191 offset:6144\n"
  "ds_read_b64_tr_b16 a[62:63], v191 offset:14336\n"
  "s_waitcnt lgkmcnt(0)\n"
  "s_add_u32 s59, s89, 0x39c000\n"
  "s_add_u32 m0, s13, 4096\n"
  "s_nop 0\n"
  "buffer_load_dwordx4 v184, s[64:67], s59 offen offset:0 lds\n"
  "buffer_load_dwordx4 v185, s[64:67], s59 offen offset:1024 lds\n"
  "buffer_load_dwordx4 v186, s[64:67], s59 offen offset:2048 lds\n"
  "buffer_load_dwordx4 v187, s[64:67], s59 offen offset:3072 lds\n"
  "v_mfma_f32_16x16x32_bf16 v[8:11], v[176:179], v[168:171], 0\n"
  "v_mfma_f32_16x16x32_bf16 v[24:27], v[176:179], v[172:175], 0\n"
  "v_mfma_f32_16x16x32_bf16 v[40:43], v[180:183], v[168:171], 0\n"
  "v_mfma_f32_16x16x32_bf16 v[56:59], v[180:183], v[172:175], 0\n"
  "s_waitcnt vmcnt(16)\n"
  "v_mfma_f32_16x16x32_bf16 v[8:11], v[160:163], v[0:3], v[8:11]\n"
  "v_mfma_f32_16x16x32_bf16 v[8:11], v[160:163], v[4:7], v[8:11]\n"
  "v_mfma_f32_16x16x32_bf16 v[8:11], v[164:167], v[16:19], v[8:11]\n"
  "v_mfma_f32_16x16x32_bf16 v[8:11], v[164:167], v[20:23], v[8:11]\n"
  "s_waitcnt vmcnt(12)\n"
  "v_mfma_f32_16x16x32_bf16 v[24:27], v[160:163], v[32:35], v[24:27]\n"
  "v_mfma_f32_16x16x32_bf16 v[24:27], v[160:163], v[36:39], v[24:27]\n"
  "v_mfma_f32_16x16x32_bf16 v[24:27], v[164:167], v[48:51], v[24:27]\n"
  "v_mfma_f32_16x16x32_bf16 v[24:27], v[164:167], v[52:55], v[24:27]\n"
  "s_waitcnt vmcnt(8)\n"
  "v_mfma_f32_16x16x32_bf16 v[40:43], v[160:163], v[64:67], v[40:43]\n"
  "v_mfma_f32_16x16x32_bf16 v[40:43], v[160:163], v[68:71], v[40:43]\n"
  "v_mfma_f32_16x16x32_bf16 v[40:43], v[164:167], v[80:83], v[40:43]\n"
  "v_mfma_f32_16x16x32_bf16 v[40:43], v[164:167], v[84:87], v[40:43]\n"
  "s_waitcnt vmcnt(4)\n"
  "v_mfma_f32_16x16x32_bf16 v[56:59], v[160:163], v[96:99], v[56:59]\n"
  "v_mfma_f32_16x16x32_bf16 v[56:59], v[160:163], v[100:103], v[56:59]\n"
  "v_mfma_f32_16x16x32_bf16 v[56:59], v[164:167], v[112:115], v[56:59]\n"
  "v_mfma_f32_16x16x32_bf16 v[56:59], v[164:167], v[116:119], v[56:59]\n"
  "s_nop 0\n"
  "buffer_load_dwordx2 v[160:161], v243, s[44:47], s62 offen offset:0\n"
  "buffer_load_dwordx2 v[162:163], v243, s[44:47], s62 offen offset:32\n"
  "buffer_load_dwordx2 v[164:165], v243, s[44:47], s62 offen offset:64\n"
  "buffer_load_dwordx2 v[166:167], v243, s[44:47], s62 offen offset:96\n"
  "s_add_u32 s59, s89, 0x3b8000\n"
  "s_add_u32 m0, s13, 8192\n"
  "s_nop 0\n"
  "buffer_load_dwordx4 v184, s[64:67], s59 offen offset:0 lds\n"
  "v_pk_add_f32 v[192:193], v[8:9], v[24:25]\n"
  "v_pk_add_f32 v[194:195], v[10:11], v[26:27]\n"
  "v_pk_add_f32 v[196:197], v[40:41], v[56:57]\n"
  "v_pk_add_f32 v[198:199], v[42:43], v[58:59]\n"
  "v_pk_add_f32 v[192:193], v[192:193], v[196:197]\n"
  "v_pk_add_f32 v[194:195], v[194:195], v[198:199]\n"
  "v_cvt_pk_bf16_f32 v208, v8, v9\n"
  "v_cvt_pk_bf16_f32 v209, v10, v11\n"
  "v_cvt_pk_bf16_f32 v210, v24, v25\n"
  "v_cvt_pk_bf16_f32 v211, v26, v27\n"
  "v_cvt_pk_bf16_f32 v212, v40, v41\n"
  "v_cvt_pk_bf16_f32 v213, v42, v43\n"
  "v_cvt_pk_bf16_f32 v214, v56, v57\n"
  "v_cvt_pk_bf16_f32 v215, v58, v59\n"
  "v_add_f32_dpp v192, v192, v192 row_ror:8 row_mask:0xf bank_mask:0xf\n"
  "v_add_f32_dpp v193, v193, v193 row_ror:8 row_mask:0xf bank_mask:0xf\n"
  "buffer_load_dwordx4 v185, s[64:67], s59 offen offset:1024 lds\n"
  "v_add_f32_dpp v194, v194, v194 row_ror:8 row_mask:0xf bank_mask:0xf\n"
  "v_add_f32_dpp v195, v195, v195 row_ror:8 row_mask:0xf bank_mask:0xf\n"
  "v_add_f32_dpp v192, v192, v192 row_ror:4 row_mask:0xf bank_mask:0xf\n"
  "v_add_f32_dpp v193, v193, v193 row_ror:4 row_mask:0xf bank_mask:0xf\n"
  "v_add_f32_dpp v194, v194, v194 row_ror:4 row_mask:0xf bank_mask:0xf\n"
  "v_add_f32_dpp v195, v195, v195 row_ror:4 row_mask:0xf bank_mask:0xf\n"
  "v_add_f32_dpp v192, v192, v192 row_ror:2 row_mask:0xf bank_mask:0xf\n"
  "v_add_f32_dpp v193, v193, v193 row_ror:2 row_mask:0xf bank_mask:0xf\n"
  "v_add_f32_dpp v194, v194, v194 row_ror:2 row_mask:0xf bank_mask:0xf\n"
  "v_add_f32_dpp v195, v195, v195 row_ror:2 row_mask:0xf bank_mask:0xf\n"
  "v_add_f32_dpp v192, v192, v192 row_ror:1 row_mask:0xf bank_mask:0xf\n"
  "v_add_f32_dpp v193, v193, v193 row_ror:1 row_mask:0xf bank_mask:0xf\n"
  "v_add_f32_dpp v194, v194, v194 row_ror:1 row_mask:0xf bank_mask:0xf\n"
  "v_add_f32_dpp v195, v195, v195 row_ror:1 row_mask:0xf bank_mask:0xf\n"
  "v_mov_b32 v196, v192\n"
  "mk_tp17:\n"
  "s_cbranch_execz mk_tp21\n"
  "v_mov_b32 v197, v193\n"
  "buffer_load_dwordx4 v186, s[64:67], s59 offen offset:2048 lds\n"
  "v_mov_b32 v198, v194\n"
  "v_mov_b32 v199, v195\n"
  "v_permlane32_swap_b32 v192, v196\n"
  "v_permlane32_swap_b32 v193, v197\n"
  "v_permlane32_swap_b32 v194, v198\n"
  "v_permlane32_swap_b32 v195, v199\n"
  "v_pk_add_f32 v[192:193], v[192:193], v[196:197]\n"
  "v_pk_add_f32 v[194:195], v[194:195], v[198:199]\n"
  "v_mul_f32 v192, 0x3c000000, v192\n"
  "v_mul_f32 v193, 0x3c000000, v193\n"
  "v_mul_f32 v194, 0x3c000000, v194\n"
  "v_mul_f32 v195, 0x3c000000, v195\n"
  "v_pk_add_f32 v[8:9], v[8:9], v[192:193] neg_lo:[0,1] neg_hi:[0,1]\n"
  "v_pk_add_f32 v[10:11], v[10:11], v[194:195] neg_lo:[0,1] neg_hi:[0,1]\n"
  "v_pk_add_f32 v[24:25], v[24:25], v[192:193] neg_lo:[0,1] neg_hi:[0,1]\n"
  "v_pk_add_f32 v[26:27], v[26:27], v[194:195] neg_lo:[0,1] neg_hi:[0,1]\n"
  "buffer_load_dwordx4 v187, s[64:67], s59 offen offset:3072 lds\n"
  "v_pk_add_f32 v[40:41], v[40:41], v[192:193] neg_lo:[0,1] neg_hi:[0,1]\n"
  "v_pk_add_f32 v[42:43], v[42:43], v[194:195] neg_lo:[0,1] neg_hi:[0,1]\n"
  "v_pk_add_f32 v[56:57], v[56:57], v[192:193] neg_lo:[0,1] neg_hi:[0,1]\n"
  "v_pk_add_f32 v[58:59], v[58:59], v[194:195] neg_lo:[0,1] neg_hi:[0,1]\n"
  "v_pk_mul_f32 v[200:201], v[8:9], v[8:9]\n"
  "v_pk_mul_f32 v[202:203], v[10:11], v[10:11]\n"
  "v_pk_fma_f32 v[200:201], v[24:25], v[24:25], v[200:201]\n"
  "v_pk_fma_f32 v[202:203], v[26:27], v[26:27], v[202:203]\n"
  "v_pk_fma_f32 v[200:201], v[40:41], v[40:41], v[200:201]\n"
  "v_pk_fma_f32 v[202:203], v[42:43], v[42:43], v[202:203]\n"
  "v_pk_fma_f32 v[200:201], v[56:57], v[56:57], v[200:201]\n"
  "v_pk_fma_f32 v[202:203], v[58:59], v[58:59], v[202:203]\n"
  "s_nop 0\n"
  "v_add_f32_dpp v200, v200, v200 row_ror:8 row_mask:0xf bank_mask:0xf\n"
  "v_add_f32_dpp v201, v201, v201 row_ror:8 row_mask:0xf bank_mask:0xf\n"
  "v_add_f32_dpp v202, v202, v202 row_ror:8 row_mask:0xf bank_mask:0xf\n"
  "v_add_f32_dpp v203, v203, v203 row_ror:8 row_mask:0xf bank_mask:0xf\n"
  "s_add_u32 s59, s89, 0x3d4000\n"
  "s_add_u32 m0, s13, 12288\n"
  "s_nop 0\n"
  "buffer_load_dwordx4 v184, s[64:67], s59 offen offset:0 lds\n"
  "v_add_f32_dpp v200, v200, v200 row_ror:4 row_mask:0xf bank_mask:0xf\n"
  "v_add_f32_dpp v201, v201, v201 row_ror:4 row_mask:0xf bank_mask:0xf\n"
  "v_add_f32_dpp v202, v202, v202 row_ror:4 row_mask:0xf bank_mask:0xf\n"
  "v_add_f32_dpp v203, v203, v203 row_ror:4 row_mask:0xf bank_mask:0xf\n"
  "v_add_f32_dpp v200, v200, v200 row_ror:2 row_mask:0xf bank_mask:0xf\n"
  "v_add_f32_dpp v201, v201, v201 row_ror:2 row_mask:0xf bank_mask:0xf\n"
  "v_add_f32_dpp v202, v202, v202 row_ror:2 row_mask:0xf bank_mask:0xf\n"
  "v_add_f32_dpp v203, v203, v203 row_ror:2 row_mask:0xf bank_mask:0xf\n"
  "v_add_f32_dpp v200, v200, v200 row_ror:1 row_mask:0xf bank_mask:0xf\n"
  "v_add_f32_dpp v201, v201, v201 row_ror:1 row_mask:0xf bank_mask:0xf\n"
  "v_add_f32_dpp v202, v202, v202 row_ror:1 row_mask:0xf bank_mask:0xf\n"
  "v_add_f32_dpp v203, v203, v203 row_ror:1 row_mask:0xf bank_mask:0xf\n"
  "v_mov_b32 v196, v200\n"
  "v_mov_b32 v197, v201\n"
  "v_mov_b32 v198, v202\n"
  "v_mov_b32 v199, v203\n"
  "buffer_load_dwordx4 v185, s[64:67], s59 offen offset:1024 lds\n"
  "v_permlane32_swap_b32 v200, v196\n"
  "v_permlane32_swap_b32 v201, v197\n"
  "v_permlane32_swap_b32 v202, v198\n"
  "v_permlane32_swap_b32 v203, v199\n"
  "v_pk_add_f32 v[200:201], v[200:201], v[196:197]\n"
  "v_pk_add_f32 v[202:203], v[202:203], v[198:199]\n"
  "s_lshl_b32 s63, %1, 6\n"
  "v_add_u32 v196, s63, v237\n"
  "s_mov_b64 exec, 0x10001\n"
  "ds_write_b128 v196, v[192:195]\n"
  "ds_write_b128 v196, v[200:203] offset:32\n"
  "s_mov_b64 exec, -1\n"
  "s_lshr_b32 s56, s33, 4\n"
  "s_and_b32 s56, s56, 0x2000\n"
  "s_xor_b32 s56, s56, s84\n"
  "s_add_u32 s56, s56, 0x6200000\n"
  "s_add_u32 s55, s56, 0x1000\n"
  "v_permlane16_swap_b32 v208, v210\n"
  "buffer_load_dwordx4 v186, s[64:67], s59 offen offset:2048 lds\n"
  "v_permlane16_swap_b32 v209, v211\n"
  "v_permlane16_swap_b32 v212, v214\n"
  "v_permlane16_swap_b32 v213, v215\n"
  "s_nop 0\n"
  "s_waitcnt lgkmcnt(0)\n"
  "s_barrier\n"
  "ds_read_b128 v[128:131], v237 offset:0\n"
  "ds_read_b128 v[132:135], v237 offset:32\n"
  "ds_read_b128 v[136:139], v237 offset:64\n"
  "ds_read_b128 v[140:143], v237 offset:96\n"
  "ds_read_b128 v[144:147], v237 offset:128\n"
  "ds_read_b128 v[148:151], v237 offset:160\n"
  "ds_read_b128 v[152:155], v237 offset:192\n"
  "ds_read_b128 v[156:159], v237 offset:224\n"
  "buffer_load_dwordx4 v187, s[64:67], s59 offen offset:3072 lds\n"
  "buffer_store_dwordx4 v[208:211], v236, s[48:51], s56 offen sc1\n"
  "buffer_store_dwordx4 v[212:215], v236, s[48:51], s55 offen sc1\n"
  "s_lshr_b32 s56, s33, 3\n"
  "s_and_b32 s56, s56, 0x2000\n"
  "s_xor_b32 s56, s56, s84\n"
  "s_add_u32 s56, s56, 0x6000000\n"
  "s_add_u32 s55, s56, 0x1000\n"
  "s_waitcnt lgkmcnt(5)\n"
  "v_pk_add_f32 v[196:197], v[128:129], v[136:137]\n"
  "v_pk_add_f32 v[198:199], v[130:131], v[138:139]\n"
  "s_waitcnt lgkmcnt(1)\n"
  "v_pk_add_f32 v[200:201], v[144:145], v[152:153]\n"
  "v_pk_add_f32 v[202:203], v[146:147], v[154:155]\n"
  "v_pk_add_f32 v[196:197], v[196:197], v[200:201]\n"
  "v_pk_add_f32 v[198:199], v[198:199], v[202:203]\n"
  "v_mul_f32 v196, 0x3e800000, v196\n"
  "v_mul_f32 v197, 0x3e800000, v197\n"
  "v_mul_f32 v198, 0x3e800000, v198\n"
  "s_add_u32 s59, s11, 0x540000\n"
  "s_add_u32 m0, s12, 0\n"
  "s_nop 0\n"
  "buffer_load_dwordx4 v184, s[64:67], s59 offen offset:0 lds\n"
  "v_mul_f32 v199, 0x3e800000, v199\n"
  "v_pk_add_f32 v[200:201], v[132:133], v[140:141]\n"
  "v_pk_add_f32 v[202:203], v[134:135], v[142:143]\n"
  "s_waitcnt lgkmcnt(0)\n"
  "v_pk_add_f32 v[204:205], v[148:149], v[156:157]\n"
  "v_pk_add_f32 v[206:207], v[150:151], v[158:159]\n"
  "v_pk_add_f32 v[200:201], v[200:201], v[204:205]\n"
  "v_pk_add_f32 v[202:203], v[202:203], v[206:207]\n"
  "v_pk_add_f32 v[128:129], v[128:129], v[196:197] neg_lo:[0,1] neg_hi:[0,1]\n"
  "v_pk_add_f32 v[130:131], v[130:131], v[198:199] neg_lo:[0,1] neg_hi:[0,1]\n"
  "v_pk_add_f32 v[136:137], v[136:137], v[196:197] neg_lo:[0,1] neg_hi:[0,1]\n"
  "v_pk_add_f32 v[138:139], v[138:139], v[198:199] neg_lo:[0,1] neg_hi:[0,1]\n"
  "v_pk_add_f32 v[144:145], v[144:145], v[196:197] neg_lo:[0,1] neg_hi:[0,1]\n"
  "v_pk_add_f32 v[146:147], v[146:147], v[198:199] neg_lo:[0,1] neg_hi:[0,1]\n"
  "v_pk_add_f32 v[152:153], v[152:153], v[196:197] neg_lo:[0,1] neg_hi:[0,1]\n"
  "v_pk_add_f32 v[154:155], v[154:155], v[198:199] neg_lo:[0,1] neg_hi:[0,1]\n"
  "v_pk_mul_f32 v[204:205], v[128:129], v[128:129]\n"
  "buffer_load_dwordx4 v185, s[64:67], s59 offen offset:1024 lds\n"
  "v_pk_mul_f32 v[206:207], v[130:131], v[130:131]\n"
  "v_pk_fma_f32 v[204:205], v[136:137], v[136:137], v[204:205]\n"
  "v_pk_fma_f32 v[206:207], v[138:139], v[138:139], v[206:207]\n"
  "v_pk_fma_f32 v[204:205], v[144:145], v[144:145], v[204:205]\n"
  "v_pk_fma_f32 v[206:207], v[146:147], v[146:147], v[206:207]\n"
  "v_pk_fma_f32 v[204:205], v[152:153], v[152:153], v[204:205]\n"
  "v_pk_fma_f32 v[206:207], v[154:155], v[154:155], v[206:207]\n"
  "v_pk_add_f32 v[192:193], v[192:193], v[196:197] neg_lo:[0,1] neg_hi:[0,1]\n"
  "v_pk_add_f32 v[194:195], v[194:195], v[198:199] neg_lo:[0,1] neg_hi:[0,1]\n"
  "v_fmac_f32 v200, 0x43000000, v204\n"
  "v_fmac_f32 v201, 0x43000000, v205\n"
  "v_fmac_f32 v202, 0x43000000, v206\n"
  "v_fmac_f32 v203, 0x43000000, v207\n"
  "v_mul_f32 v200, 0x3b000000, v200\n"
  "v_mul_f32 v201, 0x3b000000, v201\n"
  "v_mul_f32 v202, 0x3b000000, v202\n"
  "v_mov_b32 v233, 0\n"
  "buffer_atomic_add v233, v229, s[48:51], s86 offen sc0 sc1\n"
  "v_mul_f32 v203, 0x3b000000, v203\n"
  "v_add_f32 v196, 0x3727c5ac, v200\n"
  "v_add_f32 v197, 0x3727c5ac, v201\n"
  "v_add_f32 v198, 0x3727c5ac, v202\n"
  "v_add_f32 v199, 0x3727c5ac, v203\n"
  "v_rsq_f32 v196, v196\n"
  "v_rsq_f32 v197, v197\n"
  "v_rsq_f32 v198, v198\n"
  "v_rsq_f32 v199, v199\n"
  "v_pk_add_f32 v[8:9], v[8:9], v[192:193]\n"
  "v_pk_add_f32 v[10:11], v[10:11], v[194:195]\n"
  "v_pk_add_f32 v[24:25], v[24:25], v[192:193]\n"
  "v_pk_add_f32 v[26:27], v[26:27], v[194:195]\n"
  "v_pk_add_f32 v[40:41], v[40:41], v[192:193]\n"
  "v_pk_add_f32 v[42:43], v[42:43], v[194:195]\n"
  "v_pk_add_f32 v[56:57], v[56:57], v[192:193]\n"
  "v_pk_add_f32 v[58:59], v[58:59], v[194:195]\n"
  "v_pk_mul_f32 v[200:201], v[200:201], v[196:197]\n"
  "v_pk_mul_f32 v[202:203], v[202:203], v[198:199]\n"
  "v_pk_mul_f32 v[200:201], v[200:201], v[196:197]\n"
  "v_pk_mul_f32 v[202:203], v[202:203], v[198:199]\n"
  "v_add_f32 v200, 0x3727c5ac, v200\n"
  "v_add_f32 v201, 0x3727c5ac, v201\n"
  "v_add_f32 v202, 0x3727c5ac, v202\n"
  "v_add_f32 v203, 0x3727c5ac, v203\n"
  "v_rsq_f32 v200, v200\n"
  "v_rsq_f32 v201, v201\n"
  "v_rsq_f32 v202, v202\n"
  "v_rsq_f32 v203, v203\n"
  "v_pk_mul_f32 v[196:197], v[196:197], v[200:201]\n"
  "v_pk_mul_f32 v[198:199], v[198:199], v[202:203]\n"
  "v_pk_mul_f32 v[8:9], v[8:9], v[196:197]\n"
  "v_pk_mul_f32 v[10:11], v[10:11], v[198:199]\n"
  "v_pk_mul_f32 v[24:25], v[24:25], v[196:197]\n"
  "v_pk_mul_f32 v[26:27], v[26:27], v[198:199]\n"
  "v_pk_mul_f32 v[40:41], v[40:41], v[196:197]\n"
  "v_pk_mul_f32 v[42:43], v[42:43], v[198:199]\n"
  "v_pk_mul_f32 v[56:57], v[56:57], v[196:197]\n"
  "v_pk_mul_f32 v[58:59], v[58:59], v[198:199]\n"
  "v_cvt_pk_bf16_f32 v216, v8, v9\n"
  "v_cvt_pk_bf16_f32 v217, v10, v11\n"
  "v_cvt_pk_bf16_f32 v218, v24, v25\n"
  "v_cvt_pk_bf16_f32 v219, v26, v27\n"
  "v_cvt_pk_bf16_f32 v220, v40, v41\n"
  "v_cvt_pk_bf16_f32 v221, v42, v43\n"
  "v_cvt_pk_bf16_f32 v222, v56, v57\n"
  "v_cvt_pk_bf16_f32 v223, v58, v59\n"
  "v_permlane16_swap_b32 v216, v218\n"
  "v_permlane16_swap_b32 v217, v219\n"
  "v_permlane16_swap_b32 v220, v222\n"
  "v_permlane16_swap_b32 v221, v223\n"
  "buffer_store_dwordx4 v[216:219], v236, s[48:51], s56 offen sc1\n"
  "buffer_store_dwordx4 v[220:223], v236, s[48:51], s55 offen sc1\n"
  "s_nop 0\n"
  "s_waitcnt vmcnt(2)\n"
  "v_cmp_ne_u32 vcc, 16, v233\n"
  "s_and_b32 vcc_lo, vcc_lo, 0x3\n"
  "s_cbranch_scc1 mk_spin5a\n"
  "mk_spin5a_b:\n"
  "s_add_u32 s61, s88, 0x0\n"
  "buffer_load_dwordx4 a[128:131], v249, s[48:51], s61 offen sc1\n"
  "buffer_load_dwordx4 a[132:135], v250, s[48:51], s61 offen sc1\n"
  "buffer_load_dwordx4 a[136:139], v251, s[48:51], s61 offen sc1\n"
  "buffer_load_dwordx4 a[140:143], v252, s[48:51], s61 offen sc1\n"
  "s_waitcnt vmcnt(4)\n"
  "buffer_atomic_add v248, v228, s[48:51], s85 offen sc1\n"
  "s_mov_b32 s85, 0x7fff0000\n"
  "s_waitcnt vmcnt(1)\n"
  "mk_join:\n"
  "mk_tp18:\n"
  "s_cbranch_execz mk_tp22\n"
  "s_cmp_eq_u32 s52, 0\n"
  "s_cbranch_scc1 mk_skip1\n"
  "s_nop 0\n"
  "v_lshlrev_b32 v0, 16, v160\n"
  "v_and_b32 v1, 0xffff0000, v160\n"
  "v_lshlrev_b32 v2, 16, v161\n"
  "v_and_b32 v3, 0xffff0000, v161\n"
  "v_lshlrev_b32 v4, 16, v162\n"
  "v_and_b32 v5, 0xffff0000, v162\n"
  "v_lshlrev_b32 v6, 16, v163\n"
  "v_and_b32 v7, 0xffff0000, v163\n"
  "v_lshlrev_b32 v8, 16, v164\n"
  "v_and_b32 v9, 0xffff0000, v164\n"
  "v_lshlrev_b32 v10, 16, v165\n"
  "v_and_b32 v11, 0xffff0000, v165\n"
  "v_lshlrev_b32 v12, 16, v166\n"
  "v_and_b32 v13, 0xffff0000, v166\n"
  "v_lshlrev_b32 v14, 16, v167\n"
  "v_and_b32 v15, 0xffff0000, v167\n"
  "v_mfma_f32_16x16x32_bf16 v[16:19], a[64:67], a[132:135], 0\n"
  "s_add_u32 s61, s88, 0x8000\n"
  "v_mov_b32 v148, v238\n"
  "v_mfma_f32_16x16x32_bf16 v[20:23], a[68:71], a[132:135], 0\n"
  "buffer_load_dwordx4 a[144:147], v249, s[48:51], s61 offen sc1\n"
  "v_mfma_f32_16x16x32_bf16 v[24:27], a[72:75], a[132:135], 0\n"
  "v_add_u32 v149, 0x7c00, v238\n"
  "v_mfma_f32_16x16x32_bf16 v[28:31], a[76:79], a[132:135], 0\n"
  "v_add_u32 v150, 0xf800, v238\n"
  "v_mfma_f32_16x16x32_bf16 v[32:35], a[64:67], a[136:139], 0\n"
  "buffer_load_dwordx4 a[148:151], v250, s[48:51], s61 offen sc1\n"
  "v_mfma_f32_16x16x32_bf16 v[36:39], a[68:71], a[136:139], 0\n"
  "v_add_u32 v151, 0x17400, v238\n"
  "v_mfma_f32_16x16x32_bf16 v[40:43], a[72:75], a[136:139], 0\n"
  "buffer_load_dwordx4 a[152:155], v251, s[48:51], s61 offen sc1\n"
  "v_mfma_f32_16x16x32_bf16 v[44:47], a[76:79], a[136:139], 0\n"
  "s_lshl_b32 s62, %1, 5\n"
  "v_mfma_f32_16x16x32_bf16 v[48:51], a[64:67], a[140:143], 0\n"
  "buffer_load_dwordx4 a[156:159], v252, s[48:51], s61 offen sc1\n"
  "v_mfma_f32_16x16x32_bf16 v[52:55], a[68:71], a[140:143], 0\n"
  "s_add_u32 s61, s88, 0x10000\n"
  "v_xor_b32 v176, s62, v188\n"
  "v_mfma_f32_16x16x32_bf16 v[56:59], a[72:75], a[140:143], 0\n"
  "v_mfma_f32_16x16x32_bf16 v[60:63], a[76:79], a[140:143], 0\n"
  "buffer_load_dwordx4 a[160:163], v249, s[48:51], s61 offen sc1\n"
  "v_mfma_f32_16x16x32_bf16 v[0:3], a[64:67], a[128:131], v[0:3]\n"
  "v_xor_b32 v180, s62, v224\n"
  "v_mfma_f32_16x16x32_bf16 v[4:7], a[68:71], a[128:131], v[4:7]\n"
  "v_xor_b32 v177, s62, v189\n"
  "v_mfma_f32_16x16x32_bf16 v[8:11], a[72:75], a[128:131], v[8:11]\n"
  "buffer_load_dwordx4 a[164:167], v250, s[48:51], s61 offen sc1\n"
  "v_mfma_f32_16x16x32_bf16 v[12:15], a[76:79], a[128:131], v[12:15]\n"
  "s_waitcnt vmcnt(2)\n"
  "v_xor_b32 v181, s62, v225\n"
  "v_mfma_f32_16x16x32_bf16 v[16:19], a[80:83], a[148:151], v[16:19]\n"
  "buffer_load_dwordx4 a[168:171], v251, s[48:51], s61 offen sc1\n"
  "v_mfma_f32_16x16x32_bf16 v[20:23], a[84:87], a[148:151], v[20:23]\n"
  "v_xor_b32 v178, s62, v190\n"
  "v_mfma_f32_16x16x32_bf16 v[24:27], a[88:91], a[148:151], v[24:27]\n"
  "buffer_load_dwordx4 a[172:175], v252, s[48:51], s61 offen sc1\n"
  "v_mfma_f32_16x16x32_bf16 v[28:31], a[92:95], a[148:151], v[28:31]\n"
  "s_add_u32 s61, s88, 0x18000\n"
  "v_xor_b32 v182, s62, v226\n"
  "v_mfma_f32_16x16x32_bf16 v[32:35], a[80:83], a[152:155], v[32:35]\n"
  "v_mfma_f32_16x16x32_bf16 v[36:39], a[84:87], a[152:155], v[36:39]\n"
  "buffer_load_dwordx4 a[176:179], v249, s[48:51], s61 offen sc1\n"
  "v_mfma_f32_16x16x32_bf16 v[40:43], a[88:91], a[152:155], v[40:43]\n"
  "v_xor_b32 v179, s62, v191\n"
  "v_mfma_f32_16x16x32_bf16 v[44:47], a[92:95], a[152:155], v[44:47]\n"
  "v_xor_b32 v183, s62, v227\n"
  "v_mfma_f32_16x16x32_bf16 v[48:51], a[80:83], a[156:159], v[48:51]\n"
  "buffer_load_dwordx4 a[180:183], v250, s[48:51], s61 offen sc1\n"
  "v_mfma_f32_16x16x32_bf16 v[52:55], a[84:87], a[156:159], v[52:55]\n"
  "v_add_u32 v241, s12, v244\n"
  "v_mfma_f32_16x16x32_bf16 v[56:59], a[88:91], a[156:159], v[56:59]\n"
  "buffer_load_dwordx4 a[184:187], v251, s[48:51], s61 offen sc1\n"
  "v_mfma_f32_16x16x32_bf16 v[60:63], a[92:95], a[156:159], v[60:63]\n"
  "s_xor_b32 s63, %1, 1\n"
  "v_mfma_f32_16x16x32_bf16 v[0:3], a[80:83], a[144:147], v[0:3]\n"
  "buffer_load_dwordx4 a[188:191], v252, s[48:51], s61 offen sc1\n"
  "v_mfma_f32_16x16x32_bf16 v[4:7], a[84:87], a[144:147], v[4:7]\n"
  "s_add_u32 s61, s96, 0x80000\n"
  "v_mfma_f32_16x16x32_bf16 v[8:11], a[88:91], a[144:147], v[8:11]\n"
  "s_lshl_b32 s63, s63, 14\n"
  "v_mfma_f32_16x16x32_bf16 v[12:15], a[92:95], a[144:147], v[12:15]\n"
  "s_waitcnt vmcnt(4)\n"
  "buffer_load_dwordx4 a[192:195], v249, s[48:51], s61 offen sc1\n"
  "v_mfma_f32_16x16x32_bf16 v[16:19], a[96:99], a[164:167], v[16:19]\n"
  "s_add_u32 s63, s63, 0x19000\n"
  "v_mfma_f32_16x16x32_bf16 v[20:23], a[100:103], a[164:167], v[20:23]\n"
  "v_add_u32 v145, s63, v244\n"
  "v_mfma_f32_16x16x32_bf16 v[24:27], a[104:107], a[164:167], v[24:27]\n"
  "buffer_load_dwordx4 a[196:199], v250, s[48:51], s61 offen sc1\n"
  "v_mfma_f32_16x16x32_bf16 v[28:31], a[108:111], a[164:167], v[28:31]\n"
  "s_xor_b32 s63, %1, 2\n"
  "v_mfma_f32_16x16x32_bf16 v[32:35], a[96:99], a[168:171], v[32:35]\n"
  "buffer_load_dwordx4 a[200:203], v251, s[48:51], s61 offen sc1\n"
  "v_mfma_f32_16x16x32_bf16 v[36:39], a[100:103], a[168:171], v[36:39]\n"
  "s_lshl_b32 s63, s63, 14\n"
  "v_mfma_f32_16x16x32_bf16 v[40:43], a[104:107], a[168:171], v[40:43]\n"
  "buffer_load_dwordx4 a[204:207], v252, s[48:51], s61 offen sc1\n"
  "v_mfma_f32_16x16x32_bf16 v[44:47], a[108:111], a[168:171], v[44:47]\n"
  "s_add_u32 s61, s96, 0x88000\n"
  "v_mfma_f32_16x16x32_bf16 v[48:51], a[96:99], a[172:175], v[48:51]\n"
  "s_add_u32 s63, s63, 0x1a000\n"
  "v_mfma_f32_16x16x32_bf16 v[52:55], a[100:103], a[172:175], v[52:55]\n"
  "buffer_load_dwordx4 a[208:211], v249, s[48:51], s61 offen sc1\n"
  "v_mfma_f32_16x16x32_bf16 v[56:59], a[104:107], a[172:175], v[56:59]\n"
  "v_add_u32 v146, s63, v244\n"
  "v_mfma_f32_16x16x32_bf16 v[60:63], a[108:111], a[172:175], v[60:63]\n"
  "s_xor_b32 s63, %1, 3\n"
  "v_mfma_f32_16x16x32_bf16 v[0:3], a[96:99], a[160:163], v[0:3]\n"
  "buffer_load_dwordx4 a[212:215], v250, s[48:51], s61 offen sc1\n"
  "v_mfma_f32_16x16x32_bf16 v[4:7], a[100:103], a[160:163], v[4:7]\n"
  "s_lshl_b32 s63, s63, 14\n"
  "v_mfma_f32_16x16x32_bf16 v[8:11], a[104:107], a[160:163], v[8:11]\n"
  "buffer_load_dwordx4 a[216:219], v251, s[48:51], s61 offen sc1\n"
  "v_mfma_f32_16x16x32_bf16 v[12:15], a[108:111], a[160:163], v[12:15]\n"
  "s_waitcnt vmcnt(7)\n"
  "v_mfma_f32_16x16x32_bf16 v[16:19], a[112:115], a[180:183], v[16:19]\n"
  "s_add_u32 s63, s63, 0x1b000\n"
  "buffer_load_dwordx4 a[220:223], v252, s[48:51], s61 offen sc1\n"
  "v_mfma_f32_16x16x32_bf16 v[20:23], a[116:119], a[180:183], v[20:23]\n"
  "s_add_u32 s61, s96, 0x90000\n"
  "v_add_u32 v147, s63, v244\n"
  "v_mfma_f32_16x16x32_bf16 v[24:27], a[120:123], a[180:183], v[24:27]\n"
  "v_mfma_f32_16x16x32_bf16 v[28:31], a[124:127], a[180:183], v[28:31]\n"
  "buffer_load_dwordx4 a[224:227], v249, s[48:51], s61 offen sc1\n"
  "v_mfma_f32_16x16x32_bf16 v[32:35], a[112:115], a[184:187], v[32:35]\n"
  "v_mfma_f32_16x16x32_bf16 v[36:39], a[116:119], a[184:187], v[36:39]\n"
  "v_mfma_f32_16x16x32_bf16 v[40:43], a[120:123], a[184:187], v[40:43]\n"
  "buffer_load_dwordx4 a[228:231], v250, s[48:51], s61 offen sc1\n"
  "v_mfma_f32_16x16x32_bf16 v[44:47], a[124:127], a[184:187], v[44:47]\n"
  "v_mfma_f32_16x16x32_bf16 v[48:51], a[112:115], a[188:191], v[48:51]\n"
  "buffer_load_dwordx4 a[232:235], v251, s[48:51], s61 offen sc1\n"
  "v_mfma_f32_16x16x32_bf16 v[52:55], a[116:119], a[188:191], v[52:55]\n"
  "v_mfma_f32_16x16x32_bf16 v[56:59], a[120:123], a[188:191], v[56:59]\n"
  "buffer_load_dwordx4 a[236:239], v252, s[48:51], s61 offen sc1\n"
  "v_mfma_f32_16x16x32_bf16 v[60:63], a[124:127], a[188:191], v[60:63]\n"
  "s_add_u32 s61, s96, 0x98000\n"
  "v_mfma_f32_16x16x32_bf16 v[0:3], a[112:115], a[176:179], v[0:3]\n"
  "v_mfma_f32_16x16x32_bf16 v[4:7], a[116:119], a[176:179], v[4:7]\n"
  "buffer_load_dwordx4 a[240:243], v249, s[48:51], s61 offen sc1\n"
  "v_mfma_f32_16x16x32_bf16 v[8:11], a[120:123], a[176:179], v[8:11]\n"
  "v_mfma_f32_16x16x32_bf16 v[12:15], a[124:127], a[176:179], v[12:15]\n"
  "s_waitcnt vmcnt(9)\n"
  "v_mfma_f32_16x16x32_bf16 v[16:19], a[0:3], a[196:199], v[16:19]\n"
  "buffer_load_dwordx4 a[244:247], v250, s[48:51], s61 offen sc1\n"
  "v_mfma_f32_16x16x32_bf16 v[20:23], a[4:7], a[196:199], v[20:23]\n"
  "v_mfma_f32_16x16x32_bf16 v[24:27], a[8:11], a[196:199], v[24:27]\n"
  "buffer_load_dwordx4 a[248:251], v251, s[48:51], s61 offen sc1\n"
  "v_mfma_f32_16x16x32_bf16 v[28:31], a[12:15], a[196:199], v[28:31]\n"
  "v_mfma_f32_16x16x32_bf16 v[32:35], a[0:3], a[200:203], v[32:35]\n"
  "buffer_load_dwordx4 a[252:255], v252, s[48:51], s61 offen sc1\n"
  "v_mfma_f32_16x16x32_bf16 v[36:39], a[4:7], a[200:203], v[36:39]\n"
  "v_mov_b32 v233, 0\n"
  "v_mfma_f32_16x16x32_bf16 v[40:43], a[8:11], a[200:203], v[40:43]\n"
  "v_mfma_f32_16x16x32_bf16 v[44:47], a[12:15], a[200:203], v[44:47]\n"
  "buffer_atomic_add v233, v229, s[48:51], s87 offen sc0 sc1\n"
  "v_mfma_f32_16x16x32_bf16 v[48:51], a[0:3], a[204:207], v[48:51]\n"
  "v_mfma_f32_16x16x32_bf16 v[52:55], a[4:7], a[204:207], v[52:55]\n"
  "buffer_load_dwordx4 v186, s[64:67], s59 offen offset:2048 lds\n"
  "v_mfma_f32_16x16x32_bf16 v[56:59], a[8:11], a[204:207], v[56:59]\n"
  "v_mfma_f32_16x16x32_bf16 v[60:63], a[12:15], a[204:207], v[60:63]\n"
  "v_mfma_f32_16x16x32_bf16 v[0:3], a[0:3], a[192:195], v[0:3]\n"
  "buffer_load_dwordx4 v187, s[64:67], s59 offen offset:3072 lds\n"
  "v_mfma_f32_16x16x32_bf16 v[4:7], a[4:7], a[192:195], v[4:7]\n"
  "s_add_u32 s59, s11, 0x55c000\n"
  "v_mfma_f32_16x16x32_bf16 v[8:11], a[8:11], a[192:195], v[8:11]\n"
  "s_add_u32 m0, s12, 4096\n"
  "v_mfma_f32_16x16x32_bf16 v[12:15], a[12:15], a[192:195], v[12:15]\n"
  "s_waitcnt vmcnt(11)\n"
  "buffer_load_dwordx4 v184, s[64:67], s59 offen offset:0 lds\n"
  "v_mfma_f32_16x16x32_bf16 v[16:19], a[16:19], a[212:215], v[16:19]\n"
  "buffer_load_dwordx4 v185, s[64:67], s59 offen offset:1024 lds\n"
  "v_mfma_f32_16x16x32_bf16 v[20:23], a[20:23], a[212:215], v[20:23]\n"
  "v_mfma_f32_16x16x32_bf16 v[24:27], a[24:27], a[212:215], v[24:27]\n"
  "v_mfma_f32_16x16x32_bf16 v[28:31], a[28:31], a[212:215], v[28:31]\n"
  "buffer_load_dwordx4 v186, s[64:67], s59 offen offset:2048 lds\n"
  "v_mfma_f32_16x16x32_bf16 v[32:35], a[16:19], a[216:219], v[32:35]\n"
  "v_mfma_f32_16x16x32_bf16 v[36:39], a[20:23], a[216:219], v[36:39]\n"
  "v_mfma_f32_16x16x32_bf16 v[40:43], a[24:27], a[216:219], v[40:43]\n"
  "buffer_load_dwordx4 v187, s[64:67], s59 offen offset:3072 lds\n"
  "v_mfma_f32_16x16x32_bf16 v[44:47], a[28:31], a[216:219], v[44:47]\n"
  "s_add_u32 s59, s11, 0x578000\n"
  "v_mfma_f32_16x16x32_bf16 v[48:51], a[16:19], a[220:223], v[48:51]\n"
  "s_add_u32 m0, s12, 8192\n"
  "v_mfma_f32_16x16x32_bf16 v[52:55], a[20:23], a[220:223], v[52:55]\n"
  "buffer_load_dwordx4 v184, s[64:67], s59 offen offset:0 lds\n"
  "v_mfma_f32_16x16x32_bf16 v[56:59], a[24:27], a[220:223], v[56:59]\n"
  "buffer_load_dwordx4 v185, s[64:67], s59 offen offset:1024 lds\n"
  "v_mfma_f32_16x16x32_bf16 v[60:63], a[28:31], a[220:223], v[60:63]\n"
  "v_mfma_f32_16x16x32_bf16 v[0:3], a[16:19], a[208:211], v[0:3]\n"
  "v_mfma_f32_16x16x32_bf16 v[4:7], a[20:23], a[208:211], v[4:7]\n"
  "v_mfma_f32_16x16x32_bf16 v[8:11], a[24:27], a[208:211], v[8:11]\n"
  "v_mfma_f32_16x16x32_bf16 v[12:15], a[28:31], a[208:211], v[12:15]\n"
  "s_waitcnt vmcnt(13)\n"
  "v_mfma_f32_16x16x32_bf16 v[16:19], a[32:35], a[228:231], v[16:19]\n"
  "s_waitcnt vmcnt(8)\n"
  "v_cmp_ne_u32 vcc, 16, v233\n"
  "v_mfma_f32_16x16x32_bf16 v[20:23], a[36:39], a[228:231], v[20:23]\n"
  "s_and_b32 vcc_lo, vcc_lo, 0x3\n"
  "s_cbranch_scc1 mk_spin5b\n"
  "mk_spin5b_b:\n"
  "v_mfma_f32_16x16x32_bf16 v[24:27], a[40:43], a[228:231], v[24:27]\n"
  "s_add_u32 s61, s88, 0x100000\n"
  "v_mfma_f32_16x16x32_bf16 v[28:31], a[44:47], a[228:231], v[28:31]\n"
  "buffer_load_dwordx4 a[128:131], v249, s[48:51], s61 offen sc1\n"
  "v_mfma_f32_16x16x32_bf16 v[32:35], a[32:35], a[232:235], v[32:35]\n"
  "buffer_load_dwordx4 a[132:135], v250, s[48:51], s61 offen sc1\n"
  "v_mfma_f32_16x16x32_bf16 v[36:39], a[36:39], a[232:235], v[36:39]\n"
  "v_mfma_f32_16x16x32_bf16 v[40:43], a[40:43], a[232:235], v[40:43]\n"
  "v_mfma_f32_16x16x32_bf16 v[44:47], a[44:47], a[232:235], v[44:47]\n"
  "buffer_load_dwordx4 a[136:139], v251, s[48:51], s61 offen sc1\n"
  "v_mfma_f32_16x16x32_bf16 v[48:51], a[32:35], a[236:239], v[48:51]\n"
  "v_mfma_f32_16x16x32_bf16 v[52:55], a[36:39], a[236:239], v[52:55]\n"
  "v_mfma_f32_16x16x32_bf16 v[56:59], a[40:43], a[236:239], v[56:59]\n"
  "buffer_load_dwordx4 a[140:143], v252, s[48:51], s61 offen sc1\n"
  "v_mfma_f32_16x16x32_bf16 v[60:63], a[44:47], a[236:239], v[60:63]\n"
  "s_add_u32 s61, s88, 0x108000\n"
  "v_mfma_f32_16x16x32_bf16 v[0:3], a[32:35], a[224:227], v[0:3]\n"
  "buffer_load_dwordx4 a[144:147], v249, s[48:51], s61 offen sc1\n"
  "v_mfma_f32_16x16x32_bf16 v[4:7], a[36:39], a[224:227], v[4:7]\n"
  "v_mfma_f32_16x16x32_bf16 v[8:11], a[40:43], a[224:227], v[8:11]\n"
  "buffer_load_dwordx4 a[148:151], v250, s[48:51], s61 offen sc1\n"
  "v_mfma_f32_16x16x32_bf16 v[12:15], a[44:47], a[224:227], v[12:15]\n"
  "s_waitcnt vmcnt(15)\n"
  "ds_read_b64_tr_b16 v[192:193], v224 offset:0\n"
  "v_mfma_f32_16x16x32_bf16 v[16:19], a[48:51], a[244:247], v[16:19]\n"
  "ds_read_b64_tr_b16 v[194:195], v224 offset:8192\n"
  "v_mfma_f32_16x16x32_bf16 v[20:23], a[52:55], a[244:247], v[20:23]\n"
  "buffer_load_dwordx4 a[152:155], v251, s[48:51], s61 offen sc1\n"
  "v_mfma_f32_16x16x32_bf16 v[24:27], a[56:59], a[244:247], v[24:27]\n"
  "ds_read_b64_tr_b16 v[196:197], v225 offset:0\n"
  "ds_read_b64_tr_b16 v[198:199], v225 offset:8192\n"
  "v_mfma_f32_16x16x32_bf16 v[28:31], a[60:63], a[244:247], v[28:31]\n"
  "ds_read_b64_tr_b16 v[200:201], v226 offset:0\n"
  "v_mfma_f32_16x16x32_bf16 v[32:35], a[48:51], a[248:251], v[32:35]\n"
  "buffer_load_dwordx4 a[156:159], v252, s[48:51], s61 offen sc1\n"
  "v_mfma_f32_16x16x32_bf16 v[36:39], a[52:55], a[248:251], v[36:39]\n"
  "ds_read_b64_tr_b16 v[202:203], v226 offset:8192\n"
  "ds_read_b64_tr_b16 v[204:205], v227 offset:0\n"
  "v_mfma_f32_16x16x32_bf16 v[40:43], a[56:59], a[248:251], v[40:43]\n"
  "buffer_load_dwordx4 v186, s[64:67], s59 offen offset:2048 lds\n"
  "v_mfma_f32_16x16x32_bf16 v[44:47], a[60:63], a[248:251], v[44:47]\n"
  "ds_read_b64_tr_b16 v[206:207], v227 offset:8192\n"
  "v_mfma_f32_16x16x32_bf16 v[48:51], a[48:51], a[252:255], v[48:51]\n"
  "buffer_load_dwordx4 v187, s[64:67], s59 offen offset:3072 lds\n"
  "v_mfma_f32_16x16x32_bf16 v[52:55], a[52:55], a[252:255], v[52:55]\n"
  "s_add_u32 s59, s11, 0x594000\n"
  "v_mfma_f32_16x16x32_bf16 v[56:59], a[56:59], a[252:255], v[56:59]\n"
  "s_add_u32 m0, s12, 12288\n"
  "v_mfma_f32_16x16x32_bf16 v[60:63], a[60:63], a[252:255], v[60:63]\n"
  "buffer_load_dwordx4 v184, s[64:67], s59 offen offset:0 lds\n"
  "v_mfma_f32_16x16x32_bf16 v[0:3], a[48:51], a[240:243], v[0:3]\n"
  "v_mfma_f32_16x16x32_bf16 v[4:7], a[52:55], a[240:243], v[4:7]\n"
  "v_mfma_f32_16x16x32_bf16 v[8:11], a[56:59], a[240:243], v[8:11]\n"
  "buffer_load_dwordx4 v185, s[64:67], s59 offen offset:1024 lds\n"
  "v_mfma_f32_16x16x32_bf16 v[12:15], a[60:63], a[240:243], v[12:15]\n"
  "s_waitcnt lgkmcnt(0)\n"
  "s_waitcnt vmcnt(8)\n"
  "ds_read_b64_tr_b16 v[208:209], v224 offset:2048\n"
  "v_mfma_f32_16x16x32_bf16 v[16:19], v[192:195], a[132:135], v[16:19]\n"
  "buffer_load_dwordx4 v186, s[64:67], s59 offen offset:2048 lds\n"
  "v_mfma_f32_16x16x32_bf16 v[20:23], v[196:199], a[132:135], v[20:23]\n"
  "ds_read_b64_tr_b16 v[210:211], v224 offset:10240\n"
  "ds_read_b64_tr_b16 v[212:213], v225 offset:2048\n"
  "v_mfma_f32_16x16x32_bf16 v[24:27], v[200:203], a[132:135], v[24:27]\n"
  "buffer_load_dwordx4 v187, s[64:67], s59 offen offset:3072 lds\n"
  "v_mfma_f32_16x16x32_bf16 v[28:31], v[204:207], a[132:135], v[28:31]\n"
  "s_add_u32 s61, s88, 0x110000\n"
  "ds_read_b64_tr_b16 v[214:215], v225 offset:10240\n"
  "ds_read_b64_tr_b16 v[216:217], v226 offset:2048\n"
  "v_mfma_f32_16x16x32_bf16 v[32:35], v[192:195], a[136:139], v[32:35]\n"
  "ds_read_b64_tr_b16 v[218:219], v226 offset:10240\n"
  "v_mfma_f32_16x16x32_bf16 v[36:39], v[196:199], a[136:139], v[36:39]\n"
  "buffer_load_dwordx4 a[160:163], v249, s[48:51], s61 offen sc1\n"
  "v_mfma_f32_16x16x32_bf16 v[40:43], v[200:203], a[136:139], v[40:43]\n"
  "ds_read_b64_tr_b16 v[220:221], v227 offset:2048\n"
  "ds_read_b64_tr_b16 v[222:223], v227 offset:10240\n"
  "v_mfma_f32_16x16x32_bf16 v[44:47], v[204:207], a[136:139], v[44:47]\n"
  "v_mfma_f32_16x16x32_bf16 v[48:51], v[192:195], a[140:143], v[48:51]\n"
  "buffer_load_dwordx4 a[164:167], v250, s[48:51], s61 offen sc1\n"
  "v_mfma_f32_16x16x32_bf16 v[52:55], v[196:199], a[140:143], v[52:55]\n"
  "v_mfma_f32_16x16x32_bf16 v[56:59], v[200:203], a[140:143], v[56:59]\n"
  "buffer_load_dwordx4 a[168:171], v251, s[48:51], s61 offen sc1\n"
  "v_mfma_f32_16x16x32_bf16 v[60:63], v[204:207], a[140:143], v[60:63]\n"
  "v_mfma_f32_16x16x32_bf16 v[0:3], v[192:195], a[128:131], v[0:3]\n"
  "buffer_load_dwordx4 a[172:175], v252, s[48:51], s61 offen sc1\n"
  "v_mfma_f32_16x16x32_bf16 v[4:7], v[196:199], a[128:131], v[4:7]\n"
  "s_add_u32 s61, s88, 0x118000\n"
  "v_mfma_f32_16x16x32_bf16 v[8:11], v[200:203], a[128:131], v[8:11]\n"
  "v_mfma_f32_16x16x32_bf16 v[12:15], v[204:207], a[128:131], v[12:15]\n"
  "s_waitcnt lgkmcnt(0)\n"
  "s_waitcnt vmcnt(10)\n"
  "buffer_load_dwordx4 a[176:179], v249, s[48:51], s61 offen sc1\n"
  "v_mfma_f32_16x16x32_bf16 v[16:19], v[208:211], a[148:151], v[16:19]\n"
  "ds_read_b64_tr_b16 v[192:193], v224 offset:4096\n"
  "ds_read_b64_tr_b16 v[194:195], v224 offset:12288\n"
  "v_mfma_f32_16x16x32_bf16 v[20:23], v[212:215], a[148:151], v[20:23]\n"
  "ds_read_b64_tr_b16 v[196:197], v225 offset:4096\n"
  "v_mfma_f32_16x16x32_bf16 v[24:27], v[216:219], a[148:151], v[24:27]\n"
  "buffer_load_dwordx4 a[180:183], v250, s[48:51], s61 offen sc1\n"
  "v_mfma_f32_16x16x32_bf16 v[28:31], v[220:223], a[148:151], v[28:31]\n"
  "ds_read_b64_tr_b16 v[198:199], v225 offset:12288\n"
  "ds_read_b64_tr_b16 v[200:201], v226 offset:4096\n"
  "v_mfma_f32_16x16x32_bf16 v[32:35], v[208:211], a[152:155], v[32:35]\n"
  "buffer_load_dwordx4 a[184:187], v251, s[48:51], s61 offen sc1\n"
  "v_mfma_f32_16x16x32_bf16 v[36:39], v[212:215], a[152:155], v[36:39]\n"
  "ds_read_b64_tr_b16 v[202:203], v226 offset:12288\n"
  "ds_read_b64_tr_b16 v[204:205], v227 offset:4096\n"
  "v_mfma_f32_16x16x32_bf16 v[40:43], v[216:219], a[152:155], v[40:43]\n"
  "buffer_load_dwordx4 a[188:191], v252, s[48:51], s61 offen sc1\n"
  "v_mfma_f32_16x16x32_bf16 v[44:47], v[220:223], a[152:155], v[44:47]\n"
  "s_add_u32 s61, s96, 0x180000\n"
  "ds_read_b64_tr_b16 v[206:207], v227 offset:12288\n"
  "v_mfma_f32_16x16x32_bf16 v[48:51], v[208:211], a[156:159], v[48:51]\n"
  "v_mfma_f32_16x16x32_bf16 v[52:55], v[212:215], a[156:159], v[52:55]\n"
  "buffer_load_dwordx4 a[192:195], v249, s[48:51], s61 offen sc1\n"
  "v_mfma_f32_16x16x32_bf16 v[56:59], v[216:219], a[156:159], v[56:59]\n"
  "v_mfma_f32_16x16x32_bf16 v[60:63], v[220:223], a[156:159], v[60:63]\n"
  "v_mfma_f32_16x16x32_bf16 v[0:3], v[208:211], a[144:147], v[0:3]\n"
  "buffer_load_dwordx4 a[196:199], v250, s[48:51], s61 offen sc1\n"
  "v_mfma_f32_16x16x32_bf16 v[4:7], v[212:215], a[144:147], v[4:7]\n"
  "v_mfma_f32_16x16x32_bf16 v[8:11], v[216:219], a[144:147], v[8:11]\n"
  "buffer_load_dwordx4 a[200:203], v251, s[48:51], s61 offen sc1\n"
  "v_mfma_f32_16x16x32_bf16 v[12:15], v[220:223], a[144:147], v[12:15]\n"
  "s_waitcnt lgkmcnt(0)\n"
  "s_waitcnt vmcnt(7)\n"
  "ds_read_b64_tr_b16 v[208:209], v224 offset:6144\n"
  "v_mfma_f32_16x16x32_bf16 v[16:19], v[192:195], a[164:167], v[16:19]\n"
  "buffer_load_dwordx4 a[204:207], v252, s[48:51], s61 offen sc1\n"
  "v_mfma_f32_16x16x32_bf16 v[20:23], v[196:199], a[164:167], v[20:23]\n"
  "s_add_u32 s61, s96, 0x188000\n"
  "ds_read_b64_tr_b16 v[210:211], v224 offset:14336\n"
  "ds_read_b64_tr_b16 v[212:213], v225 offset:6144\n"
  "v_mfma_f32_16x16x32_bf16 v[24:27], v[200:203], a[164:167], v[24:27]\n"
  "ds_read_b64_tr_b16 v[214:215], v225 offset:14336\n"
  "v_mfma_f32_16x16x32_bf16 v[28:31], v[204:207], a[164:167], v[28:31]\n"
  "buffer_load_dwordx4 a[208:211], v249, s[48:51], s61 offen sc1\n"
  "v_mfma_f32_16x16x32_bf16 v[32:35], v[192:195], a[168:171], v[32:35]\n"
  "ds_read_b64_tr_b16 v[216:217], v226 offset:6144\n"
  "ds_read_b64_tr_b16 v[218:219], v226 offset:14336\n"
  "v_mfma_f32_16x16x32_bf16 v[36:39], v[196:199], a[168:171], v[36:39]\n"
  "mk_tp19:\n"
  "s_cbranch_execz mk_tp23\n"
  "ds_read_b64_tr_b16 v[220:221], v227 offset:6144\n"
  "v_mfma_f32_16x16x32_bf16 v[40:43], v[200:203], a[168:171], v[40:43]\n"
  "buffer_load_dwordx4 a[212:215], v250, s[48:51], s61 offen sc1\n"
  "v_mfma_f32_16x16x32_bf16 v[44:47], v[204:207], a[168:171], v[44:47]\n"
  "ds_read_b64_tr_b16 v[222:223], v227 offset:14336\n"
  "v_mfma_f32_16x16x32_bf16 v[48:51], v[192:195], a[172:175], v[48:51]\n"
  "buffer_load_dwordx4 a[216:219], v251, s[48:51], s61 offen sc1\n"
  "v_mfma_f32_16x16x32_bf16 v[52:55], v[196:199], a[172:175], v[52:55]\n"
  "v_mfma_f32_16x16x32_bf16 v[56:59], v[200:203], a[172:175], v[56:59]\n"
  "buffer_load_dwordx4 a[220:223], v252, s[48:51], s61 offen sc1\n"
  "v_mfma_f32_16x16x32_bf16 v[60:63], v[204:207], a[172:175], v[60:63]\n"
  "s_add_u32 s61, s96, 0x190000\n"
  "v_mfma_f32_16x16x32_bf16 v[0:3], v[192:195], a[160:163], v[0:3]\n"
  "v_mfma_f32_16x16x32_bf16 v[4:7], v[196:199], a[160:163], v[4:7]\n"
  "buffer_load_dwordx4 a[224:227], v249, s[48:51], s61 offen sc1\n"
  "v_mfma_f32_16x16x32_bf16 v[8:11], v[200:203], a[160:163], v[8:11]\n"
  "v_mfma_f32_16x16x32_bf16 v[12:15], v[204:207], a[160:163], v[12:15]\n"
  "s_waitcnt lgkmcnt(0)\n"
  "s_waitcnt vmcnt(9)\n"
  "ds_read_b64_tr_b16 v[192:193], v188 offset:0\n"
  "v_mfma_f32_16x16x32_bf16 v[16:19], v[208:211], a[180:183], v[16:19]\n"
  "buffer_load_dwordx4 a[228:231], v250, s[48:51], s61 offen sc1\n"
  "v_mfma_f32_16x16x32_bf16 v[20:23], v[212:215], a[180:183], v[20:23]\n"
  "ds_read_b64_tr_b16 v[194:195], v188 offset:8192\n"
  "ds_read_b64_tr_b16 v[196:197], v189 offset:0\n"
  "v_mfma_f32_16x16x32_bf16 v[24:27], v[216:219], a[180:183], v[24:27]\n"
  "buffer_load_dwordx4 a[232:235], v251, s[48:51], s61 offen sc1\n"
  "v_mfma_f32_16x16x32_bf16 v[28:31], v[220:223], a[180:183], v[28:31]\n"
  "ds_read_b64_tr_b16 v[198:199], v189 offset:8192\n"
  "ds_read_b64_tr_b16 v[200:201], v190 offset:0\n"
  "v_mfma_f32_16x16x32_bf16 v[32:35], v[208:211], a[184:187], v[32:35]\n"
  "buffer_load_dwordx4 a[236:239], v252, s[48:51], s61 offen sc1\n"
  "v_mfma_f32_16x16x32_bf16 v[36:39], v[212:215], a[184:187], v[36:39]\n"
  "s_add_u32 s61, s96, 0x198000\n"
  "ds_read_b64_tr_b16 v[202:203], v190 offset:8192\n"
  "ds_read_b64_tr_b16 v[204:205], v191 offset:0\n"
  "v_mfma_f32_16x16x32_bf16 v[40:43], v[216:219], a[184:187], v[40:43]\n"
  "ds_read_b64_tr_b16 v[206:207], v191 offset:8192\n"
  "v_mfma_f32_16x16x32_bf16 v[44:47], v[220:223], a[184:187], v[44:47]\n"
  "buffer_load_dwordx4 a[240:243], v249, s[48:51], s61 offen sc1\n"
  "v_mfma_f32_16x16x32_bf16 v[48:51], v[208:211], a[188:191], v[48:51]\n"
  "v_mfma_f32_16x16x32_bf16 v[52:55], v[212:215], a[188:191], v[52:55]\n"
  "v_mfma_f32_16x16x32_bf16 v[56:59], v[216:219], a[188:191], v[56:59]\n"
  "buffer_load_dwordx4 a[244:247], v250, s[48:51], s61 offen sc1\n"
  "v_mfma_f32_16x16x32_bf16 v[60:63], v[220:223], a[188:191], v[60:63]\n"
  "v_mfma_f32_16x16x32_bf16 v[0:3], v[208:211], a[176:179], v[0:3]\n"
  "buffer_load_dwordx4 a[248:251], v251, s[48:51], s61 offen sc1\n"
  "v_mfma_f32_16x16x32_bf16 v[4:7], v[212:215], a[176:179], v[4:7]\n"
  "v_mfma_f32_16x16x32_bf16 v[8:11], v[216:219], a[176:179], v[8:11]\n"
  "buffer_load_dwordx4 a[252:255], v252, s[48:51], s61 offen sc1\n"
  "v_mfma_f32_16x16x32_bf16 v[12:15], v[220:223], a[176:179], v[12:15]\n"
  "s_waitcnt lgkmcnt(0)\n"
  "s_waitcnt vmcnt(12)\n"
  "ds_read_b64_tr_b16 v[208:209], v188 offset:2048\n"
  "v_mfma_f32_16x16x32_bf16 v[16:19], v[192:195], a[196:199], v[16:19]\n"
  "s_add_u32 s59, s15, 0x0\n"
  "ds_read_b64_tr_b16 v[210:211], v188 offset:10240\n"
  "v_mfma_f32_16x16x32_bf16 v[20:23], v[196:199], a[196:199], v[20:23]\n"
  "s_add_u32 m0, s13, 20480\n"
  "ds_read_b64_tr_b16 v[212:213], v189 offset:2048\n"
  "v_mfma_f32_16x16x32_bf16 v[24:27], v[200:203], a[196:199], v[24:27]\n"
  "buffer_load_dwordx4 v148, s[68:71], s59 offen offset:0 lds\n"
  "v_mfma_f32_16x16x32_bf16 v[28:31], v[204:207], a[196:199], v[28:31]\n"
  "ds_read_b64_tr_b16 v[214:215], v189 offset:10240\n"
  "ds_read_b64_tr_b16 v[216:217], v190 offset:2048\n"
  "v_mfma_f32_16x16x32_bf16 v[32:35], v[192:195], a[200:203], v[32:35]\n"
  "ds_read_b64_tr_b16 v[218:219], v190 offset:10240\n"
  "v_mfma_f32_16x16x32_bf16 v[36:39], v[196:199], a[200:203], v[36:39]\n"
  "buffer_load_dwordx4 v149, s[68:71], s59 offen offset:1024 lds\n"
  "v_mfma_f32_16x16x32_bf16 v[40:43], v[200:203], a[200:203], v[40:43]\n"
  "ds_read_b64_tr_b16 v[220:221], v191 offset:2048\n"
  "ds_read_b64_tr_b16 v[222:223], v191 offset:10240\n"
  "v_mfma_f32_16x16x32_bf16 v[44:47], v[204:207], a[200:203], v[44:47]\n"
  "v_mfma_f32_16x16x32_bf16 v[48:51], v[192:195], a[204:207], v[48:51]\n"
  "v_mfma_f32_16x16x32_bf16 v[52:55], v[196:199], a[204:207], v[52:55]\n"
  "buffer_load_dwordx4 v150, s[68:71], s59 offen offset:2048 lds\n"
  "v_mfma_f32_16x16x32_bf16 v[56:59], v[200:203], a[204:207], v[56:59]\n"
  "v_mfma_f32_16x16x32_bf16 v[60:63], v[204:207], a[204:207], v[60:63]\n"
  "v_mfma_f32_16x16x32_bf16 v[0:3], v[192:195], a[192:195], v[0:3]\n"
  "v_mfma_f32_16x16x32_bf16 v[4:7], v[196:199], a[192:195], v[4:7]\n"
  "buffer_load_dwordx4 v151, s[68:71], s59 offen offset:3072 lds\n"
  "v_mfma_f32_16x16x32_bf16 v[8:11], v[200:203], a[192:195], v[8:11]\n"
  "s_add_u32 s59, s15, 0x20000\n"
  "v_mfma_f32_16x16x32_bf16 v[12:15], v[204:207], a[192:195], v[12:15]\n"
  "s_waitcnt lgkmcnt(0)\n"
  "s_waitcnt vmcnt(12)\n"
  "ds_read_b64_tr_b16 v[192:193], v188 offset:4096\n"
  "v_mfma_f32_16x16x32_bf16 v[16:19], v[208:211], a[212:215], v[16:19]\n"
  "s_add_u32 m0, s13, 0\n"
  "ds_read_b64_tr_b16 v[194:195], v188 offset:12288\n"
  "v_mfma_f32_16x16x32_bf16 v[20:23], v[212:215], a[212:215], v[20:23]\n"
  "buffer_load_dwordx4 v148, s[68:71], s59 offen offset:0 lds\n"
  "v_mfma_f32_16x16x32_bf16 v[24:27], v[216:219], a[212:215], v[24:27]\n"
  "ds_read_b64_tr_b16 v[196:197], v189 offset:4096\n"
  "ds_read_b64_tr_b16 v[198:199], v189 offset:12288\n"
  "v_mfma_f32_16x16x32_bf16 v[28:31], v[220:223], a[212:215], v[28:31]\n"
  "ds_read_b64_tr_b16 v[200:201], v190 offset:4096\n"
  "v_mfma_f32_16x16x32_bf16 v[32:35], v[208:211], a[216:219], v[32:35]\n"
  "ds_read_b64_tr_b16 v[202:203], v190 offset:12288\n"
  "v_mfma_f32_16x16x32_bf16 v[36:39], v[212:215], a[216:219], v[36:39]\n"
  "buffer_load_dwordx4 v149, s[68:71], s59 offen offset:1024 lds\n"
  "v_mfma_f32_16x16x32_bf16 v[40:43], v[216:219], a[216:219], v[40:43]\n"
  "ds_read_b64_tr_b16 v[204:205], v191 offset:4096\n"
  "ds_read_b64_tr_b16 v[206:207], v191 offset:12288\n"
  "v_mfma_f32_16x16x32_bf16 v[44:47], v[220:223], a[216:219], v[44:47]\n"
  "v_mfma_f32_16x16x32_bf16 v[48:51], v[208:211], a[220:223], v[48:51]\n"
  "v_mfma_f32_16x16x32_bf16 v[52:55], v[212:215], a[220:223], v[52:55]\n"
  "buffer_load_dwordx4 v150, s[68:71], s59 offen offset:2048 lds\n"
  "v_mfma_f32_16x16x32_bf16 v[56:59], v[216:219], a[220:223], v[56:59]\n"
  "v_mfma_f32_16x16x32_bf16 v[60:63], v[220:223], a[220:223], v[60:63]\n"
  "v_mfma_f32_16x16x32_bf16 v[0:3], v[208:211], a[208:211], v[0:3]\n"
  "v_mfma_f32_16x16x32_bf16 v[4:7], v[212:215], a[208:211], v[4:7]\n"
  "buffer_load_dwordx4 v151, s[68:71], s59 offen offset:3072 lds\n"
  "v_mfma_f32_16x16x32_bf16 v[8:11], v[216:219], a[208:211], v[8:11]\n"
  "s_add_u32 s59, s15, 0x40000\n"
  "v_mfma_f32_16x16x32_bf16 v[12:15], v[220:223], a[208:211], v[12:15]\n"
  "s_waitcnt lgkmcnt(0)\n"
  "s_waitcnt vmcnt(12)\n"
  "ds_read_b64_tr_b16 v[208:209], v188 offset:6144\n"
  "v_mfma_f32_16x16x32_bf16 v[16:19], v[192:195], a[228:231], v[16:19]\n"
  "s_add_u32 m0, s13, 4096\n"
  "ds_read_b64_tr_b16 v[210:211], v188 offset:14336\n"
  "v_mfma_f32_16x16x32_bf16 v[20:23], v[196:199], a[228:231], v[20:23]\n"
  "buffer_load_dwordx4 v148, s[68:71], s59 offen offset:0 lds\n"
  "v_mfma_f32_16x16x32_bf16 v[24:27], v[200:203], a[228:231], v[24:27]\n"
  "ds_read_b64_tr_b16 v[212:213], v189 offset:6144\n"
  "ds_read_b64_tr_b16 v[214:215], v189 offset:14336\n"
  "v_mfma_f32_16x16x32_bf16 v[28:31], v[204:207], a[228:231], v[28:31]\n"
  "ds_read_b64_tr_b16 v[216:217], v190 offset:6144\n"
  "v_mfma_f32_16x16x32_bf16 v[32:35], v[192:195], a[232:235], v[32:35]\n"
  "ds_read_b64_tr_b16 v[218:219], v190 offset:14336\n"
  "v_mfma_f32_16x16x32_bf16 v[36:39], v[196:199], a[232:235], v[36:39]\n"
  "buffer_load_dwordx4 v149, s[68:71], s59 offen offset:1024 lds\n"
  "v_mfma_f32_16x16x32_bf16 v[40:43], v[200:203], a[232:235], v[40:43]\n"
  "ds_read_b64_tr_b16 v[220:221], v191 offset:6144\n"
  "ds_read_b64_tr_b16 v[222:223], v191 offset:14336\n"
  "v_mfma_f32_16x16x32_bf16 v[44:47], v[204:207], a[232:235], v[44:47]\n"
  "v_mfma_f32_16x16x32_bf16 v[48:51], v[192:195], a[236:239], v[48:51]\n"
  "v_mfma_f32_16x16x32_bf16 v[52:55], v[196:199], a[236:239], v[52:55]\n"
  "buffer_load_dwordx4 v150, s[68:71], s59 offen offset:2048 lds\n"
  "v_mfma_f32_16x16x32_bf16 v[56:59], v[200:203], a[236:239], v[56:59]\n"
  "v_mfma_f32_16x16x32_bf16 v[60:63], v[204:207], a[236:239], v[60:63]\n"
  "v_mfma_f32_16x16x32_bf16 v[0:3], v[192:195], a[224:227], v[0:3]\n"
  "v_mfma_f32_16x16x32_bf16 v[4:7], v[196:199], a[224:227], v[4:7]\n"
  "buffer_load_dwordx4 v151, s[68:71], s59 offen offset:3072 lds\n"
  "v_mfma_f32_16x16x32_bf16 v[8:11], v[200:203], a[224:227], v[8:11]\n"
  "s_add_u32 s59, s15, 0x60000\n"
  "v_mfma_f32_16x16x32_bf16 v[12:15], v[204:207], a[224:227], v[12:15]\n"
  "s_waitcnt lgkmcnt(0)\n"
  "s_waitcnt vmcnt(12)\n"
  "v_mfma_f32_16x16x32_bf16 v[16:19], v[208:211], a[244:247], v[16:19]\n"
  "s_add_u32 m0, s13, 8192\n"
  "v_mfma_f32_16x16x32_bf16 v[20:23], v[212:215], a[244:247], v[20:23]\n"
  "buffer_load_dwordx4 v148, s[68:71], s59 offen offset:0 lds\n"
  "v_mfma_f32_16x16x32_bf16 v[24:27], v[216:219], a[244:247], v[24:27]\n"
  "v_mfma_f32_16x16x32_bf16 v[28:31], v[220:223], a[244:247], v[28:31]\n"
  "v_mfma_f32_16x16x32_bf16 v[32:35], v[208:211], a[248:251], v[32:35]\n"
  "v_mfma_f32_16x16x32_bf16 v[36:39], v[212:215], a[248:251], v[36:39]\n"
  "buffer_load_dwordx4 v149, s[68:71], s59 offen offset:1024 lds\n"
  "v_mfma_f32_16x16x32_bf16 v[40:43], v[216:219], a[248:251], v[40:43]\n"
  "ds_write_b128 v241, v[16:19] offset:4096\n"
  "v_mfma_f32_16x16x32_bf16 v[44:47], v[220:223], a[248:251], v[44:47]\n"
  "ds_write_b128 v241, v[20:23] offset:5120\n"
  "v_mfma_f32_16x16x32_bf16 v[48:51], v[208:211], a[252:255], v[48:51]\n"
  "ds_write_b128 v241, v[24:27] offset:6144\n"
  "v_mfma_f32_16x16x32_bf16 v[52:55], v[212:215], a[252:255], v[52:55]\n"
  "ds_write_b128 v241, v[28:31] offset:7168\n"
  "v_mfma_f32_16x16x32_bf16 v[56:59], v[216:219], a[252:255], v[56:59]\n"
  "buffer_load_dwordx4 v150, s[68:71], s59 offen offset:2048 lds\n"
  "v_mfma_f32_16x16x32_bf16 v[60:63], v[220:223], a[252:255], v[60:63]\n"
  "ds_write_b128 v241, v[32:35] offset:8192\n"
  "v_mfma_f32_16x16x32_bf16 v[0:3], v[208:211], a[240:243], v[0:3]\n"
  "ds_write_b128 v241, v[36:39] offset:9216\n"
  "v_mfma_f32_16x16x32_bf16 v[4:7], v[212:215], a[240:243], v[4:7]\n"
  "ds_write_b128 v241, v[40:43] offset:10240\n"
  "v_mfma_f32_16x16x32_bf16 v[8:11], v[216:219], a[240:243], v[8:11]\n"
  "ds_write_b128 v241, v[44:47] offset:11264\n"
  "v_mfma_f32_16x16x32_bf16 v[12:15], v[220:223], a[240:243], v[12:15]\n"
  "buffer_load_dwordx4 v151, s[68:71], s59 offen offset:3072 lds\n"
  "ds_write_b128 v241, v[48:51] offset:12288\n"
  "ds_write_b128 v241, v[52:55] offset:13312\n"
  "ds_write_b128 v241, v[56:59] offset:14336\n"
  "ds_write_b128 v241, v[60:63] offset:15360\n"
  "s_nop 0\n"
  "s_waitcnt lgkmcnt(0)\n"
  "s_barrier\n"
  "ds_read_b128 v[80:83], v145 offset:0\n"
  "s_add_u32 s59, s15, 0x80000\n"
  "s_add_u32 m0, s13, 12288\n"
  "s_nop 0\n"
  "buffer_load_dwordx4 v148, s[68:71], s59 offen offset:0 lds\n"
  "ds_read_b128 v[84:87], v145 offset:1024\n"
  "ds_read_b128 v[88:91], v145 offset:2048\n"
  "ds_read_b128 v[92:95], v145 offset:3072\n"
  "ds_read_b128 v[96:99], v146 offset:0\n"
  "ds_read_b128 v[100:103], v146 offset:1024\n"
  "ds_read_b128 v[104:107], v146 offset:2048\n"
  "ds_read_b128 v[108:111], v146 offset:3072\n"
  "ds_read_b128 v[112:115], v147 offset:0\n"
  "buffer_load_dwordx4 v149, s[68:71], s59 offen offset:1024 lds\n"
  "ds_read_b128 v[116:119], v147 offset:1024\n"
  "ds_read_b128 v[120:123], v147 offset:2048\n"
  "ds_read_b128 v[124:127], v147 offset:3072\n"
  "s_waitcnt lgkmcnt(11)\n"
  "v_pk_add_f32 v[0:1], v[0:1], v[80:81]\n"
  "v_pk_add_f32 v[2:3], v[2:3], v[82:83]\n"
  "s_waitcnt lgkmcnt(10)\n"
  "v_pk_add_f32 v[4:5], v[4:5], v[84:85]\n"
  "v_pk_add_f32 v[6:7], v[6:7], v[86:87]\n"
  "s_waitcnt lgkmcnt(9)\n"
  "v_pk_add_f32 v[8:9], v[8:9], v[88:89]\n"
  "buffer_load_dwordx4 v150, s[68:71], s59 offen offset:2048 lds\n"
  "v_pk_add_f32 v[10:11], v[10:11], v[90:91]\n"
  "s_waitcnt lgkmcnt(8)\n"
  "v_pk_add_f32 v[12:13], v[12:13], v[92:93]\n"
  "v_pk_add_f32 v[14:15], v[14:15], v[94:95]\n"
  "s_waitcnt lgkmcnt(3)\n"
  "v_pk_add_f32 v[96:97], v[96:97], v[112:113]\n"
  "v_pk_add_f32 v[98:99], v[98:99], v[114:115]\n"
  "s_waitcnt lgkmcnt(2)\n"
  "v_pk_add_f32 v[100:101], v[100:101], v[116:117]\n"
  "v_pk_add_f32 v[102:103], v[102:103], v[118:119]\n"
  "s_waitcnt lgkmcnt(1)\n"
  "v_pk_add_f32 v[104:105], v[104:105], v[120:121]\n"
  "buffer_load_dwordx4 v151, s[68:71], s59 offen offset:3072 lds\n"
  "v_pk_add_f32 v[106:107], v[106:107], v[122:123]\n"
  "s_waitcnt lgkmcnt(0)\n"
  "v_pk_add_f32 v[108:109], v[108:109], v[124:125]\n"
  "v_pk_add_f32 v[110:111], v[110:111], v[126:127]\n"
  "v_pk_add_f32 v[0:1], v[0:1], v[96:97]\n"
  "v_pk_add_f32 v[2:3], v[2:3], v[98:99]\n"
  "v_pk_add_f32 v[4:5], v[4:5], v[100:101]\n"
  "v_pk_add_f32 v[6:7], v[6:7], v[102:103]\n"
  "v_pk_add_f32 v[8:9], v[8:9], v[104:105]\n"
  "s_add_u32 s59, s15, 0xa0000\n"
  "s_add_u32 m0, s13, 16384\n"
  "s_nop 0\n"
  "buffer_load_dwordx4 v148, s[68:71], s59 offen offset:0 lds\n"
  "v_pk_add_f32 v[10:11], v[10:11], v[106:107]\n"
  "v_pk_add_f32 v[12:13], v[12:13], v[108:109]\n"
  "v_pk_add_f32 v[14:15], v[14:15], v[110:111]\n"
  "s_barrier\n"
  "buffer_load_dwordx4 v149, s[68:71], s59 offen offset:1024 lds\n"
  "buffer_load_dwordx4 v150, s[68:71], s59 offen offset:2048 lds\n"
  "buffer_load_dwordx4 v151, s[68:71], s59 offen offset:3072 lds\n"
  "s_add_u32 s59, s15, 0xc0000\n"
  "s_add_u32 m0, s12, 0\n"
  "s_nop 0\n"
  "buffer_load_dwordx4 v148, s[68:71], s59 offen offset:0 lds\n"
  "v_mov_b32 v112, 0xc0135761\n"
  "v_mov_b32 v114, 0xbdd2d3e8\n"
  "v_pk_mul_f32 v[64:65], v[0:1], v[0:1]\n"
  "v_pk_mul_f32 v[66:67], v[2:3], v[2:3]\n"
  "v_pk_mul_f32 v[68:69], v[4:5], v[4:5]\n"
  "v_pk_mul_f32 v[70:71], v[6:7], v[6:7]\n"
  "v_pk_mul_f32 v[72:73], v[8:9], v[8:9]\n"
  "v_pk_mul_f32 v[74:75], v[10:11], v[10:11]\n"
  "v_pk_mul_f32 v[76:77], v[12:13], v[12:13]\n"
  "v_pk_mul_f32 v[78:79], v[14:15], v[14:15]\n"
  "v_pk_fma_f32 v[64:65], v[64:65], v[114:115], v[112:113] op_sel_hi:[1,0,0]\n"
  "v_pk_fma_f32 v[66:67], v[66:67], v[114:115], v[112:113] op_sel_hi:[1,0,0]\n"
  "v_pk_fma_f32 v[68:69], v[68:69], v[114:115], v[112:113] op_sel_hi:[1,0,0]\n"
  "v_pk_fma_f32 v[70:71], v[70:71], v[114:115], v[112:113] op_sel_hi:[1,0,0]\n"
  "buffer_load_dwordx4 v149, s[68:71], s59 offen offset:1024 lds\n"
  "v_pk_fma_f32 v[72:73], v[72:73], v[114:115], v[112:113] op_sel_hi:[1,0,0]\n"
  "v_pk_fma_f32 v[74:75], v[74:75], v[114:115], v[112:113] op_sel_hi:[1,0,0]\n"
  "v_pk_fma_f32 v[76:77], v[76:77], v[114:115], v[112:113] op_sel_hi:[1,0,0]\n"
  "v_pk_fma_f32 v[78:79], v[78:79], v[114:115], v[112:113] op_sel_hi:[1,0,0]\n"
  "v_pk_mul_f32 v[64:65], v[64:65], v[0:1]\n"
  "v_pk_mul_f32 v[66:67], v[66:67], v[2:3]\n"
  "v_pk_mul_f32 v[68:69], v[68:69], v[4:5]\n"
  "v_pk_mul_f32 v[70:71], v[70:71], v[6:7]\n"
  "v_pk_mul_f32 v[72:73], v[72:73], v[8:9]\n"
  "v_pk_mul_f32 v[74:75], v[74:75], v[10:11]\n"
  "v_pk_mul_f32 v[76:77], v[76:77], v[12:13]\n"
  "v_pk_mul_f32 v[78:79], v[78:79], v[14:15]\n"
  "v_exp_f32 v64, v64\n"
  "v_exp_f32 v65, v65\n"
  "buffer_load_dwordx4 v150, s[68:71], s59 offen offset:2048 lds\n"
  "v_exp_f32 v66, v66\n"
  "v_exp_f32 v67, v67\n"
  "v_exp_f32 v68, v68\n"
  "v_exp_f32 v69, v69\n"
  "v_exp_f32 v70, v70\n"
  "v_exp_f32 v71, v71\n"
  "v_exp_f32 v72, v72\n"
  "v_exp_f32 v73, v73\n"
  "v_exp_f32 v74, v74\n"
  "v_exp_f32 v75, v75\n"
  "v_exp_f32 v76, v76\n"
  "v_exp_f32 v77, v77\n"
  "v_exp_f32 v78, v78\n"
  "v_exp_f32 v79, v79\n"
  "buffer_load_dwordx4 v151, s[68:71], s59 offen offset:3072 lds\n"
  "v_pk_add_f32 v[64:65], v[64:65], 1.0 op_sel_hi:[1,0]\n"
  "v_pk_add_f32 v[66:67], v[66:67], 1.0 op_sel_hi:[1,0]\n"
  "v_pk_add_f32 v[68:69], v[68:69], 1.0 op_sel_hi:[1,0]\n"
  "v_pk_add_f32 v[70:71], v[70:71], 1.0 op_sel_hi:[1,0]\n"
  "v_pk_add_f32 v[72:73], v[72:73], 1.0 op_sel_hi:[1,0]\n"
  "v_pk_add_f32 v[74:75], v[74:75], 1.0 op_sel_hi:[1,0]\n"
  "v_pk_add_f32 v[76:77], v[76:77], 1.0 op_sel_hi:[1,0]\n"
  "v_pk_add_f32 v[78:79], v[78:79], 1.0 op_sel_hi:[1,0]\n"
  "v_rcp_f32 v64, v64\n"
  "v_rcp_f32 v65, v65\n"
  "v_rcp_f32 v66, v66\n"
  "v_rcp_f32 v67, v67\n"
  "v_rcp_f32 v68, v68\n"
  "v_rcp_f32 v69, v69\n"
  "s_add_u32 s59, s18, 0xe0000\n"
  "s_add_u32 m0, s12, 4096\n"
  "s_nop 0\n"
  "buffer_load_dwordx4 v148, s[68:71], s59 offen offset:0 lds\n"
  "v_rcp_f32 v70, v70\n"
  "v_rcp_f32 v71, v71\n"
  "v_rcp_f32 v72, v72\n"
  "v_rcp_f32 v73, v73\n"
  "v_rcp_f32 v74, v74\n"
  "v_rcp_f32 v75, v75\n"
  "v_rcp_f32 v76, v76\n"
  "v_rcp_f32 v77, v77\n"
  "v_rcp_f32 v78, v78\n"
  "v_rcp_f32 v79, v79\n"
  "v_pk_mul_f32 v[0:1], v[0:1], v[64:65]\n"
  "v_pk_mul_f32 v[2:3], v[2:3], v[66:67]\n"
  "v_pk_mul_f32 v[4:5], v[4:5], v[68:69]\n"
  "v_pk_mul_f32 v[6:7], v[6:7], v[70:71]\n"
  "buffer_load_dwordx4 v149, s[68:71], s59 offen offset:1024 lds\n"
  "v_pk_mul_f32 v[8:9], v[8:9], v[72:73]\n"
  "v_pk_mul_f32 v[10:11], v[10:11], v[74:75]\n"
  "v_pk_mul_f32 v[12:13], v[12:13], v[76:77]\n"
  "v_pk_mul_f32 v[14:15], v[14:15], v[78:79]\n"
  "v_cvt_pk_bf16_f32 v96, v0, v1\n"
  "v_cvt_pk_bf16_f32 v97, v2, v3\n"
  "v_cvt_pk_bf16_f32 v98, v4, v5\n"
  "v_cvt_pk_bf16_f32 v99, v6, v7\n"
  "v_cvt_pk_bf16_f32 v100, v8, v9\n"
  "v_cvt_pk_bf16_f32 v101, v10, v11\n"
  "v_cvt_pk_bf16_f32 v102, v12, v13\n"
  "v_cvt_pk_bf16_f32 v103, v14, v15\n"
  "s_add_u32 s97, s92, 0x8000\n"
  "buffer_store_dwordx4 v[96:99], v244, s[48:51], s92 offen sc1\n"
  "buffer_store_dwordx4 v[100:103], v244, s[48:51], s97 offen sc1\n"
  "s_nop 0\n"
  "s_waitcnt vmcnt(16)\n"
  "ds_read_b64_tr_b16 a[0:1], v180 offset:20480\n"
  "ds_read_b64_tr_b16 a[2:3], v180 offset:22528\n"
  "ds_read_b64_tr_b16 a[4:5], v181 offset:20480\n"
  "ds_read_b64_tr_b16 a[6:7], v181 offset:22528\n"
  "ds_read_b64_tr_b16 a[8:9], v182 offset:20480\n"
  "ds_read_b64_tr_b16 a[10:11], v182 offset:22528\n"
  "ds_read_b64_tr_b16 a[12:13], v183 offset:20480\n"
  "ds_read_b64_tr_b16 a[14:15], v183 offset:22528\n"
  "ds_read_b64_tr_b16 a[16:17], v180 offset:0\n"
  "ds_read_b64_tr_b16 a[18:19], v180 offset:2048\n"
  "ds_read_b64_tr_b16 a[20:21], v181 offset:0\n"
  "ds_read_b64_tr_b16 a[22:23], v181 offset:2048\n"
  "ds_read_b64_tr_b16 a[24:25], v182 offset:0\n"
  "ds_read_b64_tr_b16 a[26:27], v182 offset:2048\n"
  "ds_read_b64_tr_b16 a[28:29], v183 offset:0\n"
  "ds_read_b64_tr_b16 a[30:31], v183 offset:2048\n"
  "ds_read_b64_tr_b16 a[32:33], v180 offset:4096\n"
  "ds_read_b64_tr_b16 a[34:35], v180 offset:6144\n"
  "ds_read_b64_tr_b16 a[36:37], v181 offset:4096\n"
  "ds_read_b64_tr_b16 a[38:39], v181 offset:6144\n"
  "ds_read_b64_tr_b16 a[40:41], v182 offset:4096\n"
  "ds_read_b64_tr_b16 a[42:43], v182 offset:6144\n"
  "ds_read_b64_tr_b16 a[44:45], v183 offset:4096\n"
  "ds_read_b64_tr_b16 a[46:47], v183 offset:6144\n"
  "ds_read_b64_tr_b16 a[48:49], v180 offset:8192\n"
  "ds_read_b64_tr_b16 a[50:51], v180 offset:10240\n"
  "ds_read_b64_tr_b16 a[52:53], v181 offset:8192\n"
  "ds_read_b64_tr_b16 a[54:55], v181 offset:10240\n"
  "ds_read_b64_tr_b16 a[56:57], v182 offset:8192\n"
  "ds_read_b64_tr_b16 a[58:59], v182 offset:10240\n"
  "ds_read_b64_tr_b16 a[60:61], v183 offset:8192\n"
  "ds_read_b64_tr_b16 a[62:63], v183 offset:10240\n"
  "v_cmp_gt_u32 vcc, 4, v239\n"
  "v_mov_b32 v232, 0x7fff0000\n"
  "v_lshlrev_b32 v242, 7, v239\n"
  "v_cndmask_b32 v232, v232, v242, vcc\n"
  "buffer_load_dwordx4 v150, s[68:71], s59 offen offset:2048 lds\n"
  "buffer_load_dwordx4 v151, s[68:71], s59 offen offset:3072 lds\n"
  "s_add_u32 s59, s18, 0x100000\n"
  "s_add_u32 m0, s12, 8192\n"
  "s_nop 0\n"
  "buffer_load_dwordx4 v148, s[68:71], s59 offen offset:0 lds\n"
  "buffer_load_dwordx4 v149, s[68:71], s59 offen offset:1024 lds\n"
  "s_waitcnt vmcnt(4)\n"
  "buffer_atomic_add v248, v232, s[48:51], s93 offen sc1\n"
  "s_nop 0\n"
  "s_waitcnt vmcnt(8)\n"
  "buffer_load_dwordx4 v150, s[68:71], s59 offen offset:2048 lds\n"
  "s_waitcnt vmcnt(8)\n"
  "buffer_load_dwordx4 v151, s[68:71], s59 offen offset:3072 lds\n"
  "s_add_u32 s59, s18, 0x120000\n"
  "s_add_u32 m0, s12, 12288\n"
  "s_nop 0\n"
  "s_waitcnt vmcnt(8)\n"
  "buffer_load_dwordx4 v148, s[68:71], s59 offen offset:0 lds\n"
  "s_waitcnt vmcnt(8)\n"
  "buffer_load_dwordx4 v149, s[68:71], s59 offen offset:1024 lds\n"
  "s_waitcnt vmcnt(8)\n"
  "buffer_load_dwordx4 v150, s[68:71], s59 offen offset:2048 lds\n"
  "s_waitcnt vmcnt(8)\n"
  "buffer_load_dwordx4 v151, s[68:71], s59 offen offset:3072 lds\n"
  "mk_mlp2:\n"
  "mk_tp20:\n"
  "s_cbranch_execz mk_tp24\n"
  "s_mov_b64 s[44:45], s[26:27]\n"
  "s_mov_b32 s46, 4096\n"
  "s_lshl_b32 s61, s73, 5\n"
  "v_and_b32 v243, 15, v239\n"
  "v_lshlrev_b32 v242, 1, v243\n"
  "buffer_load_ushort v160, v242, s[44:47], s61 offen\n"
  "v_cmp_gt_u32 vcc, 2, v239\n"
  "v_mov_b32 v232, 0x7fff0000\n"
  "v_lshlrev_b32 v242, 9, v239\n"
  "v_cndmask_b32 v232, v232, v242, vcc\n"
  "s_waitcnt lgkmcnt(0)\n"
  "s_add_u32 s59, s18, 0x140000\n"
  "s_add_u32 m0, s13, 20480\n"
  "s_nop 0\n"
  "s_waitcnt vmcnt(8)\n"
  "buffer_load_dwordx4 v148, s[68:71], s59 offen offset:0 lds\n"
  "s_waitcnt vmcnt(8)\n"
  "buffer_load_dwordx4 v149, s[68:71], s59 offen offset:1024 lds\n"
  "s_waitcnt vmcnt(8)\n"
  "buffer_load_dwordx4 v150, s[68:71], s59 offen offset:2048 lds\n"
  "s_waitcnt vmcnt(8)\n"
  "buffer_load_dwordx4 v151, s[68:71], s59 offen offset:3072 lds\n"
  "s_add_u32 s59, s18, 0x160000\n"
  "s_add_u32 m0, s13, 0\n"
  "s_nop 0\n"
  "s_waitcnt vmcnt(8)\n"
  "buffer_load_dwordx4 v148, s[68:71], s59 offen offset:0 lds\n"
  "s_waitcnt vmcnt(8)\n"
  "buffer_load_dwordx4 v149, s[68:71], s59 offen offset:1024 lds\n"
  "s_waitcnt vmcnt(8)\n"
  "buffer_load_dwordx4 v150, s[68:71], s59 offen offset:2048 lds\n"
  "s_waitcnt vmcnt(8)\n"
  "buffer_load_dwordx4 v151, s[68:71], s59 offen offset:3072 lds\n"
  "v_mov_b32 v233, 0\n"
  "buffer_atomic_add v233, v232, s[48:51], s94 offen sc0 sc1\n"
  "s_add_u32 s59, s18, 0x180000\n"
  "s_add_u32 m0, s13, 4096\n"
  "s_nop 0\n"
  "buffer_load_dwordx4 v148, s[68:71], s59 offen offset:0 lds\n"
  "buffer_load_dwordx4 v149, s[68:71], s59 offen offset:1024 lds\n"
  "buffer_load_dwordx4 v150, s[68:71], s59 offen offset:2048 lds\n"
  "buffer_load_dwordx4 v151, s[68:71], s59 offen offset:3072 lds\n"
  "s_and_b32 s61, s98, 1\n"
  "s_lshl_b32 s61, s61, 2\n"
  "v_and_b32 v242, 3, v239\n"
  "v_add_u32 v242, s61, v242\n"
  "v_lshrrev_b32 v245, 2, v243\n"
  "v_cmp_eq_u32 vcc, v245, v240\n"
  "s_nop 1\n"
  "v_cndmask_b32 v242, 64, v242, vcc\n"
  "v_and_b32 v245, 1, v242\n"
  "v_lshlrev_b32 v245, 4, v245\n"
  "v_mov_b32 v246, 0x3f80\n"
  "v_lshlrev_b32 v245, v245, v246\n"
  "v_lshrrev_b32 v246, 1, v242\n"
  "v_cmp_eq_u32 vcc, 0, v246\n"
  "s_nop 1\n"
  "v_cndmask_b32 v168, 0, v245, vcc\n"
  "v_cmp_eq_u32 vcc, 1, v246\n"
  "s_nop 1\n"
  "v_cndmask_b32 v169, 0, v245, vcc\n"
  "v_cmp_eq_u32 vcc, 2, v246\n"
  "s_nop 1\n"
  "v_cndmask_b32 v170, 0, v245, vcc\n"
  "v_cmp_eq_u32 vcc, 3, v246\n"
  "s_nop 1\n"
  "v_cndmask_b32 v171, 0, v245, vcc\n"
  "v_lshlrev_b32 v243, 10, v243\n"
  "v_and_b32 v242, 1, v240\n"
  "v_lshl_add_u32 v243, v242, 5, v243\n"
  "v_lshrrev_b32 v242, 1, v240\n"
  "v_lshl_add_u32 v234, v242, 4, v243\n"
  "v_bfe_u32 v242, v239, 3, 1\n"
  "v_mul_u32_u24 v242, 8128, v242\n"
  "v_sub_u32 v234, v234, v242\n"
  "s_mov_b64 s[44:45], s[28:29]\n"
  "s_mov_b32 s46, 0x200000\n"
  "s_waitcnt vmcnt(4)\n"
  "s_nop 0\n"
  "v_cmp_ne_u32 vcc, 14, v233\n"
  "s_and_b32 vcc_lo, vcc_lo, 0x3\n"
  "s_cbranch_scc1 mk_spin6\n"
  "mk_spin6_b:\n"
  "s_nop 0\n"
  "s_add_u32 s61, s95, 0x0\n"
  "buffer_load_dwordx4 a[128:131], v244, s[48:51], s61 offen offset:0 sc1\n"
  "buffer_load_dwordx4 a[132:135], v244, s[48:51], s61 offen offset:1024 sc1\n"
  "buffer_load_dwordx4 a[136:139], v244, s[48:51], s61 offen offset:2048 sc1\n"
  "buffer_load_dwordx4 a[140:143], v244, s[48:51], s61 offen offset:3072 sc1\n"
  "s_add_u32 s61, s95, 0x8000\n"
  "buffer_load_dwordx4 a[144:147], v244, s[48:51], s61 offen offset:0 sc1\n"
  "buffer_load_dwordx4 a[148:151], v244, s[48:51], s61 offen offset:1024 sc1\n"
  "buffer_load_dwordx4 a[152:155], v244, s[48:51], s61 offen offset:2048 sc1\n"
  "buffer_load_dwordx4 a[156:159], v244, s[48:51], s61 offen offset:3072 sc1\n"
  "s_add_u32 s61, s95, 0x10000\n"
  "buffer_load_dwordx4 a[160:163], v244, s[48:51], s61 offen offset:0 sc1\n"
  "buffer_load_dwordx4 a[164:167], v244, s[48:51], s61 offen offset:1024 sc1\n"
  "buffer_load_dwordx4 a[168:171], v244, s[48:51], s61 offen offset:2048 sc1\n"
  "buffer_load_dwordx4 a[172:175], v244, s[48:51], s61 offen offset:3072 sc1\n"
  "s_add_u32 s61, s95, 0x18000\n"
  "buffer_load_dwordx4 a[176:179], v244, s[48:51], s61 offen offset:0 sc1\n"
  "buffer_load_dwordx4 a[180:183], v244, s[48:51], s61 offen offset:1024 sc1\n"
  "buffer_load_dwordx4 a[184:187], v244, s[48:51], s61 offen offset:2048 sc1\n"
  "buffer_load_dwordx4 a[188:191], v244, s[48:51], s61 offen offset:3072 sc1\n"
  "v_lshlrev_b32 v161, 16, v160\n"
  "v_lshlrev_b32 v162, 16, v160\n"
  "v_lshlrev_b32 v163, 16, v160\n"
  "v_lshlrev_b32 v160, 16, v160\n"
  "s_waitcnt vmcnt(12)\n"
  "v_mfma_f32_16x16x32_bf16 v[16:19], a[128:131], a[4:7], 0\n"
  "s_add_u32 s61, s95, 0x20000\n"
  "v_mfma_f32_16x16x32_bf16 v[20:23], a[132:135], a[4:7], 0\n"
  "buffer_load_dwordx4 a[192:195], v244, s[48:51], s61 offen offset:0 sc1\n"
  "v_mfma_f32_16x16x32_bf16 v[24:27], a[136:139], a[4:7], 0\n"
  "v_mfma_f32_16x16x32_bf16 v[28:31], a[140:143], a[4:7], 0\n"
  "buffer_load_dwordx4 a[196:199], v244, s[48:51], s61 offen offset:1024 sc1\n"
  "v_mfma_f32_16x16x32_bf16 v[32:35], a[128:131], a[8:11], 0\n"
  "v_mfma_f32_16x16x32_bf16 v[36:39], a[132:135], a[8:11], 0\n"
  "buffer_load_dwordx4 a[200:203], v244, s[48:51], s61 offen offset:2048 sc1\n"
  "v_mfma_f32_16x16x32_bf16 v[40:43], a[136:139], a[8:11], 0\n"
  "v_mfma_f32_16x16x32_bf16 v[44:47], a[140:143], a[8:11], 0\n"
  "buffer_load_dwordx4 a[204:207], v244, s[48:51], s61 offen offset:3072 sc1\n"
  "v_mfma_f32_16x16x32_bf16 v[48:51], a[128:131], a[12:15], 0\n"
  "s_add_u32 s59, s18, 0x1a0000\n"
  "v_mfma_f32_16x16x32_bf16 v[52:55], a[132:135], a[12:15], 0\n"
  "s_add_u32 m0, s13, 8192\n"
  "v_mfma_f32_16x16x32_bf16 v[56:59], a[136:139], a[12:15], 0\n"
  "buffer_load_dwordx4 v148, s[68:71], s59 offen offset:0 lds\n"
  "v_mfma_f32_16x16x32_bf16 v[60:63], a[140:143], a[12:15], 0\n"
  "buffer_load_dwordx4 v149, s[68:71], s59 offen offset:1024 lds\n"
  "v_mfma_f32_16x16x32_bf16 v[0:3], a[128:131], a[0:3], v[160:163]\n"
  "v_mfma_f32_16x16x32_bf16 v[4:7], a[132:135], a[0:3], v[160:163]\n"
  "buffer_load_dwordx4 v150, s[68:71], s59 offen offset:2048 lds\n"
  "v_mfma_f32_16x16x32_bf16 v[8:11], a[136:139], a[0:3], v[160:163]\n"
  "v_mfma_f32_16x16x32_bf16 v[12:15], a[140:143], a[0:3], v[160:163]\n"
  "s_waitcnt vmcnt(15)\n"
  "buffer_load_dwordx4 v151, s[68:71], s59 offen offset:3072 lds\n"
  "v_mfma_f32_16x16x32_bf16 v[16:19], a[144:147], a[20:23], v[16:19]\n"
  "s_add_u32 s61, s95, 0x28000\n"
  "v_mfma_f32_16x16x32_bf16 v[20:23], a[148:151], a[20:23], v[20:23]\n"
  "buffer_load_dwordx4 a[208:211], v244, s[48:51], s61 offen offset:0 sc1\n"
  "v_mfma_f32_16x16x32_bf16 v[24:27], a[152:155], a[20:23], v[24:27]\n"
  "v_mfma_f32_16x16x32_bf16 v[28:31], a[156:159], a[20:23], v[28:31]\n"
  "v_mfma_f32_16x16x32_bf16 v[32:35], a[144:147], a[24:27], v[32:35]\n"
  "v_mfma_f32_16x16x32_bf16 v[36:39], a[148:151], a[24:27], v[36:39]\n"
  "buffer_load_dwordx4 a[212:215], v244, s[48:51], s61 offen offset:1024 sc1\n"
  "v_mfma_f32_16x16x32_bf16 v[40:43], a[152:155], a[24:27], v[40:43]\n"
  "v_mfma_f32_16x16x32_bf16 v[44:47], a[156:159], a[24:27], v[44:47]\n"
  "v_mfma_f32_16x16x32_bf16 v[48:51], a[144:147], a[28:31], v[48:51]\n"
  "v_mfma_f32_16x16x32_bf16 v[52:55], a[148:151], a[28:31], v[52:55]\n"
  "buffer_load_dwordx4 a[216:219], v244, s[48:51], s61 offen offset:2048 sc1\n"
  "v_mfma_f32_16x16x32_bf16 v[56:59], a[152:155], a[28:31], v[56:59]\n"
  "v_mfma_f32_16x16x32_bf16 v[60:63], a[156:159], a[28:31], v[60:63]\n"
  "v_mfma_f32_16x16x32_bf16 v[0:3], a[144:147], a[16:19], v[0:3]\n"
  "v_mfma_f32_16x16x32_bf16 v[4:7], a[148:151], a[16:19], v[4:7]\n"
  "buffer_load_dwordx4 a[220:223], v244, s[48:51], s61 offen offset:3072 sc1\n"
  "v_mfma_f32_16x16x32_bf16 v[8:11], a[152:155], a[16:19], v[8:11]\n"
  "s_add_u32 s61, s95, 0x30000\n"
  "v_mfma_f32_16x16x32_bf16 v[12:15], a[156:159], a[16:19], v[12:15]\n"
  "s_waitcnt vmcnt(16)\n"
  "v_mfma_f32_16x16x32_bf16 v[16:19], a[160:163], a[36:39], v[16:19]\n"
  "v_mfma_f32_16x16x32_bf16 v[20:23], a[164:167], a[36:39], v[20:23]\n"
  "buffer_load_dwordx4 a[224:227], v244, s[48:51], s61 offen offset:0 sc1\n"
  "v_mfma_f32_16x16x32_bf16 v[24:27], a[168:171], a[36:39], v[24:27]\n"
  "v_mfma_f32_16x16x32_bf16 v[28:31], a[172:175], a[36:39], v[28:31]\n"
  "v_mfma_f32_16x16x32_bf16 v[32:35], a[160:163], a[40:43], v[32:35]\n"
  "v_mfma_f32_16x16x32_bf16 v[36:39], a[164:167], a[40:43], v[36:39]\n"
  "buffer_load_dwordx4 a[228:231], v244, s[48:51], s61 offen offset:1024 sc1\n"
  "v_mfma_f32_16x16x32_bf16 v[40:43], a[168:171], a[40:43], v[40:43]\n"
  "v_mfma_f32_16x16x32_bf16 v[44:47], a[172:175], a[40:43], v[44:47]\n"
  "v_mfma_f32_16x16x32_bf16 v[48:51], a[160:163], a[44:47], v[48:51]\n"
  "v_mfma_f32_16x16x32_bf16 v[52:55], a[164:167], a[44:47], v[52:55]\n"
  "buffer_load_dwordx4 a[232:235], v244, s[48:51], s61 offen offset:2048 sc1\n"
  "v_mfma_f32_16x16x32_bf16 v[56:59], a[168:171], a[44:47], v[56:59]\n"
  "v_mfma_f32_16x16x32_bf16 v[60:63], a[172:175], a[44:47], v[60:63]\n"
  "v_mfma_f32_16x16x32_bf16 v[0:3], a[160:163], a[32:35], v[0:3]\n"
  "v_mfma_f32_16x16x32_bf16 v[4:7], a[164:167], a[32:35], v[4:7]\n"
  "mk_tp21:\n"
  "s_cbranch_execz mk_tp25\n"
  "buffer_load_dwordx4 a[236:239], v244, s[48:51], s61 offen offset:3072 sc1\n"
  "v_mfma_f32_16x16x32_bf16 v[8:11], a[168:171], a[32:35], v[8:11]\n"
  "s_add_u32 s61, s19, 0x38000\n"
  "v_mfma_f32_16x16x32_bf16 v[12:15], a[172:175], a[32:35], v[12:15]\n"
  "s_waitcnt vmcnt(16)\n"
  "ds_read_b64_tr_b16 v[196:197], v181 offset:12288\n"
  "v_mfma_f32_16x16x32_bf16 v[16:19], a[176:179], a[52:55], v[16:19]\n"
  "ds_read_b64_tr_b16 v[198:199], v181 offset:14336\n"
  "v_mfma_f32_16x16x32_bf16 v[20:23], a[180:183], a[52:55], v[20:23]\n"
  "buffer_load_dwordx4 a[240:243], v244, s[48:51], s61 offen offset:0 sc1\n"
  "v_mfma_f32_16x16x32_bf16 v[24:27], a[184:187], a[52:55], v[24:27]\n"
  "ds_read_b64_tr_b16 v[200:201], v182 offset:12288\n"
  "ds_read_b64_tr_b16 v[202:203], v182 offset:14336\n"
  "v_mfma_f32_16x16x32_bf16 v[28:31], a[188:191], a[52:55], v[28:31]\n"
  "ds_read_b64_tr_b16 v[204:205], v183 offset:12288\n"
  "v_mfma_f32_16x16x32_bf16 v[32:35], a[176:179], a[56:59], v[32:35]\n"
  "ds_read_b64_tr_b16 v[206:207], v183 offset:14336\n"
  "v_mfma_f32_16x16x32_bf16 v[36:39], a[180:183], a[56:59], v[36:39]\n"
  "buffer_load_dwordx4 a[244:247], v244, s[48:51], s61 offen offset:1024 sc1\n"
  "v_mfma_f32_16x16x32_bf16 v[40:43], a[184:187], a[56:59], v[40:43]\n"
  "ds_read_b64_tr_b16 v[192:193], v180 offset:12288\n"
  "ds_read_b64_tr_b16 v[194:195], v180 offset:14336\n"
  "v_mfma_f32_16x16x32_bf16 v[44:47], a[188:191], a[56:59], v[44:47]\n"
  "v_mfma_f32_16x16x32_bf16 v[48:51], a[176:179], a[60:63], v[48:51]\n"
  "v_mfma_f32_16x16x32_bf16 v[52:55], a[180:183], a[60:63], v[52:55]\n"
  "buffer_load_dwordx4 a[248:251], v244, s[48:51], s61 offen offset:2048 sc1\n"
  "v_mfma_f32_16x16x32_bf16 v[56:59], a[184:187], a[60:63], v[56:59]\n"
  "v_mfma_f32_16x16x32_bf16 v[60:63], a[188:191], a[60:63], v[60:63]\n"
  "v_mfma_f32_16x16x32_bf16 v[0:3], a[176:179], a[48:51], v[0:3]\n"
  "v_mfma_f32_16x16x32_bf16 v[4:7], a[180:183], a[48:51], v[4:7]\n"
  "buffer_load_dwordx4 a[252:255], v244, s[48:51], s61 offen offset:3072 sc1\n"
  "v_mfma_f32_16x16x32_bf16 v[8:11], a[184:187], a[48:51], v[8:11]\n"
  "s_add_u32 s61, s19, 0x40000\n"
  "v_mfma_f32_16x16x32_bf16 v[12:15], a[188:191], a[48:51], v[12:15]\n"
  "s_waitcnt lgkmcnt(0)\n"
  "s_waitcnt vmcnt(16)\n"
  "ds_read_b64_tr_b16 v[212:213], v181 offset:16384\n"
  "v_mfma_f32_16x16x32_bf16 v[16:19], a[192:195], v[196:199], v[16:19]\n"
  "ds_read_b64_tr_b16 v[214:215], v181 offset:18432\n"
  "v_mfma_f32_16x16x32_bf16 v[20:23], a[196:199], v[196:199], v[20:23]\n"
  "buffer_load_dwordx4 a[128:131], v244, s[48:51], s61 offen offset:0 sc1\n"
  "v_mfma_f32_16x16x32_bf16 v[24:27], a[200:203], v[196:199], v[24:27]\n"
  "ds_read_b64_tr_b16 v[216:217], v182 offset:16384\n"
  "ds_read_b64_tr_b16 v[218:219], v182 offset:18432\n"
  "v_mfma_f32_16x16x32_bf16 v[28:31], a[204:207], v[196:199], v[28:31]\n"
  "ds_read_b64_tr_b16 v[220:221], v183 offset:16384\n"
  "v_mfma_f32_16x16x32_bf16 v[32:35], a[192:195], v[200:203], v[32:35]\n"
  "ds_read_b64_tr_b16 v[222:223], v183 offset:18432\n"
  "v_mfma_f32_16x16x32_bf16 v[36:39], a[196:199], v[200:203], v[36:39]\n"
  "buffer_load_dwordx4 a[132:135], v244, s[48:51], s61 offen offset:1024 sc1\n"
  "v_mfma_f32_16x16x32_bf16 v[40:43], a[200:203], v[200:203], v[40:43]\n"
  "ds_read_b64_tr_b16 v[208:209], v180 offset:16384\n"
  "ds_read_b64_tr_b16 v[210:211], v180 offset:18432\n"
  "v_mfma_f32_16x16x32_bf16 v[44:47], a[204:207], v[200:203], v[44:47]\n"
  "v_mfma_f32_16x16x32_bf16 v[48:51], a[192:195], v[204:207], v[48:51]\n"
  "v_mfma_f32_16x16x32_bf16 v[52:55], a[196:199], v[204:207], v[52:55]\n"
  "buffer_load_dwordx4 a[136:139], v244, s[48:51], s61 offen offset:2048 sc1\n"
  "v_mfma_f32_16x16x32_bf16 v[56:59], a[200:203], v[204:207], v[56:59]\n"
  "v_mfma_f32_16x16x32_bf16 v[60:63], a[204:207], v[204:207], v[60:63]\n"
  "v_mfma_f32_16x16x32_bf16 v[0:3], a[192:195], v[192:195], v[0:3]\n"
  "v_mfma_f32_16x16x32_bf16 v[4:7], a[196:199], v[192:195], v[4:7]\n"
  "buffer_load_dwordx4 a[140:143], v244, s[48:51], s61 offen offset:3072 sc1\n"
  "v_mfma_f32_16x16x32_bf16 v[8:11], a[200:203], v[192:195], v[8:11]\n"
  "s_add_u32 s61, s19, 0x48000\n"
  "v_mfma_f32_16x16x32_bf16 v[12:15], a[204:207], v[192:195], v[12:15]\n"
  "s_waitcnt lgkmcnt(0)\n"
  "s_waitcnt vmcnt(12)\n"
  "ds_read_b64_tr_b16 v[196:197], v177 offset:0\n"
  "v_mfma_f32_16x16x32_bf16 v[16:19], a[208:211], v[212:215], v[16:19]\n"
  "ds_read_b64_tr_b16 v[198:199], v177 offset:2048\n"
  "v_mfma_f32_16x16x32_bf16 v[20:23], a[212:215], v[212:215], v[20:23]\n"
  "buffer_load_dwordx4 a[144:147], v244, s[48:51], s61 offen offset:0 sc1\n"
  "v_mfma_f32_16x16x32_bf16 v[24:27], a[216:219], v[212:215], v[24:27]\n"
  "ds_read_b64_tr_b16 v[200:201], v178 offset:0\n"
  "ds_read_b64_tr_b16 v[202:203], v178 offset:2048\n"
  "v_mfma_f32_16x16x32_bf16 v[28:31], a[220:223], v[212:215], v[28:31]\n"
  "ds_read_b64_tr_b16 v[204:205], v179 offset:0\n"
  "v_mfma_f32_16x16x32_bf16 v[32:35], a[208:211], v[216:219], v[32:35]\n"
  "ds_read_b64_tr_b16 v[206:207], v179 offset:2048\n"
  "v_mfma_f32_16x16x32_bf16 v[36:39], a[212:215], v[216:219], v[36:39]\n"
  "buffer_load_dwordx4 a[148:151], v244, s[48:51], s61 offen offset:1024 sc1\n"
  "v_mfma_f32_16x16x32_bf16 v[40:43], a[216:219], v[216:219], v[40:43]\n"
  "ds_read_b64_tr_b16 v[192:193], v176 offset:0\n"
  "ds_read_b64_tr_b16 v[194:195], v176 offset:2048\n"
  "v_mfma_f32_16x16x32_bf16 v[44:47], a[220:223], v[216:219], v[44:47]\n"
  "v_mfma_f32_16x16x32_bf16 v[48:51], a[208:211], v[220:223], v[48:51]\n"
  "v_mfma_f32_16x16x32_bf16 v[52:55], a[212:215], v[220:223], v[52:55]\n"
  "buffer_load_dwordx4 a[152:155], v244, s[48:51], s61 offen offset:2048 sc1\n"
  "v_mfma_f32_16x16x32_bf16 v[56:59], a[216:219], v[220:223], v[56:59]\n"
  "v_mfma_f32_16x16x32_bf16 v[60:63], a[220:223], v[220:223], v[60:63]\n"
  "v_mfma_f32_16x16x32_bf16 v[0:3], a[208:211], v[208:211], v[0:3]\n"
  "v_mfma_f32_16x16x32_bf16 v[4:7], a[212:215], v[208:211], v[4:7]\n"
  "buffer_load_dwordx4 a[156:159], v244, s[48:51], s61 offen offset:3072 sc1\n"
  "v_mfma_f32_16x16x32_bf16 v[8:11], a[216:219], v[208:211], v[8:11]\n"
  "s_add_u32 s61, s19, 0x50000\n"
  "v_mfma_f32_16x16x32_bf16 v[12:15], a[220:223], v[208:211], v[12:15]\n"
  "s_waitcnt lgkmcnt(0)\n"
  "s_waitcnt vmcnt(12)\n"
  "ds_read_b64_tr_b16 v[212:213], v177 offset:4096\n"
  "v_mfma_f32_16x16x32_bf16 v[16:19], a[224:227], v[196:199], v[16:19]\n"
  "ds_read_b64_tr_b16 v[214:215], v177 offset:6144\n"
  "v_mfma_f32_16x16x32_bf16 v[20:23], a[228:231], v[196:199], v[20:23]\n"
  "buffer_load_dwordx4 a[160:163], v244, s[48:51], s61 offen offset:0 sc1\n"
  "v_mfma_f32_16x16x32_bf16 v[24:27], a[232:235], v[196:199], v[24:27]\n"
  "ds_read_b64_tr_b16 v[216:217], v178 offset:4096\n"
  "ds_read_b64_tr_b16 v[218:219], v178 offset:6144\n"
  "v_mfma_f32_16x16x32_bf16 v[28:31], a[236:239], v[196:199], v[28:31]\n"
  "ds_read_b64_tr_b16 v[220:221], v179 offset:4096\n"
  "v_mfma_f32_16x16x32_bf16 v[32:35], a[224:227], v[200:203], v[32:35]\n"
  "ds_read_b64_tr_b16 v[222:223], v179 offset:6144\n"
  "v_mfma_f32_16x16x32_bf16 v[36:39], a[228:231], v[200:203], v[36:39]\n"
  "buffer_load_dwordx4 a[164:167], v244, s[48:51], s61 offen offset:1024 sc1\n"
  "v_mfma_f32_16x16x32_bf16 v[40:43], a[232:235], v[200:203], v[40:43]\n"
  "ds_read_b64_tr_b16 v[208:209], v176 offset:4096\n"
  "ds_read_b64_tr_b16 v[210:211], v176 offset:6144\n"
  "v_mfma_f32_16x16x32_bf16 v[44:47], a[236:239], v[200:203], v[44:47]\n"
  "v_mfma_f32_16x16x32_bf16 v[48:51], a[224:227], v[204:207], v[48:51]\n"
  "v_mfma_f32_16x16x32_bf16 v[52:55], a[228:231], v[204:207], v[52:55]\n"
  "buffer_load_dwordx4 a[168:171], v244, s[48:51], s61 offen offset:2048 sc1\n"
  "v_mfma_f32_16x16x32_bf16 v[56:59], a[232:235], v[204:207], v[56:59]\n"
  "v_mfma_f32_16x16x32_bf16 v[60:63], a[236:239], v[204:207], v[60:63]\n"
  "v_mfma_f32_16x16x32_bf16 v[0:3], a[224:227], v[192:195], v[0:3]\n"
  "v_mfma_f32_16x16x32_bf16 v[4:7], a[228:231], v[192:195], v[4:7]\n"
  "buffer_load_dwordx4 a[172:175], v244, s[48:51], s61 offen offset:3072 sc1\n"
  "v_mfma_f32_16x16x32_bf16 v[8:11], a[232:235], v[192:195], v[8:11]\n"
  "s_add_u32 s61, s19, 0x58000\n"
  "v_mfma_f32_16x16x32_bf16 v[12:15], a[236:239], v[192:195], v[12:15]\n"
  "s_waitcnt lgkmcnt(0)\n"
  "s_waitcnt vmcnt(12)\n"
  "ds_read_b64_tr_b16 v[196:197], v177 offset:8192\n"
  "v_mfma_f32_16x16x32_bf16 v[16:19], a[240:243], v[212:215], v[16:19]\n"
  "ds_read_b64_tr_b16 v[198:199], v177 offset:10240\n"
  "v_mfma_f32_16x16x32_bf16 v[20:23], a[244:247], v[212:215], v[20:23]\n"
  "buffer_load_dwordx4 a[176:179], v244, s[48:51], s61 offen offset:0 sc1\n"
  "v_mfma_f32_16x16x32_bf16 v[24:27], a[248:251], v[212:215], v[24:27]\n"
  "ds_read_b64_tr_b16 v[200:201], v178 offset:8192\n"
  "ds_read_b64_tr_b16 v[202:203], v178 offset:10240\n"
  "v_mfma_f32_16x16x32_bf16 v[28:31], a[252:255], v[212:215], v[28:31]\n"
  "ds_read_b64_tr_b16 v[204:205], v179 offset:8192\n"
  "v_mfma_f32_16x16x32_bf16 v[32:35], a[240:243], v[216:219], v[32:35]\n"
  "ds_read_b64_tr_b16 v[206:207], v179 offset:10240\n"
  "v_mfma_f32_16x16x32_bf16 v[36:39], a[244:247], v[216:219], v[36:39]\n"
  "buffer_load_dwordx4 a[180:183], v244, s[48:51], s61 offen offset:1024 sc1\n"
  "v_mfma_f32_16x16x32_bf16 v[40:43], a[248:251], v[216:219], v[40:43]\n"
  "ds_read_b64_tr_b16 v[192:193], v176 offset:8192\n"
  "ds_read_b64_tr_b16 v[194:195], v176 offset:10240\n"
  "v_mfma_f32_16x16x32_bf16 v[44:47], a[252:255], v[216:219], v[44:47]\n"
  "v_mfma_f32_16x16x32_bf16 v[48:51], a[240:243], v[220:223], v[48:51]\n"
  "v_mfma_f32_16x16x32_bf16 v[52:55], a[244:247], v[220:223], v[52:55]\n"
  "buffer_load_dwordx4 a[184:187], v244, s[48:51], s61 offen offset:2048 sc1\n"
  "v_mfma_f32_16x16x32_bf16 v[56:59], a[248:251], v[220:223], v[56:59]\n"
  "v_mfma_f32_16x16x32_bf16 v[60:63], a[252:255], v[220:223], v[60:63]\n"
  "v_mfma_f32_16x16x32_bf16 v[0:3], a[240:243], v[208:211], v[0:3]\n"
  "v_mfma_f32_16x16x32_bf16 v[4:7], a[244:247], v[208:211], v[4:7]\n"
  "buffer_load_dwordx4 a[188:191], v244, s[48:51], s61 offen offset:3072 sc1\n"
  "v_mfma_f32_16x16x32_bf16 v[8:11], a[248:251], v[208:211], v[8:11]\n"
  "s_add_u32 s61, s19, 0x60000\n"
  "v_mfma_f32_16x16x32_bf16 v[12:15], a[252:255], v[208:211], v[12:15]\n"
  "s_waitcnt lgkmcnt(0)\n"
  "s_waitcnt vmcnt(12)\n"
  "ds_read_b64_tr_b16 v[212:213], v177 offset:12288\n"
  "v_mfma_f32_16x16x32_bf16 v[16:19], a[128:131], v[196:199], v[16:19]\n"
  "ds_read_b64_tr_b16 v[214:215], v177 offset:14336\n"
  "v_mfma_f32_16x16x32_bf16 v[20:23], a[132:135], v[196:199], v[20:23]\n"
  "buffer_load_dwordx4 a[192:195], v244, s[48:51], s61 offen offset:0 sc1\n"
  "v_mfma_f32_16x16x32_bf16 v[24:27], a[136:139], v[196:199], v[24:27]\n"
  "ds_read_b64_tr_b16 v[216:217], v178 offset:12288\n"
  "ds_read_b64_tr_b16 v[218:219], v178 offset:14336\n"
  "v_mfma_f32_16x16x32_bf16 v[28:31], a[140:143], v[196:199], v[28:31]\n"
  "ds_read_b64_tr_b16 v[220:221], v179 offset:12288\n"
  "v_mfma_f32_16x16x32_bf16 v[32:35], a[128:131], v[200:203], v[32:35]\n"
  "ds_read_b64_tr_b16 v[222:223], v179 offset:14336\n"
  "v_mfma_f32_16x16x32_bf16 v[36:39], a[132:135], v[200:203], v[36:39]\n"
  "buffer_load_dwordx4 a[196:199], v244, s[48:51], s61 offen offset:1024 sc1\n"
  "v_mfma_f32_16x16x32_bf16 v[40:43], a[136:139], v[200:203], v[40:43]\n"
  "ds_read_b64_tr_b16 v[208:209], v176 offset:12288\n"
  "ds_read_b64_tr_b16 v[210:211], v176 offset:14336\n"
  "v_mfma_f32_16x16x32_bf16 v[44:47], a[140:143], v[200:203], v[44:47]\n"
  "v_mfma_f32_16x16x32_bf16 v[48:51], a[128:131], v[204:207], v[48:51]\n"
  "v_mfma_f32_16x16x32_bf16 v[52:55], a[132:135], v[204:207], v[52:55]\n"
  "buffer_load_dwordx4 a[200:203], v244, s[48:51], s61 offen offset:2048 sc1\n"
  "v_mfma_f32_16x16x32_bf16 v[56:59], a[136:139], v[204:207], v[56:59]\n"
  "v_mfma_f32_16x16x32_bf16 v[60:63], a[140:143], v[204:207], v[60:63]\n"
  "v_mfma_f32_16x16x32_bf16 v[0:3], a[128:131], v[192:195], v[0:3]\n"
  "v_mfma_f32_16x16x32_bf16 v[4:7], a[132:135], v[192:195], v[4:7]\n"
  "buffer_load_dwordx4 a[204:207], v244, s[48:51], s61 offen offset:3072 sc1\n"
  "v_mfma_f32_16x16x32_bf16 v[8:11], a[136:139], v[192:195], v[8:11]\n"
  "s_add_u32 s61, s19, 0x68000\n"
  "v_mfma_f32_16x16x32_bf16 v[12:15], a[140:143], v[192:195], v[12:15]\n"
  "s_waitcnt lgkmcnt(0)\n"
  "s_waitcnt vmcnt(12)\n"
  "ds_read_b64_tr_b16 v[196:197], v181 offset:20480\n"
  "v_mfma_f32_16x16x32_bf16 v[16:19], a[144:147], v[212:215], v[16:19]\n"
  "ds_read_b64_tr_b16 v[198:199], v181 offset:22528\n"
  "v_mfma_f32_16x16x32_bf16 v[20:23], a[148:151], v[212:215], v[20:23]\n"
  "buffer_load_dwordx4 a[208:211], v244, s[48:51], s61 offen offset:0 sc1\n"
  "v_mfma_f32_16x16x32_bf16 v[24:27], a[152:155], v[212:215], v[24:27]\n"
  "ds_read_b64_tr_b16 v[200:201], v182 offset:20480\n"
  "ds_read_b64_tr_b16 v[202:203], v182 offset:22528\n"
  "v_mfma_f32_16x16x32_bf16 v[28:31], a[156:159], v[212:215], v[28:31]\n"
  "ds_read_b64_tr_b16 v[204:205], v183 offset:20480\n"
  "v_mfma_f32_16x16x32_bf16 v[32:35], a[144:147], v[216:219], v[32:35]\n"
  "ds_read_b64_tr_b16 v[206:207], v183 offset:22528\n"
  "v_mfma_f32_16x16x32_bf16 v[36:39], a[148:151], v[216:219], v[36:39]\n"
  "buffer_load_dwordx4 a[212:215], v244, s[48:51], s61 offen offset:1024 sc1\n"
  "v_mfma_f32_16x16x32_bf16 v[40:43], a[152:155], v[216:219], v[40:43]\n"
  "ds_read_b64_tr_b16 v[192:193], v180 offset:20480\n"
  "ds_read_b64_tr_b16 v[194:195], v180 offset:22528\n"
  "v_mfma_f32_16x16x32_bf16 v[44:47], a[156:159], v[216:219], v[44:47]\n"
  "v_mfma_f32_16x16x32_bf16 v[48:51], a[144:147], v[220:223], v[48:51]\n"
  "v_mfma_f32_16x16x32_bf16 v[52:55], a[148:151], v[220:223], v[52:55]\n"
  "buffer_load_dwordx4 a[216:219], v244, s[48:51], s61 offen offset:2048 sc1\n"
  "v_mfma_f32_16x16x32_bf16 v[56:59], a[152:155], v[220:223], v[56:59]\n"
  "v_mfma_f32_16x16x32_bf16 v[60:63], a[156:159], v[220:223], v[60:63]\n"
  "v_mfma_f32_16x16x32_bf16 v[0:3], a[144:147], v[208:211], v[0:3]\n"
  "v_mfma_f32_16x16x32_bf16 v[4:7], a[148:151], v[208:211], v[4:7]\n"
  "buffer_load_dwordx4 a[220:223], v244, s[48:51], s61 offen offset:3072 sc1\n"
  "v_mfma_f32_16x16x32_bf16 v[8:11], a[152:155], v[208:211], v[8:11]\n"
  "v_mfma_f32_16x16x32_bf16 v[12:15], a[156:159], v[208:211], v[12:15]\n"
  "s_waitcnt lgkmcnt(0)\n"
  "s_waitcnt vmcnt(12)\n"
  "ds_read_b64_tr_b16 v[212:213], v181 offset:0\n"
  "v_mfma_f32_16x16x32_bf16 v[16:19], a[160:163], v[196:199], v[16:19]\n"
  "ds_read_b64_tr_b16 v[214:215], v181 offset:2048\n"
  "v_mfma_f32_16x16x32_bf16 v[20:23], a[164:167], v[196:199], v[20:23]\n"
  "buffer_load_dwordx4 v[128:131], v244, s[48:51], s72 offen offset:0 sc1\n"
  "v_mfma_f32_16x16x32_bf16 v[24:27], a[168:171], v[196:199], v[24:27]\n"
  "ds_read_b64_tr_b16 v[216:217], v182 offset:0\n"
  "ds_read_b64_tr_b16 v[218:219], v182 offset:2048\n"
  "v_mfma_f32_16x16x32_bf16 v[28:31], a[172:175], v[196:199], v[28:31]\n"
  "ds_read_b64_tr_b16 v[220:221], v183 offset:0\n"
  "v_mfma_f32_16x16x32_bf16 v[32:35], a[160:163], v[200:203], v[32:35]\n"
  "ds_read_b64_tr_b16 v[222:223], v183 offset:2048\n"
  "v_mfma_f32_16x16x32_bf16 v[36:39], a[164:167], v[200:203], v[36:39]\n"
  "buffer_load_dwordx4 v[132:135], v244, s[48:51], s72 offen offset:1024 sc1\n"
  "v_mfma_f32_16x16x32_bf16 v[40:43], a[168:171], v[200:203], v[40:43]\n"
  "ds_read_b64_tr_b16 v[208:209], v180 offset:0\n"
  "ds_read_b64_tr_b16 v[210:211], v180 offset:2048\n"
  "v_mfma_f32_16x16x32_bf16 v[44:47], a[172:175], v[200:203], v[44:47]\n"
  "v_mfma_f32_16x16x32_bf16 v[48:51], a[160:163], v[204:207], v[48:51]\n"
  "v_mfma_f32_16x16x32_bf16 v[52:55], a[164:167], v[204:207], v[52:55]\n"
  "buffer_load_dwordx4 v[136:139], v244, s[48:51], s72 offen offset:2048 sc1\n"
  "v_mfma_f32_16x16x32_bf16 v[56:59], a[168:171], v[204:207], v[56:59]\n"
  "v_mfma_f32_16x16x32_bf16 v[60:63], a[172:175], v[204:207], v[60:63]\n"
  "v_mfma_f32_16x16x32_bf16 v[0:3], a[160:163], v[192:195], v[0:3]\n"
  "v_mfma_f32_16x16x32_bf16 v[4:7], a[164:167], v[192:195], v[4:7]\n"
  "buffer_load_dwordx4 v[140:143], v244, s[48:51], s72 offen offset:3072 sc1\n"
  "v_mfma_f32_16x16x32_bf16 v[8:11], a[168:171], v[192:195], v[8:11]\n"
  "v_mfma_f32_16x16x32_bf16 v[12:15], a[172:175], v[192:195], v[12:15]\n"
  "s_waitcnt lgkmcnt(0)\n"
  "s_waitcnt vmcnt(12)\n"
  "ds_read_b64_tr_b16 v[196:197], v181 offset:4096\n"
  "v_mfma_f32_16x16x32_bf16 v[16:19], a[176:179], v[212:215], v[16:19]\n"
  "ds_read_b64_tr_b16 v[198:199], v181 offset:6144\n"
  "v_mfma_f32_16x16x32_bf16 v[20:23], a[180:183], v[212:215], v[20:23]\n"
  "ds_read_b64_tr_b16 v[200:201], v182 offset:4096\n"
  "v_mfma_f32_16x16x32_bf16 v[24:27], a[184:187], v[212:215], v[24:27]\n"
  "ds_read_b64_tr_b16 v[202:203], v182 offset:6144\n"
  "v_mfma_f32_16x16x32_bf16 v[28:31], a[188:191], v[212:215], v[28:31]\n"
  "ds_read_b64_tr_b16 v[204:205], v183 offset:4096\n"
  "v_mfma_f32_16x16x32_bf16 v[32:35], a[176:179], v[216:219], v[32:35]\n"
  "ds_read_b64_tr_b16 v[206:207], v183 offset:6144\n"
  "v_mfma_f32_16x16x32_bf16 v[36:39], a[180:183], v[216:219], v[36:39]\n"
  "ds_read_b64_tr_b16 v[192:193], v180 offset:4096\n"
  "v_mfma_f32_16x16x32_bf16 v[40:43], a[184:187], v[216:219], v[40:43]\n"
  "ds_read_b64_tr_b16 v[194:195], v180 offset:6144\n"
  "v_mfma_f32_16x16x32_bf16 v[44:47], a[188:191], v[216:219], v[44:47]\n"
  "v_mfma_f32_16x16x32_bf16 v[48:51], a[176:179], v[220:223], v[48:51]\n"
  "v_mfma_f32_16x16x32_bf16 v[52:55], a[180:183], v[220:223], v[52:55]\n"
  "v_mfma_f32_16x16x32_bf16 v[56:59], a[184:187], v[220:223], v[56:59]\n"
  "v_mfma_f32_16x16x32_bf16 v[60:63], a[188:191], v[220:223], v[60:63]\n"
  "v_mfma_f32_16x16x32_bf16 v[0:3], a[176:179], v[208:211], v[0:3]\n"
  "v_mfma_f32_16x16x32_bf16 v[4:7], a[180:183], v[208:211], v[4:7]\n"
  "v_mfma_f32_16x16x32_bf16 v[8:11], a[184:187], v[208:211], v[8:11]\n"
  "v_mfma_f32_16x16x32_bf16 v[12:15], a[188:191], v[208:211], v[12:15]\n"
  "s_waitcnt lgkmcnt(0)\n"
  "s_waitcnt vmcnt(8)\n"
  "ds_read_b64_tr_b16 v[212:213], v181 offset:8192\n"
  "v_mfma_f32_16x16x32_bf16 v[16:19], a[192:195], v[196:199], v[16:19]\n"
  "ds_read_b64_tr_b16 v[214:215], v181 offset:10240\n"
  "v_mfma_f32_16x16x32_bf16 v[20:23], a[196:199], v[196:199], v[20:23]\n"
  "ds_read_b64_tr_b16 v[216:217], v182 offset:8192\n"
  "v_mfma_f32_16x16x32_bf16 v[24:27], a[200:203], v[196:199], v[24:27]\n"
  "ds_read_b64_tr_b16 v[218:219], v182 offset:10240\n"
  "v_mfma_f32_16x16x32_bf16 v[28:31], a[204:207], v[196:199], v[28:31]\n"
  "ds_read_b64_tr_b16 v[220:221], v183 offset:8192\n"
  "v_mfma_f32_16x16x32_bf16 v[32:35], a[192:195], v[200:203], v[32:35]\n"
  "ds_read_b64_tr_b16 v[222:223], v183 offset:10240\n"
  "v_mfma_f32_16x16x32_bf16 v[36:39], a[196:199], v[200:203], v[36:39]\n"
  "ds_read_b64_tr_b16 v[208:209], v180 offset:8192\n"
  "v_mfma_f32_16x16x32_bf16 v[40:43], a[200:203], v[200:203], v[40:43]\n"
  "ds_read_b64_tr_b16 v[210:211], v180 offset:10240\n"
  "v_mfma_f32_16x16x32_bf16 v[44:47], a[204:207], v[200:203], v[44:47]\n"
  "v_mfma_f32_16x16x32_bf16 v[48:51], a[192:195], v[204:207], v[48:51]\n"
  "v_mfma_f32_16x16x32_bf16 v[52:55], a[196:199], v[204:207], v[52:55]\n"
  "v_mfma_f32_16x16x32_bf16 v[56:59], a[200:203], v[204:207], v[56:59]\n"
  "v_mfma_f32_16x16x32_bf16 v[60:63], a[204:207], v[204:207], v[60:63]\n"
  "v_mfma_f32_16x16x32_bf16 v[0:3], a[192:195], v[192:195], v[0:3]\n"
  "v_mfma_f32_16x16x32_bf16 v[4:7], a[196:199], v[192:195], v[4:7]\n"
  "v_mfma_f32_16x16x32_bf16 v[8:11], a[200:203], v[192:195], v[8:11]\n"
  "v_mfma_f32_16x16x32_bf16 v[12:15], a[204:207], v[192:195], v[12:15]\n"
  "s_waitcnt lgkmcnt(0)\n"
  "s_waitcnt vmcnt(4)\n"
  "v_mfma_f32_16x16x32_bf16 v[16:19], a[208:211], v[212:215], v[16:19]\n"
  "v_mfma_f32_16x16x32_bf16 v[20:23], a[212:215], v[212:215], v[20:23]\n"
  "v_mfma_f32_16x16x32_bf16 v[24:27], a[216:219], v[212:215], v[24:27]\n"
  "v_mfma_f32_16x16x32_bf16 v[28:31], a[220:223], v[212:215], v[28:31]\n"
  "v_mfma_f32_16x16x32_bf16 v[32:35], a[208:211], v[216:219], v[32:35]\n"
  "v_mfma_f32_16x16x32_bf16 v[36:39], a[212:215], v[216:219], v[36:39]\n"
  "v_mfma_f32_16x16x32_bf16 v[40:43], a[216:219], v[216:219], v[40:43]\n"
  "ds_write_b128 v241, v[16:19] offset:4096\n"
  "v_mfma_f32_16x16x32_bf16 v[44:47], a[220:223], v[216:219], v[44:47]\n"
  "ds_write_b128 v241, v[20:23] offset:5120\n"
  "v_mfma_f32_16x16x32_bf16 v[48:51], a[208:211], v[220:223], v[48:51]\n"
  "ds_write_b128 v241, v[24:27] offset:6144\n"
  "v_mfma_f32_16x16x32_bf16 v[52:55], a[212:215], v[220:223], v[52:55]\n"
  "ds_write_b128 v241, v[28:31] offset:7168\n"
  "v_mfma_f32_16x16x32_bf16 v[56:59], a[216:219], v[220:223], v[56:59]\n"
  "ds_write_b128 v241, v[32:35] offset:8192\n"
  "v_mfma_f32_16x16x32_bf16 v[60:63], a[220:223], v[220:223], v[60:63]\n"
  "ds_write_b128 v241, v[36:39] offset:9216\n"
  "v_mfma_f32_16x16x32_bf16 v[0:3], a[208:211], v[208:211], v[0:3]\n"
  "ds_write_b128 v241, v[40:43] offset:10240\n"
  "v_mfma_f32_16x16x32_bf16 v[4:7], a[212:215], v[208:211], v[4:7]\n"
  "ds_write_b128 v241, v[44:47] offset:11264\n"
  "v_mfma_f32_16x16x32_bf16 v[8:11], a[216:219], v[208:211], v[8:11]\n"
  "ds_write_b128 v241, v[48:51] offset:12288\n"
  "v_mfma_f32_16x16x32_bf16 v[12:15], a[220:223], v[208:211], v[12:15]\n"
  "s_waitcnt vmcnt(0)\n"
  "ds_write_b128 v241, v[52:55] offset:13312\n"
  "v_mfma_f32_16x16x32_bf16 v[0:3], v[128:131], v[168:171], v[0:3]\n"
  "ds_write_b128 v241, v[56:59] offset:14336\n"
  "v_mfma_f32_16x16x32_bf16 v[4:7], v[132:135], v[168:171], v[4:7]\n"
  "ds_write_b128 v241, v[60:63] offset:15360\n"
  "v_mfma_f32_16x16x32_bf16 v[8:11], v[136:139], v[168:171], v[8:11]\n"
  "v_mfma_f32_16x16x32_bf16 v[12:15], v[140:143], v[168:171], v[12:15]\n"
  "s_nop 0\n"
  "s_waitcnt lgkmcnt(0)\n"
  "s_barrier\n"
  "ds_read_b128 v[80:83], v145 offset:0\n"
  "ds_read_b128 v[84:87], v145 offset:1024\n"
  "ds_read_b128 v[88:91], v145 offset:2048\n"
  "ds_read_b128 v[92:95], v145 offset:3072\n"
  "ds_read_b128 v[96:99], v146 offset:0\n"
  "ds_read_b128 v[100:103], v146 offset:1024\n"
  "ds_read_b128 v[104:107], v146 offset:2048\n"
  "ds_read_b128 v[108:111], v146 offset:3072\n"
  "ds_read_b128 v[112:115], v147 offset:0\n"
  "ds_read_b128 v[116:119], v147 offset:1024\n"
  "ds_read_b128 v[120:123], v147 offset:2048\n"
  "ds_read_b128 v[124:127], v147 offset:3072\n"
  "s_waitcnt lgkmcnt(11)\n"
  "v_pk_add_f32 v[0:1], v[0:1], v[80:81]\n"
  "v_pk_add_f32 v[2:3], v[2:3], v[82:83]\n"
  "s_waitcnt lgkmcnt(10)\n"
  "v_pk_add_f32 v[4:5], v[4:5], v[84:85]\n"
  "v_pk_add_f32 v[6:7], v[6:7], v[86:87]\n"
  "s_waitcnt lgkmcnt(9)\n"
  "v_pk_add_f32 v[8:9], v[8:9], v[88:89]\n"
  "v_pk_add_f32 v[10:11], v[10:11], v[90:91]\n"
  "s_waitcnt lgkmcnt(8)\n"
  "v_pk_add_f32 v[12:13], v[12:13], v[92:93]\n"
  "v_pk_add_f32 v[14:15], v[14:15], v[94:95]\n"
  "s_waitcnt lgkmcnt(3)\n"
  "v_pk_add_f32 v[96:97], v[96:97], v[112:113]\n"
  "v_pk_add_f32 v[98:99], v[98:99], v[114:115]\n"
  "s_waitcnt lgkmcnt(2)\n"
  "v_pk_add_f32 v[100:101], v[100:101], v[116:117]\n"
  "v_pk_add_f32 v[102:103], v[102:103], v[118:119]\n"
  "s_waitcnt lgkmcnt(1)\n"
  "v_pk_add_f32 v[104:105], v[104:105], v[120:121]\n"
  "v_pk_add_f32 v[106:107], v[106:107], v[122:123]\n"
  "s_waitcnt lgkmcnt(0)\n"
  "v_pk_add_f32 v[108:109], v[108:109], v[124:125]\n"
  "v_pk_add_f32 v[110:111], v[110:111], v[126:127]\n"
  "v_pk_add_f32 v[0:1], v[0:1], v[96:97]\n"
  "v_pk_add_f32 v[2:3], v[2:3], v[98:99]\n"
  "v_pk_add_f32 v[4:5], v[4:5], v[100:101]\n"
  "v_pk_add_f32 v[6:7], v[6:7], v[102:103]\n"
  "v_pk_add_f32 v[8:9], v[8:9], v[104:105]\n"
  "v_pk_add_f32 v[10:11], v[10:11], v[106:107]\n"
  "v_pk_add_f32 v[12:13], v[12:13], v[108:109]\n"
  "v_pk_add_f32 v[14:15], v[14:15], v[110:111]\n"
  "v_cvt_pk_bf16_f32 v64, v0, v1\n"
  "v_cvt_pk_bf16_f32 v65, v2, v3\n"
  "v_cvt_pk_bf16_f32 v66, v4, v5\n"
  "v_cvt_pk_bf16_f32 v67, v6, v7\n"
  "v_cvt_pk_bf16_f32 v68, v8, v9\n"
  "v_cvt_pk_bf16_f32 v69, v10, v11\n"
  "v_cvt_pk_bf16_f32 v70, v12, v13\n"
  "v_cvt_pk_bf16_f32 v71, v14, v15\n"
  "v_permlane16_swap_b32 v64, v66\n"
  "v_permlane16_swap_b32 v65, v67\n"
  "v_permlane16_swap_b32 v68, v70\n"
  "v_permlane16_swap_b32 v69, v71\n"
  "v_mov_b32 v72, v64\n"
  "v_mov_b32 v73, v65\n"
  "v_mov_b32 v74, v66\n"
  "v_mov_b32 v75, v67\n"
  "v_mov_b32_dpp v64, v68 row_ror:8 row_mask:0xf bank_mask:0xc\n"
  "v_mov_b32_dpp v65, v69 row_ror:8 row_mask:0xf bank_mask:0xc\n"
  "v_mov_b32_dpp v66, v70 row_ror:8 row_mask:0xf bank_mask:0xc\n"
  "v_mov_b32_dpp v67, v71 row_ror:8 row_mask:0xf bank_mask:0xc\n"
  "v_mov_b32_dpp v68, v72 row_ror:8 row_mask:0xf bank_mask:0x3\n"
  "v_mov_b32_dpp v69, v73 row_ror:8 row_mask:0xf bank_mask:0x3\n"
  "v_mov_b32_dpp v70, v74 row_ror:8 row_mask:0xf bank_mask:0x3\n"
  "v_mov_b32_dpp v71, v75 row_ror:8 row_mask:0xf bank_mask:0x3\n"
  "s_add_u32 s61, s74, 0x2000\n"
  "buffer_store_dwordx4 v[64:67], v234, s[44:47], s74 offen sc1\n"
  "buffer_store_dwordx4 v[68:71], v234, s[44:47], s61 offen sc1\n"
  "s_endpgm\n"
  "mk_skip1:\n"
  "mk_tp22:\n"
  "s_cbranch_execz mk_tret\n"
  "s_waitcnt vmcnt(0)\n"
  "buffer_atomic_add v248, v228, s[48:51], s85 offen sc1\n"
  "s_barrier\n"
  "v_mov_b32 v148, v238\n"
  "v_add_u32 v149, 0x7c00, v238\n"
  "v_add_u32 v150, 0xf800, v238\n"
  "v_add_u32 v151, 0x17400, v238\n"
  "s_lshl_b32 s62, %1, 5\n"
  "v_xor_b32 v176, s62, v188\n"
  "v_xor_b32 v180, s62, v224\n"
  "v_xor_b32 v177, s62, v189\n"
  "v_xor_b32 v181, s62, v225\n"
  "v_xor_b32 v178, s62, v190\n"
  "v_xor_b32 v182, s62, v226\n"
  "v_xor_b32 v179, s62, v191\n"
  "v_xor_b32 v183, s62, v227\n"
  "v_add_u32 v241, s12, v244\n"
  "s_xor_b32 s63, %1, 1\n"
  "s_lshl_b32 s63, s63, 14\n"
  "s_add_u32 s63, s63, 0x19000\n"
  "v_add_u32 v145, s63, v244\n"
  "s_xor_b32 s63, %1, 2\n"
  "s_lshl_b32 s63, s63, 14\n"
  "s_add_u32 s63, s63, 0x1a000\n"
  "v_add_u32 v146, s63, v244\n"
  "s_xor_b32 s63, %1, 3\n"
  "s_lshl_b32 s63, s63, 14\n"
  "s_add_u32 s63, s63, 0x1b000\n"
  "v_add_u32 v147, s63, v244\n"
  "s_add_u32 s59, s15, 0x0\n"
  "s_add_u32 m0, s13, 20480\n"
  "s_nop 0\n"
  "buffer_load_dwordx4 v148, s[68:71], s59 offen offset:0 lds\n"
  "buffer_load_dwordx4 v149, s[68:71], s59 offen offset:1024 lds\n"
  "buffer_load_dwordx4 v150, s[68:71], s59 offen offset:2048 lds\n"
  "buffer_load_dwordx4 v151, s[68:71], s59 offen offset:3072 lds\n"
  "s_add_u32 s59, s15, 0x20000\n"
  "s_add_u32 m0, s13, 0\n"
  "s_nop 0\n"
  "buffer_load_dwordx4 v148, s[68:71], s59 offen offset:0 lds\n"
  "buffer_load_dwordx4 v149, s[68:71], s59 offen offset:1024 lds\n"
  "buffer_load_dwordx4 v150, s[68:71], s59 offen offset:2048 lds\n"
  "buffer_load_dwordx4 v151, s[68:71], s59 offen offset:3072 lds\n"
  "s_add_u32 s59, s15, 0x40000\n"
  "s_add_u32 m0, s13, 4096\n"
  "s_nop 0\n"
  "buffer_load_dwordx4 v148, s[68:71], s59 offen offset:0 lds\n"
  "buffer_load_dwordx4 v149, s[68:71], s59 offen offset:1024 lds\n"
  "buffer_load_dwordx4 v150, s[68:71], s59 offen offset:2048 lds\n"
  "buffer_load_dwordx4 v151, s[68:71], s59 offen offset:3072 lds\n"
  "s_add_u32 s59, s15, 0x60000\n"
  "s_add_u32 m0, s13, 8192\n"
  "s_nop 0\n"
  "buffer_load_dwordx4 v148, s[68:71], s59 offen offset:0 lds\n"
  "buffer_load_dwordx4 v149, s[68:71], s59 offen offset:1024 lds\n"
  "buffer_load_dwordx4 v150, s[68:71], s59 offen offset:2048 lds\n"
  "buffer_load_dwordx4 v151, s[68:71], s59 offen offset:3072 lds\n"
  "s_add_u32 s59, s15, 0x80000\n"
  "s_add_u32 m0, s13, 12288\n"
  "s_nop 0\n"
  "buffer_load_dwordx4 v148, s[68:71], s59 offen offset:0 lds\n"
  "buffer_load_dwordx4 v149, s[68:71], s59 offen offset:1024 lds\n"
  "buffer_load_dwordx4 v150, s[68:71], s59 offen offset:2048 lds\n"
  "buffer_load_dwordx4 v151, s[68:71], s59 offen offset:3072 lds\n"
  "s_add_u32 s59, s15, 0xa0000\n"
  "s_add_u32 m0, s13, 16384\n"
  "s_nop 0\n"
  "buffer_load_dwordx4 v148, s[68:71], s59 offen offset:0 lds\n"
  "buffer_load_dwordx4 v149, s[68:71], s59 offen offset:1024 lds\n"
  "buffer_load_dwordx4 v150, s[68:71], s59 offen offset:2048 lds\n"
  "buffer_load_dwordx4 v151, s[68:71], s59 offen offset:3072 lds\n"
  "s_add_u32 s59, s15, 0xc0000\n"
  "s_add_u32 m0, s12, 0\n"
  "s_nop 0\n"
  "buffer_load_dwordx4 v148, s[68:71], s59 offen offset:0 lds\n"
  "buffer_load_dwordx4 v149, s[68:71], s59 offen offset:1024 lds\n"
  "buffer_load_dwordx4 v150, s[68:71], s59 offen offset:2048 lds\n"
  "buffer_load_dwordx4 v151, s[68:71], s59 offen offset:3072 lds\n"
  "s_add_u32 s59, s18, 0xe0000\n"
  "s_add_u32 m0, s12, 4096\n"
  "s_nop 0\n"
  "buffer_load_dwordx4 v148, s[68:71], s59 offen offset:0 lds\n"
  "buffer_load_dwordx4 v149, s[68:71], s59 offen offset:1024 lds\n"
  "buffer_load_dwordx4 v150, s[68:71], s59 offen offset:2048 lds\n"
  "buffer_load_dwordx4 v151, s[68:71], s59 offen offset:3072 lds\n"
  "s_add_u32 s59, s18, 0x100000\n"
  "s_add_u32 m0, s12, 8192\n"
  "s_nop 0\n"
  "buffer_load_dwordx4 v148, s[68:71], s59 offen offset:0 lds\n"
  "buffer_load_dwordx4 v149, s[68:71], s59 offen offset:1024 lds\n"
  "buffer_load_dwordx4 v150, s[68:71], s59 offen offset:2048 lds\n"
  "buffer_load_dwordx4 v151, s[68:71], s59 offen offset:3072 lds\n"
  "s_add_u32 s59, s18, 0x120000\n"
  "s_add_u32 m0, s12, 12288\n"
  "s_nop 0\n"
  "buffer_load_dwordx4 v148, s[68:71], s59 offen offset:0 lds\n"
  "buffer_load_dwordx4 v149, s[68:71], s59 offen offset:1024 lds\n"
  "buffer_load_dwordx4 v150, s[68:71], s59 offen offset:2048 lds\n"
  "buffer_load_dwordx4 v151, s[68:71], s59 offen offset:3072 lds\n"
  "s_waitcnt vmcnt(24)\n"
  "ds_read_b64_tr_b16 a[0:1], v180 offset:20480\n"
  "ds_read_b64_tr_b16 a[2:3], v180 offset:22528\n"
  "ds_read_b64_tr_b16 a[4:5], v181 offset:20480\n"
  "ds_read_b64_tr_b16 a[6:7], v181 offset:22528\n"
  "ds_read_b64_tr_b16 a[8:9], v182 offset:20480\n"
  "ds_read_b64_tr_b16 a[10:11], v182 offset:22528\n"
  "ds_read_b64_tr_b16 a[12:13], v183 offset:20480\n"
  "ds_read_b64_tr_b16 a[14:15], v183 offset:22528\n"
  "ds_read_b64_tr_b16 a[16:17], v180 offset:0\n"
  "ds_read_b64_tr_b16 a[18:19], v180 offset:2048\n"
  "ds_read_b64_tr_b16 a[20:21], v181 offset:0\n"
  "ds_read_b64_tr_b16 a[22:23], v181 offset:2048\n"
  "ds_read_b64_tr_b16 a[24:25], v182 offset:0\n"
  "ds_read_b64_tr_b16 a[26:27], v182 offset:2048\n"
  "ds_read_b64_tr_b16 a[28:29], v183 offset:0\n"
  "ds_read_b64_tr_b16 a[30:31], v183 offset:2048\n"
  "ds_read_b64_tr_b16 a[32:33], v180 offset:4096\n"
  "ds_read_b64_tr_b16 a[34:35], v180 offset:6144\n"
  "ds_read_b64_tr_b16 a[36:37], v181 offset:4096\n"
  "ds_read_b64_tr_b16 a[38:39], v181 offset:6144\n"
  "ds_read_b64_tr_b16 a[40:41], v182 offset:4096\n"
  "ds_read_b64_tr_b16 a[42:43], v182 offset:6144\n"
  "ds_read_b64_tr_b16 a[44:45], v183 offset:4096\n"
  "ds_read_b64_tr_b16 a[46:47], v183 offset:6144\n"
  "ds_read_b64_tr_b16 a[48:49], v180 offset:8192\n"
  "ds_read_b64_tr_b16 a[50:51], v180 offset:10240\n"
  "ds_read_b64_tr_b16 a[52:53], v181 offset:8192\n"
  "ds_read_b64_tr_b16 a[54:55], v181 offset:10240\n"
  "ds_read_b64_tr_b16 a[56:57], v182 offset:8192\n"
  "ds_read_b64_tr_b16 a[58:59], v182 offset:10240\n"
  "ds_read_b64_tr_b16 a[60:61], v183 offset:8192\n"
  "ds_read_b64_tr_b16 a[62:63], v183 offset:10240\n"
  "s_nop 0\n"
  "s_nop 0\n"
  "s_nop 0\n"
  "s_nop 0\n"
  "s_nop 0\n"
  "s_nop 0\n"
  "s_nop 0\n"
  "s_branch mk_mlp2\n"
  "mk_fixA:\n"
  "mk_tp23:\n"
  "s_cbranch_execz mk_tret\n"
  "v_cndmask_b32_e64 v208, v192, v246, s[64:65]\n"
  "v_cndmask_b32_e64 v209, v192, v246, s[66:67]\n"
  "v_cndmask_b32_e64 v210, v192, v246, s[68:69]\n"
  "v_cndmask_b32_e64 v211, v192, v246, s[70:71]\n"
  "v_cndmask_b32_e64 v212, v192, v246, s[72:73]\n"
  "v_cndmask_b32_e64 v213, v192, v246, s[74:75]\n"
  "v_cndmask_b32_e64 v214, v192, v246, s[76:77]\n"
  "v_cndmask_b32_e64 v215, v192, v246, s[78:79]\n"
  "mk_tp24:\n"
  "s_cbranch_execz mk_tret\n"
  "v_cndmask_b32_e64 v216, v192, v246, s[80:81]\n"
  "v_cndmask_b32_e64 v217, v192, v246, s[82:83]\n"
  "v_cndmask_b32_e64 v218, v192, v246, s[84:85]\n"
  "v_cndmask_b32_e64 v219, v192, v246, s[86:87]\n"
  "v_cndmask_b32_e64 v220, v192, v246, s[88:89]\n"
  "v_cndmask_b32_e64 v221, v192, v246, s[90:91]\n"
  "v_cndmask_b32_e64 v222, v192, v246, s[92:93]\n"
  "v_cndmask_b32_e64 v223, v192, v246, s[94:95]\n"
  "s_branch mk_LA2\n"
  "mk_fixB:\n"
  "mk_tp25:\n"
  "s_cbranch_execz mk_tret\n"
  "v_lshlrev_b32 v52, 17, v244\n"
  "v_mov_b32 v53, 0\n"
  "s_mov_b32 s59, 0x1fff80\n"
  "buffer_load_dword v54, v52, s[48:51], s59 offen\n"
  "global_load_dword v55, v53, s[28:29]\n"
  "global_load_dword v4, v53, s[24:25]\n"
  "global_load_dword v5, v53, s[20:21]\n"
  "s_cmp_eq_u32 s10, 0\n"
  "s_cselect_b32 s59, s11, 0x7fff0000\n"
  "buffer_atomic_add v248, v253, s[48:51], s59 offen sc1\n"
  "s_or_b32 s10, s10, 2\n"
  "buffer_load_dword v36, v37, s[12:15], 0 offen\n"
  "v_cndmask_b32_e64 v208, v200, v246, s[64:65]\n"
  "v_cndmask_b32_e64 v209, v200, v246, s[66:67]\n"
  "v_cndmask_b32_e64 v210, v200, v246, s[68:69]\n"
  "v_cndmask_b32_e64 v211, v200, v246, s[70:71]\n"
  "v_cndmask_b32_e64 v212, v200, v246, s[72:73]\n"
  "v_cndmask_b32_e64 v213, v200, v246, s[74:75]\n"
  "v_cndmask_b32_e64 v214, v200, v246, s[76:77]\n"
  "v_cndmask_b32_e64 v215, v200, v246, s[78:79]\n"
  "v_cndmask_b32_e64 v216, v200, v246, s[80:81]\n"
  "v_cndmask_b32_e64 v217, v200, v246, s[82:83]\n"
  "v_cndmask_b32_e64 v218, v200, v246, s[84:85]\n"
  "v_cndmask_b32_e64 v219, v200, v246, s[86:87]\n"
  "v_cndmask_b32_e64 v220, v200, v246, s[88:89]\n"
  "v_cndmask_b32_e64 v221, v200, v246, s[90:91]\n"
  "v_cndmask_b32_e64 v222, v200, v246, s[92:93]\n"
  "v_cndmask_b32_e64 v223, v200, v246, s[94:95]\n"
  "s_branch mk_LB2\n"
  "mk_spinV:\n"
  "s_mov_b32 s99, 0\n"
  "mk_spinV_l:\n"
  "s_sleep 1\n"
  "v_mov_b32 v252, 0\n"
  "buffer_atomic_add v252, v249, s[48:51], s58 offen sc0 sc1\n"
  "s_waitcnt vmcnt(0)\n"
  "s_add_u32 s99, s99, 1\n"
  "s_cmp_gt_u32 s99, 0x20000\n"
  "s_cbranch_scc1 mk_spinV_b\n"
  "v_cmp_ne_u32 vcc, 8, v252\n"
  "s_and_b32 vcc_lo, vcc_lo, 0x1\n"
  "s_cbranch_scc1 mk_spinV_l\n"
  "s_branch mk_spinV_b\n"
  "mk_spinG:\n"
  "s_mov_b32 s99, 0\n"
  "mk_spinG_l:\n"
  "s_sleep 1\n"
  "buffer_load_dword v242, v249, s[48:51], s97 offen sc1\n"
  "v_mov_b32 v252, 0\n"
  "buffer_atomic_add v252, v249, s[48:51], s96 offen sc0 sc1\n"
  "s_waitcnt vmcnt(0)\n"
  "v_cmp_ne_u32 vcc, v242, v253\n"
  "v_max_u32 v243, v250, v251\n"
  "v_mov_b32 v253, v242\n"
  "v_cndmask_b32 v243, v243, v250, vcc\n"
  "buffer_store_dword v253, v243, s[48:51], s96 offen sc1\n"
  "s_add_u32 s99, s99, 1\n"
  "s_cmp_gt_u32 s99, 0x20000\n"
  "s_cbranch_scc1 mk_spinG_b\n"
  "v_cmp_eq_u32 vcc, v252, v248\n"
  "s_and_b32 vcc_lo, vcc_lo, 0xff\n"
  "s_cbranch_scc1 mk_spinG_l\n"
  "s_branch mk_spinG_b\n"
  "mk_spinH:\n"
  "s_mov_b32 s99, 0\n"
  "mk_spinH_l:\n"
  "s_sleep 1\n"
  "buffer_load_dword v242, v249, s[48:51], s97 offen sc1\n"
  "v_mov_b32 v252, 0\n"
  "buffer_atomic_add v252, v249, s[48:51], s96 offen sc0 sc1\n"
  "s_waitcnt vmcnt(0)\n"
  "v_cmp_ne_u32 vcc, v242, v253\n"
  "v_max_u32 v243, v250, v251\n"
  "v_mov_b32 v253, v242\n"
  "v_cndmask_b32 v243, v243, v251, vcc\n"
  "buffer_store_dword v253, v243, s[48:51], s96 offen sc1\n"
  "s_add_u32 s99, s99, 1\n"
  "s_cmp_gt_u32 s99, 0x20000\n"
  "s_cbranch_scc1 mk_spinH_b\n"
  "v_cmp_eq_u32 vcc, v252, v248\n"
  "s_and_b32 vcc_lo, vcc_lo, 0x300\n"
  "s_cbranch_scc1 mk_spinH_l\n"
  "s_branch mk_spinH_b\n"
  "mk_spinP:\n"
  "s_mov_b32 s99, 0\n"
  "mk_spinP_l:\n"
  "s_sleep 1\n"
  "buffer_load_dword v252, v251, s[48:51], s33 offen sc1\n"
  "s_waitcnt vmcnt(0)\n"
  "s_add_u32 s99, s99, 1\n"
  "s_cmp_gt_u32 s99, 0x20000\n"
  "s_cbranch_scc1 mk_spinP_b\n"
  "v_and_b32 v36, 0xff, v252\n"
  "v_cmp_ne_u32 vcc, 8, v36\n"
  "s_and_b32 vcc_lo, vcc_lo, 0xfffff\n"
  "s_cbranch_scc1 mk_spinP_l\n"
  "s_branch mk_spinP_b\n"
  "mk_spin3a:\n"
  "s_mov_b32 s99, 0\n"
  "mk_spin3a_l:\n"
  "s_sleep 1\n"
  "v_mov_b32 v39, 0\n"
  "buffer_atomic_add v39, v253, s[48:51], s11 offen sc0 sc1\n"
  "s_waitcnt vmcnt(0)\n"
  "s_add_u32 s99, s99, 1\n"
  "s_cmp_gt_u32 s99, 0x20000\n"
  "s_cbranch_scc1 mk_spin3a_b\n"
  "v_cmp_ne_u32 vcc, 4, v39\n"
  "s_and_b32 vcc_lo, vcc_lo, 0x1\n"
  "s_cbranch_scc1 mk_spin3a_l\n"
  "s_branch mk_spin3a_b\n"
  "mk_spin3b:\n"
  "s_mov_b32 s99, 0\n"
  "mk_spin3b_l:\n"
  "s_sleep 1\n"
  "v_mov_b32 v233, 0\n"
  "buffer_atomic_add v233, v253, s[48:51], s97 offen sc0 sc1\n"
  "s_waitcnt vmcnt(0)\n"
  "s_add_u32 s99, s99, 1\n"
  "s_cmp_gt_u32 s99, 0x20000\n"
  "s_cbranch_scc1 mk_spin3b_b\n"
  "v_cmp_ne_u32 vcc, 4, v233\n"
  "s_and_b32 vcc_lo, vcc_lo, 0x1\n"
  "s_cbranch_scc1 mk_spin3b_l\n"
  "s_branch mk_spin3b_b\n"
  "mk_spin4:\n"
  "s_mov_b32 s99, 0\n"
  "mk_spin4_l:\n"
  "s_sleep 1\n"
  "v_mov_b32 v233, 0\n"
  "buffer_atomic_add v233, v253, s[48:51], s80 offen sc0 sc1\n"
  "s_waitcnt vmcnt(0)\n"
  "s_add_u32 s99, s99, 1\n"
  "s_cmp_gt_u32 s99, 0x20000\n"
  "s_cbranch_scc1 mk_spin4_b\n"
  "v_cmp_ne_u32 vcc, 8, v233\n"
  "s_and_b32 vcc_lo, vcc_lo, 0x1\n"
  "s_cbranch_scc1 mk_spin4_l\n"
  "s_branch mk_spin4_b\n"
  "mk_spin5a:\n"
  "s_waitcnt vmcnt(0)\n"
  "buffer_atomic_add v248, v228, s[48:51], s85 offen sc1\n"
  "s_mov_b32 s85, 0x7fff0000\n"
  "s_mov_b32 s99, 0\n"
  "v_mov_b32 v230, 0\n"
  "buffer_atomic_add v230, v229, s[48:51], s86 offen sc0 sc1\n"
  "s_sleep 4\n"
  "mk_spin5a_l:\n"
  "v_mov_b32 v233, 0\n"
  "buffer_atomic_add v233, v229, s[48:51], s86 offen sc0 sc1\n"
  "s_waitcnt vmcnt(1)\n"
  "v_cmp_ne_u32 vcc, 16, v230\n"
  "s_and_b32 vcc_lo, vcc_lo, 0x3\n"
  "s_cbranch_scc0 mk_spin5a_b\n"
  "v_mov_b32 v230, 0\n"
  "buffer_atomic_add v230, v229, s[48:51], s86 offen sc0 sc1\n"
  "s_waitcnt vmcnt(1)\n"
  "v_cmp_ne_u32 vcc, 16, v233\n"
  "s_and_b32 vcc_lo, vcc_lo, 0x3\n"
  "s_cbranch_scc0 mk_spin5a_b\n"
  "s_add_u32 s99, s99, 1\n"
  "s_cmp_lt_u32 s99, 0x20000\n"
  "s_cbranch_scc1 mk_spin5a_l\n"
  "s_waitcnt vmcnt(0)\n"
  "s_branch mk_spin5a_b\n"
  "mk_spin5b:\n"
  "s_mov_b32 s99, 0\n"
  "v_mov_b32 v230, 0\n"
  "buffer_atomic_add v230, v229, s[48:51], s87 offen sc0 sc1\n"
  "s_sleep 4\n"
  "mk_spin5b_l:\n"
  "v_mov_b32 v233, 0\n"
  "buffer_atomic_add v233, v229, s[48:51], s87 offen sc0 sc1\n"
  "s_waitcnt vmcnt(1)\n"
  "v_cmp_ne_u32 vcc, 16, v230\n"
  "s_and_b32 vcc_lo, vcc_lo, 0x3\n"
  "s_cbranch_scc0 mk_spin5b_b\n"
  "v_mov_b32 v230, 0\n"
  "buffer_atomic_add v230, v229, s[48:51], s87 offen sc0 sc1\n"
  "s_waitcnt vmcnt(1)\n"
  "v_cmp_ne_u32 vcc, 16, v233\n"
  "s_and_b32 vcc_lo, vcc_lo, 0x3\n"
  "s_cbranch_scc0 mk_spin5b_b\n"
  "s_add_u32 s99, s99, 1\n"
  "s_cmp_lt_u32 s99, 0x20000\n"
  "s_cbranch_scc1 mk_spin5b_l\n"
  "s_waitcnt vmcnt(0)\n"
  "s_branch mk_spin5b_b\n"
  "mk_spin5c:\n"
  "s_mov_b32 s99, 0\n"
  "v_mov_b32 v230, 0\n"
  "buffer_atomic_add v230, v229, s[48:51], s86 offen sc0 sc1\n"
  "s_sleep 4\n"
  "mk_spin5c_l:\n"
  "v_mov_b32 v233, 0\n"
  "buffer_atomic_add v233, v229, s[48:51], s86 offen sc0 sc1\n"
  "s_waitcnt vmcnt(1)\n"
  "v_cmp_ne_u32 vcc, 16, v230\n"
  "s_and_b32 vcc_lo, vcc_lo, 0x3\n"
  "s_cbranch_scc0 mk_spin5c_b\n"
  "v_mov_b32 v230, 0\n"
  "buffer_atomic_add v230, v229, s[48:51], s86 offen sc0 sc1\n"
  "s_waitcnt vmcnt(1)\n"
  "v_cmp_ne_u32 vcc, 16, v233\n"
  "s_and_b32 vcc_lo, vcc_lo, 0x3\n"
  "s_cbranch_scc0 mk_spin5c_b\n"
  "s_add_u32 s99, s99, 1\n"
  "s_cmp_lt_u32 s99, 0x20000\n"
  "s_cbranch_scc1 mk_spin5c_l\n"
  "s_waitcnt vmcnt(0)\n"
  "s_branch mk_spin5c_b\n"
  "mk_spin4b:\n"
  "s_mov_b32 s99, 0\n"
  "v_mov_b32 v230, 0\n"
  "buffer_atomic_add v230, v253, s[48:51], s80 offen sc0 sc1\n"
  "s_sleep 4\n"
  "mk_spin4b_l:\n"
  "v_mov_b32 v233, 0\n"
  "buffer_atomic_add v233, v253, s[48:51], s80 offen sc0 sc1\n"
  "s_waitcnt vmcnt(1)\n"
  "v_cmp_ne_u32 vcc, 8, v230\n"
  "s_and_b32 vcc_lo, vcc_lo, 0x1\n"
  "s_cbranch_scc0 mk_spin4b_b\n"
  "v_mov_b32 v230, 0\n"
  "buffer_atomic_add v230, v253, s[48:51], s80 offen sc0 sc1\n"
  "s_waitcnt vmcnt(1)\n"
  "v_cmp_ne_u32 vcc, 8, v233\n"
  "s_and_b32 vcc_lo, vcc_lo, 0x1\n"
  "s_cbranch_scc0 mk_spin4b_b\n"
  "s_add_u32 s99, s99, 1\n"
  "s_cmp_lt_u32 s99, 0x20000\n"
  "s_cbranch_scc1 mk_spin4b_l\n"
  "s_waitcnt vmcnt(0)\n"
  "s_branch mk_spin4b_b\n"
  "mk_spin6:\n"
  "s_mov_b32 s99, 0\n"
  "v_mov_b32 v230, 0\n"
  "buffer_atomic_add v230, v232, s[48:51], s94 offen sc0 sc1\n"
  "s_sleep 4\n"
  "mk_spin6_l:\n"
  "v_mov_b32 v233, 0\n"
  "buffer_atomic_add v233, v232, s[48:51], s94 offen sc0 sc1\n"
  "s_waitcnt vmcnt(1)\n"
  "v_cmp_ne_u32 vcc, 14, v230\n"
  "s_and_b32 vcc_lo, vcc_lo, 0x3\n"
  "s_cbranch_scc0 mk_spin6_b\n"
  "v_mov_b32 v230, 0\n"
  "buffer_atomic_add v230, v232, s[48:51], s94 offen sc0 sc1\n"
  "s_waitcnt vmcnt(1)\n"
  "v_cmp_ne_u32 vcc, 14, v233\n"
  "s_and_b32 vcc_lo, vcc_lo, 0x3\n"
  "s_cbranch_scc0 mk_spin6_b\n"
  "s_add_u32 s99, s99, 1\n"
  "s_cmp_lt_u32 s99, 0x20000\n"
  "s_cbranch_scc1 mk_spin6_l\n"
  "s_waitcnt vmcnt(0)\n"
  "s_branch mk_spin6_b\n"
  "mk_end:\n"
  "mk_slow:\n"
  "s_nop 7\n"
  "v_lshrrev_b32 v220, 4, v244\n"
  "v_and_b32 v221, 15, v220\n"
  "v_lshrrev_b32 v220, 4, v220\n"
  "v_lshlrev_b32 v220, 2, v220\n"
  "v_sub_u32 v220, v221, v220\n"
  "s_bfe_u32 s61, s40, 0x70011\n"
  "s_lshl_b32 s61, s61, 4\n"
  "v_add_u32 v208, s61, v220\n"
  "v_mov_b32 v212, v246\n"
  "v_mov_b32 v224, 0\n"
  "v_mov_b32 v225, 0\n"
  "v_mov_b32 v226, 0\n"
  "v_mov_b32 v227, 0\n"
  "s_bfe_u32 s61, s41, 0x70011\n"
  "s_lshl_b32 s61, s61, 4\n"
  "v_add_u32 v209, s61, v220\n"
  "v_mov_b32 v213, v246\n"
  "v_mov_b32 v228, 0\n"
  "v_mov_b32 v229, 0\n"
  "v_mov_b32 v230, 0\n"
  "v_mov_b32 v231, 0\n"
  "s_bfe_u32 s61, s42, 0x70011\n"
  "s_lshl_b32 s61, s61, 4\n"
  "v_add_u32 v210, s61, v220\n"
  "v_mov_b32 v214, v246\n"
  "v_mov_b32 v232, 0\n"
  "v_mov_b32 v233, 0\n"
  "v_mov_b32 v234, 0\n"
  "v_mov_b32 v235, 0\n"
  "s_bfe_u32 s61, s43, 0x70011\n"
  "s_lshl_b32 s61, s61, 4\n"
  "v_add_u32 v211, s61, v220\n"
  "v_mov_b32 v215, v246\n"
  "v_mov_b32 v236, 0\n"
  "v_mov_b32 v237, 0\n"
  "v_mov_b32 v238, 0\n"
  "v_mov_b32 v239, 0\n"
  "v_accvgpr_write_b32 a0, 0\n"
  "v_accvgpr_write_b32 a1, 0\n"
  "v_accvgpr_write_b32 a2, 0\n"
  "v_accvgpr_write_b32 a3, 0\n"
  "v_accvgpr_write_b32 a4, 0\n"
  "v_accvgpr_write_b32 a5, 0\n"
  "v_accvgpr_write_b32 a6, 0\n"
  "v_accvgpr_write_b32 a7, 0\n"
  "v_accvgpr_write_b32 a8, 0\n"
  "v_accvgpr_write_b32 a9, 0\n"
  "v_accvgpr_write_b32 a10, 0\n"
  "v_accvgpr_write_b32 a11, 0\n"
  "v_accvgpr_write_b32 a12, 0\n"
  "v_accvgpr_write_b32 a13, 0\n"
  "v_accvgpr_write_b32 a14, 0\n"
  "v_accvgpr_write_b32 a15, 0\n"
  "v_accvgpr_write_b32 a16, 0\n"
  "v_accvgpr_write_b32 a17, 0\n"
  "v_accvgpr_write_b32 a18, 0\n"
  "v_accvgpr_write_b32 a19, 0\n"
  "v_accvgpr_write_b32 a20, 0\n"
  "v_accvgpr_write_b32 a21, 0\n"
  "v_accvgpr_write_b32 a22, 0\n"
  "v_accvgpr_write_b32 a23, 0\n"
  "v_accvgpr_write_b32 a24, 0\n"
  "v_accvgpr_write_b32 a25, 0\n"
  "v_accvgpr_write_b32 a26, 0\n"
  "v_accvgpr_write_b32 a27, 0\n"
  "v_accvgpr_write_b32 a28, 0\n"
  "v_accvgpr_write_b32 a29, 0\n"
  "v_accvgpr_write_b32 a30, 0\n"
  "v_accvgpr_write_b32 a31, 0\n"
  "v_accvgpr_write_b32 a32, 0\n"
  "v_accvgpr_write_b32 a33, 0\n"
  "v_accvgpr_write_b32 a34, 0\n"
  "v_accvgpr_write_b32 a35, 0\n"
  "v_accvgpr_write_b32 a36, 0\n"
  "v_accvgpr_write_b32 a37, 0\n"
  "v_accvgpr_write_b32 a38, 0\n"
  "v_accvgpr_write_b32 a39, 0\n"
  "v_accvgpr_write_b32 a40, 0\n"
  "v_accvgpr_write_b32 a41, 0\n"
  "v_accvgpr_write_b32 a42, 0\n"
  "v_accvgpr_write_b32 a43, 0\n"
  "v_accvgpr_write_b32 a44, 0\n"
  "v_accvgpr_write_b32 a45, 0\n"
  "v_accvgpr_write_b32 a46, 0\n"
  "v_accvgpr_write_b32 a47, 0\n"
  "v_accvgpr_write_b32 a48, 0\n"
  "v_accvgpr_write_b32 a49, 0\n"
  "v_accvgpr_write_b32 a50, 0\n"
  "v_accvgpr_write_b32 a51, 0\n"
  "v_accvgpr_write_b32 a52, 0\n"
  "v_accvgpr_write_b32 a53, 0\n"
  "v_accvgpr_write_b32 a54, 0\n"
  "v_accvgpr_write_b32 a55, 0\n"
  "v_accvgpr_write_b32 a56, 0\n"
  "v_accvgpr_write_b32 a57, 0\n"
  "v_accvgpr_write_b32 a58, 0\n"
  "v_accvgpr_write_b32 a59, 0\n"
  "v_accvgpr_write_b32 a60, 0\n"
  "v_accvgpr_write_b32 a61, 0\n"
  "v_accvgpr_write_b32 a62, 0\n"
  "v_accvgpr_write_b32 a63, 0\n"
  "v_accvgpr_write_b32 a64, 0\n"
  "v_accvgpr_write_b32 a65, 0\n"
  "v_accvgpr_write_b32 a66, 0\n"
  "v_accvgpr_write_b32 a67, 0\n"
  "v_accvgpr_write_b32 a68, 0\n"
  "v_accvgpr_write_b32 a69, 0\n"
  "v_accvgpr_write_b32 a70, 0\n"
  "v_accvgpr_write_b32 a71, 0\n"
  "v_accvgpr_write_b32 a72, 0\n"
  "v_accvgpr_write_b32 a73, 0\n"
  "v_accvgpr_write_b32 a74, 0\n"
  "v_accvgpr_write_b32 a75, 0\n"
  "v_accvgpr_write_b32 a76, 0\n"
  "v_accvgpr_write_b32 a77, 0\n"
  "v_accvgpr_write_b32 a78, 0\n"
  "v_accvgpr_write_b32 a79, 0\n"
  "v_accvgpr_write_b32 a80, 0\n"
  "v_accvgpr_write_b32 a81, 0\n"
  "v_accvgpr_write_b32 a82, 0\n"
  "v_accvgpr_write_b32 a83, 0\n"
  "v_accvgpr_write_b32 a84, 0\n"
  "v_accvgpr_write_b32 a85, 0\n"
  "v_accvgpr_write_b32 a86, 0\n"
  "v_accvgpr_write_b32 a87, 0\n"
  "v_accvgpr_write_b32 a88, 0\n"
  "v_accvgpr_write_b32 a89, 0\n"
  "v_accvgpr_write_b32 a90, 0\n"
  "v_accvgpr_write_b32 a91, 0\n"
  "v_accvgpr_write_b32 a92, 0\n"
  "v_accvgpr_write_b32 a93, 0\n"
  "v_accvgpr_write_b32 a94, 0\n"
  "v_accvgpr_write_b32 a95, 0\n"
  "v_accvgpr_write_b32 a96, 0\n"
  "v_accvgpr_write_b32 a97, 0\n"
  "v_accvgpr_write_b32 a98, 0\n"
  "v_accvgpr_write_b32 a99, 0\n"
  "v_accvgpr_write_b32 a100, 0\n"
  "v_accvgpr_write_b32 a101, 0\n"
  "v_accvgpr_write_b32 a102, 0\n"
  "v_accvgpr_write_b32 a103, 0\n"
  "v_accvgpr_write_b32 a104, 0\n"
  "v_accvgpr_write_b32 a105, 0\n"
  "v_accvgpr_write_b32 a106, 0\n"
  "v_accvgpr_write_b32 a107, 0\n"
  "v_accvgpr_write_b32 a108, 0\n"
  "v_accvgpr_write_b32 a109, 0\n"
  "v_accvgpr_write_b32 a110, 0\n"
  "v_accvgpr_write_b32 a111, 0\n"
  "v_accvgpr_write_b32 a112, 0\n"
  "v_accvgpr_write_b32 a113, 0\n"
  "v_accvgpr_write_b32 a114, 0\n"
  "v_accvgpr_write_b32 a115, 0\n"
  "v_accvgpr_write_b32 a116, 0\n"
  "v_accvgpr_write_b32 a117, 0\n"
  "v_accvgpr_write_b32 a118, 0\n"
  "v_accvgpr_write_b32 a119, 0\n"
  "v_accvgpr_write_b32 a120, 0\n"
  "v_accvgpr_write_b32 a121, 0\n"
  "v_accvgpr_write_b32 a122, 0\n"
  "v_accvgpr_write_b32 a123, 0\n"
  "v_accvgpr_write_b32 a124, 0\n"
  "v_accvgpr_write_b32 a125, 0\n"
  "v_accvgpr_write_b32 a126, 0\n"
  "v_accvgpr_write_b32 a127, 0\n"
  "s_mov_b32 s96, 0\n"
  "mk_slow_l:\n"
  "s_add_u32 s61, s96, 0\n"
  "buffer_load_dwordx4 a[128:131], v244, s[44:47], s61 offen offset:0 sc1\n"
  "buffer_load_dwordx4 a[132:135], v244, s[44:47], s61 offen offset:1024 sc1\n"
  "buffer_load_dwordx4 a[136:139], v244, s[44:47], s61 offen offset:2048 sc1\n"
  "buffer_load_dwordx4 a[140:143], v244, s[44:47], s61 offen offset:3072 sc1\n"
  "s_add_u32 s61, s96, 4096\n"
  "buffer_load_dwordx4 a[144:147], v244, s[44:47], s61 offen offset:0 sc1\n"
  "buffer_load_dwordx4 a[148:151], v244, s[44:47], s61 offen offset:1024 sc1\n"
  "buffer_load_dwordx4 a[152:155], v244, s[44:47], s61 offen offset:2048 sc1\n"
  "buffer_load_dwordx4 a[156:159], v244, s[44:47], s61 offen offset:3072 sc1\n"
  "s_add_u32 s61, s96, 8192\n"
  "buffer_load_dwordx4 a[160:163], v244, s[44:47], s61 offen offset:0 sc1\n"
  "buffer_load_dwordx4 a[164:167], v244, s[44:47], s61 offen offset:1024 sc1\n"
  "buffer_load_dwordx4 a[168:171], v244, s[44:47], s61 offen offset:2048 sc1\n"
  "buffer_load_dwordx4 a[172:175], v244, s[44:47], s61 offen offset:3072 sc1\n"
  "s_add_u32 s61, s96, 12288\n"
  "buffer_load_dwordx4 a[176:179], v244, s[44:47], s61 offen offset:0 sc1\n"
  "buffer_load_dwordx4 a[180:183], v244, s[44:47], s61 offen offset:1024 sc1\n"
  "buffer_load_dwordx4 a[184:187], v244, s[44:47], s61 offen offset:2048 sc1\n"
  "buffer_load_dwordx4 a[188:191], v244, s[44:47], s61 offen offset:3072 sc1\n"
  "s_add_u32 s61, s96, 16384\n"
  "buffer_load_dwordx4 a[192:195], v244, s[44:47], s61 offen offset:0 sc1\n"
  "buffer_load_dwordx4 a[196:199], v244, s[44:47], s61 offen offset:1024 sc1\n"
  "buffer_load_dwordx4 a[200:203], v244, s[44:47], s61 offen offset:2048 sc1\n"
  "buffer_load_dwordx4 a[204:207], v244, s[44:47], s61 offen offset:3072 sc1\n"
  "s_add_u32 s61, s96, 20480\n"
  "buffer_load_dwordx4 a[208:211], v244, s[44:47], s61 offen offset:0 sc1\n"
  "buffer_load_dwordx4 a[212:215], v244, s[44:47], s61 offen offset:1024 sc1\n"
  "buffer_load_dwordx4 a[216:219], v244, s[44:47], s61 offen offset:2048 sc1\n"
  "buffer_load_dwordx4 a[220:223], v244, s[44:47], s61 offen offset:3072 sc1\n"
  "s_add_u32 s61, s96, 24576\n"
  "buffer_load_dwordx4 a[224:227], v244, s[44:47], s61 offen offset:0 sc1\n"
  "buffer_load_dwordx4 a[228:231], v244, s[44:47], s61 offen offset:1024 sc1\n"
  "buffer_load_dwordx4 a[232:235], v244, s[44:47], s61 offen offset:2048 sc1\n"
  "buffer_load_dwordx4 a[236:239], v244, s[44:47], s61 offen offset:3072 sc1\n"
  "s_add_u32 s61, s96, 28672\n"
  "buffer_load_dwordx4 a[240:243], v244, s[44:47], s61 offen offset:0 sc1\n"
  "buffer_load_dwordx4 a[244:247], v244, s[44:47], s61 offen offset:1024 sc1\n"
  "buffer_load_dwordx4 a[248:251], v244, s[44:47], s61 offen offset:2048 sc1\n"
  "buffer_load_dwordx4 a[252:255], v244, s[44:47], s61 offen offset:3072 sc1\n"
  "s_waitcnt vmcnt(0)\n"
  "v_mfma_f32_16x16x32_bf16 v[0:3], a[128:131], v[128:131], 0\n"
  "v_mfma_f32_16x16x32_bf16 v[0:3], a[132:135], v[132:135], v[0:3]\n"
  "v_mfma_f32_16x16x32_bf16 v[0:3], a[136:139], v[136:139], v[0:3]\n"
  "v_mfma_f32_16x16x32_bf16 v[0:3], a[140:143], v[140:143], v[0:3]\n"
  "v_mfma_f32_16x16x32_bf16 v[16:19], a[144:147], v[128:131], 0\n"
  "v_mfma_f32_16x16x32_bf16 v[16:19], a[148:151], v[132:135], v[16:19]\n"
  "v_mfma_f32_16x16x32_bf16 v[16:19], a[152:155], v[136:139], v[16:19]\n"
  "v_mfma_f32_16x16x32_bf16 v[16:19], a[156:159], v[140:143], v[16:19]\n"
  "v_mfma_f32_16x16x32_bf16 v[32:35], a[160:163], v[128:131], 0\n"
  "v_mfma_f32_16x16x32_bf16 v[32:35], a[164:167], v[132:135], v[32:35]\n"
  "v_mfma_f32_16x16x32_bf16 v[32:35], a[168:171], v[136:139], v[32:35]\n"
  "v_mfma_f32_16x16x32_bf16 v[32:35], a[172:175], v[140:143], v[32:35]\n"
  "v_mfma_f32_16x16x32_bf16 v[48:51], a[176:179], v[128:131], 0\n"
  "v_mfma_f32_16x16x32_bf16 v[48:51], a[180:183], v[132:135], v[48:51]\n"
  "v_mfma_f32_16x16x32_bf16 v[48:51], a[184:187], v[136:139], v[48:51]\n"
  "v_mfma_f32_16x16x32_bf16 v[48:51], a[188:191], v[140:143], v[48:51]\n"
  "v_mfma_f32_16x16x32_bf16 v[4:7], a[128:131], v[144:147], 0\n"
  "v_mfma_f32_16x16x32_bf16 v[4:7], a[132:135], v[148:151], v[4:7]\n"
  "v_mfma_f32_16x16x32_bf16 v[4:7], a[136:139], v[152:155], v[4:7]\n"
  "v_mfma_f32_16x16x32_bf16 v[4:7], a[140:143], v[156:159], v[4:7]\n"
  "v_mfma_f32_16x16x32_bf16 v[20:23], a[144:147], v[144:147], 0\n"
  "v_mfma_f32_16x16x32_bf16 v[20:23], a[148:151], v[148:151], v[20:23]\n"
  "v_mfma_f32_16x16x32_bf16 v[20:23], a[152:155], v[152:155], v[20:23]\n"
  "v_mfma_f32_16x16x32_bf16 v[20:23], a[156:159], v[156:159], v[20:23]\n"
  "v_mfma_f32_16x16x32_bf16 v[36:39], a[160:163], v[144:147], 0\n"
  "v_mfma_f32_16x16x32_bf16 v[36:39], a[164:167], v[148:151], v[36:39]\n"
  "v_mfma_f32_16x16x32_bf16 v[36:39], a[168:171], v[152:155], v[36:39]\n"
  "v_mfma_f32_16x16x32_bf16 v[36:39], a[172:175], v[156:159], v[36:39]\n"
  "v_mfma_f32_16x16x32_bf16 v[52:55], a[176:179], v[144:147], 0\n"
  "v_mfma_f32_16x16x32_bf16 v[52:55], a[180:183], v[148:151], v[52:55]\n"
  "v_mfma_f32_16x16x32_bf16 v[52:55], a[184:187], v[152:155], v[52:55]\n"
  "v_mfma_f32_16x16x32_bf16 v[52:55], a[188:191], v[156:159], v[52:55]\n"
  "v_mfma_f32_16x16x32_bf16 v[8:11], a[128:131], v[160:163], 0\n"
  "v_mfma_f32_16x16x32_bf16 v[8:11], a[132:135], v[164:167], v[8:11]\n"
  "v_mfma_f32_16x16x32_bf16 v[8:11], a[136:139], v[168:171], v[8:11]\n"
  "v_mfma_f32_16x16x32_bf16 v[8:11], a[140:143], v[172:175], v[8:11]\n"
  "v_mfma_f32_16x16x32_bf16 v[24:27], a[144:147], v[160:163], 0\n"
  "v_mfma_f32_16x16x32_bf16 v[24:27], a[148:151], v[164:167], v[24:27]\n"
  "v_mfma_f32_16x16x32_bf16 v[24:27], a[152:155], v[168:171], v[24:27]\n"
  "v_mfma_f32_16x16x32_bf16 v[24:27], a[156:159], v[172:175], v[24:27]\n"
  "v_mfma_f32_16x16x32_bf16 v[40:43], a[160:163], v[160:163], 0\n"
  "v_mfma_f32_16x16x32_bf16 v[40:43], a[164:167], v[164:167], v[40:43]\n"
  "v_mfma_f32_16x16x32_bf16 v[40:43], a[168:171], v[168:171], v[40:43]\n"
  "v_mfma_f32_16x16x32_bf16 v[40:43], a[172:175], v[172:175], v[40:43]\n"
  "v_mfma_f32_16x16x32_bf16 v[56:59], a[176:179], v[160:163], 0\n"
  "v_mfma_f32_16x16x32_bf16 v[56:59], a[180:183], v[164:167], v[56:59]\n"
  "v_mfma_f32_16x16x32_bf16 v[56:59], a[184:187], v[168:171], v[56:59]\n"
  "v_mfma_f32_16x16x32_bf16 v[56:59], a[188:191], v[172:175], v[56:59]\n"
  "v_mfma_f32_16x16x32_bf16 v[12:15], a[128:131], v[176:179], 0\n"
  "v_mfma_f32_16x16x32_bf16 v[12:15], a[132:135], v[180:183], v[12:15]\n"
  "v_mfma_f32_16x16x32_bf16 v[12:15], a[136:139], v[184:187], v[12:15]\n"
  "v_mfma_f32_16x16x32_bf16 v[12:15], a[140:143], v[188:191], v[12:15]\n"
  "v_mfma_f32_16x16x32_bf16 v[28:31], a[144:147], v[176:179], 0\n"
  "v_mfma_f32_16x16x32_bf16 v[28:31], a[148:151], v[180:183], v[28:31]\n"
  "v_mfma_f32_16x16x32_bf16 v[28:31], a[152:155], v[184:187], v[28:31]\n"
  "v_mfma_f32_16x16x32_bf16 v[28:31], a[156:159], v[188:191], v[28:31]\n"
  "v_mfma_f32_16x16x32_bf16 v[44:47], a[160:163], v[176:179], 0\n"
  "v_mfma_f32_16x16x32_bf16 v[44:47], a[164:167], v[180:183], v[44:47]\n"
  "v_mfma_f32_16x16x32_bf16 v[44:47], a[168:171], v[184:187], v[44:47]\n"
  "v_mfma_f32_16x16x32_bf16 v[44:47], a[172:175], v[188:191], v[44:47]\n"
  "v_mfma_f32_16x16x32_bf16 v[60:63], a[176:179], v[176:179], 0\n"
  "v_mfma_f32_16x16x32_bf16 v[60:63], a[180:183], v[180:183], v[60:63]\n"
  "v_mfma_f32_16x16x32_bf16 v[60:63], a[184:187], v[184:187], v[60:63]\n"
  "v_mfma_f32_16x16x32_bf16 v[60:63], a[188:191], v[188:191], v[60:63]\n"
  "v_mov_b32 v220, v212\n"
  "v_cmp_gt_i32 vcc, 0, v208\n"
  "s_nop 1\n"
  "v_cndmask_b32 v0, v0, v246, vcc\n"
  "v_max_f32 v220, v220, v0\n"
  "v_cmp_gt_i32 vcc, 1, v208\n"
  "s_nop 1\n"
  "v_cndmask_b32 v1, v1, v246, vcc\n"
  "v_max_f32 v220, v220, v1\n"
  "v_cmp_gt_i32 vcc, 2, v208\n"
  "s_nop 1\n"
  "v_cndmask_b32 v2, v2, v246, vcc\n"
  "v_max_f32 v220, v220, v2\n"
  "v_cmp_gt_i32 vcc, 3, v208\n"
  "s_nop 1\n"
  "v_cndmask_b32 v3, v3, v246, vcc\n"
  "v_max_f32 v220, v220, v3\n"
  "v_cmp_gt_i32 vcc, 16, v208\n"
  "s_nop 1\n"
  "v_cndmask_b32 v16, v16, v246, vcc\n"
  "v_max_f32 v220, v220, v16\n"
  "v_cmp_gt_i32 vcc, 17, v208\n"
  "s_nop 1\n"
  "v_cndmask_b32 v17, v17, v246, vcc\n"
  "v_max_f32 v220, v220, v17\n"
  "v_cmp_gt_i32 vcc, 18, v208\n"
  "s_nop 1\n"
  "v_cndmask_b32 v18, v18, v246, vcc\n"
  "v_max_f32 v220, v220, v18\n"
  "v_cmp_gt_i32 vcc, 19, v208\n"
  "s_nop 1\n"
  "v_cndmask_b32 v19, v19, v246, vcc\n"
  "v_max_f32 v220, v220, v19\n"
  "v_cmp_gt_i32 vcc, 32, v208\n"
  "s_nop 1\n"
  "v_cndmask_b32 v32, v32, v246, vcc\n"
  "v_max_f32 v220, v220, v32\n"
  "v_cmp_gt_i32 vcc, 33, v208\n"
  "s_nop 1\n"
  "v_cndmask_b32 v33, v33, v246, vcc\n"
  "v_max_f32 v220, v220, v33\n"
  "v_cmp_gt_i32 vcc, 34, v208\n"
  "s_nop 1\n"
  "v_cndmask_b32 v34, v34, v246, vcc\n"
  "v_max_f32 v220, v220, v34\n"
  "v_cmp_gt_i32 vcc, 35, v208\n"
  "s_nop 1\n"
  "v_cndmask_b32 v35, v35, v246, vcc\n"
  "v_max_f32 v220, v220, v35\n"
  "v_cmp_gt_i32 vcc, 48, v208\n"
  "s_nop 1\n"
  "v_cndmask_b32 v48, v48, v246, vcc\n"
  "v_max_f32 v220, v220, v48\n"
  "v_cmp_gt_i32 vcc, 49, v208\n"
  "s_nop 1\n"
  "v_cndmask_b32 v49, v49, v246, vcc\n"
  "v_max_f32 v220, v220, v49\n"
  "v_cmp_gt_i32 vcc, 50, v208\n"
  "s_nop 1\n"
  "v_cndmask_b32 v50, v50, v246, vcc\n"
  "v_max_f32 v220, v220, v50\n"
  "v_cmp_gt_i32 vcc, 51, v208\n"
  "s_nop 1\n"
  "v_cndmask_b32 v51, v51, v246, vcc\n"
  "v_max_f32 v220, v220, v51\n"
  "v_mov_b32 v221, v220\n"
  "s_nop 1\n"
  "v_permlane16_swap_b32 v220, v221\n"
  "v_max_f32 v220, v220, v221\n"
  "v_mov_b32 v221, v220\n"
  "s_nop 1\n"
  "v_permlane32_swap_b32 v220, v221\n"
  "v_max_f32 v220, v220, v221\n"
  "v_sub_f32 v216, v212, v220\n"
  "v_exp_f32 v216, v216\n"
  "v_mov_b32 v212, v220\n"
  "v_subrev_u32 v208, 64, v208\n"
  "v_mul_f32 v224, v224, v216\n"
  "v_mul_f32 v225, v225, v216\n"
  "v_mul_f32 v226, v226, v216\n"
  "v_mul_f32 v227, v227, v216\n"
  "v_accvgpr_read_b32 v221, a0\n"
  "v_mul_f32 v221, v221, v216\n"
  "v_accvgpr_write_b32 a0, v221\n"
  "v_accvgpr_read_b32 v221, a1\n"
  "v_mul_f32 v221, v221, v216\n"
  "v_accvgpr_write_b32 a1, v221\n"
  "v_accvgpr_read_b32 v221, a2\n"
  "v_mul_f32 v221, v221, v216\n"
  "v_accvgpr_write_b32 a2, v221\n"
  "v_accvgpr_read_b32 v221, a3\n"
  "v_mul_f32 v221, v221, v216\n"
  "v_accvgpr_write_b32 a3, v221\n"
  "v_accvgpr_read_b32 v221, a4\n"
  "v_mul_f32 v221, v221, v216\n"
  "v_accvgpr_write_b32 a4, v221\n"
  "v_accvgpr_read_b32 v221, a5\n"
  "v_mul_f32 v221, v221, v216\n"
  "v_accvgpr_write_b32 a5, v221\n"
  "v_accvgpr_read_b32 v221, a6\n"
  "v_mul_f32 v221, v221, v216\n"
  "v_accvgpr_write_b32 a6, v221\n"
  "v_accvgpr_read_b32 v221, a7\n"
  "v_mul_f32 v221, v221, v216\n"
  "v_accvgpr_write_b32 a7, v221\n"
  "v_accvgpr_read_b32 v221, a8\n"
  "v_mul_f32 v221, v221, v216\n"
  "v_accvgpr_write_b32 a8, v221\n"
  "v_accvgpr_read_b32 v221, a9\n"
  "v_mul_f32 v221, v221, v216\n"
  "v_accvgpr_write_b32 a9, v221\n"
  "v_accvgpr_read_b32 v221, a10\n"
  "v_mul_f32 v221, v221, v216\n"
  "v_accvgpr_write_b32 a10, v221\n"
  "v_accvgpr_read_b32 v221, a11\n"
  "v_mul_f32 v221, v221, v216\n"
  "v_accvgpr_write_b32 a11, v221\n"
  "v_accvgpr_read_b32 v221, a12\n"
  "v_mul_f32 v221, v221, v216\n"
  "v_accvgpr_write_b32 a12, v221\n"
  "v_accvgpr_read_b32 v221, a13\n"
  "v_mul_f32 v221, v221, v216\n"
  "v_accvgpr_write_b32 a13, v221\n"
  "v_accvgpr_read_b32 v221, a14\n"
  "v_mul_f32 v221, v221, v216\n"
  "v_accvgpr_write_b32 a14, v221\n"
  "v_accvgpr_read_b32 v221, a15\n"
  "v_mul_f32 v221, v221, v216\n"
  "v_accvgpr_write_b32 a15, v221\n"
  "v_accvgpr_read_b32 v221, a16\n"
  "v_mul_f32 v221, v221, v216\n"
  "v_accvgpr_write_b32 a16, v221\n"
  "v_accvgpr_read_b32 v221, a17\n"
  "v_mul_f32 v221, v221, v216\n"
  "v_accvgpr_write_b32 a17, v221\n"
  "v_accvgpr_read_b32 v221, a18\n"
  "v_mul_f32 v221, v221, v216\n"
  "v_accvgpr_write_b32 a18, v221\n"
  "v_accvgpr_read_b32 v221, a19\n"
  "v_mul_f32 v221, v221, v216\n"
  "v_accvgpr_write_b32 a19, v221\n"
  "v_accvgpr_read_b32 v221, a20\n"
  "v_mul_f32 v221, v221, v216\n"
  "v_accvgpr_write_b32 a20, v221\n"
  "v_accvgpr_read_b32 v221, a21\n"
  "v_mul_f32 v221, v221, v216\n"
  "v_accvgpr_write_b32 a21, v221\n"
  "v_accvgpr_read_b32 v221, a22\n"
  "v_mul_f32 v221, v221, v216\n"
  "v_accvgpr_write_b32 a22, v221\n"
  "v_accvgpr_read_b32 v221, a23\n"
  "v_mul_f32 v221, v221, v216\n"
  "v_accvgpr_write_b32 a23, v221\n"
  "v_accvgpr_read_b32 v221, a24\n"
  "v_mul_f32 v221, v221, v216\n"
  "v_accvgpr_write_b32 a24, v221\n"
  "v_accvgpr_read_b32 v221, a25\n"
  "v_mul_f32 v221, v221, v216\n"
  "v_accvgpr_write_b32 a25, v221\n"
  "v_accvgpr_read_b32 v221, a26\n"
  "v_mul_f32 v221, v221, v216\n"
  "v_accvgpr_write_b32 a26, v221\n"
  "v_accvgpr_read_b32 v221, a27\n"
  "v_mul_f32 v221, v221, v216\n"
  "v_accvgpr_write_b32 a27, v221\n"
  "v_accvgpr_read_b32 v221, a28\n"
  "v_mul_f32 v221, v221, v216\n"
  "v_accvgpr_write_b32 a28, v221\n"
  "v_accvgpr_read_b32 v221, a29\n"
  "v_mul_f32 v221, v221, v216\n"
  "v_accvgpr_write_b32 a29, v221\n"
  "v_accvgpr_read_b32 v221, a30\n"
  "v_mul_f32 v221, v221, v216\n"
  "v_accvgpr_write_b32 a30, v221\n"
  "v_accvgpr_read_b32 v221, a31\n"
  "v_mul_f32 v221, v221, v216\n"
  "v_accvgpr_write_b32 a31, v221\n"
  "v_sub_f32 v0, v0, v220\n"
  "v_exp_f32 v0, v0\n"
  "v_sub_f32 v1, v1, v220\n"
  "v_exp_f32 v1, v1\n"
  "v_sub_f32 v2, v2, v220\n"
  "v_exp_f32 v2, v2\n"
  "v_sub_f32 v3, v3, v220\n"
  "v_exp_f32 v3, v3\n"
  "v_cvt_pk_bf16_f32 v64, v0, v1\n"
  "v_cvt_pk_bf16_f32 v65, v2, v3\n"
  "v_sub_f32 v16, v16, v220\n"
  "v_exp_f32 v16, v16\n"
  "v_sub_f32 v17, v17, v220\n"
  "v_exp_f32 v17, v17\n"
  "v_sub_f32 v18, v18, v220\n"
  "v_exp_f32 v18, v18\n"
  "v_sub_f32 v19, v19, v220\n"
  "v_exp_f32 v19, v19\n"
  "v_cvt_pk_bf16_f32 v66, v16, v17\n"
  "v_cvt_pk_bf16_f32 v67, v18, v19\n"
  "v_sub_f32 v32, v32, v220\n"
  "v_exp_f32 v32, v32\n"
  "v_sub_f32 v33, v33, v220\n"
  "v_exp_f32 v33, v33\n"
  "v_sub_f32 v34, v34, v220\n"
  "v_exp_f32 v34, v34\n"
  "v_sub_f32 v35, v35, v220\n"
  "v_exp_f32 v35, v35\n"
  "v_cvt_pk_bf16_f32 v80, v32, v33\n"
  "v_cvt_pk_bf16_f32 v81, v34, v35\n"
  "v_sub_f32 v48, v48, v220\n"
  "v_exp_f32 v48, v48\n"
  "v_sub_f32 v49, v49, v220\n"
  "v_exp_f32 v49, v49\n"
  "v_sub_f32 v50, v50, v220\n"
  "v_exp_f32 v50, v50\n"
  "v_sub_f32 v51, v51, v220\n"
  "v_exp_f32 v51, v51\n"
  "v_cvt_pk_bf16_f32 v82, v48, v49\n"
  "v_cvt_pk_bf16_f32 v83, v50, v51\n"
  "v_mov_b32 v220, v213\n"
  "v_cmp_gt_i32 vcc, 0, v209\n"
  "s_nop 1\n"
  "v_cndmask_b32 v4, v4, v246, vcc\n"
  "v_max_f32 v220, v220, v4\n"
  "v_cmp_gt_i32 vcc, 1, v209\n"
  "s_nop 1\n"
  "v_cndmask_b32 v5, v5, v246, vcc\n"
  "v_max_f32 v220, v220, v5\n"
  "v_cmp_gt_i32 vcc, 2, v209\n"
  "s_nop 1\n"
  "v_cndmask_b32 v6, v6, v246, vcc\n"
  "v_max_f32 v220, v220, v6\n"
  "v_cmp_gt_i32 vcc, 3, v209\n"
  "s_nop 1\n"
  "v_cndmask_b32 v7, v7, v246, vcc\n"
  "v_max_f32 v220, v220, v7\n"
  "v_cmp_gt_i32 vcc, 16, v209\n"
  "s_nop 1\n"
  "v_cndmask_b32 v20, v20, v246, vcc\n"
  "v_max_f32 v220, v220, v20\n"
  "v_cmp_gt_i32 vcc, 17, v209\n"
  "s_nop 1\n"
  "v_cndmask_b32 v21, v21, v246, vcc\n"
  "v_max_f32 v220, v220, v21\n"
  "v_cmp_gt_i32 vcc, 18, v209\n"
  "s_nop 1\n"
  "v_cndmask_b32 v22, v22, v246, vcc\n"
  "v_max_f32 v220, v220, v22\n"
  "v_cmp_gt_i32 vcc, 19, v209\n"
  "s_nop 1\n"
  "v_cndmask_b32 v23, v23, v246, vcc\n"
  "v_max_f32 v220, v220, v23\n"
  "v_cmp_gt_i32 vcc, 32, v209\n"
  "s_nop 1\n"
  "v_cndmask_b32 v36, v36, v246, vcc\n"
  "v_max_f32 v220, v220, v36\n"
  "v_cmp_gt_i32 vcc, 33, v209\n"
  "s_nop 1\n"
  "v_cndmask_b32 v37, v37, v246, vcc\n"
  "v_max_f32 v220, v220, v37\n"
  "v_cmp_gt_i32 vcc, 34, v209\n"
  "s_nop 1\n"
  "v_cndmask_b32 v38, v38, v246, vcc\n"
  "v_max_f32 v220, v220, v38\n"
  "v_cmp_gt_i32 vcc, 35, v209\n"
  "s_nop 1\n"
  "v_cndmask_b32 v39, v39, v246, vcc\n"
  "v_max_f32 v220, v220, v39\n"
  "v_cmp_gt_i32 vcc, 48, v209\n"
  "s_nop 1\n"
  "v_cndmask_b32 v52, v52, v246, vcc\n"
  "v_max_f32 v220, v220, v52\n"
  "v_cmp_gt_i32 vcc, 49, v209\n"
  "s_nop 1\n"
  "v_cndmask_b32 v53, v53, v246, vcc\n"
  "v_max_f32 v220, v220, v53\n"
  "v_cmp_gt_i32 vcc, 50, v209\n"
  "s_nop 1\n"
  "v_cndmask_b32 v54, v54, v246, vcc\n"
  "v_max_f32 v220, v220, v54\n"
  "v_cmp_gt_i32 vcc, 51, v209\n"
  "s_nop 1\n"
  "v_cndmask_b32 v55, v55, v246, vcc\n"
  "v_max_f32 v220, v220, v55\n"
  "v_mov_b32 v221, v220\n"
  "s_nop 1\n"
  "v_permlane16_swap_b32 v220, v221\n"
  "v_max_f32 v220, v220, v221\n"
  "v_mov_b32 v221, v220\n"
  "s_nop 1\n"
  "v_permlane32_swap_b32 v220, v221\n"
  "v_max_f32 v220, v220, v221\n"
  "v_sub_f32 v216, v213, v220\n"
  "v_exp_f32 v216, v216\n"
  "v_mov_b32 v213, v220\n"
  "v_subrev_u32 v209, 64, v209\n"
  "v_mul_f32 v228, v228, v216\n"
  "v_mul_f32 v229, v229, v216\n"
  "v_mul_f32 v230, v230, v216\n"
  "v_mul_f32 v231, v231, v216\n"
  "v_accvgpr_read_b32 v221, a32\n"
  "v_mul_f32 v221, v221, v216\n"
  "v_accvgpr_write_b32 a32, v221\n"
  "v_accvgpr_read_b32 v221, a33\n"
  "v_mul_f32 v221, v221, v216\n"
  "v_accvgpr_write_b32 a33, v221\n"
  "v_accvgpr_read_b32 v221, a34\n"
  "v_mul_f32 v221, v221, v216\n"
  "v_accvgpr_write_b32 a34, v221\n"
  "v_accvgpr_read_b32 v221, a35\n"
  "v_mul_f32 v221, v221, v216\n"
  "v_accvgpr_write_b32 a35, v221\n"
  "v_accvgpr_read_b32 v221, a36\n"
  "v_mul_f32 v221, v221, v216\n"
  "v_accvgpr_write_b32 a36, v221\n"
  "v_accvgpr_read_b32 v221, a37\n"
  "v_mul_f32 v221, v221, v216\n"
  "v_accvgpr_write_b32 a37, v221\n"
  "v_accvgpr_read_b32 v221, a38\n"
  "v_mul_f32 v221, v221, v216\n"
  "v_accvgpr_write_b32 a38, v221\n"
  "v_accvgpr_read_b32 v221, a39\n"
  "v_mul_f32 v221, v221, v216\n"
  "v_accvgpr_write_b32 a39, v221\n"
  "v_accvgpr_read_b32 v221, a40\n"
  "v_mul_f32 v221, v221, v216\n"
  "v_accvgpr_write_b32 a40, v221\n"
  "v_accvgpr_read_b32 v221, a41\n"
  "v_mul_f32 v221, v221, v216\n"
  "v_accvgpr_write_b32 a41, v221\n"
  "v_accvgpr_read_b32 v221, a42\n"
  "v_mul_f32 v221, v221, v216\n"
  "v_accvgpr_write_b32 a42, v221\n"
  "v_accvgpr_read_b32 v221, a43\n"
  "v_mul_f32 v221, v221, v216\n"
  "v_accvgpr_write_b32 a43, v221\n"
  "v_accvgpr_read_b32 v221, a44\n"
  "v_mul_f32 v221, v221, v216\n"
  "v_accvgpr_write_b32 a44, v221\n"
  "v_accvgpr_read_b32 v221, a45\n"
  "v_mul_f32 v221, v221, v216\n"
  "v_accvgpr_write_b32 a45, v221\n"
  "v_accvgpr_read_b32 v221, a46\n"
  "v_mul_f32 v221, v221, v216\n"
  "v_accvgpr_write_b32 a46, v221\n"
  "v_accvgpr_read_b32 v221, a47\n"
  "v_mul_f32 v221, v221, v216\n"
  "v_accvgpr_write_b32 a47, v221\n"
  "v_accvgpr_read_b32 v221, a48\n"
  "v_mul_f32 v221, v221, v216\n"
  "v_accvgpr_write_b32 a48, v221\n"
  "v_accvgpr_read_b32 v221, a49\n"
  "v_mul_f32 v221, v221, v216\n"
  "v_accvgpr_write_b32 a49, v221\n"
  "v_accvgpr_read_b32 v221, a50\n"
  "v_mul_f32 v221, v221, v216\n"
  "v_accvgpr_write_b32 a50, v221\n"
  "v_accvgpr_read_b32 v221, a51\n"
  "v_mul_f32 v221, v221, v216\n"
  "v_accvgpr_write_b32 a51, v221\n"
  "v_accvgpr_read_b32 v221, a52\n"
  "v_mul_f32 v221, v221, v216\n"
  "v_accvgpr_write_b32 a52, v221\n"
  "v_accvgpr_read_b32 v221, a53\n"
  "v_mul_f32 v221, v221, v216\n"
  "v_accvgpr_write_b32 a53, v221\n"
  "v_accvgpr_read_b32 v221, a54\n"
  "v_mul_f32 v221, v221, v216\n"
  "v_accvgpr_write_b32 a54, v221\n"
  "v_accvgpr_read_b32 v221, a55\n"
  "v_mul_f32 v221, v221, v216\n"
  "v_accvgpr_write_b32 a55, v221\n"
  "v_accvgpr_read_b32 v221, a56\n"
  "v_mul_f32 v221, v221, v216\n"
  "v_accvgpr_write_b32 a56, v221\n"
  "v_accvgpr_read_b32 v221, a57\n"
  "v_mul_f32 v221, v221, v216\n"
  "v_accvgpr_write_b32 a57, v221\n"
  "v_accvgpr_read_b32 v221, a58\n"
  "v_mul_f32 v221, v221, v216\n"
  "v_accvgpr_write_b32 a58, v221\n"
  "v_accvgpr_read_b32 v221, a59\n"
  "v_mul_f32 v221, v221, v216\n"
  "v_accvgpr_write_b32 a59, v221\n"
  "v_accvgpr_read_b32 v221, a60\n"
  "v_mul_f32 v221, v221, v216\n"
  "v_accvgpr_write_b32 a60, v221\n"
  "v_accvgpr_read_b32 v221, a61\n"
  "v_mul_f32 v221, v221, v216\n"
  "v_accvgpr_write_b32 a61, v221\n"
  "v_accvgpr_read_b32 v221, a62\n"
  "v_mul_f32 v221, v221, v216\n"
  "v_accvgpr_write_b32 a62, v221\n"
  "v_accvgpr_read_b32 v221, a63\n"
  "v_mul_f32 v221, v221, v216\n"
  "v_accvgpr_write_b32 a63, v221\n"
  "v_sub_f32 v4, v4, v220\n"
  "v_exp_f32 v4, v4\n"
  "v_sub_f32 v5, v5, v220\n"
  "v_exp_f32 v5, v5\n"
  "v_sub_f32 v6, v6, v220\n"
  "v_exp_f32 v6, v6\n"
  "v_sub_f32 v7, v7, v220\n"
  "v_exp_f32 v7, v7\n"
  "v_cvt_pk_bf16_f32 v68, v4, v5\n"
  "v_cvt_pk_bf16_f32 v69, v6, v7\n"
  "v_sub_f32 v20, v20, v220\n"
  "v_exp_f32 v20, v20\n"
  "v_sub_f32 v21, v21, v220\n"
  "v_exp_f32 v21, v21\n"
  "v_sub_f32 v22, v22, v220\n"
  "v_exp_f32 v22, v22\n"
  "v_sub_f32 v23, v23, v220\n"
  "v_exp_f32 v23, v23\n"
  "v_cvt_pk_bf16_f32 v70, v20, v21\n"
  "v_cvt_pk_bf16_f32 v71, v22, v23\n"
  "v_sub_f32 v36, v36, v220\n"
  "v_exp_f32 v36, v36\n"
  "v_sub_f32 v37, v37, v220\n"
  "v_exp_f32 v37, v37\n"
  "v_sub_f32 v38, v38, v220\n"
  "v_exp_f32 v38, v38\n"
  "v_sub_f32 v39, v39, v220\n"
  "v_exp_f32 v39, v39\n"
  "v_cvt_pk_bf16_f32 v84, v36, v37\n"
  "v_cvt_pk_bf16_f32 v85, v38, v39\n"
  "v_sub_f32 v52, v52, v220\n"
  "v_exp_f32 v52, v52\n"
  "v_sub_f32 v53, v53, v220\n"
  "v_exp_f32 v53, v53\n"
  "v_sub_f32 v54, v54, v220\n"
  "v_exp_f32 v54, v54\n"
  "v_sub_f32 v55, v55, v220\n"
  "v_exp_f32 v55, v55\n"
  "v_cvt_pk_bf16_f32 v86, v52, v53\n"
  "v_cvt_pk_bf16_f32 v87, v54, v55\n"
  "v_mov_b32 v220, v214\n"
  "v_cmp_gt_i32 vcc, 0, v210\n"
  "s_nop 1\n"
  "v_cndmask_b32 v8, v8, v246, vcc\n"
  "v_max_f32 v220, v220, v8\n"
  "v_cmp_gt_i32 vcc, 1, v210\n"
  "s_nop 1\n"
  "v_cndmask_b32 v9, v9, v246, vcc\n"
  "v_max_f32 v220, v220, v9\n"
  "v_cmp_gt_i32 vcc, 2, v210\n"
  "s_nop 1\n"
  "v_cndmask_b32 v10, v10, v246, vcc\n"
  "v_max_f32 v220, v220, v10\n"
  "v_cmp_gt_i32 vcc, 3, v210\n"
  "s_nop 1\n"
  "v_cndmask_b32 v11, v11, v246, vcc\n"
  "v_max_f32 v220, v220, v11\n"
  "v_cmp_gt_i32 vcc, 16, v210\n"
  "s_nop 1\n"
  "v_cndmask_b32 v24, v24, v246, vcc\n"
  "v_max_f32 v220, v220, v24\n"
  "v_cmp_gt_i32 vcc, 17, v210\n"
  "s_nop 1\n"
  "v_cndmask_b32 v25, v25, v246, vcc\n"
  "v_max_f32 v220, v220, v25\n"
  "v_cmp_gt_i32 vcc, 18, v210\n"
  "s_nop 1\n"
  "v_cndmask_b32 v26, v26, v246, vcc\n"
  "v_max_f32 v220, v220, v26\n"
  "v_cmp_gt_i32 vcc, 19, v210\n"
  "s_nop 1\n"
  "v_cndmask_b32 v27, v27, v246, vcc\n"
  "v_max_f32 v220, v220, v27\n"
  "v_cmp_gt_i32 vcc, 32, v210\n"
  "s_nop 1\n"
  "v_cndmask_b32 v40, v40, v246, vcc\n"
  "v_max_f32 v220, v220, v40\n"
  "v_cmp_gt_i32 vcc, 33, v210\n"
  "s_nop 1\n"
  "v_cndmask_b32 v41, v41, v246, vcc\n"
  "v_max_f32 v220, v220, v41\n"
  "v_cmp_gt_i32 vcc, 34, v210\n"
  "s_nop 1\n"
  "v_cndmask_b32 v42, v42, v246, vcc\n"
  "v_max_f32 v220, v220, v42\n"
  "v_cmp_gt_i32 vcc, 35, v210\n"
  "s_nop 1\n"
  "v_cndmask_b32 v43, v43, v246, vcc\n"
  "v_max_f32 v220, v220, v43\n"
  "v_cmp_gt_i32 vcc, 48, v210\n"
  "s_nop 1\n"
  "v_cndmask_b32 v56, v56, v246, vcc\n"
  "v_max_f32 v220, v220, v56\n"
  "v_cmp_gt_i32 vcc, 49, v210\n"
  "s_nop 1\n"
  "v_cndmask_b32 v57, v57, v246, vcc\n"
  "v_max_f32 v220, v220, v57\n"
  "v_cmp_gt_i32 vcc, 50, v210\n"
  "s_nop 1\n"
  "v_cndmask_b32 v58, v58, v246, vcc\n"
  "v_max_f32 v220, v220, v58\n"
  "v_cmp_gt_i32 vcc, 51, v210\n"
  "s_nop 1\n"
  "v_cndmask_b32 v59, v59, v246, vcc\n"
  "v_max_f32 v220, v220, v59\n"
  "v_mov_b32 v221, v220\n"
  "s_nop 1\n"
  "v_permlane16_swap_b32 v220, v221\n"
  "v_max_f32 v220, v220, v221\n"
  "v_mov_b32 v221, v220\n"
  "s_nop 1\n"
  "v_permlane32_swap_b32 v220, v221\n"
  "v_max_f32 v220, v220, v221\n"
  "v_sub_f32 v216, v214, v220\n"
  "v_exp_f32 v216, v216\n"
  "v_mov_b32 v214, v220\n"
  "v_subrev_u32 v210, 64, v210\n"
  "v_mul_f32 v232, v232, v216\n"
  "v_mul_f32 v233, v233, v216\n"
  "v_mul_f32 v234, v234, v216\n"
  "v_mul_f32 v235, v235, v216\n"
  "v_accvgpr_read_b32 v221, a64\n"
  "v_mul_f32 v221, v221, v216\n"
  "v_accvgpr_write_b32 a64, v221\n"
  "v_accvgpr_read_b32 v221, a65\n"
  "v_mul_f32 v221, v221, v216\n"
  "v_accvgpr_write_b32 a65, v221\n"
  "v_accvgpr_read_b32 v221, a66\n"
  "v_mul_f32 v221, v221, v216\n"
  "v_accvgpr_write_b32 a66, v221\n"
  "v_accvgpr_read_b32 v221, a67\n"
  "v_mul_f32 v221, v221, v216\n"
  "v_accvgpr_write_b32 a67, v221\n"
  "v_accvgpr_read_b32 v221, a68\n"
  "v_mul_f32 v221, v221, v216\n"
  "v_accvgpr_write_b32 a68, v221\n"
  "v_accvgpr_read_b32 v221, a69\n"
  "v_mul_f32 v221, v221, v216\n"
  "v_accvgpr_write_b32 a69, v221\n"
  "v_accvgpr_read_b32 v221, a70\n"
  "v_mul_f32 v221, v221, v216\n"
  "v_accvgpr_write_b32 a70, v221\n"
  "v_accvgpr_read_b32 v221, a71\n"
  "v_mul_f32 v221, v221, v216\n"
  "v_accvgpr_write_b32 a71, v221\n"
  "v_accvgpr_read_b32 v221, a72\n"
  "v_mul_f32 v221, v221, v216\n"
  "v_accvgpr_write_b32 a72, v221\n"
  "v_accvgpr_read_b32 v221, a73\n"
  "v_mul_f32 v221, v221, v216\n"
  "v_accvgpr_write_b32 a73, v221\n"
  "v_accvgpr_read_b32 v221, a74\n"
  "v_mul_f32 v221, v221, v216\n"
  "v_accvgpr_write_b32 a74, v221\n"
  "v_accvgpr_read_b32 v221, a75\n"
  "v_mul_f32 v221, v221, v216\n"
  "v_accvgpr_write_b32 a75, v221\n"
  "v_accvgpr_read_b32 v221, a76\n"
  "v_mul_f32 v221, v221, v216\n"
  "v_accvgpr_write_b32 a76, v221\n"
  "v_accvgpr_read_b32 v221, a77\n"
  "v_mul_f32 v221, v221, v216\n"
  "v_accvgpr_write_b32 a77, v221\n"
  "v_accvgpr_read_b32 v221, a78\n"
  "v_mul_f32 v221, v221, v216\n"
  "v_accvgpr_write_b32 a78, v221\n"
  "v_accvgpr_read_b32 v221, a79\n"
  "v_mul_f32 v221, v221, v216\n"
  "v_accvgpr_write_b32 a79, v221\n"
  "v_accvgpr_read_b32 v221, a80\n"
  "v_mul_f32 v221, v221, v216\n"
  "v_accvgpr_write_b32 a80, v221\n"
  "v_accvgpr_read_b32 v221, a81\n"
  "v_mul_f32 v221, v221, v216\n"
  "v_accvgpr_write_b32 a81, v221\n"
  "v_accvgpr_read_b32 v221, a82\n"
  "v_mul_f32 v221, v221, v216\n"
  "v_accvgpr_write_b32 a82, v221\n"
  "v_accvgpr_read_b32 v221, a83\n"
  "v_mul_f32 v221, v221, v216\n"
  "v_accvgpr_write_b32 a83, v221\n"
  "v_accvgpr_read_b32 v221, a84\n"
  "v_mul_f32 v221, v221, v216\n"
  "v_accvgpr_write_b32 a84, v221\n"
  "v_accvgpr_read_b32 v221, a85\n"
  "v_mul_f32 v221, v221, v216\n"
  "v_accvgpr_write_b32 a85, v221\n"
  "v_accvgpr_read_b32 v221, a86\n"
  "v_mul_f32 v221, v221, v216\n"
  "v_accvgpr_write_b32 a86, v221\n"
  "v_accvgpr_read_b32 v221, a87\n"
  "v_mul_f32 v221, v221, v216\n"
  "v_accvgpr_write_b32 a87, v221\n"
  "v_accvgpr_read_b32 v221, a88\n"
  "v_mul_f32 v221, v221, v216\n"
  "v_accvgpr_write_b32 a88, v221\n"
  "v_accvgpr_read_b32 v221, a89\n"
  "v_mul_f32 v221, v221, v216\n"
  "v_accvgpr_write_b32 a89, v221\n"
  "v_accvgpr_read_b32 v221, a90\n"
  "v_mul_f32 v221, v221, v216\n"
  "v_accvgpr_write_b32 a90, v221\n"
  "v_accvgpr_read_b32 v221, a91\n"
  "v_mul_f32 v221, v221, v216\n"
  "v_accvgpr_write_b32 a91, v221\n"
  "v_accvgpr_read_b32 v221, a92\n"
  "v_mul_f32 v221, v221, v216\n"
  "v_accvgpr_write_b32 a92, v221\n"
  "v_accvgpr_read_b32 v221, a93\n"
  "v_mul_f32 v221, v221, v216\n"
  "v_accvgpr_write_b32 a93, v221\n"
  "v_accvgpr_read_b32 v221, a94\n"
  "v_mul_f32 v221, v221, v216\n"
  "v_accvgpr_write_b32 a94, v221\n"
  "v_accvgpr_read_b32 v221, a95\n"
  "v_mul_f32 v221, v221, v216\n"
  "v_accvgpr_write_b32 a95, v221\n"
  "v_sub_f32 v8, v8, v220\n"
  "v_exp_f32 v8, v8\n"
  "v_sub_f32 v9, v9, v220\n"
  "v_exp_f32 v9, v9\n"
  "v_sub_f32 v10, v10, v220\n"
  "v_exp_f32 v10, v10\n"
  "v_sub_f32 v11, v11, v220\n"
  "v_exp_f32 v11, v11\n"
  "v_cvt_pk_bf16_f32 v72, v8, v9\n"
  "v_cvt_pk_bf16_f32 v73, v10, v11\n"
  "v_sub_f32 v24, v24, v220\n"
  "v_exp_f32 v24, v24\n"
  "v_sub_f32 v25, v25, v220\n"
  "v_exp_f32 v25, v25\n"
  "v_sub_f32 v26, v26, v220\n"
  "v_exp_f32 v26, v26\n"
  "v_sub_f32 v27, v27, v220\n"
  "v_exp_f32 v27, v27\n"
  "v_cvt_pk_bf16_f32 v74, v24, v25\n"
  "v_cvt_pk_bf16_f32 v75, v26, v27\n"
  "v_sub_f32 v40, v40, v220\n"
  "v_exp_f32 v40, v40\n"
  "v_sub_f32 v41, v41, v220\n"
  "v_exp_f32 v41, v41\n"
  "v_sub_f32 v42, v42, v220\n"
  "v_exp_f32 v42, v42\n"
  "v_sub_f32 v43, v43, v220\n"
  "v_exp_f32 v43, v43\n"
  "v_cvt_pk_bf16_f32 v88, v40, v41\n"
  "v_cvt_pk_bf16_f32 v89, v42, v43\n"
  "v_sub_f32 v56, v56, v220\n"
  "v_exp_f32 v56, v56\n"
  "v_sub_f32 v57, v57, v220\n"
  "v_exp_f32 v57, v57\n"
  "v_sub_f32 v58, v58, v220\n"
  "v_exp_f32 v58, v58\n"
  "v_sub_f32 v59, v59, v220\n"
  "v_exp_f32 v59, v59\n"
  "v_cvt_pk_bf16_f32 v90, v56, v57\n"
  "v_cvt_pk_bf16_f32 v91, v58, v59\n"
  "v_mov_b32 v220, v215\n"
  "v_cmp_gt_i32 vcc, 0, v211\n"
  "s_nop 1\n"
  "v_cndmask_b32 v12, v12, v246, vcc\n"
  "v_max_f32 v220, v220, v12\n"
  "v_cmp_gt_i32 vcc, 1, v211\n"
  "s_nop 1\n"
  "v_cndmask_b32 v13, v13, v246, vcc\n"
  "v_max_f32 v220, v220, v13\n"
  "v_cmp_gt_i32 vcc, 2, v211\n"
  "s_nop 1\n"
  "v_cndmask_b32 v14, v14, v246, vcc\n"
  "v_max_f32 v220, v220, v14\n"
  "v_cmp_gt_i32 vcc, 3, v211\n"
  "s_nop 1\n"
  "v_cndmask_b32 v15, v15, v246, vcc\n"
  "v_max_f32 v220, v220, v15\n"
  "v_cmp_gt_i32 vcc, 16, v211\n"
  "s_nop 1\n"
  "v_cndmask_b32 v28, v28, v246, vcc\n"
  "v_max_f32 v220, v220, v28\n"
  "v_cmp_gt_i32 vcc, 17, v211\n"
  "s_nop 1\n"
  "v_cndmask_b32 v29, v29, v246, vcc\n"
  "v_max_f32 v220, v220, v29\n"
  "v_cmp_gt_i32 vcc, 18, v211\n"
  "s_nop 1\n"
  "v_cndmask_b32 v30, v30, v246, vcc\n"
  "v_max_f32 v220, v220, v30\n"
  "v_cmp_gt_i32 vcc, 19, v211\n"
  "s_nop 1\n"
  "v_cndmask_b32 v31, v31, v246, vcc\n"
  "v_max_f32 v220, v220, v31\n"
  "v_cmp_gt_i32 vcc, 32, v211\n"
  "s_nop 1\n"
  "v_cndmask_b32 v44, v44, v246, vcc\n"
  "v_max_f32 v220, v220, v44\n"
  "v_cmp_gt_i32 vcc, 33, v211\n"
  "s_nop 1\n"
  "v_cndmask_b32 v45, v45, v246, vcc\n"
  "v_max_f32 v220, v220, v45\n"
  "v_cmp_gt_i32 vcc, 34, v211\n"
  "s_nop 1\n"
  "v_cndmask_b32 v46, v46, v246, vcc\n"
  "v_max_f32 v220, v220, v46\n"
  "v_cmp_gt_i32 vcc, 35, v211\n"
  "s_nop 1\n"
  "v_cndmask_b32 v47, v47, v246, vcc\n"
  "v_max_f32 v220, v220, v47\n"
  "v_cmp_gt_i32 vcc, 48, v211\n"
  "s_nop 1\n"
  "v_cndmask_b32 v60, v60, v246, vcc\n"
  "v_max_f32 v220, v220, v60\n"
  "v_cmp_gt_i32 vcc, 49, v211\n"
  "s_nop 1\n"
  "v_cndmask_b32 v61, v61, v246, vcc\n"
  "v_max_f32 v220, v220, v61\n"
  "v_cmp_gt_i32 vcc, 50, v211\n"
  "s_nop 1\n"
  "v_cndmask_b32 v62, v62, v246, vcc\n"
  "v_max_f32 v220, v220, v62\n"
  "v_cmp_gt_i32 vcc, 51, v211\n"
  "s_nop 1\n"
  "v_cndmask_b32 v63, v63, v246, vcc\n"
  "v_max_f32 v220, v220, v63\n"
  "v_mov_b32 v221, v220\n"
  "s_nop 1\n"
  "v_permlane16_swap_b32 v220, v221\n"
  "v_max_f32 v220, v220, v221\n"
  "v_mov_b32 v221, v220\n"
  "s_nop 1\n"
  "v_permlane32_swap_b32 v220, v221\n"
  "v_max_f32 v220, v220, v221\n"
  "v_sub_f32 v216, v215, v220\n"
  "v_exp_f32 v216, v216\n"
  "v_mov_b32 v215, v220\n"
  "v_subrev_u32 v211, 64, v211\n"
  "v_mul_f32 v236, v236, v216\n"
  "v_mul_f32 v237, v237, v216\n"
  "v_mul_f32 v238, v238, v216\n"
  "v_mul_f32 v239, v239, v216\n"
  "v_accvgpr_read_b32 v221, a96\n"
  "v_mul_f32 v221, v221, v216\n"
  "v_accvgpr_write_b32 a96, v221\n"
  "v_accvgpr_read_b32 v221, a97\n"
  "v_mul_f32 v221, v221, v216\n"
  "v_accvgpr_write_b32 a97, v221\n"
  "v_accvgpr_read_b32 v221, a98\n"
  "v_mul_f32 v221, v221, v216\n"
  "v_accvgpr_write_b32 a98, v221\n"
  "v_accvgpr_read_b32 v221, a99\n"
  "v_mul_f32 v221, v221, v216\n"
  "v_accvgpr_write_b32 a99, v221\n"
  "v_accvgpr_read_b32 v221, a100\n"
  "v_mul_f32 v221, v221, v216\n"
  "v_accvgpr_write_b32 a100, v221\n"
  "v_accvgpr_read_b32 v221, a101\n"
  "v_mul_f32 v221, v221, v216\n"
  "v_accvgpr_write_b32 a101, v221\n"
  "v_accvgpr_read_b32 v221, a102\n"
  "v_mul_f32 v221, v221, v216\n"
  "v_accvgpr_write_b32 a102, v221\n"
  "v_accvgpr_read_b32 v221, a103\n"
  "v_mul_f32 v221, v221, v216\n"
  "v_accvgpr_write_b32 a103, v221\n"
  "v_accvgpr_read_b32 v221, a104\n"
  "v_mul_f32 v221, v221, v216\n"
  "v_accvgpr_write_b32 a104, v221\n"
  "v_accvgpr_read_b32 v221, a105\n"
  "v_mul_f32 v221, v221, v216\n"
  "v_accvgpr_write_b32 a105, v221\n"
  "v_accvgpr_read_b32 v221, a106\n"
  "v_mul_f32 v221, v221, v216\n"
  "v_accvgpr_write_b32 a106, v221\n"
  "v_accvgpr_read_b32 v221, a107\n"
  "v_mul_f32 v221, v221, v216\n"
  "v_accvgpr_write_b32 a107, v221\n"
  "v_accvgpr_read_b32 v221, a108\n"
  "v_mul_f32 v221, v221, v216\n"
  "v_accvgpr_write_b32 a108, v221\n"
  "v_accvgpr_read_b32 v221, a109\n"
  "v_mul_f32 v221, v221, v216\n"
  "v_accvgpr_write_b32 a109, v221\n"
  "v_accvgpr_read_b32 v221, a110\n"
  "v_mul_f32 v221, v221, v216\n"
  "v_accvgpr_write_b32 a110, v221\n"
  "v_accvgpr_read_b32 v221, a111\n"
  "v_mul_f32 v221, v221, v216\n"
  "v_accvgpr_write_b32 a111, v221\n"
  "v_accvgpr_read_b32 v221, a112\n"
  "v_mul_f32 v221, v221, v216\n"
  "v_accvgpr_write_b32 a112, v221\n"
  "v_accvgpr_read_b32 v221, a113\n"
  "v_mul_f32 v221, v221, v216\n"
  "v_accvgpr_write_b32 a113, v221\n"
  "v_accvgpr_read_b32 v221, a114\n"
  "v_mul_f32 v221, v221, v216\n"
  "v_accvgpr_write_b32 a114, v221\n"
  "v_accvgpr_read_b32 v221, a115\n"
  "v_mul_f32 v221, v221, v216\n"
  "v_accvgpr_write_b32 a115, v221\n"
  "v_accvgpr_read_b32 v221, a116\n"
  "v_mul_f32 v221, v221, v216\n"
  "v_accvgpr_write_b32 a116, v221\n"
  "v_accvgpr_read_b32 v221, a117\n"
  "v_mul_f32 v221, v221, v216\n"
  "v_accvgpr_write_b32 a117, v221\n"
  "v_accvgpr_read_b32 v221, a118\n"
  "v_mul_f32 v221, v221, v216\n"
  "v_accvgpr_write_b32 a118, v221\n"
  "v_accvgpr_read_b32 v221, a119\n"
  "v_mul_f32 v221, v221, v216\n"
  "v_accvgpr_write_b32 a119, v221\n"
  "v_accvgpr_read_b32 v221, a120\n"
  "v_mul_f32 v221, v221, v216\n"
  "v_accvgpr_write_b32 a120, v221\n"
  "v_accvgpr_read_b32 v221, a121\n"
  "v_mul_f32 v221, v221, v216\n"
  "v_accvgpr_write_b32 a121, v221\n"
  "v_accvgpr_read_b32 v221, a122\n"
  "v_mul_f32 v221, v221, v216\n"
  "v_accvgpr_write_b32 a122, v221\n"
  "v_accvgpr_read_b32 v221, a123\n"
  "v_mul_f32 v221, v221, v216\n"
  "v_accvgpr_write_b32 a123, v221\n"
  "v_accvgpr_read_b32 v221, a124\n"
  "v_mul_f32 v221, v221, v216\n"
  "v_accvgpr_write_b32 a124, v221\n"
  "v_accvgpr_read_b32 v221, a125\n"
  "v_mul_f32 v221, v221, v216\n"
  "v_accvgpr_write_b32 a125, v221\n"
  "v_accvgpr_read_b32 v221, a126\n"
  "v_mul_f32 v221, v221, v216\n"
  "v_accvgpr_write_b32 a126, v221\n"
  "v_accvgpr_read_b32 v221, a127\n"
  "v_mul_f32 v221, v221, v216\n"
  "v_accvgpr_write_b32 a127, v221\n"
  "v_sub_f32 v12, v12, v220\n"
  "v_exp_f32 v12, v12\n"
  "v_sub_f32 v13, v13, v220\n"
  "v_exp_f32 v13, v13\n"
  "v_sub_f32 v14, v14, v220\n"
  "v_exp_f32 v14, v14\n"
  "v_sub_f32 v15, v15, v220\n"
  "v_exp_f32 v15, v15\n"
  "v_cvt_pk_bf16_f32 v76, v12, v13\n"
  "v_cvt_pk_bf16_f32 v77, v14, v15\n"
  "v_sub_f32 v28, v28, v220\n"
  "v_exp_f32 v28, v28\n"
  "v_sub_f32 v29, v29, v220\n"
  "v_exp_f32 v29, v29\n"
  "v_sub_f32 v30, v30, v220\n"
  "v_exp_f32 v30, v30\n"
  "v_sub_f32 v31, v31, v220\n"
  "v_exp_f32 v31, v31\n"
  "v_cvt_pk_bf16_f32 v78, v28, v29\n"
  "v_cvt_pk_bf16_f32 v79, v30, v31\n"
  "v_sub_f32 v44, v44, v220\n"
  "v_exp_f32 v44, v44\n"
  "v_sub_f32 v45, v45, v220\n"
  "v_exp_f32 v45, v45\n"
  "v_sub_f32 v46, v46, v220\n"
  "v_exp_f32 v46, v46\n"
  "v_sub_f32 v47, v47, v220\n"
  "v_exp_f32 v47, v47\n"
  "v_cvt_pk_bf16_f32 v92, v44, v45\n"
  "v_cvt_pk_bf16_f32 v93, v46, v47\n"
  "v_sub_f32 v60, v60, v220\n"
  "v_exp_f32 v60, v60\n"
  "v_sub_f32 v61, v61, v220\n"
  "v_exp_f32 v61, v61\n"
  "v_sub_f32 v62, v62, v220\n"
  "v_exp_f32 v62, v62\n"
  "v_sub_f32 v63, v63, v220\n"
  "v_exp_f32 v63, v63\n"
  "v_cvt_pk_bf16_f32 v94, v60, v61\n"
  "v_cvt_pk_bf16_f32 v95, v62, v63\n"
  "v_mfma_f32_16x16x32_bf16 v[224:227], v[240:243], v[64:67], v[224:227]\n"
  "v_mfma_f32_16x16x32_bf16 v[224:227], v[240:243], v[80:83], v[224:227]\n"
  "v_mfma_f32_16x16x32_bf16 a[0:3], a[192:195], v[64:67], a[0:3]\n"
  "v_mfma_f32_16x16x32_bf16 a[0:3], a[196:199], v[80:83], a[0:3]\n"
  "v_mfma_f32_16x16x32_bf16 a[4:7], a[200:203], v[64:67], a[4:7]\n"
  "v_mfma_f32_16x16x32_bf16 a[4:7], a[204:207], v[80:83], a[4:7]\n"
  "v_mfma_f32_16x16x32_bf16 a[8:11], a[208:211], v[64:67], a[8:11]\n"
  "v_mfma_f32_16x16x32_bf16 a[8:11], a[212:215], v[80:83], a[8:11]\n"
  "v_mfma_f32_16x16x32_bf16 a[12:15], a[216:219], v[64:67], a[12:15]\n"
  "v_mfma_f32_16x16x32_bf16 a[12:15], a[220:223], v[80:83], a[12:15]\n"
  "v_mfma_f32_16x16x32_bf16 a[16:19], a[224:227], v[64:67], a[16:19]\n"
  "v_mfma_f32_16x16x32_bf16 a[16:19], a[228:231], v[80:83], a[16:19]\n"
  "v_mfma_f32_16x16x32_bf16 a[20:23], a[232:235], v[64:67], a[20:23]\n"
  "v_mfma_f32_16x16x32_bf16 a[20:23], a[236:239], v[80:83], a[20:23]\n"
  "v_mfma_f32_16x16x32_bf16 a[24:27], a[240:243], v[64:67], a[24:27]\n"
  "v_mfma_f32_16x16x32_bf16 a[24:27], a[244:247], v[80:83], a[24:27]\n"
  "v_mfma_f32_16x16x32_bf16 a[28:31], a[248:251], v[64:67], a[28:31]\n"
  "v_mfma_f32_16x16x32_bf16 a[28:31], a[252:255], v[80:83], a[28:31]\n"
  "v_mfma_f32_16x16x32_bf16 v[228:231], v[240:243], v[68:71], v[228:231]\n"
  "v_mfma_f32_16x16x32_bf16 v[228:231], v[240:243], v[84:87], v[228:231]\n"
  "v_mfma_f32_16x16x32_bf16 a[32:35], a[192:195], v[68:71], a[32:35]\n"
  "v_mfma_f32_16x16x32_bf16 a[32:35], a[196:199], v[84:87], a[32:35]\n"
  "v_mfma_f32_16x16x32_bf16 a[36:39], a[200:203], v[68:71], a[36:39]\n"
  "v_mfma_f32_16x16x32_bf16 a[36:39], a[204:207], v[84:87], a[36:39]\n"
  "v_mfma_f32_16x16x32_bf16 a[40:43], a[208:211], v[68:71], a[40:43]\n"
  "v_mfma_f32_16x16x32_bf16 a[40:43], a[212:215], v[84:87], a[40:43]\n"
  "v_mfma_f32_16x16x32_bf16 a[44:47], a[216:219], v[68:71], a[44:47]\n"
  "v_mfma_f32_16x16x32_bf16 a[44:47], a[220:223], v[84:87], a[44:47]\n"
  "v_mfma_f32_16x16x32_bf16 a[48:51], a[224:227], v[68:71], a[48:51]\n"
  "v_mfma_f32_16x16x32_bf16 a[48:51], a[228:231], v[84:87], a[48:51]\n"
  "v_mfma_f32_16x16x32_bf16 a[52:55], a[232:235], v[68:71], a[52:55]\n"
  "v_mfma_f32_16x16x32_bf16 a[52:55], a[236:239], v[84:87], a[52:55]\n"
  "v_mfma_f32_16x16x32_bf16 a[56:59], a[240:243], v[68:71], a[56:59]\n"
  "v_mfma_f32_16x16x32_bf16 a[56:59], a[244:247], v[84:87], a[56:59]\n"
  "v_mfma_f32_16x16x32_bf16 a[60:63], a[248:251], v[68:71], a[60:63]\n"
  "v_mfma_f32_16x16x32_bf16 a[60:63], a[252:255], v[84:87], a[60:63]\n"
  "v_mfma_f32_16x16x32_bf16 v[232:235], v[240:243], v[72:75], v[232:235]\n"
  "v_mfma_f32_16x16x32_bf16 v[232:235], v[240:243], v[88:91], v[232:235]\n"
  "v_mfma_f32_16x16x32_bf16 a[64:67], a[192:195], v[72:75], a[64:67]\n"
  "v_mfma_f32_16x16x32_bf16 a[64:67], a[196:199], v[88:91], a[64:67]\n"
  "v_mfma_f32_16x16x32_bf16 a[68:71], a[200:203], v[72:75], a[68:71]\n"
  "v_mfma_f32_16x16x32_bf16 a[68:71], a[204:207], v[88:91], a[68:71]\n"
  "v_mfma_f32_16x16x32_bf16 a[72:75], a[208:211], v[72:75], a[72:75]\n"
  "v_mfma_f32_16x16x32_bf16 a[72:75], a[212:215], v[88:91], a[72:75]\n"
  "v_mfma_f32_16x16x32_bf16 a[76:79], a[216:219], v[72:75], a[76:79]\n"
  "v_mfma_f32_16x16x32_bf16 a[76:79], a[220:223], v[88:91], a[76:79]\n"
  "v_mfma_f32_16x16x32_bf16 a[80:83], a[224:227], v[72:75], a[80:83]\n"
  "v_mfma_f32_16x16x32_bf16 a[80:83], a[228:231], v[88:91], a[80:83]\n"
  "v_mfma_f32_16x16x32_bf16 a[84:87], a[232:235], v[72:75], a[84:87]\n"
  "v_mfma_f32_16x16x32_bf16 a[84:87], a[236:239], v[88:91], a[84:87]\n"
  "v_mfma_f32_16x16x32_bf16 a[88:91], a[240:243], v[72:75], a[88:91]\n"
  "v_mfma_f32_16x16x32_bf16 a[88:91], a[244:247], v[88:91], a[88:91]\n"
  "v_mfma_f32_16x16x32_bf16 a[92:95], a[248:251], v[72:75], a[92:95]\n"
  "v_mfma_f32_16x16x32_bf16 a[92:95], a[252:255], v[88:91], a[92:95]\n"
  "v_mfma_f32_16x16x32_bf16 v[236:239], v[240:243], v[76:79], v[236:239]\n"
  "v_mfma_f32_16x16x32_bf16 v[236:239], v[240:243], v[92:95], v[236:239]\n"
  "v_mfma_f32_16x16x32_bf16 a[96:99], a[192:195], v[76:79], a[96:99]\n"
  "v_mfma_f32_16x16x32_bf16 a[96:99], a[196:199], v[92:95], a[96:99]\n"
  "v_mfma_f32_16x16x32_bf16 a[100:103], a[200:203], v[76:79], a[100:103]\n"
  "v_mfma_f32_16x16x32_bf16 a[100:103], a[204:207], v[92:95], a[100:103]\n"
  "v_mfma_f32_16x16x32_bf16 a[104:107], a[208:211], v[76:79], a[104:107]\n"
  "v_mfma_f32_16x16x32_bf16 a[104:107], a[212:215], v[92:95], a[104:107]\n"
  "v_mfma_f32_16x16x32_bf16 a[108:111], a[216:219], v[76:79], a[108:111]\n"
  "v_mfma_f32_16x16x32_bf16 a[108:111], a[220:223], v[92:95], a[108:111]\n"
  "v_mfma_f32_16x16x32_bf16 a[112:115], a[224:227], v[76:79], a[112:115]\n"
  "v_mfma_f32_16x16x32_bf16 a[112:115], a[228:231], v[92:95], a[112:115]\n"
  "v_mfma_f32_16x16x32_bf16 a[116:119], a[232:235], v[76:79], a[116:119]\n"
  "v_mfma_f32_16x16x32_bf16 a[116:119], a[236:239], v[92:95], a[116:119]\n"
  "v_mfma_f32_16x16x32_bf16 a[120:123], a[240:243], v[76:79], a[120:123]\n"
  "v_mfma_f32_16x16x32_bf16 a[120:123], a[244:247], v[92:95], a[120:123]\n"
  "v_mfma_f32_16x16x32_bf16 a[124:127], a[248:251], v[76:79], a[124:127]\n"
  "v_mfma_f32_16x16x32_bf16 a[124:127], a[252:255], v[92:95], a[124:127]\n"
  "s_nop 7\n"
  "s_add_u32 s96, s96, 0x8000\n"
  "s_cmp_lt_u32 s96, s46\n"
  "s_cbranch_scc1 mk_slow_l\n"
  "s_bitcmp0_b32 s10, 0\n"
  "s_cbranch_scc1 mk_slow_r\n"
  "v_rcp_f32 v226, v224\n"
  "v_rcp_f32 v230, v228\n"
  "s_and_b32 s61, s53, 14\n"
  "s_and_b32 s63, s53, 15\n"
  "s_add_u32 s61, s61, s63\n"
  "s_lshl_b32 s61, s61, 12\n"
  "s_add_u32 s59, s40, s61\n"
  "v_accvgpr_read_b32 v192, a0\n"
  "v_accvgpr_read_b32 v193, a1\n"
  "v_accvgpr_read_b32 v194, a2\n"
  "v_accvgpr_read_b32 v195, a3\n"
  "v_accvgpr_read_b32 v196, a4\n"
  "v_accvgpr_read_b32 v197, a5\n"
  "v_accvgpr_read_b32 v198, a6\n"
  "v_accvgpr_read_b32 v199, a7\n"
  "v_pk_mul_f32 v[192:193], v[192:193], v[226:227] op_sel_hi:[1,0]\n"
  "v_pk_mul_f32 v[194:195], v[194:195], v[226:227] op_sel_hi:[1,0]\n"
  "v_pk_mul_f32 v[196:197], v[196:197], v[226:227] op_sel_hi:[1,0]\n"
  "v_pk_mul_f32 v[198:199], v[198:199], v[226:227] op_sel_hi:[1,0]\n"
  "v_cvt_pk_bf16_f32 v128, v192, v193\n"
  "v_cvt_pk_bf16_f32 v129, v194, v195\n"
  "v_cvt_pk_bf16_f32 v130, v196, v197\n"
  "v_cvt_pk_bf16_f32 v131, v198, v199\n"
  "buffer_store_dwordx4 v[128:131], v244, s[48:51], s59 offen offset:0 sc1\n"
  "v_accvgpr_read_b32 v4, a8\n"
  "v_accvgpr_read_b32 v5, a9\n"
  "v_accvgpr_read_b32 v6, a10\n"
  "v_accvgpr_read_b32 v7, a11\n"
  "v_accvgpr_read_b32 v20, a12\n"
  "v_accvgpr_read_b32 v21, a13\n"
  "v_accvgpr_read_b32 v22, a14\n"
  "v_accvgpr_read_b32 v23, a15\n"
  "v_pk_mul_f32 v[4:5], v[4:5], v[226:227] op_sel_hi:[1,0]\n"
  "v_pk_mul_f32 v[6:7], v[6:7], v[226:227] op_sel_hi:[1,0]\n"
  "v_pk_mul_f32 v[20:21], v[20:21], v[226:227] op_sel_hi:[1,0]\n"
  "v_pk_mul_f32 v[22:23], v[22:23], v[226:227] op_sel_hi:[1,0]\n"
  "v_cvt_pk_bf16_f32 v132, v4, v5\n"
  "v_cvt_pk_bf16_f32 v133, v6, v7\n"
  "v_cvt_pk_bf16_f32 v134, v20, v21\n"
  "v_cvt_pk_bf16_f32 v135, v22, v23\n"
  "buffer_store_dwordx4 v[132:135], v244, s[48:51], s59 offen offset:1024 sc1\n"
  "v_accvgpr_read_b32 v192, a16\n"
  "v_accvgpr_read_b32 v193, a17\n"
  "v_accvgpr_read_b32 v194, a18\n"
  "v_accvgpr_read_b32 v195, a19\n"
  "v_accvgpr_read_b32 v196, a20\n"
  "v_accvgpr_read_b32 v197, a21\n"
  "v_accvgpr_read_b32 v198, a22\n"
  "v_accvgpr_read_b32 v199, a23\n"
  "v_pk_mul_f32 v[192:193], v[192:193], v[226:227] op_sel_hi:[1,0]\n"
  "v_pk_mul_f32 v[194:195], v[194:195], v[226:227] op_sel_hi:[1,0]\n"
  "v_pk_mul_f32 v[196:197], v[196:197], v[226:227] op_sel_hi:[1,0]\n"
  "v_pk_mul_f32 v[198:199], v[198:199], v[226:227] op_sel_hi:[1,0]\n"
  "v_cvt_pk_bf16_f32 v136, v192, v193\n"
  "v_cvt_pk_bf16_f32 v137, v194, v195\n"
  "v_cvt_pk_bf16_f32 v138, v196, v197\n"
  "v_cvt_pk_bf16_f32 v139, v198, v199\n"
  "buffer_store_dwordx4 v[136:139], v244, s[48:51], s59 offen offset:2048 sc1\n"
  "v_accvgpr_read_b32 v4, a24\n"
  "v_accvgpr_read_b32 v5, a25\n"
  "v_accvgpr_read_b32 v6, a26\n"
  "v_accvgpr_read_b32 v7, a27\n"
  "v_accvgpr_read_b32 v20, a28\n"
  "v_accvgpr_read_b32 v21, a29\n"
  "v_accvgpr_read_b32 v22, a30\n"
  "v_accvgpr_read_b32 v23, a31\n"
  "v_pk_mul_f32 v[4:5], v[4:5], v[226:227] op_sel_hi:[1,0]\n"
  "v_pk_mul_f32 v[6:7], v[6:7], v[226:227] op_sel_hi:[1,0]\n"
  "v_pk_mul_f32 v[20:21], v[20:21], v[226:227] op_sel_hi:[1,0]\n"
  "v_pk_mul_f32 v[22:23], v[22:23], v[226:227] op_sel_hi:[1,0]\n"
  "v_cvt_pk_bf16_f32 v140, v4, v5\n"
  "v_cvt_pk_bf16_f32 v141, v6, v7\n"
  "v_cvt_pk_bf16_f32 v142, v20, v21\n"
  "v_cvt_pk_bf16_f32 v143, v22, v23\n"
  "buffer_store_dwordx4 v[140:143], v244, s[48:51], s59 offen offset:3072 sc1\n"
  "s_add_u32 s59, s41, s61\n"
  "v_accvgpr_read_b32 v192, a32\n"
  "v_accvgpr_read_b32 v193, a33\n"
  "v_accvgpr_read_b32 v194, a34\n"
  "v_accvgpr_read_b32 v195, a35\n"
  "v_accvgpr_read_b32 v196, a36\n"
  "v_accvgpr_read_b32 v197, a37\n"
  "v_accvgpr_read_b32 v198, a38\n"
  "v_accvgpr_read_b32 v199, a39\n"
  "v_pk_mul_f32 v[192:193], v[192:193], v[230:231] op_sel_hi:[1,0]\n"
  "v_pk_mul_f32 v[194:195], v[194:195], v[230:231] op_sel_hi:[1,0]\n"
  "v_pk_mul_f32 v[196:197], v[196:197], v[230:231] op_sel_hi:[1,0]\n"
  "v_pk_mul_f32 v[198:199], v[198:199], v[230:231] op_sel_hi:[1,0]\n"
  "v_cvt_pk_bf16_f32 v144, v192, v193\n"
  "v_cvt_pk_bf16_f32 v145, v194, v195\n"
  "v_cvt_pk_bf16_f32 v146, v196, v197\n"
  "v_cvt_pk_bf16_f32 v147, v198, v199\n"
  "buffer_store_dwordx4 v[144:147], v244, s[48:51], s59 offen offset:0 sc1\n"
  "v_accvgpr_read_b32 v4, a40\n"
  "v_accvgpr_read_b32 v5, a41\n"
  "v_accvgpr_read_b32 v6, a42\n"
  "v_accvgpr_read_b32 v7, a43\n"
  "v_accvgpr_read_b32 v20, a44\n"
  "v_accvgpr_read_b32 v21, a45\n"
  "v_accvgpr_read_b32 v22, a46\n"
  "v_accvgpr_read_b32 v23, a47\n"
  "v_pk_mul_f32 v[4:5], v[4:5], v[230:231] op_sel_hi:[1,0]\n"
  "v_pk_mul_f32 v[6:7], v[6:7], v[230:231] op_sel_hi:[1,0]\n"
  "v_pk_mul_f32 v[20:21], v[20:21], v[230:231] op_sel_hi:[1,0]\n"
  "v_pk_mul_f32 v[22:23], v[22:23], v[230:231] op_sel_hi:[1,0]\n"
  "v_cvt_pk_bf16_f32 v148, v4, v5\n"
  "v_cvt_pk_bf16_f32 v149, v6, v7\n"
  "v_cvt_pk_bf16_f32 v150, v20, v21\n"
  "v_cvt_pk_bf16_f32 v151, v22, v23\n"
  "buffer_store_dwordx4 v[148:151], v244, s[48:51], s59 offen offset:1024 sc1\n"
  "v_accvgpr_read_b32 v192, a48\n"
  "v_accvgpr_read_b32 v193, a49\n"
  "v_accvgpr_read_b32 v194, a50\n"
  "v_accvgpr_read_b32 v195, a51\n"
  "v_accvgpr_read_b32 v196, a52\n"
  "v_accvgpr_read_b32 v197, a53\n"
  "v_accvgpr_read_b32 v198, a54\n"
  "v_accvgpr_read_b32 v199, a55\n"
  "v_pk_mul_f32 v[192:193], v[192:193], v[230:231] op_sel_hi:[1,0]\n"
  "v_pk_mul_f32 v[194:195], v[194:195], v[230:231] op_sel_hi:[1,0]\n"
  "v_pk_mul_f32 v[196:197], v[196:197], v[230:231] op_sel_hi:[1,0]\n"
  "v_pk_mul_f32 v[198:199], v[198:199], v[230:231] op_sel_hi:[1,0]\n"
  "v_cvt_pk_bf16_f32 v152, v192, v193\n"
  "v_cvt_pk_bf16_f32 v153, v194, v195\n"
  "v_cvt_pk_bf16_f32 v154, v196, v197\n"
  "v_cvt_pk_bf16_f32 v155, v198, v199\n"
  "buffer_store_dwordx4 v[152:155], v244, s[48:51], s59 offen offset:2048 sc1\n"
  "v_accvgpr_read_b32 v4, a56\n"
  "v_accvgpr_read_b32 v5, a57\n"
  "v_accvgpr_read_b32 v6, a58\n"
  "v_accvgpr_read_b32 v7, a59\n"
  "v_accvgpr_read_b32 v20, a60\n"
  "v_accvgpr_read_b32 v21, a61\n"
  "v_accvgpr_read_b32 v22, a62\n"
  "v_accvgpr_read_b32 v23, a63\n"
  "v_pk_mul_f32 v[4:5], v[4:5], v[230:231] op_sel_hi:[1,0]\n"
  "v_pk_mul_f32 v[6:7], v[6:7], v[230:231] op_sel_hi:[1,0]\n"
  "v_pk_mul_f32 v[20:21], v[20:21], v[230:231] op_sel_hi:[1,0]\n"
  "v_pk_mul_f32 v[22:23], v[22:23], v[230:231] op_sel_hi:[1,0]\n"
  "v_cvt_pk_bf16_f32 v156, v4, v5\n"
  "v_cvt_pk_bf16_f32 v157, v6, v7\n"
  "v_cvt_pk_bf16_f32 v158, v20, v21\n"
  "v_cvt_pk_bf16_f32 v159, v22, v23\n"
  "buffer_store_dwordx4 v[156:159], v244, s[48:51], s59 offen offset:3072 sc1\n"
  "s_waitcnt vmcnt(0)\n"
  "buffer_atomic_add v248, v253, s[48:51], s11 offen sc1\n"
  "mk_slow_r:\n"
  "v_bfe_u32 v192, v244, 6, 4\n"
  "v_bfe_u32 v193, v244, 1, 5\n"
  "v_lshl_add_u32 v193, v192, 8, v193\n"
  "v_add_u32 v193, 0x20000, v193\n"
  "v_and_b32 v192, 7, v192\n"
  "v_xor_b32 v194, 0, v192\n"
  "v_lshl_add_u32 v224, v194, 5, v193\n"
  "v_xor_b32 v194, 1, v192\n"
  "v_lshl_add_u32 v225, v194, 5, v193\n"
  "v_xor_b32 v194, 2, v192\n"
  "v_lshl_add_u32 v226, v194, 5, v193\n"
  "v_xor_b32 v194, 3, v192\n"
  "v_lshl_add_u32 v227, v194, 5, v193\n"
  "v_xor_b32 v194, 4, v192\n"
  "v_lshl_add_u32 v228, v194, 5, v193\n"
  "v_xor_b32 v194, 5, v192\n"
  "v_lshl_add_u32 v229, v194, 5, v193\n"
  "v_xor_b32 v194, 6, v192\n"
  "v_lshl_add_u32 v230, v194, 5, v193\n"
  "v_xor_b32 v194, 7, v192\n"
  "v_lshl_add_u32 v231, v194, 5, v193\n"
  "v_mov_b32 v39, 0\n"
  "s_branch mk_norm\n"
  :: "s"(__builtin_amdgcn_kernarg_segment_ptr()), "s"(wid), "s"(blockIdx.x), "s"((unsigned)(size_t)lds)
  : "memory", "vcc", "scc", "v0","v1","v2","v3","v4","v5","v6","v7","v8","v9","v10","v11","v12","v13","v14","v15","v16","v17","v18","v19","v20","v21","v22","v23","v24","v25","v26","v27","v28","v29","v30","v31","v32","v33","v34","v35","v36","v37","v38","v39","v40","v41","v42","v43","v44","v45","v46","v47","v48","v49","v50","v51","v52","v53","v54","v55","v56","v57","v58","v59","v60","v61","v62","v63","v64","v65","v66","v67","v68","v69","v70","v71","v72","v73","v74","v75","v76","v77","v78","v79","v80","v81","v82","v83","v84","v85","v86","v87","v88","v89","v90","v91","v92","v93","v94","v95","v96","v97","v98","v99","v100","v101","v102","v103","v104","v105","v106","v107","v108","v109","v110","v111","v112","v113","v114","v115","v116","v117","v118","v119","v120","v121","v122","v123","v124","v125","v126","v127","v128","v129","v130","v131","v132","v133","v134","v135","v136","v137","v138","v139","v140","v141","v142","v143","v144","v145","v146","v147","v148","v149","v150","v151","v152","v153","v154","v155","v156","v157","v158","v159","v160","v161","v162","v163","v164","v165","v166","v167","v168","v169","v170","v171","v172","v173","v174","v175","v176","v177","v178","v179","v180","v181","v182","v183","v184","v185","v186","v187","v188","v189","v190","v191","v192","v193","v194","v195","v196","v197","v198","v199","v200","v201","v202","v203","v204","v205","v206","v207","v208","v209","v210","v211","v212","v213","v214","v215","v216","v217","v218","v219","v220","v221","v222","v223","v224","v225","v226","v227","v228","v229","v230","v231","v232","v233","v234","v235","v236","v237","v238","v239","v240","v241","v242","v243","v244","v245","v246","v247","v248","v249","v250","v251","v252","v253","v254","v255","a0","a1","a2","a3","a4","a5","a6","a7","a8","a9","a10","a11","a12","a13","a14","a15","a16","a17","a18","a19","a20","a21","a22","a23","a24","a25","a26","a27","a28","a29","a30","a31","a32","a33","a34","a35","a36","a37","a38","a39","a40","a41","a42","a43","a44","a45","a46","a47","a48","a49","a50","a51","a52","a53","a54","a55","a56","a57","a58","a59","a60","a61","a62","a63","a64","a65","a66","a67","a68","a69","a70","a71","a72","a73","a74","a75","a76","a77","a78","a79","a80","a81","a82","a83","a84","a85","a86","a87","a88","a89","a90","a91","a92","a93","a94","a95","a96","a97","a98","a99","a100","a101","a102","a103","a104","a105","a106","a107","a108","a109","a110","a111","a112","a113","a114","a115","a116","a117","a118","a119","a120","a121","a122","a123","a124","a125","a126","a127","a128","a129","a130","a131","a132","a133","a134","a135","a136","a137","a138","a139","a140","a141","a142","a143","a144","a145","a146","a147","a148","a149","a150","a151","a152","a153","a154","a155","a156","a157","a158","a159","a160","a161","a162","a163","a164","a165","a166","a167","a168","a169","a170","a171","a172","a173","a174","a175","a176","a177","a178","a179","a180","a181","a182","a183","a184","a185","a186","a187","a188","a189","a190","a191","a192","a193","a194","a195","a196","a197","a198","a199","a200","a201","a202","a203","a204","a205","a206","a207","a208","a209","a210","a211","a212","a213","a214","a215","a216","a217","a218","a219","a220","a221","a222","a223","a224","a225","a226","a227","a228","a229","a230","a231","a232","a233","a234","a235","a236","a237","a238","a239","a240","a241","a242","a243","a244","a245","a246","a247","a248","a249","a250","a251","a252","a253","a254","a255","s5","s6","s7","s8","s9","s10","s11","s12","s13","s14","s15","s16","s17","s18","s19","s20","s21","s22","s23","s24","s25","s26","s27","s28","s29","s30","s31","s33","s34","s35","s36","s37","s38","s39","s40","s41","s42","s43","s44","s45","s46","s47","s48","s49","s50","s51","s52","s53","s54","s55","s56","s57","s58","s59","s60","s61","s62","s63","s64","s65","s66","s67","s68","s69","s70","s71","s72","s73","s74","s75","s76","s77","s78","s79","s80","s81","s82","s83","s84","s85","s86","s87","s88","s89","s90","s91","s92","s93","s94","s95","s96","s97","s98","s99");
}
extern "C" void kernel_launch(void* const* d_in, const int* in_sizes, int n_in, void* d_out, int out_size, void* d_ws, size_t ws_size, hipStream_t stream) {
  fused_block<<<256, 256, 0, stream>>>(d_in[0], d_in[1], d_in[2], d_in[3], d_in[4], d_in[5], d_in[7], d_in[8], d_in[9], d_in[10], d_out, d_ws);
}
```
